```python
import torch

def _softmax(x, axis=-1):
    return torch.softmax(x, dim=axis)

def _gelu(x):
    return torch.nn.functional.gelu(x, approximate='tanh')

def _silu(x):
    return torch.nn.functional.silu(x)

def _sigmoid(x):
    return torch.sigmoid(x)

def _layernorm(x, axis=-1, eps=1e-05):
    m = x.mean(dim=axis, keepdim=True)
    v = x.var(dim=axis, keepdim=True, unbiased=False)
    return (x - m) / torch.sqrt(v + eps)

def _scatter_add(tgt, idx, src, axis=0):
    return tgt.clone().index_add_(axis, idx, src)

def _segment_sum(src, seg, n):
    out = torch.zeros((n,) + src.shape[1:], dtype=src.dtype)
    return out.index_add_(0, seg, src)

def _pad(x, pw):
    flat = tuple((v for pair in reversed(pw) for v in pair))
    return torch.nn.functional.pad(x, flat)
kernel_name = 'adaLN_cond__compressed_kv_cla__mixer_s_mlp__prompt'

def setup_inputs():

    _w = lambda a: (lambda t: t.to(torch.bfloat16) if t.is_floating_point() else t)(torch.as_tensor(a))

    def _A_setup():
        B, H, Sq, Sk, D, M = (1, 32, 2048, 2048, 128, 640)
        Dc = 4 * D
        return {'x': _w((lambda *s: torch.randn(*s, dtype=torch.bfloat16) * 0.05)(B, Sq, M)), 'c_kv': _w((lambda *s: torch.randn(*s, dtype=torch.bfloat16) * 1.6)(B, Sk, Dc)), 'w_q': _w((lambda *s: torch.randn(*s, dtype=torch.bfloat16) * 0.8)(M, H, D) * M ** (-0.5)), 'w_uk': _w((lambda *s: torch.randn(*s, dtype=torch.bfloat16) * 0.8)(Dc, H, D) * Dc ** (-0.5)), 'w_uv': _w((lambda *s: torch.randn(*s, dtype=torch.bfloat16) * 1.6)(Dc, H, D) * Dc ** (-0.5)), 'w_o': _w((lambda *s: torch.randn(*s, dtype=torch.bfloat16) * 1.6)(H, D, M) * (H * D) ** (-0.5))}
    _A_in = _A_setup()

    def _B_setup():
        B, S, D, F = (1, 2048, 640, 1728)
        return {'x': _w((lambda *s: torch.randn(*s, dtype=torch.bfloat16) * 0.05)(B, S, D)), 'w1': _w((lambda *s: torch.randn(*s, dtype=torch.bfloat16) * 3.2)(D, D) * D ** (-0.5)), 'w2': _w((lambda *s: torch.randn(*s, dtype=torch.bfloat16) * 3.2)(D, D) * D ** (-0.5)), 'w3': _w((lambda *s: torch.randn(*s, dtype=torch.bfloat16) * 3.2)(D, F) * D ** (-0.5)), 'w4': _w((lambda *s: torch.randn(*s, dtype=torch.bfloat16) * 3.2)(F, D) * F ** (-0.5))}
    _B_in = _B_setup()
    return {'A_x': _A_in['x'], 'A_c_kv': _A_in['c_kv'], 'A_w_q': _A_in['w_q'], 'A_w_uk': _A_in['w_uk'], 'A_w_uv': _A_in['w_uv'], 'A_w_o': _A_in['w_o'], 'B_x': _B_in['x'], 'B_w1': _B_in['w1'], 'B_w2': _B_in['w2'], 'B_w3': _B_in['w3'], 'B_w4': _B_in['w4'], 'c': _w((lambda *s: torch.randn(*s, dtype=torch.bfloat16) * 0.1)(1, 640) * 8.0), 'w_mod': _w((lambda *s: torch.randn(*s, dtype=torch.bfloat16) * 0.1)(640, 6 * 640) * 640 ** (-0.5) * 8.0)}

def reference(A_x, A_c_kv, A_w_q, A_w_uk, A_w_uv, A_w_o, B_x, B_w1, B_w2, B_w3, B_w4, c, w_mod):

    def _A_ref(x, c_kv, w_q, w_uk, w_uv, w_o):
        B, Sq, M = (x.shape[0], x.shape[1], x.shape[2])
        Sk = c_kv.shape[1]
        H, D = (w_q.shape[1], w_q.shape[2])
        q = torch.einsum('bqm,mhd->bhqd', x, w_q)
        k = torch.einsum('bkc,chd->bhkd', c_kv, w_uk)
        v = torch.einsum('bkc,chd->bhkd', c_kv, w_uv)
        scores = torch.einsum('bhqd,bhkd->bhqk', q, k) * D ** (-0.5)
        off = Sk - Sq
        qi = torch.arange(Sq)[:, None]
        kj = torch.arange(Sk)[None, :]
        cmask = kj <= qi + off
        scores = torch.where(cmask[None, None], scores, -1000000000.0)
        p = _softmax(scores, axis=-1)
        ctx = torch.einsum('bhqk,bhkd->bhqd', p, v)
        return torch.einsum('bhqd,hdm->bqm', ctx, w_o)

    def _B_ref(x, w1, w2, w3, w4):
        B, S, D = x.shape
        h = _gelu(_layernorm(x, axis=-1) @ w1)
        c = D // 4
        g0 = _pad(h[:, :, 0 * c:1 * c], ((0, 0), (1, 0), (0, 0)))[:, :S, :]
        g1 = _pad(h[:, :, 1 * c:2 * c], ((0, 0), (0, 1), (0, 0)))[:, 1:, :]
        g2 = _pad(h[:, :, 2 * c:3 * c], ((0, 0), (2, 0), (0, 0)))[:, :S, :]
        g3 = _pad(h[:, :, 3 * c:], ((0, 0), (0, 2), (0, 0)))[:, 2:, :]
        hs = torch.concatenate([g0, g1, g2, g3], axis=-1) @ w2
        x1 = x + hs
        h2 = _gelu(_layernorm(x1, axis=-1) @ w3) @ w4
        return x1 + h2
    x = A_x
    mod = (_silu(c) @ w_mod).reshape(c.shape[0], 6, 1, -1)
    sa, ca, ga, sb, cb, gb = (mod[:, 0].clone(), mod[:, 1].clone(), mod[:, 2].clone(), mod[:, 3].clone(), mod[:, 4].clone(), mod[:, 5].clone())
    h = _layernorm(x, axis=-1) * (1 + ca) + sa
    x = x + ga * _A_ref(h, A_c_kv, A_w_q, A_w_uk, A_w_uv, A_w_o)
    h = _layernorm(x, axis=-1) * (1 + cb) + sb
    x = x + gb * _B_ref(h, B_w1, B_w2, B_w3, B_w4)
    return x
```

```cpp
#include <hip/hip_runtime.h>
#include <hip/hip_bf16.h>
typedef unsigned short u16;
__device__ __forceinline__ float bf2f(u16 v){ return __uint_as_float(((unsigned)v)<<16); }
__device__ __forceinline__ u16 f2bf(float f){ unsigned u=__float_as_uint(f); u += 0x7fffu + ((u>>16)&1u); return (u16)(u>>16); }
__device__ __forceinline__ float rb(float f){ return bf2f(f2bf(f)); }
__device__ __forceinline__ float gelu(float x){ float t = 0.7978845608028654f*(x+0.044715f*x*x*x); return 0.5f*x*(1.f+tanhf(t)); }

template<int ACT>
__global__ void gemm(const u16* A, int lda, const u16* B, int ldb, u16* C, int ldc, int M, int N, int K){
  __shared__ float As[16][65]; __shared__ float Bs[16][65];
  int tx=threadIdx.x&15, ty=threadIdx.x>>4; int m0=blockIdx.y*64, n0=blockIdx.x*64;
  float acc[4][4]={};
  for(int k0=0;k0<K;k0+=16){
    for(int i=threadIdx.x;i<1024;i+=256){ int r=i>>4,c=i&15; As[c][r]=bf2f(A[(size_t)(m0+r)*lda+k0+c]); }
    for(int i=threadIdx.x;i<1024;i+=256){ int r=i>>6,c=i&63; Bs[r][c]= (n0+c<N)? bf2f(B[(size_t)(k0+r)*ldb+n0+c]):0.f; }
    __syncthreads();
    for(int k=0;k<16;k++){ float a[4],b[4]; for(int i=0;i<4;i++){a[i]=As[k][ty*4+i]; b[i]=Bs[k][tx*4+i];}
      for(int i=0;i<4;i++)for(int j=0;j<4;j++)acc[i][j]+=a[i]*b[j]; }
    __syncthreads();
  }
  for(int i=0;i<4;i++)for(int j=0;j<4;j++){ int n=n0+tx*4+j; if(n<N){ float v=acc[i][j]; if(ACT==1) v=gelu(v); C[(size_t)(m0+ty*4+i)*ldc+n]=f2bf(v);} }
}
__global__ void modk(const u16* c, const u16* w, float* mod){
  int n=blockIdx.x*256+threadIdx.x; if(n>=3840) return; float s=0;
  for(int k=0;k<640;k++){ float x=bf2f(c[k]); float si=rb(x/(1.f+__expf(-x))); s+=si*bf2f(w[k*3840+n]); }
  mod[n]=rb(s);
}
__device__ float bsum(float v, float* sh){ for(int o=32;o;o>>=1) v+=__shfl_xor(v,o); __syncthreads(); if((threadIdx.x&63)==0) sh[threadIdx.x>>6]=v; __syncthreads(); return sh[0]+sh[1]; }
__global__ void ln(const u16* in, u16* out, const float* sc, const float* sh_){
  __shared__ float sh[2]; int r=blockIdx.x; float v[5]; float s=0;
  for(int i=0;i<5;i++){ v[i]=bf2f(in[r*640+threadIdx.x+128*i]); s+=v[i]; }
  float m=bsum(s,sh)/640.f; float q=0; for(int i=0;i<5;i++){ float d=v[i]-m; q+=d*d; }
  float var=bsum(q,sh)/640.f; float rs=rsqrtf(var+1e-5f);
  for(int i=0;i<5;i++){ int c=threadIdx.x+128*i; float y=(v[i]-m)*rs; if(sc) y=y*(1.f+sc[c])+sh_[c]; out[r*640+c]=f2bf(y); }
}
__global__ void attn(const u16* q, const u16* k, const u16* v, u16* ctx){
  __shared__ float p[2048]; __shared__ float qs[128]; __shared__ float sh[2];
  int h=blockIdx.y, r=blockIdx.x, t=threadIdx.x; qs[t]=bf2f(q[(size_t)r*4096+h*128+t]); __syncthreads();
  float mx=-1e30f;
  for(int j=t;j<=r;j+=128){ float s=0; const u16* kp=k+(size_t)j*4096+h*128; for(int d=0;d<128;d++) s+=qs[d]*bf2f(kp[d]); s*=0.08838834764831845f; p[j]=s; mx=fmaxf(mx,s);}
  for(int o=32;o;o>>=1) mx=fmaxf(mx,__shfl_xor(mx,o)); if((t&63)==0) sh[t>>6]=mx; __syncthreads(); mx=fmaxf(sh[0],sh[1]);
  float l=0; for(int j=t;j<=r;j+=128){ float e=__expf(p[j]-mx); p[j]=e; l+=e; }
  l=bsum(l,sh); __syncthreads();
  float o=0; for(int j=0;j<=r;j++) o+=p[j]*bf2f(v[(size_t)j*4096+h*128+t]);
  ctx[(size_t)r*4096+h*128+t]=f2bf(o/l);
}
__global__ void axpy(const u16* x, const float* g, const u16* y, const u16* y2, u16* out){
  int i=blockIdx.x*256+threadIdx.x; int c=i%640; float a=bf2f(y[i]); if(y2) a=rb(a+bf2f(y2[i])); out[i]=f2bf(bf2f(x[i])+(g? g[c]:1.f)*a);
}
__global__ void shiftk(const u16* h, u16* o){ int i=blockIdx.x*256+threadIdx.x; int r=i/640,c=i%640; int g=c/160; int d = g==0?-1: g==1?1: g==2?-2:2; int rr=r+d; o[i]=(rr>=0&&rr<2048)? h[rr*640+c]:(u16)0; }

extern "C" void kernel_launch(void* const* d_in, const int* in_sizes, int n_in, void* d_out, int out_size, void* d_ws, size_t ws_size, hipStream_t st){
  const u16 *x=(const u16*)d_in[0], *ckv=(const u16*)d_in[1], *wq=(const u16*)d_in[2], *wuk=(const u16*)d_in[3], *wuv=(const u16*)d_in[4], *wo=(const u16*)d_in[5];
  const u16 *w1=(const u16*)d_in[7], *w2=(const u16*)d_in[8], *w3=(const u16*)d_in[9], *w4=(const u16*)d_in[10], *c=(const u16*)d_in[11], *wm=(const u16*)d_in[12];
  char* ws=(char*)d_ws; size_t MB=1<<20;
  float* mod=(float*)ws; u16 *h=(u16*)(ws+1*MB), *q=(u16*)(ws+4*MB), *k=(u16*)(ws+24*MB), *v=(u16*)(ws+44*MB), *ctx=(u16*)(ws+64*MB);
  u16 *ao=(u16*)(ws+84*MB), *x2=(u16*)(ws+88*MB), *hb=(u16*)(ws+92*MB), *u=(u16*)(ws+96*MB), *h1=(u16*)(ws+100*MB), *hs=(u16*)(ws+104*MB), *t2=(u16*)(ws+108*MB), *x1=(u16*)(ws+112*MB), *u2=(u16*)(ws+116*MB), *g=(u16*)(ws+120*MB), *h2=(u16*)(ws+130*MB);
  modk<<<15,256,0,st>>>(c,wm,mod);
  ln<<<2048,128,0,st>>>(x,h,mod+640,mod);
  gemm<0><<<dim3(64,32),256,0,st>>>(h,640,wq,4096,q,4096,2048,4096,640);
  gemm<0><<<dim3(64,32),256,0,st>>>(ckv,512,wuk,4096,k,4096,2048,4096,512);
  gemm<0><<<dim3(64,32),256,0,st>>>(ckv,512,wuv,4096,v,4096,2048,4096,512);
  attn<<<dim3(2048,32),128,0,st>>>(q,k,v,ctx);
  gemm<0><<<dim3(10,32),256,0,st>>>(ctx,4096,wo,640,ao,640,2048,640,4096);
  axpy<<<5120,256,0,st>>>(x,mod+2*640,ao,nullptr,x2);
  ln<<<2048,128,0,st>>>(x2,hb,mod+4*640,mod+3*640);
  ln<<<2048,128,0,st>>>(hb,u,nullptr,nullptr);
  gemm<1><<<dim3(10,32),256,0,st>>>(u,640,w1,640,h1,640,2048,640,640);
  shiftk<<<5120,256,0,st>>>(h1,hs);
  gemm<0><<<dim3(10,32),256,0,st>>>(hs,640,w2,640,t2,640,2048,640,640);
  axpy<<<5120,256,0,st>>>(hb,nullptr,t2,nullptr,x1);
  ln<<<2048,128,0,st>>>(x1,u2,nullptr,nullptr);
  gemm<1><<<dim3(27,32),256,0,st>>>(u2,640,w3,1728,g,1728,2048,1728,640);
  gemm<0><<<dim3(10,32),256,0,st>>>(g,1728,w4,640,h2,640,2048,640,1728);
  axpy<<<5120,256,0,st>>>(x2,mod+5*640,x1,h2,(u16*)d_out);
}
```

```cpp
#include <hip/hip_runtime.h>

__global__ __launch_bounds__(256, 1) void mega(const void* ckv, const void* wuk, const void* wuv, const void* wq, char* ws, const void* x, void* out, unsigned* dbg,
                                               const void* wo, const void* w1, const void* w2, const void* w3, const void* w4, const void* c, const void* wm) {
  extern __shared__ __attribute__((aligned(16))) char lds[];
  const unsigned b = blockIdx.x;
  const unsigned w = __builtin_amdgcn_readfirstlane(threadIdx.x >> 6);
  const unsigned lb = (unsigned)(size_t)lds;
  const unsigned long long ka = (unsigned long long)__builtin_amdgcn_kernarg_segment_ptr();
  asm volatile(
"s_branch ENTRY_%=\n"
".p2align 12\n"
"ENTRY_%=:\n"
"s_load_dwordx16 s[8:23], %0, 0\n"
"s_load_dwordx8 s[28:35], %0, 64\n"
"s_load_dwordx4 s[36:39], %0, 96\n"
"s_load_dwordx2 s[40:41], %0, 112\n"
"s_mov_b32 s66, 2097152\n"
"s_mov_b32 s67, 0x20000\n"
"s_mov_b32 s70, 4194304\n"
"s_mov_b32 s71, 0x20000\n"
"v_mbcnt_lo_u32_b32 v252, -1, 0\n"
"v_mbcnt_hi_u32_b32 v252, -1, v252\n"
"s_and_b32 s85, %1, 7\n"
"s_lshl_b32 s85, s85, 2\n"
"s_bfe_u32 s86, %1, 0x20003\n"
"s_add_u32 s24, s85, s86\n"
"s_lshr_b32 s25, %1, 5\n"
"s_lshl_b32 s80, s25, 18\n"
"s_lshl_b32 s85, %2, 16\n"
"s_add_u32 s80, s80, s85\n"
"s_lshl_b32 s81, %2, 17\n"
"s_lshl_b32 s85, s24, 8\n"
"s_add_u32 s81, s81, s85\n"
"s_mov_b32 s63, s81\n"
"s_lshl_b32 s85, %2, 13\n"
"s_add_u32 s82, %3, s85\n"
"s_lshl_b32 s85, %2, 12\n"
"s_add_u32 s57, %3, s85\n"
"v_lshrrev_b32 v154, 4, v252\n"
"v_and_b32 v155, 7, v252\n"
"v_xor_b32 v155, v155, v154\n"
"v_lshlrev_b32 v155, 4, v155\n"
"v_lshrrev_b32 v156, 3, v252\n"
"v_lshl_add_u32 v155, v156, 10, v155\n"
"v_xor_b32 v156, 64, v155\n"
"v_add_u32 v140, 0, v155\n"
"v_add_u32 v141, 7168, v155\n"
"v_add_u32 v142, 14336, v156\n"
"v_add_u32 v143, 21504, v156\n"
"v_add_u32 v144, 32768, v155\n"
"v_add_u32 v145, 39936, v155\n"
"v_add_u32 v146, 47104, v156\n"
"v_add_u32 v147, 54272, v156\n"
"v_and_b32 v155, 1, v154\n"
"v_lshrrev_b32 v156, 1, v154\n"
"v_lshl_or_b32 v155, v156, 2, v155\n"
"v_and_b32 v156, 15, v252\n"
"v_xor_b32 v155, v155, v156\n"
"v_lshlrev_b32 v155, 4, v155\n"
"v_lshl_add_u32 v155, v154, 13, v155\n"
"v_xor_b32 v157, 128, v155\n"
"v_add_u32 v240, 0, v155\n"
"v_add_u32 v241, 31744, v155\n"
"v_add_u32 v242, 63488, v157\n"
"v_add_u32 v243, 95232, v157\n"
"v_lshrrev_b32 v154, 4, v252\n"
"v_and_b32 v156, 15, v252\n"
"v_lshlrev_b32 v155, 1, v154\n"
"v_xor_b32 v155, v155, v156\n"
"v_lshlrev_b32 v155, 4, v155\n"
"v_lshl_add_u32 v155, v154, 13, v155\n"
"v_xor_b32 v157, 128, v155\n"
"v_add_u32 v148, 0, v155\n"
"v_add_u32 v149, 31744, v155\n"
"v_add_u32 v150, 63488, v157\n"
"v_add_u32 v151, 95232, v157\n"
"s_mov_b32 s74, 4194304\n"
"s_mov_b32 s75, 0x20000\n"
"s_mov_b32 s98, 134217728\n"
"s_mov_b32 s99, 0x20000\n"
"v_mov_b32 v167, 0\n"
"s_mov_b32 s42, 105938944\n"
"s_lshl_b32 s94, s25, 1\n"
"s_and_b32 s95, %2, 1\n"
"s_add_u32 s94, s94, s95\n"
"s_lshl_b32 s94, s94, 15\n"
"s_lshl_b32 s95, s24, 8\n"
"s_add_u32 s94, s94, s95\n"
"s_add_u32 s94, s94, 1048576\n"
"s_lshl_b32 s93, s25, 2\n"
"s_add_u32 s93, s93, %2\n"
"s_mul_i32 s93, s93, 163840\n"
"s_add_u32 s93, s93, s95\n"
"s_lshl_b32 s95, s25, 2\n"
"s_bfe_u32 s92, %1, 0x20003\n"
"s_add_u32 s95, s95, s92\n"
"s_lshl_b32 s95, s95, 2\n"
"s_add_u32 s95, s95, %2\n"
"s_lshl_b32 s95, s95, 14\n"
"s_add_u32 s95, s95, 256\n"
"s_waitcnt lgkmcnt(0)\n"
"s_mov_b64 s[64:65], s[8:9]\n"
"s_mov_b64 s[68:69], s[10:11]\n"
"s_mov_b64 s[72:73], s[12:13]\n"
"s_mov_b64 s[96:97], s[16:17]\n"
"s_mov_b64 s[76:77], s[14:15]\n"
"s_cmp_lt_u32 %2, 2\n"
"s_cselect_b64 s[88:89], s[68:69], s[72:73]\n"
"s_mov_b32 s90, 4194304\n"
"s_mov_b32 s91, 0x20000\n"
"s_getpc_b64 s[60:61]\n"
"s_and_b32 s60, s60, 0xfffff000\n"
"s_lshr_b32 s62, %1, 3\n"
"s_lshl_b32 s62, s62, 2\n"
"s_add_u32 s62, s62, %2\n"
"s_lshl_b32 s62, s62, 10\n"
"v_min_u32 v130, 7, v252\n"
"v_lshlrev_b32 v130, 7, v130\n"
"v_add_u32 v130, s62, v130\n"
"v_min_u32 v130, CODE_END_%=-ENTRY_%=, v130\n"
"global_load_dword v227, v130, s[60:61]\n"
"s_add_u32 m0, s82, 32768\n"
"s_nop 0\n"
"buffer_load_dwordx4 v140, s[64:67], s80 offen offset:0 lds\n"
"buffer_load_dwordx4 v141, s[64:67], s80 offen offset:1024 lds\n"
"buffer_load_dwordx4 v142, s[64:67], s80 offen offset:2048 lds\n"
"buffer_load_dwordx4 v143, s[64:67], s80 offen offset:3072 lds\n"
"s_add_u32 m0, s82, 36864\n"
"s_nop 0\n"
"buffer_load_dwordx4 v144, s[64:67], s80 offen offset:0 lds\n"
"buffer_load_dwordx4 v145, s[64:67], s80 offen offset:1024 lds\n"
"buffer_load_dwordx4 v146, s[64:67], s80 offen offset:2048 lds\n"
"buffer_load_dwordx4 v147, s[64:67], s80 offen offset:3072 lds\n"
"s_add_u32 m0, s57, 65536\n"
"s_nop 0\n"
"buffer_load_dwordx4 v240, s[68:71], s81 offen offset:0 lds\n"
"buffer_load_dwordx4 v241, s[68:71], s81 offen offset:1024 lds\n"
"buffer_load_dwordx4 v242, s[68:71], s81 offen offset:2048 lds\n"
"buffer_load_dwordx4 v243, s[68:71], s81 offen offset:3072 lds\n"
"s_add_u32 m0, s57, 81920\n"
"s_nop 0\n"
"buffer_load_dwordx4 v148, s[72:75], s81 offen offset:0 lds\n"
"buffer_load_dwordx4 v149, s[72:75], s81 offen offset:1024 lds\n"
"buffer_load_dwordx4 v150, s[72:75], s81 offen offset:2048 lds\n"
"buffer_load_dwordx4 v151, s[72:75], s81 offen offset:3072 lds\n"
"s_add_u32 s80, s80, 128\n"
"s_add_u32 s81, s81, 524288\n"
"s_and_b32 s85, %2, 1\n"
"s_lshl_b32 s85, s85, 14\n"
"s_add_u32 s85, s85, %3\n"
"v_and_b32 v155, 7, v252\n"
"v_bfe_u32 v156, v252, 3, 1\n"
"v_lshl_or_b32 v155, v156, 4, v155\n"
"v_lshlrev_b32 v155, 7, v155\n"
"v_bfe_u32 v156, v252, 1, 3\n"
"v_xor_b32 v156, v156, v154\n"
"v_lshl_add_u32 v155, v156, 4, v155\n"
"v_add_u32 v128, s85, v155\n"
"v_xor_b32 v155, 64, v155\n"
"v_add_u32 v129, s85, v155\n"
"s_lshr_b32 s86, %2, 1\n"
"v_bfe_u32 v155, v252, 2, 2\n"
"v_lshl_add_u32 v156, v154, 3, v155\n"
"v_lshlrev_b32 v255, 7, v156\n"
"v_lshlrev_b32 v156, 8, v156\n"
"v_and_b32 v157, 1, v252\n"
"v_lshl_or_b32 v156, v157, 3, v156\n"
"v_and_b32 v157, 1, v154\n"
"v_lshl_or_b32 v156, v157, 7, v156\n"
"v_and_b32 v157, 1, v155\n"
"v_lshl_or_b32 v152, v157, 4, v156\n"
"v_bfe_u32 v157, v252, 1, 1\n"
"v_lshl_or_b32 v152, v157, 5, v152\n"
"v_lshl_or_b32 v153, v157, 4, v156\n"
"v_lshl_or_b32 v153, v155, 5, v153\n"
"v_lshrrev_b32 v157, 1, v155\n"
"v_lshl_or_b32 v152, v157, 6, v152\n"
"s_lshl_b32 s86, s86, 7\n"
"s_add_u32 s85, %3, 32768\n"
"v_xor_b32 v232, 0, v152\n"
"v_add_u32 v232, s85, v232\n"
"v_xor_b32 v233, 16, v152\n"
"v_add_u32 v233, s85, v233\n"
"v_xor_b32 v234, 64, v152\n"
"v_add_u32 v234, s85, v234\n"
"v_xor_b32 v235, 80, v152\n"
"v_add_u32 v235, s85, v235\n"
"v_xor_b32 v236, 128, v152\n"
"v_add_u32 v236, s85, v236\n"
"v_xor_b32 v237, 144, v152\n"
"v_add_u32 v237, s85, v237\n"
"v_xor_b32 v238, 192, v152\n"
"v_add_u32 v238, s85, v238\n"
"v_xor_b32 v239, 208, v152\n"
"v_add_u32 v239, s85, v239\n"
"v_xor_b32 v152, s86, v152\n"
"v_xor_b32 v153, s86, v153\n"
"s_add_u32 s85, %3, 65536\n"
"v_xor_b32 v132, 0, v152\n"
"v_add_u32 v132, s85, v132\n"
"v_xor_b32 v133, 16, v152\n"
"v_add_u32 v133, s85, v133\n"
"v_xor_b32 v134, 64, v152\n"
"v_add_u32 v134, s85, v134\n"
"v_xor_b32 v135, 80, v152\n"
"v_add_u32 v135, s85, v135\n"
"s_add_u32 s85, %3, 81920\n"
"v_xor_b32 v136, 0, v153\n"
"v_add_u32 v136, s85, v136\n"
"v_xor_b32 v137, 32, v153\n"
"v_add_u32 v137, s85, v137\n"
"v_xor_b32 v138, 64, v153\n"
"v_add_u32 v138, s85, v138\n"
"v_xor_b32 v139, 96, v153\n"
"v_add_u32 v139, s85, v139\n"
"v_lshlrev_b32 v251, 4, v252\n"
"s_lshl_b32 s9, s25, 1\n"
"s_add_u32 s9, s9, 2\n"
"s_mov_b32 s78, 5242880\n"
"s_mov_b32 s79, 0x20000\n"
"s_mov_b64 exec, 0\n"
"s_cmp_lg_u32 %2, 0\n"
"s_cbranch_scc1 TWA0_%=\n"
"s_branch PG1_%=\n"
"RPG1_%=:\n"
"s_branch PG5_%=\n"
"RPG5_%=:\n"
"s_nop 0\n"
"s_nop 0\n"
"s_nop 0\n"
"s_nop 0\n"
"TWA0_%=:\n"
"s_cmp_lg_u32 %2, 1\n"
"s_cbranch_scc1 TWA1_%=\n"
"s_branch PG2_%=\n"
"RPG2_%=:\n"
"s_branch PG6_%=\n"
"RPG6_%=:\n"
"s_nop 0\n"
"s_nop 0\n"
"s_nop 0\n"
"s_nop 0\n"
"TWA1_%=:\n"
"s_cmp_lg_u32 %2, 2\n"
"s_cbranch_scc1 TWA2_%=\n"
"s_branch PG3_%=\n"
"RPG3_%=:\n"
"s_branch PG7_%=\n"
"RPG7_%=:\n"
"s_nop 0\n"
"s_nop 0\n"
"s_nop 0\n"
"s_nop 0\n"
"TWA2_%=:\n"
"s_cmp_lg_u32 %2, 3\n"
"s_cbranch_scc1 TWA3_%=\n"
"s_branch PG4_%=\n"
"RPG4_%=:\n"
"s_nop 0\n"
"s_nop 0\n"
"s_nop 0\n"
"s_nop 0\n"
"s_nop 0\n"
"TWA3_%=:\n"
"s_mov_b64 exec, -1\n"
"buffer_load_dwordx2 v[168:169], v167, s[96:99], s42 offen sc1\n"
"s_mul_i32 s50, %1, 1093\n"
"s_lshr_b32 s50, s50, 16\n"
"s_mul_i32 s52, s50, 160\n"
"s_mov_b64 s[44:45], s[38:39]\n"
"s_mov_b32 s46, 1280\n"
"s_mov_b32 s47, 0x20000\n"
"s_mul_i32 s53, %2, 40\n"
"v_add_u32 v162, s53, v252\n"
"v_lshlrev_b32 v162, 1, v162\n"
"s_lshl_b32 s54, s52, 1\n"
"buffer_load_ushort v163, v162, s[44:47], s54 offen\n"
"buffer_load_dword v227, v240, s[88:91], s94 offen\n"
"s_add_u32 s94, s94, 524288\n"
"buffer_load_dword v227, v140, s[64:67], s95 offen\n"
"buffer_load_dword v227, v141, s[64:67], s95 offen offset:1024\n"
"s_add_u32 s95, s95, 128\n"
"s_mul_i32 s51, s50, 60\n"
"s_sub_u32 s51, %1, s51\n"
"s_mov_b64 s[44:45], s[40:41]\n"
"s_cmp_lt_u32 %1, 240\n"
"s_cselect_b32 s46, 4915200, 0\n"
"s_add_u32 s53, s53, s52\n"
"s_mul_i32 s53, s53, 7680\n"
"s_lshl_b32 s54, s51, 7\n"
"s_add_u32 s53, s53, s54\n"
"v_lshrrev_b32 v160, 3, v252\n"
"v_mul_u32_u24 v160, 7680, v160\n"
"v_and_b32 v161, 7, v252\n"
"v_lshl_add_u32 v160, v161, 4, v160\n"
"s_mul_i32 s54, %2, 5120\n"
"s_add_u32 s54, s54, %3\n"
"s_add_u32 m0, s54, 131072\n"
"s_nop 0\n"
"buffer_load_dwordx4 v160, s[44:47], s53 offen nt lds\n"
"s_add_u32 m0, s54, 132096\n"
"s_add_u32 s53, s53, 61440\n"
"buffer_load_dwordx4 v160, s[44:47], s53 offen nt lds\n"
"s_add_u32 m0, s54, 133120\n"
"s_add_u32 s53, s53, 61440\n"
"buffer_load_dwordx4 v160, s[44:47], s53 offen nt lds\n"
"s_add_u32 m0, s54, 134144\n"
"s_add_u32 s53, s53, 61440\n"
"buffer_load_dwordx4 v160, s[44:47], s53 offen nt lds\n"
"s_add_u32 m0, s54, 135168\n"
"s_add_u32 s53, s53, 61440\n"
"buffer_load_dwordx4 v160, s[44:47], s53 offen nt lds\n"
"s_add_u32 m0, s82, 0\n"
"s_nop 0\n"
"buffer_load_dwordx4 v140, s[64:67], s80 offen offset:0 lds\n"
"buffer_load_dwordx4 v141, s[64:67], s80 offen offset:1024 lds\n"
"buffer_load_dwordx4 v142, s[64:67], s80 offen offset:2048 lds\n"
"buffer_load_dwordx4 v143, s[64:67], s80 offen offset:3072 lds\n"
"s_add_u32 m0, s82, 4096\n"
"s_nop 0\n"
"buffer_load_dwordx4 v144, s[64:67], s80 offen offset:0 lds\n"
"buffer_load_dwordx4 v145, s[64:67], s80 offen offset:1024 lds\n"
"buffer_load_dwordx4 v146, s[64:67], s80 offen offset:2048 lds\n"
"buffer_load_dwordx4 v147, s[64:67], s80 offen offset:3072 lds\n"
"s_waitcnt vmcnt(18)\n"
"s_barrier\n"
"KPASS_%=:\n"
"ds_read_b64_tr_b16 a[32:33], v132 offset:0\n"
"ds_read_b64_tr_b16 a[34:35], v132 offset:1024\n"
"ds_read_b128 a[0:3], v128 offset:32768\n"
"ds_read_b128 a[4:7], v128 offset:33792\n"
"ds_read_b128 a[8:11], v128 offset:36864\n"
"ds_read_b128 a[12:15], v128 offset:37888\n"
"ds_read_b128 a[16:19], v128 offset:40960\n"
"ds_read_b128 a[20:23], v128 offset:41984\n"
"ds_read_b128 a[24:27], v128 offset:45056\n"
"ds_read_b128 a[28:31], v128 offset:46080\n"
"ds_read_b64_tr_b16 a[36:37], v133 offset:0\n"
"ds_read_b64_tr_b16 a[38:39], v133 offset:1024\n"
"ds_read_b64_tr_b16 a[40:41], v134 offset:0\n"
"ds_read_b64_tr_b16 a[42:43], v134 offset:1024\n"
"ds_read_b64_tr_b16 a[44:45], v135 offset:0\n"
"ds_read_b64_tr_b16 a[46:47], v135 offset:1024\n"
"ds_read_b64_tr_b16 a[48:49], v136 offset:0\n"
"ds_read_b64_tr_b16 a[50:51], v136 offset:1024\n"
"ds_read_b64_tr_b16 a[52:53], v137 offset:0\n"
"ds_read_b64_tr_b16 a[54:55], v137 offset:1024\n"
"ds_read_b64_tr_b16 a[56:57], v138 offset:0\n"
"ds_read_b64_tr_b16 a[58:59], v138 offset:1024\n"
"ds_read_b64_tr_b16 a[60:61], v139 offset:0\n"
"ds_read_b64_tr_b16 a[62:63], v139 offset:1024\n"
"s_waitcnt lgkmcnt(0)\n"
"v_mfma_f32_16x16x32_bf16 a[128:131], a[32:35], a[0:3], 0\n"
"s_add_u32 m0, s57, 98304\n"
"v_mfma_f32_16x16x32_bf16 a[132:135], a[32:35], a[4:7], 0\n"
"ds_read_b64_tr_b16 a[96:97], v132 offset:8192\n"
"v_mfma_f32_16x16x32_bf16 a[136:139], a[32:35], a[8:11], 0\n"
"buffer_load_dwordx4 v240, s[68:71], s81 offen offset:0 lds\n"
"v_mfma_f32_16x16x32_bf16 a[140:143], a[32:35], a[12:15], 0\n"
"ds_read_b64_tr_b16 a[98:99], v132 offset:9216\n"
"v_mfma_f32_16x16x32_bf16 a[144:147], a[32:35], a[16:19], 0\n"
"buffer_load_dwordx4 v241, s[68:71], s81 offen offset:1024 lds\n"
"v_mfma_f32_16x16x32_bf16 a[148:151], a[32:35], a[20:23], 0\n"
"v_mfma_f32_16x16x32_bf16 a[152:155], a[32:35], a[24:27], 0\n"
"buffer_load_dwordx4 v242, s[68:71], s81 offen offset:2048 lds\n"
"v_mfma_f32_16x16x32_bf16 a[156:159], a[32:35], a[28:31], 0\n"
"ds_read_b128 a[64:67], v129 offset:32768\n"
"v_mfma_f32_16x16x32_bf16 a[160:163], a[36:39], a[0:3], 0\n"
"buffer_load_dwordx4 v243, s[68:71], s81 offen offset:3072 lds\n"
"v_mfma_f32_16x16x32_bf16 a[164:167], a[36:39], a[4:7], 0\n"
"v_mfma_f32_16x16x32_bf16 a[168:171], a[36:39], a[8:11], 0\n"
"s_add_u32 m0, s57, 114688\n"
"v_mfma_f32_16x16x32_bf16 a[172:175], a[36:39], a[12:15], 0\n"
"ds_read_b128 a[68:71], v129 offset:33792\n"
"v_mfma_f32_16x16x32_bf16 a[176:179], a[36:39], a[16:19], 0\n"
"buffer_load_dwordx4 v148, s[72:75], s81 offen offset:0 lds\n"
"v_mfma_f32_16x16x32_bf16 a[180:183], a[36:39], a[20:23], 0\n"
"v_mfma_f32_16x16x32_bf16 a[184:187], a[36:39], a[24:27], 0\n"
"buffer_load_dwordx4 v149, s[72:75], s81 offen offset:1024 lds\n"
"v_mfma_f32_16x16x32_bf16 a[188:191], a[36:39], a[28:31], 0\n"
"ds_read_b128 a[72:75], v129 offset:36864\n"
"v_mfma_f32_16x16x32_bf16 a[192:195], a[40:43], a[0:3], 0\n"
"buffer_load_dwordx4 v150, s[72:75], s81 offen offset:2048 lds\n"
"v_mfma_f32_16x16x32_bf16 a[196:199], a[40:43], a[4:7], 0\n"
"v_mfma_f32_16x16x32_bf16 a[200:203], a[40:43], a[8:11], 0\n"
"buffer_load_dwordx4 v151, s[72:75], s81 offen offset:3072 lds\n"
"v_mfma_f32_16x16x32_bf16 a[204:207], a[40:43], a[12:15], 0\n"
"ds_read_b128 a[76:79], v129 offset:37888\n"
"v_mfma_f32_16x16x32_bf16 a[208:211], a[40:43], a[16:19], 0\n"
"s_add_u32 s80, s80, 128\n"
"v_mfma_f32_16x16x32_bf16 a[212:215], a[40:43], a[20:23], 0\n"
"v_mfma_f32_16x16x32_bf16 a[216:219], a[40:43], a[24:27], 0\n"
"s_add_u32 s81, s81, 524288\n"
"v_mfma_f32_16x16x32_bf16 a[220:223], a[40:43], a[28:31], 0\n"
"ds_read_b128 a[80:83], v129 offset:40960\n"
"v_mfma_f32_16x16x32_bf16 a[224:227], a[44:47], a[0:3], 0\n"
"ds_read_b128 a[84:87], v129 offset:41984\n"
"s_waitcnt vmcnt(24)\n"
"v_lshlrev_b32 v163, 16, v163\n"
"v_mfma_f32_16x16x32_bf16 a[228:231], a[44:47], a[4:7], 0\n"
"v_mul_f32 v164, 0xbfb8aa3b, v163\n"
"v_exp_f32 v164, v164\n"
"v_mfma_f32_16x16x32_bf16 a[232:235], a[44:47], a[8:11], 0\n"
"ds_read_b128 a[88:91], v129 offset:45056\n"
"v_min_u32 v165, 40, v252\n"
"v_mfma_f32_16x16x32_bf16 a[236:239], a[44:47], a[12:15], 0\n"
"v_add_f32 v164, 1.0, v164\n"
"s_mul_i32 s60, %2, 80\n"
"v_mfma_f32_16x16x32_bf16 a[240:243], a[44:47], a[16:19], 0\n"
"ds_read_b128 a[92:95], v129 offset:46080\n"
"v_rcp_f32 v164, v164\n"
"v_mfma_f32_16x16x32_bf16 a[244:247], a[44:47], a[20:23], 0\n"
"v_cmp_gt_u32 vcc, 40, v252\n"
"v_mov_b32 v166, 448\n"
"v_mfma_f32_16x16x32_bf16 a[248:251], a[44:47], a[24:27], 0\n"
"ds_read_b64_tr_b16 a[100:101], v133 offset:8192\n"
"v_mul_f32 v163, v163, v164\n"
"v_mfma_f32_16x16x32_bf16 a[252:255], a[44:47], a[28:31], 0\n"
"v_cndmask_b32 v162, v166, v162, vcc\n"
"s_add_u32 s60, %3, 151552\n"
"v_mfma_f32_16x16x32_bf16 v[0:3], a[0:3], a[48:51], 0\n"
"ds_read_b64_tr_b16 a[102:103], v133 offset:9216\n"
"v_cvt_pk_bf16_f32 v163, v163, v163\n"
"v_mfma_f32_16x16x32_bf16 v[4:7], a[0:3], a[52:55], 0\n"
"v_add_u32 v162, s60, v162\n"
"ds_write_b16 v162, v163\n"
"v_mfma_f32_16x16x32_bf16 v[8:11], a[0:3], a[56:59], 0\n"
"ds_read_b64_tr_b16 a[104:105], v134 offset:8192\n"
"v_lshrrev_b32 v164, 4, v252\n"
"v_mfma_f32_16x16x32_bf16 v[12:15], a[0:3], a[60:63], 0\n"
"ds_read_b64_tr_b16 a[106:107], v134 offset:9216\n"
"v_bfe_u32 v165, v252, 2, 2\n"
"v_mfma_f32_16x16x32_bf16 v[16:19], a[4:7], a[48:51], 0\n"
"v_lshl_add_u32 v165, v164, 3, v165\n"
"v_lshlrev_b32 v165, 7, v165\n"
"v_mfma_f32_16x16x32_bf16 v[20:23], a[4:7], a[52:55], 0\n"
"ds_read_b64_tr_b16 a[108:109], v135 offset:8192\n"
"v_and_b32 v166, 3, v252\n"
"v_mfma_f32_16x16x32_bf16 v[24:27], a[4:7], a[56:59], 0\n"
"v_lshl_add_u32 v165, v166, 3, v165\n"
"v_lshl_add_u32 v164, v164, 4, s60\n"
"v_mfma_f32_16x16x32_bf16 v[28:31], a[4:7], a[60:63], 0\n"
"ds_read_b64_tr_b16 a[110:111], v135 offset:9216\n"
"s_lshl_b32 s60, %2, 5\n"
"v_mfma_f32_16x16x32_bf16 v[32:35], a[8:11], a[48:51], 0\n"
"s_add_u32 s60, s60, %3\n"
"s_add_u32 s60, s60, 131072\n"
"v_mfma_f32_16x16x32_bf16 v[36:39], a[8:11], a[52:55], 0\n"
"ds_read_b64_tr_b16 a[112:113], v136 offset:8192\n"
"v_add_u32 v165, s60, v165\n"
"v_mfma_f32_16x16x32_bf16 v[40:43], a[8:11], a[56:59], 0\n"
"v_and_b32 v160, 15, v252\n"
"v_min_u32 v160, 1, v160\n"
"v_mfma_f32_16x16x32_bf16 v[44:47], a[8:11], a[60:63], 0\n"
"ds_read_b64_tr_b16 a[114:115], v136 offset:9216\n"
"v_lshlrev_b32 v160, 30, v160\n"
"v_mfma_f32_16x16x32_bf16 v[48:51], a[12:15], a[48:51], 0\n"
"v_lshrrev_b32 v161, 4, v252\n"
"v_lshl_add_u32 v160, v161, 6, v160\n"
"v_mfma_f32_16x16x32_bf16 v[52:55], a[12:15], a[52:55], 0\n"
"ds_read_b64_tr_b16 a[116:117], v137 offset:8192\n"
"s_lshl_b32 s52, s51, 10\n"
"v_mfma_f32_16x16x32_bf16 v[56:59], a[12:15], a[56:59], 0\n"
"ds_read_b64_tr_b16 a[118:119], v137 offset:9216\n"
"s_lshl_b32 s53, %2, 8\n"
"v_mfma_f32_16x16x32_bf16 v[60:63], a[12:15], a[60:63], 0\n"
"s_add_u32 s52, s52, s53\n"
"s_lshl_b32 s53, s50, 2\n"
"v_mfma_f32_16x16x32_bf16 v[64:67], a[16:19], a[48:51], 0\n"
"ds_read_b64_tr_b16 a[120:121], v138 offset:8192\n"
"s_add_u32 s52, s52, s53\n"
"v_mfma_f32_16x16x32_bf16 v[68:71], a[16:19], a[52:55], 0\n"
"s_add_u32 s52, s52, 104988672\n"
"s_cmp_lt_u32 %1, 240\n"
"s_cselect_b32 s52, s52, 1073741824\n"
"v_mfma_f32_16x16x32_bf16 v[72:75], a[16:19], a[56:59], 0\n"
"ds_read_b64_tr_b16 a[122:123], v138 offset:9216\n"
"s_lshl_b32 s53, s50, 6\n"
"v_mfma_f32_16x16x32_bf16 v[76:79], a[16:19], a[60:63], 0\n"
"s_add_u32 s53, s53, s51\n"
"s_sub_u32 s60, %1, 240\n"
"v_mfma_f32_16x16x32_bf16 v[80:83], a[20:23], a[48:51], 0\n"
"ds_read_b64_tr_b16 a[124:125], v139 offset:8192\n"
"s_lshr_b32 s61, s60, 2\n"
"v_mfma_f32_16x16x32_bf16 v[84:87], a[20:23], a[52:55], 0\n"
"s_lshl_b32 s61, s61, 6\n"
"s_and_b32 s60, s60, 3\n"
"v_mfma_f32_16x16x32_bf16 v[88:91], a[20:23], a[56:59], 0\n"
"ds_read_b64_tr_b16 a[126:127], v139 offset:9216\n"
"s_add_u32 s60, s60, s61\n"
"v_mfma_f32_16x16x32_bf16 v[92:95], a[20:23], a[60:63], 0\n"
"s_add_u32 s60, s60, 60\n"
"s_cmp_lt_u32 %1, 240\n"
"s_cselect_b32 s53, s53, s60\n"
"v_mfma_f32_16x16x32_bf16 v[96:99], a[24:27], a[48:51], 0\n"
"s_lshl_b32 s53, s53, 3\n"
"s_add_u32 s53, s53, 105922560\n"
"v_mfma_f32_16x16x32_bf16 v[100:103], a[24:27], a[52:55], 0\n"
"v_readfirstlane_b32 s14, v168\n"
"v_readfirstlane_b32 s15, v169\n"
"v_mfma_f32_16x16x32_bf16 v[104:107], a[24:27], a[56:59], 0\n"
"s_add_u32 s14, s14, 1\n"
"s_addc_u32 s15, s15, 0\n"
"v_mfma_f32_16x16x32_bf16 v[108:111], a[24:27], a[60:63], 0\n"
"v_mfma_f32_16x16x32_bf16 v[112:115], a[28:31], a[48:51], 0\n"
"s_waitcnt lgkmcnt(0)\n"
"s_waitcnt vmcnt(0)\n"
"s_barrier\n"
"v_mfma_f32_16x16x32_bf16 v[116:119], a[28:31], a[52:55], 0\n"
"v_mfma_f32_16x16x32_bf16 v[120:123], a[28:31], a[56:59], 0\n"
"v_mfma_f32_16x16x32_bf16 v[124:127], a[28:31], a[60:63], 0\n"
"v_mfma_f32_16x16x32_bf16 a[128:131], a[96:99], a[64:67], a[128:131]\n"
"ds_read_b128 v[196:199], v164 offset:0\n"
"ds_read_b64_tr_b16 v[176:177], v165 offset:0\n"
"s_add_u32 m0, s82, 32768\n"
"v_mfma_f32_16x16x32_bf16 a[132:135], a[96:99], a[68:71], a[132:135]\n"
"ds_read_b64_tr_b16 v[178:179], v165 offset:512\n"
"buffer_load_dwordx4 v140, s[64:67], s80 offen offset:0 lds\n"
"v_mfma_f32_16x16x32_bf16 a[136:139], a[96:99], a[72:75], a[136:139]\n"
"ds_read_b64_tr_b16 a[32:33], v132 offset:32768\n"
"ds_read_b128 v[200:203], v164 offset:64\n"
"v_mfma_f32_16x16x32_bf16 a[140:143], a[96:99], a[76:79], a[140:143]\n"
"ds_read_b64_tr_b16 a[34:35], v132 offset:33792\n"
"ds_read_b64_tr_b16 v[180:181], v165 offset:4096\n"
"v_mfma_f32_16x16x32_bf16 a[144:147], a[96:99], a[80:83], a[144:147]\n"
"ds_read_b64_tr_b16 v[182:183], v165 offset:4608\n"
"ds_read_b128 v[204:207], v164 offset:128\n"
"v_mfma_f32_16x16x32_bf16 a[148:151], a[96:99], a[84:87], a[148:151]\n"
"ds_read_b64_tr_b16 v[184:185], v165 offset:8192\n"
"buffer_load_dwordx4 v141, s[64:67], s80 offen offset:1024 lds\n"
"v_mfma_f32_16x16x32_bf16 a[152:155], a[96:99], a[88:91], a[152:155]\n"
"ds_read_b128 a[0:3], v128 offset:0\n"
"ds_read_b64_tr_b16 v[186:187], v165 offset:8704\n"
"v_mfma_f32_16x16x32_bf16 a[156:159], a[96:99], a[92:95], a[156:159]\n"
"ds_read_b128 a[4:7], v128 offset:1024\n"
"ds_read_b128 v[208:211], v164 offset:192\n"
"v_mfma_f32_16x16x32_bf16 a[160:163], a[100:103], a[64:67], a[160:163]\n"
"ds_read_b64_tr_b16 v[188:189], v165 offset:12288\n"
"ds_read_b64_tr_b16 v[190:191], v165 offset:12800\n"
"v_mfma_f32_16x16x32_bf16 a[164:167], a[100:103], a[68:71], a[164:167]\n"
"ds_read_b128 v[212:215], v164 offset:256\n"
"buffer_load_dwordx4 v142, s[64:67], s80 offen offset:2048 lds\n"
"v_mfma_f32_16x16x32_bf16 a[168:171], a[100:103], a[72:75], a[168:171]\n"
"ds_read_b128 a[8:11], v128 offset:4096\n"
"ds_read_b64_tr_b16 v[192:193], v165 offset:16384\n"
"v_mfma_f32_16x16x32_bf16 a[172:175], a[100:103], a[76:79], a[172:175]\n"
"ds_read_b128 a[12:15], v128 offset:5120\n"
"ds_read_b64_tr_b16 v[194:195], v165 offset:16896\n"
"v_mfma_f32_16x16x32_bf16 a[176:179], a[100:103], a[80:83], a[176:179]\n"
"v_mfma_f32_16x16x32_bf16 a[180:183], a[100:103], a[84:87], a[180:183]\n"
"buffer_load_dwordx4 v143, s[64:67], s80 offen offset:3072 lds\n"
"v_mfma_f32_16x16x32_bf16 a[184:187], a[100:103], a[88:91], a[184:187]\n"
"ds_read_b128 a[16:19], v128 offset:8192\n"
"v_mfma_f32_16x16x32_bf16 a[188:191], a[100:103], a[92:95], a[188:191]\n"
"v_mfma_f32_16x16x32_bf16 a[192:195], a[104:107], a[64:67], a[192:195]\n"
"s_waitcnt lgkmcnt(11)\n"
"v_mfma_f32_16x16x32_bf16 v[172:175], v[176:179], v[196:199], 0\n"
"s_add_u32 m0, s82, 36864\n"
"v_mfma_f32_16x16x32_bf16 a[196:199], a[104:107], a[68:71], a[196:199]\n"
"buffer_load_dwordx4 v144, s[64:67], s80 offen offset:0 lds\n"
"v_mfma_f32_16x16x32_bf16 a[200:203], a[104:107], a[72:75], a[200:203]\n"
"ds_read_b128 a[20:23], v128 offset:9216\n"
"v_mfma_f32_16x16x32_bf16 v[172:175], v[180:183], v[200:203], v[172:175]\n"
"v_mfma_f32_16x16x32_bf16 a[204:207], a[104:107], a[76:79], a[204:207]\n"
"ds_read_b128 a[24:27], v128 offset:12288\n"
"v_mfma_f32_16x16x32_bf16 a[208:211], a[104:107], a[80:83], a[208:211]\n"
"s_waitcnt lgkmcnt(3)\n"
"v_mfma_f32_16x16x32_bf16 v[172:175], v[184:187], v[204:207], v[172:175]\n"
"v_mfma_f32_16x16x32_bf16 a[212:215], a[104:107], a[84:87], a[212:215]\n"
"buffer_load_dwordx4 v145, s[64:67], s80 offen offset:1024 lds\n"
"v_mfma_f32_16x16x32_bf16 a[216:219], a[104:107], a[88:91], a[216:219]\n"
"ds_read_b128 a[28:31], v128 offset:13312\n"
"v_mfma_f32_16x16x32_bf16 v[172:175], v[188:191], v[208:211], v[172:175]\n"
"v_mfma_f32_16x16x32_bf16 a[220:223], a[104:107], a[92:95], a[220:223]\n"
"ds_read_b64_tr_b16 a[36:37], v133 offset:32768\n"
"v_mfma_f32_16x16x32_bf16 a[224:227], a[108:111], a[64:67], a[224:227]\n"
"v_mfma_f32_16x16x32_bf16 v[172:175], v[192:195], v[212:215], v[172:175]\n"
"v_mfma_f32_16x16x32_bf16 a[228:231], a[108:111], a[68:71], a[228:231]\n"
"buffer_load_dwordx4 v146, s[64:67], s80 offen offset:2048 lds\n"
"v_mfma_f32_16x16x32_bf16 a[232:235], a[108:111], a[72:75], a[232:235]\n"
"ds_read_b64_tr_b16 a[38:39], v133 offset:33792\n"
"v_mfma_f32_16x16x32_bf16 a[236:239], a[108:111], a[76:79], a[236:239]\n"
"v_mfma_f32_16x16x32_bf16 a[240:243], a[108:111], a[80:83], a[240:243]\n"
"ds_read_b64_tr_b16 a[40:41], v134 offset:32768\n"
"v_mfma_f32_16x16x32_bf16 a[244:247], a[108:111], a[84:87], a[244:247]\n"
"buffer_load_dwordx4 v147, s[64:67], s80 offen offset:3072 lds\n"
"v_mfma_f32_16x16x32_bf16 a[248:251], a[108:111], a[88:91], a[248:251]\n"
"ds_read_b64_tr_b16 a[42:43], v134 offset:33792\n"
"s_mov_b64 s[44:45], s[18:19]\n"
"v_mfma_f32_16x16x32_bf16 a[252:255], a[108:111], a[92:95], a[252:255]\n"
"v_mfma_f32_16x16x32_bf16 v[0:3], a[64:67], a[112:115], v[0:3]\n"
"s_add_u32 m0, s57, 65536\n"
"s_mov_b32 s46, 2621440\n"
"v_mfma_f32_16x16x32_bf16 v[4:7], a[64:67], a[116:119], v[4:7]\n"
"buffer_load_dwordx4 v240, s[68:71], s81 offen offset:0 lds\n"
"v_mfma_f32_16x16x32_bf16 v[8:11], a[64:67], a[120:123], v[8:11]\n"
"ds_read_b64_tr_b16 a[44:45], v135 offset:32768\n"
"v_mfma_f32_16x16x32_bf16 v[12:15], a[64:67], a[124:127], v[12:15]\n"
"ds_read_b64_tr_b16 a[46:47], v135 offset:33792\n"
"v_mfma_f32_16x16x32_bf16 v[16:19], a[68:71], a[112:115], v[16:19]\n"
"s_lshl_b32 s60, %1, 3\n"
"v_mfma_f32_16x16x32_bf16 v[20:23], a[68:71], a[116:119], v[20:23]\n"
"buffer_load_dwordx4 v241, s[68:71], s81 offen offset:1024 lds\n"
"v_mfma_f32_16x16x32_bf16 v[24:27], a[68:71], a[120:123], v[24:27]\n"
"ds_read_b64_tr_b16 a[48:49], v136 offset:32768\n"
"s_lshl_b32 s61, %2, 1\n"
"v_mfma_f32_16x16x32_bf16 v[28:31], a[68:71], a[124:127], v[28:31]\n"
"ds_read_b64_tr_b16 a[50:51], v136 offset:33792\n"
"v_mfma_f32_16x16x32_bf16 v[32:35], a[72:75], a[112:115], v[32:35]\n"
"v_mfma_f32_16x16x32_bf16 v[36:39], a[72:75], a[116:119], v[36:39]\n"
"buffer_load_dwordx4 v242, s[68:71], s81 offen offset:2048 lds\n"
"s_add_u32 s60, s60, s61\n"
"v_mfma_f32_16x16x32_bf16 v[40:43], a[72:75], a[120:123], v[40:43]\n"
"ds_read_b64_tr_b16 a[52:53], v137 offset:32768\n"
"v_mfma_f32_16x16x32_bf16 v[44:47], a[72:75], a[124:127], v[44:47]\n"
"v_lshrrev_b32 v220, 5, v252\n"
"v_mfma_f32_16x16x32_bf16 v[48:51], a[76:79], a[112:115], v[48:51]\n"
"ds_read_b64_tr_b16 a[54:55], v137 offset:33792\n"
"v_mfma_f32_16x16x32_bf16 v[52:55], a[76:79], a[116:119], v[52:55]\n"
"buffer_load_dwordx4 v243, s[68:71], s81 offen offset:3072 lds\n"
"v_mfma_f32_16x16x32_bf16 v[56:59], a[76:79], a[120:123], v[56:59]\n"
"ds_read_b64_tr_b16 a[56:57], v138 offset:32768\n"
"v_mfma_f32_16x16x32_bf16 v[60:63], a[76:79], a[124:127], v[60:63]\n"
"v_add_u32 v220, s60, v220\n"
"v_mfma_f32_16x16x32_bf16 v[64:67], a[80:83], a[112:115], v[64:67]\n"
"s_add_u32 m0, s57, 81920\n"
"v_mfma_f32_16x16x32_bf16 v[68:71], a[80:83], a[116:119], v[68:71]\n"
"buffer_load_dwordx4 v148, s[72:75], s81 offen offset:0 lds\n"
"v_mfma_f32_16x16x32_bf16 v[72:75], a[80:83], a[120:123], v[72:75]\n"
"ds_read_b64_tr_b16 a[58:59], v138 offset:33792\n"
"v_and_b32 v221, 31, v252\n"
"v_mfma_f32_16x16x32_bf16 v[76:79], a[80:83], a[124:127], v[76:79]\n"
"ds_read_b64_tr_b16 a[60:61], v139 offset:32768\n"
"v_mfma_f32_16x16x32_bf16 v[80:83], a[84:87], a[112:115], v[80:83]\n"
"v_mul_u32_u24 v224, 1280, v220\n"
"v_mfma_f32_16x16x32_bf16 v[84:87], a[84:87], a[116:119], v[84:87]\n"
"buffer_load_dwordx4 v149, s[72:75], s81 offen offset:1024 lds\n"
"v_mfma_f32_16x16x32_bf16 v[88:91], a[84:87], a[120:123], v[88:91]\n"
"ds_read_b64_tr_b16 a[62:63], v139 offset:33792\n"
"v_mfma_f32_16x16x32_bf16 v[92:95], a[84:87], a[124:127], v[92:95]\n"
"v_lshl_add_u32 v224, v221, 4, v224\n"
"v_mfma_f32_16x16x32_bf16 v[96:99], a[88:91], a[112:115], v[96:99]\n"
"v_mfma_f32_16x16x32_bf16 v[100:103], a[88:91], a[116:119], v[100:103]\n"
"buffer_load_dwordx4 v150, s[72:75], s81 offen offset:2048 lds\n"
"v_and_b32 v225, 16, v252\n"
"v_mfma_f32_16x16x32_bf16 v[104:107], a[88:91], a[120:123], v[104:107]\n"
"v_mfma_f32_16x16x32_bf16 v[108:111], a[88:91], a[124:127], v[108:111]\n"
"v_lshl_add_u32 v225, v225, 26, v224\n"
"PG1_%=:\n"
"s_cbranch_execz RPG1_%=\n"
"v_mfma_f32_16x16x32_bf16 v[112:115], a[92:95], a[112:115], v[112:115]\n"
"v_mfma_f32_16x16x32_bf16 v[116:119], a[92:95], a[116:119], v[116:119]\n"
"buffer_load_dwordx4 v151, s[72:75], s81 offen offset:3072 lds\n"
"v_mfma_f32_16x16x32_bf16 v[120:123], a[92:95], a[120:123], v[120:123]\n"
"s_add_u32 s80, s80, 128\n"
"s_add_u32 s81, s81, 524288\n"
"v_mfma_f32_16x16x32_bf16 v[124:127], a[92:95], a[124:127], v[124:127]\n"
"buffer_store_dword v172, v160, s[96:99], s52 offen offset:0 sc1\n"
"buffer_store_dword v173, v160, s[96:99], s52 offen offset:16 sc1\n"
"buffer_store_dword v174, v160, s[96:99], s52 offen offset:32 sc1\n"
"buffer_store_dword v175, v160, s[96:99], s52 offen offset:48 sc1\n"
"s_waitcnt lgkmcnt(0)\n"
"v_mfma_f32_16x16x32_bf16 a[128:131], a[32:35], a[0:3], a[128:131]\n"
"buffer_load_dword v227, v240, s[88:91], s94 offen\n"
"s_add_u32 s94, s94, 524288\n"
"v_mfma_f32_16x16x32_bf16 a[132:135], a[32:35], a[4:7], a[132:135]\n"
"buffer_load_dword v227, v140, s[64:67], s95 offen\n"
"v_mfma_f32_16x16x32_bf16 a[136:139], a[32:35], a[8:11], a[136:139]\n"
"buffer_load_dword v227, v141, s[64:67], s95 offen offset:1024\n"
"v_mfma_f32_16x16x32_bf16 a[140:143], a[32:35], a[12:15], a[140:143]\n"
"s_add_u32 s95, s95, 128\n"
"v_mfma_f32_16x16x32_bf16 a[144:147], a[32:35], a[16:19], a[144:147]\n"
"buffer_load_dwordx4 v[160:163], v224, s[44:47], 0 offen nt\n"
"v_mfma_f32_16x16x32_bf16 a[148:151], a[32:35], a[20:23], a[148:151]\n"
"buffer_load_dwordx4 v[164:167], v224, s[44:47], 0 offen offset:512 nt\n"
"v_mfma_f32_16x16x32_bf16 a[152:155], a[32:35], a[24:27], a[152:155]\n"
"buffer_load_dwordx4 v[168:171], v225, s[44:47], 0 offen offset:1024 nt\n"
"v_mfma_f32_16x16x32_bf16 a[156:159], a[32:35], a[28:31], a[156:159]\n"
"ds_read_b64_tr_b16 a[96:97], v132 offset:40960\n"
"v_mfma_f32_16x16x32_bf16 a[160:163], a[36:39], a[0:3], a[160:163]\n"
"s_lshl_b32 s40, s24, 19\n"
"s_lshl_b32 s41, %2, 12\n"
"v_mfma_f32_16x16x32_bf16 a[164:167], a[36:39], a[4:7], a[164:167]\n"
"ds_read_b64_tr_b16 a[98:99], v132 offset:41984\n"
"s_add_u32 s40, s40, s41\n"
"v_mfma_f32_16x16x32_bf16 a[168:171], a[36:39], a[8:11], a[168:171]\n"
"s_sub_u32 s41, 33, s9\n"
"s_cmp_lt_u32 %2, 2\n"
"s_cselect_b32 s42, 0, 1.0\n"
"v_mfma_f32_16x16x32_bf16 a[172:175], a[36:39], a[12:15], a[172:175]\n"
"ds_read_b128 a[64:67], v129 offset:0\n"
"v_mov_b32 v204, 1073741824\n"
"v_mfma_f32_16x16x32_bf16 a[176:179], a[36:39], a[16:19], a[176:179]\n"
"v_and_b32 v200, 31, v252\n"
"v_add_u32 v200, 0, v200\n"
"v_mfma_f32_16x16x32_bf16 a[180:183], a[36:39], a[20:23], a[180:183]\n"
"ds_read_b128 a[68:71], v129 offset:1024\n"
"s_mul_i32 s60, %1, 1639\n"
"v_mfma_f32_16x16x32_bf16 a[184:187], a[36:39], a[24:27], a[184:187]\n"
"v_lshlrev_b32 v201, 1, v200\n"
"v_subrev_u32 v201, s9, v201\n"
"v_mfma_f32_16x16x32_bf16 a[188:191], a[36:39], a[28:31], a[188:191]\n"
"ds_read_b128 a[72:75], v129 offset:4096\n"
"v_max_i32 v201, v200, v201\n"
"v_mfma_f32_16x16x32_bf16 a[192:195], a[40:43], a[0:3], a[192:195]\n"
"v_lshrrev_b32 v202, 5, v252\n"
"s_lshr_b32 s60, s60, 16\n"
"v_mfma_f32_16x16x32_bf16 a[196:199], a[40:43], a[4:7], a[196:199]\n"
"ds_read_b128 a[76:79], v129 offset:5120\n"
"v_sub_u32 v201, v201, v202\n"
"v_mfma_f32_16x16x32_bf16 a[200:203], a[40:43], a[8:11], a[200:203]\n"
"v_mov_b32 v203, s41\n"
"s_lshl_b32 s61, %1, 6\n"
"v_mfma_f32_16x16x32_bf16 a[204:207], a[40:43], a[12:15], a[204:207]\n"
"ds_read_b128 a[80:83], v129 offset:8192\n"
"v_lshl_add_u32 v205, v202, 6, v200\n"
"v_mfma_f32_16x16x32_bf16 a[208:211], a[40:43], a[16:19], a[208:211]\n"
"s_add_u32 s38, s9, 64\n"
"v_cmp_eq_u32 vcc, s38, v205\n"
"v_mfma_f32_16x16x32_bf16 a[212:215], a[40:43], a[20:23], a[212:215]\n"
"ds_read_b128 a[84:87], v129 offset:9216\n"
"s_add_u32 s48, s61, 104857600\n"
"v_mfma_f32_16x16x32_bf16 a[216:219], a[40:43], a[24:27], a[216:219]\n"
"v_cndmask_b32 v201, v201, v203, vcc\n"
"v_lshlrev_b32 v201, 14, v201\n"
"v_mfma_f32_16x16x32_bf16 a[220:223], a[40:43], a[28:31], a[220:223]\n"
"ds_read_b128 a[88:91], v129 offset:12288\n"
"s_add_u32 s39, s40, 33554432\n"
"v_mfma_f32_16x16x32_bf16 a[224:227], a[44:47], a[0:3], a[224:227]\n"
"v_add_u32 v201, s39, v201\n"
"s_add_u32 s49, s61, 104862720\n"
"v_mfma_f32_16x16x32_bf16 a[228:231], a[44:47], a[4:7], a[228:231]\n"
"ds_read_b128 a[92:95], v129 offset:13312\n"
"v_cmp_lt_u32 vcc, 16, v200\n"
"v_mfma_f32_16x16x32_bf16 a[232:235], a[44:47], a[8:11], a[232:235]\n"
"s_nop 0\n"
"v_cndmask_b32 v201, v201, v204, vcc\n"
"v_subrev_u32 v205, 64, v205\n"
"v_mfma_f32_16x16x32_bf16 a[236:239], a[44:47], a[12:15], a[236:239]\n"
"ds_read_b64_tr_b16 a[100:101], v133 offset:40960\n"
"s_lshl_b32 s61, %1, 5\n"
"v_mfma_f32_16x16x32_bf16 a[240:243], a[44:47], a[16:19], a[240:243]\n"
"v_cmp_gt_u32 vcc, s9, v205\n"
"s_nop 1\n"
"v_cndmask_b32 v229, v201, v204, vcc\n"
"v_mfma_f32_16x16x32_bf16 a[244:247], a[44:47], a[20:23], a[244:247]\n"
"ds_read_b64_tr_b16 a[102:103], v133 offset:41984\n"
"v_and_b32 v200, 31, v252\n"
"v_mfma_f32_16x16x32_bf16 a[248:251], a[44:47], a[24:27], a[248:251]\n"
"v_add_u32 v200, 2, v200\n"
"s_add_u32 s54, s61, 104918016\n"
"v_mfma_f32_16x16x32_bf16 a[252:255], a[44:47], a[28:31], a[252:255]\n"
"ds_read_b64_tr_b16 a[104:105], v134 offset:40960\n"
"v_lshlrev_b32 v201, 1, v200\n"
"v_mfma_f32_16x16x32_bf16 v[0:3], a[0:3], a[48:51], v[0:3]\n"
"v_subrev_u32 v201, s9, v201\n"
"s_add_u32 s55, s61, 104920576\n"
"v_mfma_f32_16x16x32_bf16 v[4:7], a[0:3], a[52:55], v[4:7]\n"
"ds_read_b64_tr_b16 a[106:107], v134 offset:41984\n"
"v_max_i32 v201, v200, v201\n"
"v_mfma_f32_16x16x32_bf16 v[8:11], a[0:3], a[56:59], v[8:11]\n"
"v_lshrrev_b32 v202, 5, v252\n"
"v_sub_u32 v201, v201, v202\n"
"v_mfma_f32_16x16x32_bf16 v[12:15], a[0:3], a[60:63], v[12:15]\n"
"ds_read_b64_tr_b16 a[108:109], v135 offset:40960\n"
"s_add_u32 s61, s61, 104920576\n"
"v_mfma_f32_16x16x32_bf16 v[16:19], a[4:7], a[48:51], v[16:19]\n"
"v_mov_b32 v203, s41\n"
"v_lshl_add_u32 v205, v202, 6, v200\n"
"v_mfma_f32_16x16x32_bf16 v[20:23], a[4:7], a[52:55], v[20:23]\n"
"ds_read_b64_tr_b16 a[110:111], v135 offset:41984\n"
"s_add_u32 s38, s9, 64\n"
"v_mfma_f32_16x16x32_bf16 v[24:27], a[4:7], a[56:59], v[24:27]\n"
"v_cmp_eq_u32 vcc, s38, v205\n"
"s_cmp_eq_u32 s60, 4\n"
"s_cselect_b32 s43, 1.0, 0\n"
"s_cselect_b32 s49, s49, 1073741824\n"
"s_cselect_b32 s55, s55, 1073741824\n"
"s_cselect_b32 s54, s54, 1073741824\n"
"v_mfma_f32_16x16x32_bf16 v[28:31], a[4:7], a[60:63], v[28:31]\n"
"ds_read_b64_tr_b16 a[112:113], v136 offset:40960\n"
"v_cndmask_b32 v201, v201, v203, vcc\n"
"v_mfma_f32_16x16x32_bf16 v[32:35], a[8:11], a[48:51], v[32:35]\n"
"v_lshlrev_b32 v201, 14, v201\n"
"s_cmp_eq_u32 s60, 3\n"
"s_cselect_b32 s54, s61, s54\n"
"v_mfma_f32_16x16x32_bf16 v[36:39], a[8:11], a[52:55], v[36:39]\n"
"ds_read_b64_tr_b16 a[114:115], v136 offset:41984\n"
"s_add_u32 s39, s40, 16777216\n"
"v_mfma_f32_16x16x32_bf16 v[40:43], a[8:11], a[56:59], v[40:43]\n"
"v_add_u32 v201, s39, v201\n"
"v_cmp_lt_u32 vcc, 16, v200\n"
"v_mfma_f32_16x16x32_bf16 v[44:47], a[8:11], a[60:63], v[44:47]\n"
"ds_read_b64_tr_b16 a[116:117], v137 offset:40960\n"
"v_cndmask_b32 v201, v201, v204, vcc\n"
"v_mfma_f32_16x16x32_bf16 v[48:51], a[12:15], a[48:51], v[48:51]\n"
"v_subrev_u32 v205, 64, v205\n"
"s_cmp_lt_u32 %1, 240\n"
"s_cselect_b32 s48, s48, 1073741824\n"
"v_mfma_f32_16x16x32_bf16 v[52:55], a[12:15], a[52:55], v[52:55]\n"
"ds_read_b64_tr_b16 a[118:119], v137 offset:41984\n"
"v_cmp_gt_u32 vcc, s9, v205\n"
"v_mfma_f32_16x16x32_bf16 v[56:59], a[12:15], a[56:59], v[56:59]\n"
"s_nop 0\n"
"v_cndmask_b32 v230, v201, v204, vcc\n"
"s_lshl_b32 s56, %1, 8\n"
"v_mfma_f32_16x16x32_bf16 v[60:63], a[12:15], a[60:63], v[60:63]\n"
"ds_read_b64_tr_b16 a[120:121], v138 offset:40960\n"
"v_and_b32 v200, 3, v252\n"
"v_mfma_f32_16x16x32_bf16 v[64:67], a[16:19], a[48:51], v[64:67]\n"
"v_lshlrev_b32 v200, 8, v200\n"
"v_add_u32 v200, %1, v200\n"
"v_mfma_f32_16x16x32_bf16 v[68:71], a[16:19], a[52:55], v[68:71]\n"
"ds_read_b64_tr_b16 a[122:123], v138 offset:41984\n"
"s_add_u32 s56, s56, 104988672\n"
"v_mfma_f32_16x16x32_bf16 v[72:75], a[16:19], a[56:59], v[72:75]\n"
"v_lshlrev_b32 v200, 2, v200\n"
"v_add_u32 v200, %2, v200\n"
"v_mfma_f32_16x16x32_bf16 v[76:79], a[16:19], a[60:63], v[76:79]\n"
"ds_read_b64_tr_b16 a[124:125], v139 offset:40960\n"
"v_mov_b32 v201, 0\n"
"v_mfma_f32_16x16x32_bf16 v[80:83], a[20:23], a[48:51], v[80:83]\n"
"v_mov_b32 v202, s28\n"
"v_mfma_f32_16x16x32_bf16 v[84:87], a[20:23], a[52:55], v[84:87]\n"
"ds_read_b64_tr_b16 a[126:127], v139 offset:41984\n"
"v_mov_b32 v203, s29\n"
"v_mfma_f32_16x16x32_bf16 v[88:91], a[20:23], a[56:59], v[88:91]\n"
"v_mov_b32 v204, 5242880\n"
"v_mfma_f32_16x16x32_bf16 v[92:95], a[20:23], a[60:63], v[92:95]\n"
"v_mov_b32 v205, 1280\n"
"v_mov_b32 v206, 67108864\n"
"v_mfma_f32_16x16x32_bf16 v[96:99], a[24:27], a[48:51], v[96:99]\n"
"v_mov_b32 v207, 131072\n"
"v_mfma_f32_16x16x32_bf16 v[100:103], a[24:27], a[52:55], v[100:103]\n"
"v_mov_b32 v208, 6554\n"
"v_mov_b32 v209, 10\n"
"v_mfma_f32_16x16x32_bf16 v[104:107], a[24:27], a[56:59], v[104:107]\n"
"v_cmp_le_u32 vcc, 1280, v200\n"
"v_mfma_f32_16x16x32_bf16 v[108:111], a[24:27], a[60:63], v[108:111]\n"
"v_mfma_f32_16x16x32_bf16 v[112:115], a[28:31], a[48:51], v[112:115]\n"
"s_waitcnt lgkmcnt(0)\n"
"s_waitcnt vmcnt(6)\n"
"s_barrier\n"
"s_cmp_lg_u32 %2, 0\n"
"s_cbranch_scc1 PUBM_%=\n"
"v_min_u32 v130, 1, v252\n"
"v_lshlrev_b32 v130, 30, v130\n"
"v_mov_b32 v222, s14\n"
"v_mov_b32 v223, s15\n"
"buffer_store_dwordx2 v[222:223], v130, s[96:99], s53 offen sc1\n"
"PUBM_%=:\n"
"v_mfma_f32_16x16x32_bf16 v[116:119], a[28:31], a[52:55], v[116:119]\n"
"v_mfma_f32_16x16x32_bf16 v[120:123], a[28:31], a[56:59], v[120:123]\n"
"v_mfma_f32_16x16x32_bf16 v[124:127], a[28:31], a[60:63], v[124:127]\n"
"v_mfma_f32_16x16x32_bf16 a[128:131], a[96:99], a[64:67], a[128:131]\n"
"v_mov_b32 v212, 1280\n"
"v_cndmask_b32 v201, v201, v212, vcc\n"
"s_add_u32 m0, s82, 0\n"
"v_mfma_f32_16x16x32_bf16 a[132:135], a[96:99], a[68:71], a[132:135]\n"
"buffer_load_dwordx4 v140, s[64:67], s80 offen offset:0 lds\n"
"v_mov_b32 v212, s30\n"
"v_mfma_f32_16x16x32_bf16 a[136:139], a[96:99], a[72:75], a[136:139]\n"
"ds_read_b64_tr_b16 a[32:33], v132 offset:0\n"
"v_cndmask_b32 v202, v202, v212, vcc\n"
"v_mfma_f32_16x16x32_bf16 a[140:143], a[96:99], a[76:79], a[140:143]\n"
"ds_read_b64_tr_b16 a[34:35], v132 offset:1024\n"
"v_mov_b32 v212, s31\n"
"v_mfma_f32_16x16x32_bf16 a[144:147], a[96:99], a[80:83], a[144:147]\n"
"v_cndmask_b32 v203, v203, v212, vcc\n"
"v_mov_b32 v212, 819200\n"
"v_mfma_f32_16x16x32_bf16 a[148:151], a[96:99], a[84:87], a[148:151]\n"
"buffer_load_dwordx4 v141, s[64:67], s80 offen offset:1024 lds\n"
"v_cndmask_b32 v204, v204, v212, vcc\n"
"v_mfma_f32_16x16x32_bf16 a[152:155], a[96:99], a[88:91], a[152:155]\n"
"ds_read_b128 a[0:3], v128 offset:32768\n"
"v_mov_b32 v212, 73400320\n"
"v_mfma_f32_16x16x32_bf16 a[156:159], a[96:99], a[92:95], a[156:159]\n"
"ds_read_b128 a[4:7], v128 offset:33792\n"
"v_cndmask_b32 v206, v206, v212, vcc\n"
"v_mfma_f32_16x16x32_bf16 a[160:163], a[100:103], a[64:67], a[160:163]\n"
"v_mov_b32 v212, 20480\n"
"v_cndmask_b32 v207, v207, v212, vcc\n"
"v_mfma_f32_16x16x32_bf16 a[164:167], a[100:103], a[68:71], a[164:167]\n"
"buffer_load_dwordx4 v142, s[64:67], s80 offen offset:2048 lds\n"
"v_cmp_le_u32 vcc, 1480, v200\n"
"v_mfma_f32_16x16x32_bf16 a[168:171], a[100:103], a[72:75], a[168:171]\n"
"ds_read_b128 a[8:11], v128 offset:36864\n"
"v_mov_b32 v212, 1480\n"
"v_mfma_f32_16x16x32_bf16 a[172:175], a[100:103], a[76:79], a[172:175]\n"
"ds_read_b128 a[12:15], v128 offset:37888\n"
"v_cndmask_b32 v201, v201, v212, vcc\n"
"v_mfma_f32_16x16x32_bf16 a[176:179], a[100:103], a[80:83], a[176:179]\n"
"v_mov_b32 v212, s32\n"
"v_cndmask_b32 v202, v202, v212, vcc\n"
"v_mfma_f32_16x16x32_bf16 a[180:183], a[100:103], a[84:87], a[180:183]\n"
"buffer_load_dwordx4 v143, s[64:67], s80 offen offset:3072 lds\n"
"v_mov_b32 v212, s33\n"
"v_mfma_f32_16x16x32_bf16 a[184:187], a[100:103], a[88:91], a[184:187]\n"
"ds_read_b128 a[16:19], v128 offset:40960\n"
"v_cndmask_b32 v203, v203, v212, vcc\n"
"v_mov_b32 v212, 74448896\n"
"v_mfma_f32_16x16x32_bf16 a[188:191], a[100:103], a[92:95], a[188:191]\n"
"v_cndmask_b32 v206, v206, v212, vcc\n"
"v_mfma_f32_16x16x32_bf16 a[192:195], a[104:107], a[64:67], a[192:195]\n"
"v_cmp_le_u32 vcc, 1680, v200\n"
"s_add_u32 m0, s82, 4096\n"
"v_mfma_f32_16x16x32_bf16 a[196:199], a[104:107], a[68:71], a[196:199]\n"
"buffer_load_dwordx4 v144, s[64:67], s80 offen offset:0 lds\n"
"v_mov_b32 v212, 1680\n"
"v_cndmask_b32 v201, v201, v212, vcc\n"
"v_mfma_f32_16x16x32_bf16 a[200:203], a[104:107], a[72:75], a[200:203]\n"
"ds_read_b128 a[20:23], v128 offset:41984\n"
"v_mov_b32 v212, s34\n"
"v_mfma_f32_16x16x32_bf16 a[204:207], a[104:107], a[76:79], a[204:207]\n"
"ds_read_b128 a[24:27], v128 offset:45056\n"
"v_cndmask_b32 v202, v202, v212, vcc\n"
"v_mfma_f32_16x16x32_bf16 a[208:211], a[104:107], a[80:83], a[208:211]\n"
"v_mov_b32 v212, s35\n"
"v_cndmask_b32 v203, v203, v212, vcc\n"
"v_mfma_f32_16x16x32_bf16 a[212:215], a[104:107], a[84:87], a[212:215]\n"
"buffer_load_dwordx4 v145, s[64:67], s80 offen offset:1024 lds\n"
"v_mov_b32 v212, 2211840\n"
"v_mfma_f32_16x16x32_bf16 a[216:219], a[104:107], a[88:91], a[216:219]\n"
"ds_read_b128 a[28:31], v128 offset:46080\n"
"v_cndmask_b32 v204, v204, v212, vcc\n"
"v_mfma_f32_16x16x32_bf16 a[220:223], a[104:107], a[92:95], a[220:223]\n"
"ds_read_b64_tr_b16 a[36:37], v133 offset:0\n"
"v_mov_b32 v212, 3456\n"
"v_mfma_f32_16x16x32_bf16 a[224:227], a[108:111], a[64:67], a[224:227]\n"
"v_cndmask_b32 v205, v205, v212, vcc\n"
"v_mov_b32 v212, 75497472\n"
"v_mfma_f32_16x16x32_bf16 a[228:231], a[108:111], a[68:71], a[228:231]\n"
"buffer_load_dwordx4 v146, s[64:67], s80 offen offset:2048 lds\n"
"v_cndmask_b32 v206, v206, v212, vcc\n"
"v_mfma_f32_16x16x32_bf16 a[232:235], a[108:111], a[72:75], a[232:235]\n"
"ds_read_b64_tr_b16 a[38:39], v133 offset:1024\n"
"v_mov_b32 v212, 2341\n"
"v_mfma_f32_16x16x32_bf16 a[236:239], a[108:111], a[76:79], a[236:239]\n"
"v_cndmask_b32 v208, v208, v212, vcc\n"
"v_mov_b32 v212, 28\n"
"v_mfma_f32_16x16x32_bf16 a[240:243], a[108:111], a[80:83], a[240:243]\n"
"ds_read_b64_tr_b16 a[40:41], v134 offset:0\n"
"v_cndmask_b32 v209, v209, v212, vcc\n"
"v_mfma_f32_16x16x32_bf16 a[244:247], a[108:111], a[84:87], a[244:247]\n"
"buffer_load_dwordx4 v147, s[64:67], s80 offen offset:3072 lds\n"
"v_cmp_le_u32 vcc, 2240, v200\n"
"v_mfma_f32_16x16x32_bf16 a[248:251], a[108:111], a[88:91], a[248:251]\n"
"ds_read_b64_tr_b16 a[42:43], v134 offset:1024\n"
"v_mov_b32 v212, 2240\n"
"v_mfma_f32_16x16x32_bf16 a[252:255], a[108:111], a[92:95], a[252:255]\n"
"v_cndmask_b32 v201, v201, v212, vcc\n"
"v_mov_b32 v212, s36\n"
"v_mfma_f32_16x16x32_bf16 v[0:3], a[64:67], a[112:115], v[0:3]\n"
"v_cndmask_b32 v202, v202, v212, vcc\n"
"s_add_u32 m0, s57, 98304\n"
"v_mfma_f32_16x16x32_bf16 v[4:7], a[64:67], a[116:119], v[4:7]\n"
"buffer_load_dwordx4 v240, s[68:71], s81 offen offset:0 lds\n"
"v_mov_b32 v212, s37\n"
"v_mfma_f32_16x16x32_bf16 v[8:11], a[64:67], a[120:123], v[8:11]\n"
"ds_read_b64_tr_b16 a[44:45], v135 offset:0\n"
"v_cndmask_b32 v203, v203, v212, vcc\n"
"v_mfma_f32_16x16x32_bf16 v[12:15], a[64:67], a[124:127], v[12:15]\n"
"ds_read_b64_tr_b16 a[46:47], v135 offset:1024\n"
"v_mov_b32 v212, 1280\n"
"v_cndmask_b32 v205, v205, v212, vcc\n"
"v_mfma_f32_16x16x32_bf16 v[16:19], a[68:71], a[112:115], v[16:19]\n"
"v_mov_b32 v212, 78643200\n"
"v_mfma_f32_16x16x32_bf16 v[20:23], a[68:71], a[116:119], v[20:23]\n"
"buffer_load_dwordx4 v241, s[68:71], s81 offen offset:1024 lds\n"
"v_cndmask_b32 v206, v206, v212, vcc\n"
"v_mov_b32 v212, 57344\n"
"v_mfma_f32_16x16x32_bf16 v[24:27], a[68:71], a[120:123], v[24:27]\n"
"ds_read_b64_tr_b16 a[48:49], v136 offset:0\n"
"v_cndmask_b32 v207, v207, v212, vcc\n"
"v_mfma_f32_16x16x32_bf16 v[28:31], a[68:71], a[124:127], v[28:31]\n"
"ds_read_b64_tr_b16 a[50:51], v136 offset:1024\n"
"v_mov_b32 v212, 6554\n"
"v_mfma_f32_16x16x32_bf16 v[32:35], a[72:75], a[112:115], v[32:35]\n"
"v_cndmask_b32 v208, v208, v212, vcc\n"
"v_mov_b32 v212, 10\n"
"v_mfma_f32_16x16x32_bf16 v[36:39], a[72:75], a[116:119], v[36:39]\n"
"buffer_load_dwordx4 v242, s[68:71], s81 offen offset:2048 lds\n"
"v_cndmask_b32 v209, v209, v212, vcc\n"
"v_mfma_f32_16x16x32_bf16 v[40:43], a[72:75], a[120:123], v[40:43]\n"
"ds_read_b64_tr_b16 a[52:53], v137 offset:0\n"
"v_sub_u32 v201, v200, v201\n"
"v_mfma_f32_16x16x32_bf16 v[44:47], a[72:75], a[124:127], v[44:47]\n"
"v_mul_u32_u24 v210, v201, v208\n"
"v_lshrrev_b32 v210, 16, v210\n"
"v_mfma_f32_16x16x32_bf16 v[48:51], a[76:79], a[112:115], v[48:51]\n"
"ds_read_b64_tr_b16 a[54:55], v137 offset:1024\n"
"v_mul_u32_u24 v211, v210, v209\n"
"v_mfma_f32_16x16x32_bf16 v[52:55], a[76:79], a[116:119], v[52:55]\n"
"buffer_load_dwordx4 v243, s[68:71], s81 offen offset:3072 lds\n"
"v_sub_u32 v211, v201, v211\n"
"v_mfma_f32_16x16x32_bf16 v[56:59], a[76:79], a[120:123], v[56:59]\n"
"ds_read_b64_tr_b16 a[56:57], v138 offset:0\n"
"v_mul_u32_u24 v201, v210, v205\n"
"v_mfma_f32_16x16x32_bf16 v[60:63], a[76:79], a[124:127], v[60:63]\n"
"v_lshlrev_b32 v201, 5, v201\n"
"v_lshl_add_u32 v201, v211, 7, v201\n"
"v_mfma_f32_16x16x32_bf16 v[64:67], a[80:83], a[112:115], v[64:67]\n"
"v_cmp_ne_u32 vcc, 27, v211\n"
"s_add_u32 m0, s57, 114688\n"
"v_mfma_f32_16x16x32_bf16 v[68:71], a[80:83], a[116:119], v[68:71]\n"
"buffer_load_dwordx4 v148, s[72:75], s81 offen offset:0 lds\n"
"v_mul_u32_u24 v208, v211, v207\n"
"v_mfma_f32_16x16x32_bf16 v[72:75], a[80:83], a[120:123], v[72:75]\n"
"ds_read_b64_tr_b16 a[58:59], v138 offset:1024\n"
"v_lshl_add_u32 v206, v208, 2, v206\n"
"v_mfma_f32_16x16x32_bf16 v[76:79], a[80:83], a[124:127], v[76:79]\n"
"ds_read_b64_tr_b16 a[60:61], v139 offset:0\n"
"v_cndmask_b32 v204, 0, v204, vcc\n"
"v_mfma_f32_16x16x32_bf16 v[80:83], a[84:87], a[112:115], v[80:83]\n"
"v_cmp_gt_u32 vcc, 2800, v200\n"
"v_lshl_add_u32 v206, v210, 10, v206\n"
"v_mfma_f32_16x16x32_bf16 v[84:87], a[84:87], a[116:119], v[84:87]\n"
"buffer_load_dwordx4 v149, s[72:75], s81 offen offset:1024 lds\n"
"v_mov_b32 v212, 1073741824\n"
"v_mfma_f32_16x16x32_bf16 v[88:91], a[84:87], a[120:123], v[88:91]\n"
"ds_read_b64_tr_b16 a[62:63], v139 offset:1024\n"
"v_cndmask_b32 v204, 0, v204, vcc\n"
"v_mfma_f32_16x16x32_bf16 v[92:95], a[84:87], a[124:127], v[92:95]\n"
"v_cndmask_b32 v206, v212, v206, vcc\n"
"v_mov_b32_dpp v254, v202 quad_perm:[0,1,2,3] row_mask:0x1 bank_mask:0x1\n"
"v_mfma_f32_16x16x32_bf16 v[96:99], a[88:91], a[112:115], v[96:99]\n"
"s_nop 0\n"
"v_mov_b32_dpp v254, v203 quad_perm:[0,1,2,3] row_mask:0x1 bank_mask:0x2\n"
"s_nop 1\n"
"v_mov_b32_dpp v254, v204 quad_perm:[0,1,2,3] row_mask:0x1 bank_mask:0x4\n"
"v_mfma_f32_16x16x32_bf16 v[100:103], a[88:91], a[116:119], v[100:103]\n"
"buffer_load_dwordx4 v150, s[72:75], s81 offen offset:2048 lds\n"
"v_mov_b32_dpp v254, v201 quad_perm:[0,1,2,3] row_mask:0x1 bank_mask:0x8\n"
"v_mfma_f32_16x16x32_bf16 v[104:107], a[88:91], a[120:123], v[104:107]\n"
"s_nop 0\n"
"v_mov_b32_dpp v254, v205 quad_perm:[0,1,2,3] row_mask:0x2 bank_mask:0x1\n"
"s_nop 1\n"
"v_mov_b32_dpp v254, v206 quad_perm:[0,1,2,3] row_mask:0x2 bank_mask:0x2\n"
"v_mfma_f32_16x16x32_bf16 v[108:111], a[88:91], a[124:127], v[108:111]\n"
"s_nop 0\n"
"v_mov_b32_dpp v254, v207 quad_perm:[0,1,2,3] row_mask:0x2 bank_mask:0x4\n"
"v_mfma_f32_16x16x32_bf16 v[112:115], a[92:95], a[112:115], v[112:115]\n"
"v_mfma_f32_16x16x32_bf16 v[116:119], a[92:95], a[116:119], v[116:119]\n"
"buffer_load_dwordx4 v151, s[72:75], s81 offen offset:3072 lds\n"
"s_mov_b32 s86, 105922560\n"
"v_mfma_f32_16x16x32_bf16 v[120:123], a[92:95], a[120:123], v[120:123]\n"
"s_add_u32 s80, s80, 128\n"
"s_add_u32 s81, s81, 524288\n"
"v_mfma_f32_16x16x32_bf16 v[124:127], a[92:95], a[124:127], v[124:127]\n"
"s_waitcnt lgkmcnt(0)\n"
"v_mfma_f32_16x16x32_bf16 a[128:131], a[32:35], a[0:3], a[128:131]\n"
"buffer_load_dword v227, v240, s[88:91], s94 offen\n"
"s_add_u32 s94, s94, 524288\n"
"v_mfma_f32_16x16x32_bf16 a[132:135], a[32:35], a[4:7], a[132:135]\n"
"buffer_load_dword v227, v140, s[64:67], s95 offen\n"
"v_mfma_f32_16x16x32_bf16 a[136:139], a[32:35], a[8:11], a[136:139]\n"
"buffer_load_dword v227, v141, s[64:67], s95 offen offset:1024\n"
"v_mfma_f32_16x16x32_bf16 a[140:143], a[32:35], a[12:15], a[140:143]\n"
"buffer_load_dwordx4 v[204:207], v251, s[96:99], s86 offen sc1\n"
"s_add_u32 s95, s95, 128\n"
"v_mfma_f32_16x16x32_bf16 a[144:147], a[32:35], a[16:19], a[144:147]\n"
"buffer_load_dwordx4 v[208:211], v251, s[96:99], s86 offen offset:1024 sc1\n"
"v_mfma_f32_16x16x32_bf16 a[148:151], a[32:35], a[20:23], a[148:151]\n"
"buffer_load_dword v227, v240, s[76:79], s93 offen\n"
"v_mfma_f32_16x16x32_bf16 a[152:155], a[32:35], a[24:27], a[152:155]\n"
"s_add_u32 s93, s93, 32768\n"
"v_mfma_f32_16x16x32_bf16 a[156:159], a[32:35], a[28:31], a[156:159]\n"
"ds_read_b64_tr_b16 a[96:97], v132 offset:8192\n"
"v_mfma_f32_16x16x32_bf16 a[160:163], a[36:39], a[0:3], a[160:163]\n"
"s_waitcnt vmcnt(22)\n"
"v_lshlrev_b32 v172, 16, v160\n"
"v_and_b32 v173, 0xffff0000, v160\n"
"v_mfma_f32_16x16x32_bf16 a[164:167], a[36:39], a[4:7], a[164:167]\n"
"ds_read_b64_tr_b16 a[98:99], v132 offset:9216\n"
"v_lshlrev_b32 v174, 16, v161\n"
"v_and_b32 v175, 0xffff0000, v161\n"
"v_mfma_f32_16x16x32_bf16 a[168:171], a[36:39], a[8:11], a[168:171]\n"
"v_lshlrev_b32 v176, 16, v162\n"
"v_and_b32 v177, 0xffff0000, v162\n"
"v_mfma_f32_16x16x32_bf16 a[172:175], a[36:39], a[12:15], a[172:175]\n"
"ds_read_b128 a[64:67], v129 offset:32768\n"
"v_lshlrev_b32 v178, 16, v163\n"
"v_mfma_f32_16x16x32_bf16 a[176:179], a[36:39], a[16:19], a[176:179]\n"
"v_and_b32 v179, 0xffff0000, v163\n"
"v_lshlrev_b32 v180, 16, v164\n"
"v_mfma_f32_16x16x32_bf16 a[180:183], a[36:39], a[20:23], a[180:183]\n"
"ds_read_b128 a[68:71], v129 offset:33792\n"
"v_and_b32 v181, 0xffff0000, v164\n"
"v_mfma_f32_16x16x32_bf16 a[184:187], a[36:39], a[24:27], a[184:187]\n"
"v_lshlrev_b32 v182, 16, v165\n"
"v_and_b32 v183, 0xffff0000, v165\n"
"v_mfma_f32_16x16x32_bf16 a[188:191], a[36:39], a[28:31], a[188:191]\n"
"ds_read_b128 a[72:75], v129 offset:36864\n"
"v_lshlrev_b32 v184, 16, v166\n"
"v_and_b32 v185, 0xffff0000, v166\n"
"v_mfma_f32_16x16x32_bf16 a[192:195], a[40:43], a[0:3], a[192:195]\n"
"v_lshlrev_b32 v186, 16, v167\n"
"v_and_b32 v187, 0xffff0000, v167\n"
"v_mfma_f32_16x16x32_bf16 a[196:199], a[40:43], a[4:7], a[196:199]\n"
"ds_read_b128 a[76:79], v129 offset:37888\n"
"v_lshlrev_b32 v188, 16, v168\n"
"v_mfma_f32_16x16x32_bf16 a[200:203], a[40:43], a[8:11], a[200:203]\n"
"v_and_b32 v189, 0xffff0000, v168\n"
"v_lshlrev_b32 v190, 16, v169\n"
"v_mfma_f32_16x16x32_bf16 a[204:207], a[40:43], a[12:15], a[204:207]\n"
"ds_read_b128 a[80:83], v129 offset:40960\n"
"v_and_b32 v191, 0xffff0000, v169\n"
"v_mfma_f32_16x16x32_bf16 a[208:211], a[40:43], a[16:19], a[208:211]\n"
"v_lshlrev_b32 v192, 16, v170\n"
"v_and_b32 v193, 0xffff0000, v170\n"
"v_mfma_f32_16x16x32_bf16 a[212:215], a[40:43], a[20:23], a[212:215]\n"
"ds_read_b128 a[84:87], v129 offset:41984\n"
"v_lshlrev_b32 v194, 16, v171\n"
"v_and_b32 v195, 0xffff0000, v171\n"
"v_mfma_f32_16x16x32_bf16 a[216:219], a[40:43], a[24:27], a[216:219]\n"
"v_add_f32 v214, v172, v173\n"
"v_mul_f32 v216, v172, v172\n"
"v_mfma_f32_16x16x32_bf16 a[220:223], a[40:43], a[28:31], a[220:223]\n"
"ds_read_b128 a[88:91], v129 offset:45056\n"
"v_mul_f32 v217, v173, v173\n"
"v_mfma_f32_16x16x32_bf16 a[224:227], a[44:47], a[0:3], a[224:227]\n"
"v_add_f32 v215, v174, v175\n"
"v_fmac_f32 v216, v174, v174\n"
"v_mfma_f32_16x16x32_bf16 a[228:231], a[44:47], a[4:7], a[228:231]\n"
"ds_read_b128 a[92:95], v129 offset:46080\n"
"v_fmac_f32 v217, v175, v175\n"
"v_mfma_f32_16x16x32_bf16 a[232:235], a[44:47], a[8:11], a[232:235]\n"
"v_add_f32 v214, v214, v176\n"
"v_fmac_f32 v216, v176, v176\n"
"v_mfma_f32_16x16x32_bf16 a[236:239], a[44:47], a[12:15], a[236:239]\n"
"ds_read_b64_tr_b16 a[100:101], v133 offset:8192\n"
"v_add_f32 v215, v215, v177\n"
"v_fmac_f32 v217, v177, v177\n"
"v_mfma_f32_16x16x32_bf16 a[240:243], a[44:47], a[16:19], a[240:243]\n"
"v_add_f32 v214, v214, v178\n"
"v_fmac_f32 v216, v178, v178\n"
"v_mfma_f32_16x16x32_bf16 a[244:247], a[44:47], a[20:23], a[244:247]\n"
"ds_read_b64_tr_b16 a[102:103], v133 offset:9216\n"
"v_add_f32 v215, v215, v179\n"
"v_mfma_f32_16x16x32_bf16 a[248:251], a[44:47], a[24:27], a[248:251]\n"
"v_fmac_f32 v217, v179, v179\n"
"v_add_f32 v214, v214, v180\n"
"v_mfma_f32_16x16x32_bf16 a[252:255], a[44:47], a[28:31], a[252:255]\n"
"ds_read_b64_tr_b16 a[104:105], v134 offset:8192\n"
"v_fmac_f32 v216, v180, v180\n"
"v_mfma_f32_16x16x32_bf16 v[0:3], a[0:3], a[48:51], v[0:3]\n"
"v_add_f32 v215, v215, v181\n"
"v_fmac_f32 v217, v181, v181\n"
"v_mfma_f32_16x16x32_bf16 v[4:7], a[0:3], a[52:55], v[4:7]\n"
"ds_read_b64_tr_b16 a[106:107], v134 offset:9216\n"
"v_add_f32 v214, v214, v182\n"
"v_fmac_f32 v216, v182, v182\n"
"v_mfma_f32_16x16x32_bf16 v[8:11], a[0:3], a[56:59], v[8:11]\n"
"v_add_f32 v215, v215, v183\n"
"v_fmac_f32 v217, v183, v183\n"
"v_mfma_f32_16x16x32_bf16 v[12:15], a[0:3], a[60:63], v[12:15]\n"
"ds_read_b64_tr_b16 a[108:109], v135 offset:8192\n"
"v_add_f32 v214, v214, v184\n"
"v_mfma_f32_16x16x32_bf16 v[16:19], a[4:7], a[48:51], v[16:19]\n"
"v_fmac_f32 v216, v184, v184\n"
"v_add_f32 v215, v215, v185\n"
"v_mfma_f32_16x16x32_bf16 v[20:23], a[4:7], a[52:55], v[20:23]\n"
"ds_read_b64_tr_b16 a[110:111], v135 offset:9216\n"
"v_fmac_f32 v217, v185, v185\n"
"v_mfma_f32_16x16x32_bf16 v[24:27], a[4:7], a[56:59], v[24:27]\n"
"v_add_f32 v214, v214, v186\n"
"v_fmac_f32 v216, v186, v186\n"
"v_mfma_f32_16x16x32_bf16 v[28:31], a[4:7], a[60:63], v[28:31]\n"
"ds_read_b64_tr_b16 a[112:113], v136 offset:8192\n"
"v_add_f32 v215, v215, v187\n"
"v_fmac_f32 v217, v187, v187\n"
"v_mfma_f32_16x16x32_bf16 v[32:35], a[8:11], a[48:51], v[32:35]\n"
"v_add_f32 v214, v214, v188\n"
"v_fmac_f32 v216, v188, v188\n"
"v_mfma_f32_16x16x32_bf16 v[36:39], a[8:11], a[52:55], v[36:39]\n"
"ds_read_b64_tr_b16 a[114:115], v136 offset:9216\n"
"v_add_f32 v215, v215, v189\n"
"v_mfma_f32_16x16x32_bf16 v[40:43], a[8:11], a[56:59], v[40:43]\n"
"v_fmac_f32 v217, v189, v189\n"
"v_add_f32 v214, v214, v190\n"
"v_mfma_f32_16x16x32_bf16 v[44:47], a[8:11], a[60:63], v[44:47]\n"
"ds_read_b64_tr_b16 a[116:117], v137 offset:8192\n"
"v_fmac_f32 v216, v190, v190\n"
"v_mfma_f32_16x16x32_bf16 v[48:51], a[12:15], a[48:51], v[48:51]\n"
"s_waitcnt vmcnt(2)\n"
"v_xor_b32 v204, s14, v204\n"
"v_xor_b32 v205, s15, v205\n"
"v_mfma_f32_16x16x32_bf16 v[52:55], a[12:15], a[52:55], v[52:55]\n"
"ds_read_b64_tr_b16 a[118:119], v137 offset:9216\n"
"v_xor_b32 v206, s14, v206\n"
"v_mfma_f32_16x16x32_bf16 v[56:59], a[12:15], a[56:59], v[56:59]\n"
"v_xor_b32 v207, s15, v207\n"
"v_mfma_f32_16x16x32_bf16 v[60:63], a[12:15], a[60:63], v[60:63]\n"
"ds_read_b64_tr_b16 a[120:121], v138 offset:8192\n"
"s_waitcnt vmcnt(1)\n"
"v_xor_b32 v208, s14, v208\n"
"v_mfma_f32_16x16x32_bf16 v[64:67], a[16:19], a[48:51], v[64:67]\n"
"v_xor_b32 v209, s15, v209\n"
"v_mfma_f32_16x16x32_bf16 v[68:71], a[16:19], a[52:55], v[68:71]\n"
"ds_read_b64_tr_b16 a[122:123], v138 offset:9216\n"
"v_xor_b32 v210, s14, v210\n"
"v_mfma_f32_16x16x32_bf16 v[72:75], a[16:19], a[56:59], v[72:75]\n"
"v_xor_b32 v211, s15, v211\n"
"v_mfma_f32_16x16x32_bf16 v[76:79], a[16:19], a[60:63], v[76:79]\n"
"ds_read_b64_tr_b16 a[124:125], v139 offset:8192\n"
"v_or3_b32 v204, v204, v205, v206\n"
"v_mfma_f32_16x16x32_bf16 v[80:83], a[20:23], a[48:51], v[80:83]\n"
"v_or3_b32 v207, v207, v208, v209\n"
"v_mfma_f32_16x16x32_bf16 v[84:87], a[20:23], a[52:55], v[84:87]\n"
"ds_read_b64_tr_b16 a[126:127], v139 offset:9216\n"
"v_or3_b32 v204, v204, v210, v211\n"
"v_mfma_f32_16x16x32_bf16 v[88:91], a[20:23], a[56:59], v[88:91]\n"
"v_or_b32 v204, v204, v207\n"
"v_mfma_f32_16x16x32_bf16 v[92:95], a[20:23], a[60:63], v[92:95]\n"
"v_cmp_ne_u32 vcc, 0, v204\n"
"v_mfma_f32_16x16x32_bf16 v[96:99], a[24:27], a[48:51], v[96:99]\n"
"s_nop 0\n"
"s_cbranch_vccnz SLOW_M_%=\n"
"OK_M_%=:\n"
"v_mfma_f32_16x16x32_bf16 v[100:103], a[24:27], a[52:55], v[100:103]\n"
"v_mfma_f32_16x16x32_bf16 v[104:107], a[24:27], a[56:59], v[104:107]\n"
"v_mfma_f32_16x16x32_bf16 v[108:111], a[24:27], a[60:63], v[108:111]\n"
"v_mfma_f32_16x16x32_bf16 v[112:115], a[28:31], a[48:51], v[112:115]\n"
"s_waitcnt lgkmcnt(0)\n"
"s_barrier\n"
"v_mfma_f32_16x16x32_bf16 v[116:119], a[28:31], a[52:55], v[116:119]\n"
"v_mfma_f32_16x16x32_bf16 v[120:123], a[28:31], a[56:59], v[120:123]\n"
"v_mfma_f32_16x16x32_bf16 v[124:127], a[28:31], a[60:63], v[124:127]\n"
"v_mfma_f32_16x16x32_bf16 a[128:131], a[96:99], a[64:67], a[128:131]\n"
"v_add_f32 v215, v215, v191\n"
"v_fmac_f32 v217, v191, v191\n"
"s_add_u32 m0, s82, 32768\n"
"v_mfma_f32_16x16x32_bf16 a[132:135], a[96:99], a[68:71], a[132:135]\n"
"buffer_load_dwordx4 v140, s[64:67], s80 offen offset:0 lds\n"
"v_add_f32 v214, v214, v192\n"
"v_mfma_f32_16x16x32_bf16 a[136:139], a[96:99], a[72:75], a[136:139]\n"
"ds_read_b64_tr_b16 a[32:33], v132 offset:32768\n"
"v_fmac_f32 v216, v192, v192\n"
"v_mfma_f32_16x16x32_bf16 a[140:143], a[96:99], a[76:79], a[140:143]\n"
"ds_read_b64_tr_b16 a[34:35], v132 offset:33792\n"
"v_add_f32 v215, v215, v193\n"
"v_mfma_f32_16x16x32_bf16 a[144:147], a[96:99], a[80:83], a[144:147]\n"
"v_fmac_f32 v217, v193, v193\n"
"v_add_f32 v214, v214, v194\n"
"v_mfma_f32_16x16x32_bf16 a[148:151], a[96:99], a[84:87], a[148:151]\n"
"buffer_load_dwordx4 v141, s[64:67], s80 offen offset:1024 lds\n"
"v_fmac_f32 v216, v194, v194\n"
"v_mfma_f32_16x16x32_bf16 a[152:155], a[96:99], a[88:91], a[152:155]\n"
"ds_read_b128 a[0:3], v128 offset:0\n"
"v_add_f32 v215, v215, v195\n"
"v_fmac_f32 v217, v195, v195\n"
"v_mfma_f32_16x16x32_bf16 a[156:159], a[96:99], a[92:95], a[156:159]\n"
"ds_read_b128 a[4:7], v128 offset:1024\n"
"v_add_f32 v214, v214, v215\n"
"v_mfma_f32_16x16x32_bf16 a[160:163], a[100:103], a[64:67], a[160:163]\n"
"v_add_f32 v216, v216, v217\n"
"v_add_f32_dpp v214, v214, v214 row_ror:8 row_mask:0xf bank_mask:0xf\n"
"v_mfma_f32_16x16x32_bf16 a[164:167], a[100:103], a[68:71], a[164:167]\n"
"buffer_load_dwordx4 v142, s[64:67], s80 offen offset:2048 lds\n"
"v_add_f32_dpp v216, v216, v216 row_ror:8 row_mask:0xf bank_mask:0xf\n"
"v_mfma_f32_16x16x32_bf16 a[168:171], a[100:103], a[72:75], a[168:171]\n"
"ds_read_b128 a[8:11], v128 offset:4096\n"
"v_add_f32_dpp v214, v214, v214 row_ror:4 row_mask:0xf bank_mask:0xf\n"
"v_mfma_f32_16x16x32_bf16 a[172:175], a[100:103], a[76:79], a[172:175]\n"
"ds_read_b128 a[12:15], v128 offset:5120\n"
"v_add_f32_dpp v216, v216, v216 row_ror:4 row_mask:0xf bank_mask:0xf\n"
"v_mfma_f32_16x16x32_bf16 a[176:179], a[100:103], a[80:83], a[176:179]\n"
"v_add_f32_dpp v214, v214, v214 row_ror:2 row_mask:0xf bank_mask:0xf\n"
"v_add_f32_dpp v216, v216, v216 row_ror:2 row_mask:0xf bank_mask:0xf\n"
"PG2_%=:\n"
"s_cbranch_execz RPG2_%=\n"
"v_mfma_f32_16x16x32_bf16 a[180:183], a[100:103], a[84:87], a[180:183]\n"
"buffer_load_dwordx4 v143, s[64:67], s80 offen offset:3072 lds\n"
"v_add_f32_dpp v214, v214, v214 row_ror:1 row_mask:0xf bank_mask:0xf\n"
"v_add_f32_dpp v216, v216, v216 row_ror:1 row_mask:0xf bank_mask:0xf\n"
"v_mfma_f32_16x16x32_bf16 a[184:187], a[100:103], a[88:91], a[184:187]\n"
"ds_read_b128 a[16:19], v128 offset:8192\n"
"v_mov_b32 v215, v214\n"
"v_mfma_f32_16x16x32_bf16 a[188:191], a[100:103], a[92:95], a[188:191]\n"
"v_mov_b32 v217, v216\n"
"v_permlane16_swap_b32 v214, v215\n"
"v_mfma_f32_16x16x32_bf16 a[192:195], a[104:107], a[64:67], a[192:195]\n"
"v_permlane16_swap_b32 v216, v217\n"
"s_add_u32 m0, s82, 36864\n"
"v_mfma_f32_16x16x32_bf16 a[196:199], a[104:107], a[68:71], a[196:199]\n"
"buffer_load_dwordx4 v144, s[64:67], s80 offen offset:0 lds\n"
"v_add_f32 v214, v214, v215\n"
"v_mfma_f32_16x16x32_bf16 a[200:203], a[104:107], a[72:75], a[200:203]\n"
"ds_read_b128 a[20:23], v128 offset:9216\n"
"v_add_f32 v216, v216, v217\n"
"v_mfma_f32_16x16x32_bf16 a[204:207], a[104:107], a[76:79], a[204:207]\n"
"ds_read_b128 a[24:27], v128 offset:12288\n"
"v_mul_f32 v218, 0x3acccccd, v214\n"
"v_mul_f32 v219, 0x3acccccd, v216\n"
"v_mfma_f32_16x16x32_bf16 a[208:211], a[104:107], a[80:83], a[208:211]\n"
"v_fma_f32 v219, -v218, v218, v219\n"
"v_mfma_f32_16x16x32_bf16 a[212:215], a[104:107], a[84:87], a[212:215]\n"
"buffer_load_dwordx4 v145, s[64:67], s80 offen offset:1024 lds\n"
"v_max_f32 v219, 0, v219\n"
"v_add_f32 v219, 0x3727c5ac, v219\n"
"v_mfma_f32_16x16x32_bf16 a[216:219], a[104:107], a[88:91], a[216:219]\n"
"ds_read_b128 a[28:31], v128 offset:13312\n"
"v_rsq_f32 v219, v219\n"
"v_mfma_f32_16x16x32_bf16 a[220:223], a[104:107], a[92:95], a[220:223]\n"
"ds_read_b64_tr_b16 a[36:37], v133 offset:32768\n"
"v_mfma_f32_16x16x32_bf16 a[224:227], a[108:111], a[64:67], a[224:227]\n"
"v_sub_f32 v172, v172, v218\n"
"v_sub_f32 v173, v173, v218\n"
"v_mfma_f32_16x16x32_bf16 a[228:231], a[108:111], a[68:71], a[228:231]\n"
"buffer_load_dwordx4 v146, s[64:67], s80 offen offset:2048 lds\n"
"v_sub_f32 v174, v174, v218\n"
"v_mfma_f32_16x16x32_bf16 a[232:235], a[108:111], a[72:75], a[232:235]\n"
"ds_read_b64_tr_b16 a[38:39], v133 offset:33792\n"
"v_sub_f32 v175, v175, v218\n"
"v_mfma_f32_16x16x32_bf16 a[236:239], a[108:111], a[76:79], a[236:239]\n"
"v_sub_f32 v176, v176, v218\n"
"v_sub_f32 v177, v177, v218\n"
"v_mfma_f32_16x16x32_bf16 a[240:243], a[108:111], a[80:83], a[240:243]\n"
"ds_read_b64_tr_b16 a[40:41], v134 offset:32768\n"
"v_sub_f32 v178, v178, v218\n"
"v_mfma_f32_16x16x32_bf16 a[244:247], a[108:111], a[84:87], a[244:247]\n"
"buffer_load_dwordx4 v147, s[64:67], s80 offen offset:3072 lds\n"
"v_sub_f32 v179, v179, v218\n"
"v_sub_f32 v180, v180, v218\n"
"v_mfma_f32_16x16x32_bf16 a[248:251], a[108:111], a[88:91], a[248:251]\n"
"ds_read_b64_tr_b16 a[42:43], v134 offset:33792\n"
"v_sub_f32 v181, v181, v218\n"
"v_mfma_f32_16x16x32_bf16 a[252:255], a[108:111], a[92:95], a[252:255]\n"
"v_sub_f32 v182, v182, v218\n"
"v_sub_f32 v183, v183, v218\n"
"v_mfma_f32_16x16x32_bf16 v[0:3], a[64:67], a[112:115], v[0:3]\n"
"v_sub_f32 v184, v184, v218\n"
"s_add_u32 m0, s57, 65536\n"
"v_mfma_f32_16x16x32_bf16 v[4:7], a[64:67], a[116:119], v[4:7]\n"
"buffer_load_dwordx4 v240, s[68:71], s81 offen offset:0 lds\n"
"v_sub_f32 v185, v185, v218\n"
"v_mfma_f32_16x16x32_bf16 v[8:11], a[64:67], a[120:123], v[8:11]\n"
"ds_read_b64_tr_b16 a[44:45], v135 offset:32768\n"
"v_sub_f32 v186, v186, v218\n"
"v_mfma_f32_16x16x32_bf16 v[12:15], a[64:67], a[124:127], v[12:15]\n"
"ds_read_b64_tr_b16 a[46:47], v135 offset:33792\n"
"v_sub_f32 v187, v187, v218\n"
"v_sub_f32 v188, v188, v218\n"
"v_mfma_f32_16x16x32_bf16 v[16:19], a[68:71], a[112:115], v[16:19]\n"
"v_sub_f32 v189, v189, v218\n"
"v_sub_f32 v190, v190, v218\n"
"v_mfma_f32_16x16x32_bf16 v[20:23], a[68:71], a[116:119], v[20:23]\n"
"buffer_load_dwordx4 v241, s[68:71], s81 offen offset:1024 lds\n"
"v_sub_f32 v191, v191, v218\n"
"v_mfma_f32_16x16x32_bf16 v[24:27], a[68:71], a[120:123], v[24:27]\n"
"ds_read_b64_tr_b16 a[48:49], v136 offset:32768\n"
"v_sub_f32 v192, v192, v218\n"
"v_mfma_f32_16x16x32_bf16 v[28:31], a[68:71], a[124:127], v[28:31]\n"
"ds_read_b64_tr_b16 a[50:51], v136 offset:33792\n"
"v_sub_f32 v193, v193, v218\n"
"v_mfma_f32_16x16x32_bf16 v[32:35], a[72:75], a[112:115], v[32:35]\n"
"v_sub_f32 v194, v194, v218\n"
"v_sub_f32 v195, v195, v218\n"
"v_mfma_f32_16x16x32_bf16 v[36:39], a[72:75], a[116:119], v[36:39]\n"
"buffer_load_dwordx4 v242, s[68:71], s81 offen offset:2048 lds\n"
"v_mul_f32 v172, v172, v219\n"
"v_mfma_f32_16x16x32_bf16 v[40:43], a[72:75], a[120:123], v[40:43]\n"
"ds_read_b64_tr_b16 a[52:53], v137 offset:32768\n"
"v_mul_f32 v173, v173, v219\n"
"v_mul_f32 v174, v174, v219\n"
"v_mfma_f32_16x16x32_bf16 v[44:47], a[72:75], a[124:127], v[44:47]\n"
"v_mul_f32 v175, v175, v219\n"
"v_mfma_f32_16x16x32_bf16 v[48:51], a[76:79], a[112:115], v[48:51]\n"
"ds_read_b64_tr_b16 a[54:55], v137 offset:33792\n"
"v_mul_f32 v176, v176, v219\n"
"v_mul_f32 v177, v177, v219\n"
"v_mfma_f32_16x16x32_bf16 v[52:55], a[76:79], a[116:119], v[52:55]\n"
"buffer_load_dwordx4 v243, s[68:71], s81 offen offset:3072 lds\n"
"v_mul_f32 v178, v178, v219\n"
"v_mfma_f32_16x16x32_bf16 v[56:59], a[76:79], a[120:123], v[56:59]\n"
"ds_read_b64_tr_b16 a[56:57], v138 offset:32768\n"
"v_mul_f32 v179, v179, v219\n"
"v_mfma_f32_16x16x32_bf16 v[60:63], a[76:79], a[124:127], v[60:63]\n"
"v_mul_f32 v180, v180, v219\n"
"v_mul_f32 v181, v181, v219\n"
"v_mfma_f32_16x16x32_bf16 v[64:67], a[80:83], a[112:115], v[64:67]\n"
"v_mul_f32 v182, v182, v219\n"
"s_add_u32 m0, s57, 81920\n"
"v_mfma_f32_16x16x32_bf16 v[68:71], a[80:83], a[116:119], v[68:71]\n"
"buffer_load_dwordx4 v148, s[72:75], s81 offen offset:0 lds\n"
"v_mul_f32 v183, v183, v219\n"
"v_mfma_f32_16x16x32_bf16 v[72:75], a[80:83], a[120:123], v[72:75]\n"
"ds_read_b64_tr_b16 a[58:59], v138 offset:33792\n"
"v_mul_f32 v184, v184, v219\n"
"v_mul_f32 v185, v185, v219\n"
"v_mfma_f32_16x16x32_bf16 v[76:79], a[80:83], a[124:127], v[76:79]\n"
"ds_read_b64_tr_b16 a[60:61], v139 offset:32768\n"
"v_mul_f32 v186, v186, v219\n"
"v_mfma_f32_16x16x32_bf16 v[80:83], a[84:87], a[112:115], v[80:83]\n"
"v_mul_f32 v187, v187, v219\n"
"v_mul_f32 v188, v188, v219\n"
"v_mfma_f32_16x16x32_bf16 v[84:87], a[84:87], a[116:119], v[84:87]\n"
"buffer_load_dwordx4 v149, s[72:75], s81 offen offset:1024 lds\n"
"v_mul_f32 v189, v189, v219\n"
"v_mfma_f32_16x16x32_bf16 v[88:91], a[84:87], a[120:123], v[88:91]\n"
"ds_read_b64_tr_b16 a[62:63], v139 offset:33792\n"
"v_mul_f32 v190, v190, v219\n"
"v_mfma_f32_16x16x32_bf16 v[92:95], a[84:87], a[124:127], v[92:95]\n"
"v_mul_f32 v191, v191, v219\n"
"v_mul_f32 v192, v192, v219\n"
"v_mfma_f32_16x16x32_bf16 v[96:99], a[88:91], a[112:115], v[96:99]\n"
"v_mul_f32 v193, v193, v219\n"
"v_mul_f32 v194, v194, v219\n"
"v_mfma_f32_16x16x32_bf16 v[100:103], a[88:91], a[116:119], v[100:103]\n"
"buffer_load_dwordx4 v150, s[72:75], s81 offen offset:2048 lds\n"
"v_mul_f32 v195, v195, v219\n"
"v_mfma_f32_16x16x32_bf16 v[104:107], a[88:91], a[120:123], v[104:107]\n"
"v_lshl_add_u32 v160, %2, 10, v251\n"
"s_mov_b32 s60, 104988672\n"
"v_mfma_f32_16x16x32_bf16 v[108:111], a[88:91], a[124:127], v[108:111]\n"
"v_lshl_add_u32 v161, %2, 6, v252\n"
"v_lshrrev_b32 v162, 4, v161\n"
"v_mfma_f32_16x16x32_bf16 v[112:115], a[92:95], a[112:115], v[112:115]\n"
"v_min_u32 v162, 1, v162\n"
"v_lshlrev_b32 v162, 30, v162\n"
"v_mfma_f32_16x16x32_bf16 v[116:119], a[92:95], a[116:119], v[116:119]\n"
"buffer_load_dwordx4 v151, s[72:75], s81 offen offset:3072 lds\n"
"v_add_u32 v163, v162, v251\n"
"v_mfma_f32_16x16x32_bf16 v[120:123], a[92:95], a[120:123], v[120:123]\n"
"s_add_u32 s80, s80, 128\n"
"s_add_u32 s81, s81, 524288\n"
"v_mfma_f32_16x16x32_bf16 v[124:127], a[92:95], a[124:127], v[124:127]\n"
"s_waitcnt lgkmcnt(0)\n"
"v_mfma_f32_16x16x32_bf16 a[128:131], a[32:35], a[0:3], a[128:131]\n"
"buffer_load_dword v227, v240, s[88:91], s94 offen\n"
"buffer_load_dword v227, v140, s[64:67], s95 offen\n"
"s_add_u32 s94, s94, 524288\n"
"v_mfma_f32_16x16x32_bf16 a[132:135], a[32:35], a[4:7], a[132:135]\n"
"buffer_load_dword v227, v141, s[64:67], s95 offen offset:1024\n"
"s_add_u32 s95, s95, 128\n"
"v_mfma_f32_16x16x32_bf16 a[136:139], a[32:35], a[8:11], a[136:139]\n"
"buffer_load_dwordx4 v[196:199], v160, s[96:99], s60 offen sc1\n"
"s_add_u32 s60, s60, 4096\n"
"v_mfma_f32_16x16x32_bf16 a[140:143], a[32:35], a[12:15], a[140:143]\n"
"buffer_load_dwordx4 v[200:203], v160, s[96:99], s60 offen sc1\n"
"s_add_u32 s60, s60, 4096\n"
"buffer_load_dwordx4 v[204:207], v160, s[96:99], s60 offen sc1\n"
"v_mfma_f32_16x16x32_bf16 a[144:147], a[32:35], a[16:19], a[144:147]\n"
"s_add_u32 s60, s60, 4096\n"
"buffer_load_dwordx4 v[208:211], v160, s[96:99], s60 offen sc1\n"
"v_mfma_f32_16x16x32_bf16 a[148:151], a[32:35], a[20:23], a[148:151]\n"
"s_add_u32 s60, s60, 4096\n"
"buffer_load_dwordx4 v[212:215], v160, s[96:99], s60 offen sc1\n"
"v_mfma_f32_16x16x32_bf16 a[152:155], a[32:35], a[24:27], a[152:155]\n"
"buffer_load_dwordx4 v[216:219], v163, s[96:99], s56 offen sc1\n"
"s_add_u32 s60, s60, 4096\n"
"v_mfma_f32_16x16x32_bf16 a[156:159], a[32:35], a[28:31], a[156:159]\n"
"ds_read_b64_tr_b16 a[96:97], v132 offset:40960\n"
"v_mfma_f32_16x16x32_bf16 a[160:163], a[36:39], a[0:3], a[160:163]\n"
"v_and_b32 v157, 3, v252\n"
"v_lshl_or_b32 v255, v157, 4, v255\n"
"v_mfma_f32_16x16x32_bf16 a[164:167], a[36:39], a[4:7], a[164:167]\n"
"ds_read_b64_tr_b16 a[98:99], v132 offset:41984\n"
"s_lshl_b32 s85, %2, 13\n"
"v_mfma_f32_16x16x32_bf16 a[168:171], a[36:39], a[8:11], a[168:171]\n"
"s_add_u32 s85, s85, %3\n"
"s_add_u32 s85, s85, 131072\n"
"v_mfma_f32_16x16x32_bf16 a[172:175], a[36:39], a[12:15], a[172:175]\n"
"ds_read_b128 a[64:67], v129 offset:0\n"
"v_add_u32 v255, s85, v255\n"
"v_mfma_f32_16x16x32_bf16 a[176:179], a[36:39], a[16:19], a[176:179]\n"
"v_and_b32 v155, 7, v252\n"
"v_bfe_u32 v156, v252, 3, 1\n"
"v_mfma_f32_16x16x32_bf16 a[180:183], a[36:39], a[20:23], a[180:183]\n"
"ds_read_b128 a[68:71], v129 offset:1024\n"
"v_lshl_or_b32 v155, v156, 4, v155\n"
"v_mfma_f32_16x16x32_bf16 a[184:187], a[36:39], a[24:27], a[184:187]\n"
"v_bfe_u32 v156, v252, 4, 1\n"
"v_lshl_or_b32 v155, v156, 5, v155\n"
"v_mfma_f32_16x16x32_bf16 a[188:191], a[36:39], a[28:31], a[188:191]\n"
"ds_read_b128 a[72:75], v129 offset:4096\n"
"v_lshlrev_b32 v155, 4, v155\n"
"v_mfma_f32_16x16x32_bf16 a[192:195], a[40:43], a[0:3], a[192:195]\n"
"v_lshrrev_b32 v157, 5, v252\n"
"v_lshl_or_b32 v152, v157, 10, v155\n"
"v_mfma_f32_16x16x32_bf16 a[196:199], a[40:43], a[4:7], a[196:199]\n"
"ds_read_b128 a[76:79], v129 offset:5120\n"
"v_and_b32 v155, 15, v252\n"
"v_mfma_f32_16x16x32_bf16 a[200:203], a[40:43], a[8:11], a[200:203]\n"
"v_lshl_or_b32 v155, v156, 5, v155\n"
"v_lshlrev_b32 v155, 4, v155\n"
"v_mfma_f32_16x16x32_bf16 a[204:207], a[40:43], a[12:15], a[204:207]\n"
"ds_read_b128 a[80:83], v129 offset:8192\n"
"v_lshl_or_b32 v250, v157, 10, v155\n"
"v_mfma_f32_16x16x32_bf16 a[208:211], a[40:43], a[16:19], a[208:211]\n"
"v_and_b32 v155, 3, v252\n"
"v_lshl_or_b32 v155, v156, 5, v155\n"
"v_mfma_f32_16x16x32_bf16 a[212:215], a[40:43], a[20:23], a[212:215]\n"
"ds_read_b128 a[84:87], v129 offset:9216\n"
"v_bfe_u32 v156, v252, 2, 1\n"
"v_mfma_f32_16x16x32_bf16 a[216:219], a[40:43], a[24:27], a[216:219]\n"
"v_lshl_or_b32 v155, v156, 3, v155\n"
"v_bfe_u32 v156, v252, 3, 1\n"
"v_mfma_f32_16x16x32_bf16 a[220:223], a[40:43], a[28:31], a[220:223]\n"
"ds_read_b128 a[88:91], v129 offset:12288\n"
"v_lshl_or_b32 v155, v156, 2, v155\n"
"v_mfma_f32_16x16x32_bf16 a[224:227], a[44:47], a[0:3], a[224:227]\n"
"v_lshlrev_b32 v155, 4, v155\n"
"v_lshl_or_b32 v153, v157, 12, v155\n"
"v_mfma_f32_16x16x32_bf16 a[228:231], a[44:47], a[4:7], a[228:231]\n"
"ds_read_b128 a[92:95], v129 offset:13312\n"
"s_lshl_b32 s83, s24, 19\n"
"v_mfma_f32_16x16x32_bf16 a[232:235], a[44:47], a[8:11], a[232:235]\n"
"s_lshl_b32 s85, s25, 16\n"
"s_add_u32 s83, s83, s85\n"
"v_mfma_f32_16x16x32_bf16 a[236:239], a[44:47], a[12:15], a[236:239]\n"
"ds_read_b64_tr_b16 a[100:101], v133 offset:40960\n"
"s_and_b32 s85, %2, 1\n"
"v_mfma_f32_16x16x32_bf16 a[240:243], a[44:47], a[16:19], a[240:243]\n"
"s_lshl_b32 s85, s85, 15\n"
"s_add_u32 s83, s83, s85\n"
"v_mfma_f32_16x16x32_bf16 a[244:247], a[44:47], a[20:23], a[244:247]\n"
"ds_read_b64_tr_b16 a[102:103], v133 offset:41984\n"
"s_lshr_b32 s85, %2, 1\n"
"v_mfma_f32_16x16x32_bf16 a[248:251], a[44:47], a[24:27], a[248:251]\n"
"s_lshl_b32 s85, s85, 11\n"
"s_add_u32 s84, s83, s85\n"
"v_mfma_f32_16x16x32_bf16 a[252:255], a[44:47], a[28:31], a[252:255]\n"
"ds_read_b64_tr_b16 a[104:105], v134 offset:40960\n"
"s_add_u32 s83, s84, s85\n"
"v_mfma_f32_16x16x32_bf16 v[0:3], a[0:3], a[48:51], v[0:3]\n"
"s_add_u32 s83, s83, 16777216\n"
"s_add_u32 s84, s84, 33554432\n"
"v_mfma_f32_16x16x32_bf16 v[4:7], a[0:3], a[52:55], v[4:7]\n"
"ds_read_b64_tr_b16 a[106:107], v134 offset:41984\n"
"v_add_u32 v244, 0, v251\n"
"v_mfma_f32_16x16x32_bf16 v[8:11], a[0:3], a[56:59], v[8:11]\n"
"v_add_u32 v245, 20480, v251\n"
"v_add_u32 v246, 40960, v251\n"
"v_mfma_f32_16x16x32_bf16 v[12:15], a[0:3], a[60:63], v[12:15]\n"
"ds_read_b64_tr_b16 a[108:109], v135 offset:40960\n"
"v_add_u32 v247, 61440, v251\n"
"v_mfma_f32_16x16x32_bf16 v[16:19], a[4:7], a[48:51], v[16:19]\n"
"s_lshl_b32 s85, s25, 3\n"
"s_sub_u32 s10, 120, s85\n"
"v_mfma_f32_16x16x32_bf16 v[20:23], a[4:7], a[52:55], v[20:23]\n"
"ds_read_b64_tr_b16 a[110:111], v135 offset:41984\n"
"s_cmp_lt_u32 %2, 2\n"
"s_cselect_b32 s8, s85, s10\n"
"v_mfma_f32_16x16x32_bf16 v[24:27], a[4:7], a[56:59], v[24:27]\n"
"v_mfma_f32_16x16x32_bf16 v[28:31], a[4:7], a[60:63], v[28:31]\n"
"ds_read_b64_tr_b16 a[112:113], v136 offset:40960\n"
"v_mfma_f32_16x16x32_bf16 v[32:35], a[8:11], a[48:51], v[32:35]\n"
"v_mfma_f32_16x16x32_bf16 v[36:39], a[8:11], a[52:55], v[36:39]\n"
"ds_read_b64_tr_b16 a[114:115], v136 offset:41984\n"
"v_mfma_f32_16x16x32_bf16 v[40:43], a[8:11], a[56:59], v[40:43]\n"
"v_mfma_f32_16x16x32_bf16 v[44:47], a[8:11], a[60:63], v[44:47]\n"
"ds_read_b64_tr_b16 a[116:117], v137 offset:40960\n"
"v_mfma_f32_16x16x32_bf16 v[48:51], a[12:15], a[48:51], v[48:51]\n"
"v_mfma_f32_16x16x32_bf16 v[52:55], a[12:15], a[52:55], v[52:55]\n"
"ds_read_b64_tr_b16 a[118:119], v137 offset:41984\n"
"v_mfma_f32_16x16x32_bf16 v[56:59], a[12:15], a[56:59], v[56:59]\n"
"v_mfma_f32_16x16x32_bf16 v[60:63], a[12:15], a[60:63], v[60:63]\n"
"ds_read_b64_tr_b16 a[120:121], v138 offset:40960\n"
"v_mfma_f32_16x16x32_bf16 v[64:67], a[16:19], a[48:51], v[64:67]\n"
"v_mfma_f32_16x16x32_bf16 v[68:71], a[16:19], a[52:55], v[68:71]\n"
"ds_read_b64_tr_b16 a[122:123], v138 offset:41984\n"
"v_mfma_f32_16x16x32_bf16 v[72:75], a[16:19], a[56:59], v[72:75]\n"
"v_mfma_f32_16x16x32_bf16 v[76:79], a[16:19], a[60:63], v[76:79]\n"
"ds_read_b64_tr_b16 a[124:125], v139 offset:40960\n"
"v_mfma_f32_16x16x32_bf16 v[80:83], a[20:23], a[48:51], v[80:83]\n"
"v_mfma_f32_16x16x32_bf16 v[84:87], a[20:23], a[52:55], v[84:87]\n"
"ds_read_b64_tr_b16 a[126:127], v139 offset:41984\n"
"v_mfma_f32_16x16x32_bf16 v[88:91], a[20:23], a[56:59], v[88:91]\n"
"v_mfma_f32_16x16x32_bf16 v[92:95], a[20:23], a[60:63], v[92:95]\n"
"v_mfma_f32_16x16x32_bf16 v[96:99], a[24:27], a[48:51], v[96:99]\n"
"v_mfma_f32_16x16x32_bf16 v[100:103], a[24:27], a[52:55], v[100:103]\n"
"v_mfma_f32_16x16x32_bf16 v[104:107], a[24:27], a[56:59], v[104:107]\n"
"v_mfma_f32_16x16x32_bf16 v[108:111], a[24:27], a[60:63], v[108:111]\n"
"v_mfma_f32_16x16x32_bf16 v[112:115], a[28:31], a[48:51], v[112:115]\n"
"s_waitcnt lgkmcnt(0)\n"
"s_waitcnt vmcnt(9)\n"
"s_barrier\n"
"v_mfma_f32_16x16x32_bf16 v[116:119], a[28:31], a[52:55], v[116:119]\n"
"v_mfma_f32_16x16x32_bf16 v[120:123], a[28:31], a[56:59], v[120:123]\n"
"v_mfma_f32_16x16x32_bf16 v[124:127], a[28:31], a[60:63], v[124:127]\n"
"v_mfma_f32_16x16x32_bf16 a[128:131], a[96:99], a[64:67], a[128:131]\n"
"s_add_u32 m0, s82, 0\n"
"v_mfma_f32_16x16x32_bf16 a[132:135], a[96:99], a[68:71], a[132:135]\n"
"buffer_load_dwordx4 v140, s[64:67], s80 offen offset:0 lds\n"
"v_mfma_f32_16x16x32_bf16 a[136:139], a[96:99], a[72:75], a[136:139]\n"
"ds_read_b64_tr_b16 a[32:33], v132 offset:0\n"
"v_mfma_f32_16x16x32_bf16 a[140:143], a[96:99], a[76:79], a[140:143]\n"
"ds_read_b64_tr_b16 a[34:35], v132 offset:1024\n"
"v_mfma_f32_16x16x32_bf16 a[144:147], a[96:99], a[80:83], a[144:147]\n"
"v_mfma_f32_16x16x32_bf16 a[148:151], a[96:99], a[84:87], a[148:151]\n"
"buffer_load_dwordx4 v141, s[64:67], s80 offen offset:1024 lds\n"
"v_mfma_f32_16x16x32_bf16 a[152:155], a[96:99], a[88:91], a[152:155]\n"
"ds_read_b128 a[0:3], v128 offset:32768\n"
"v_mfma_f32_16x16x32_bf16 a[156:159], a[96:99], a[92:95], a[156:159]\n"
"ds_read_b128 a[4:7], v128 offset:33792\n"
"v_mfma_f32_16x16x32_bf16 a[160:163], a[100:103], a[64:67], a[160:163]\n"
"v_mfma_f32_16x16x32_bf16 a[164:167], a[100:103], a[68:71], a[164:167]\n"
"buffer_load_dwordx4 v142, s[64:67], s80 offen offset:2048 lds\n"
"v_mfma_f32_16x16x32_bf16 a[168:171], a[100:103], a[72:75], a[168:171]\n"
"ds_read_b128 a[8:11], v128 offset:36864\n"
"s_add_u32 s60, %3, 152576\n"
"v_mfma_f32_16x16x32_bf16 a[172:175], a[100:103], a[76:79], a[172:175]\n"
"ds_read_b128 a[12:15], v128 offset:37888\n"
"v_lshlrev_b32 v161, 2, v161\n"
"v_mfma_f32_16x16x32_bf16 a[176:179], a[100:103], a[80:83], a[176:179]\n"
"v_add_u32 v161, s60, v161\n"
"v_mfma_f32_16x16x32_bf16 a[180:183], a[100:103], a[84:87], a[180:183]\n"
"buffer_load_dwordx4 v143, s[64:67], s80 offen offset:3072 lds\n"
"s_waitcnt vmcnt(9)\n"
"v_add_f32 v196, v196, v197\n"
"v_mfma_f32_16x16x32_bf16 a[184:187], a[100:103], a[88:91], a[184:187]\n"
"ds_read_b128 a[16:19], v128 offset:40960\n"
"v_mfma_f32_16x16x32_bf16 a[188:191], a[100:103], a[92:95], a[188:191]\n"
"v_add_f32 v198, v198, v199\n"
"v_add_f32 v196, v196, v198\n"
"v_mfma_f32_16x16x32_bf16 a[192:195], a[104:107], a[64:67], a[192:195]\n"
"s_add_u32 m0, s82, 4096\n"
"v_mfma_f32_16x16x32_bf16 a[196:199], a[104:107], a[68:71], a[196:199]\n"
"buffer_load_dwordx4 v144, s[64:67], s80 offen offset:0 lds\n"
"v_cvt_pk_bf16_f32 v196, v196, v196\n"
"v_mfma_f32_16x16x32_bf16 a[200:203], a[104:107], a[72:75], a[200:203]\n"
"ds_read_b128 a[20:23], v128 offset:41984\n"
"v_and_b32 v196, 0xffff0000, v196\n"
"v_mfma_f32_16x16x32_bf16 a[204:207], a[104:107], a[76:79], a[204:207]\n"
"ds_read_b128 a[24:27], v128 offset:45056\n"
"v_mfma_f32_16x16x32_bf16 a[208:211], a[104:107], a[80:83], a[208:211]\n"
"v_mul_f32 v199, 0x3e0293ee, v196\n"
"v_mfma_f32_16x16x32_bf16 a[212:215], a[104:107], a[84:87], a[212:215]\n"
"buffer_load_dwordx4 v145, s[64:67], s80 offen offset:1024 lds\n"
"v_mfma_f32_16x16x32_bf16 a[216:219], a[104:107], a[88:91], a[216:219]\n"
"ds_read_b128 a[28:31], v128 offset:46080\n"
"v_mfma_f32_16x16x32_bf16 a[220:223], a[104:107], a[92:95], a[220:223]\n"
"ds_read_b64_tr_b16 a[36:37], v133 offset:0\n"
"v_mfma_f32_16x16x32_bf16 a[224:227], a[108:111], a[64:67], a[224:227]\n"
"ds_write_b32 v161, v199 offset:0\n"
"v_mfma_f32_16x16x32_bf16 a[228:231], a[108:111], a[68:71], a[228:231]\n"
"buffer_load_dwordx4 v146, s[64:67], s80 offen offset:2048 lds\n"
"s_waitcnt vmcnt(7)\n"
"v_add_f32 v200, v200, v201\n"
"v_mfma_f32_16x16x32_bf16 a[232:235], a[108:111], a[72:75], a[232:235]\n"
"ds_read_b64_tr_b16 a[38:39], v133 offset:1024\n"
"v_add_f32 v202, v202, v203\n"
"v_mfma_f32_16x16x32_bf16 a[236:239], a[108:111], a[76:79], a[236:239]\n"
"v_add_f32 v200, v200, v202\n"
"v_cvt_pk_bf16_f32 v200, v200, v200\n"
"v_mfma_f32_16x16x32_bf16 a[240:243], a[108:111], a[80:83], a[240:243]\n"
"ds_read_b64_tr_b16 a[40:41], v134 offset:0\n"
"v_and_b32 v200, 0xffff0000, v200\n"
"v_mfma_f32_16x16x32_bf16 a[244:247], a[108:111], a[84:87], a[244:247]\n"
"buffer_load_dwordx4 v147, s[64:67], s80 offen offset:3072 lds\n"
"v_mul_f32 v203, 0x3e0293ee, v200\n"
"v_mfma_f32_16x16x32_bf16 a[248:251], a[108:111], a[88:91], a[248:251]\n"
"ds_read_b64_tr_b16 a[42:43], v134 offset:1024\n"
"v_mfma_f32_16x16x32_bf16 a[252:255], a[108:111], a[92:95], a[252:255]\n"
"ds_write_b32 v161, v203 offset:1024\n"
"v_mfma_f32_16x16x32_bf16 v[0:3], a[64:67], a[112:115], v[0:3]\n"
"s_add_u32 m0, s57, 98304\n"
"v_mfma_f32_16x16x32_bf16 v[4:7], a[64:67], a[116:119], v[4:7]\n"
"buffer_load_dwordx4 v240, s[68:71], s81 offen offset:0 lds\n"
"v_add_f32 v204, v204, v205\n"
"v_mfma_f32_16x16x32_bf16 v[8:11], a[64:67], a[120:123], v[8:11]\n"
"ds_read_b64_tr_b16 a[44:45], v135 offset:0\n"
"v_add_f32 v206, v206, v207\n"
"v_mfma_f32_16x16x32_bf16 v[12:15], a[64:67], a[124:127], v[12:15]\n"
"ds_read_b64_tr_b16 a[46:47], v135 offset:1024\n"
"v_mfma_f32_16x16x32_bf16 v[16:19], a[68:71], a[112:115], v[16:19]\n"
"v_add_f32 v204, v204, v206\n"
"v_mfma_f32_16x16x32_bf16 v[20:23], a[68:71], a[116:119], v[20:23]\n"
"buffer_load_dwordx4 v241, s[68:71], s81 offen offset:1024 lds\n"
"v_cvt_pk_bf16_f32 v204, v204, v204\n"
"v_mfma_f32_16x16x32_bf16 v[24:27], a[68:71], a[120:123], v[24:27]\n"
"ds_read_b64_tr_b16 a[48:49], v136 offset:0\n"
"v_and_b32 v204, 0xffff0000, v204\n"
"v_mfma_f32_16x16x32_bf16 v[28:31], a[68:71], a[124:127], v[28:31]\n"
"ds_read_b64_tr_b16 a[50:51], v136 offset:1024\n"
"v_mfma_f32_16x16x32_bf16 v[32:35], a[72:75], a[112:115], v[32:35]\n"
"v_add_f32 v205, s42, v204\n"
"v_cvt_pk_bf16_f32 v206, v205, v205\n"
"v_mfma_f32_16x16x32_bf16 v[36:39], a[72:75], a[116:119], v[36:39]\n"
"buffer_load_dwordx4 v242, s[68:71], s81 offen offset:2048 lds\n"
"v_mfma_f32_16x16x32_bf16 v[40:43], a[72:75], a[120:123], v[40:43]\n"
"ds_read_b64_tr_b16 a[52:53], v137 offset:0\n"
"v_and_b32 v205, 0xffff0000, v206\n"
"v_mfma_f32_16x16x32_bf16 v[44:47], a[72:75], a[124:127], v[44:47]\n"
"v_mul_f32 v207, 0x3e0293ee, v205\n"
"v_mfma_f32_16x16x32_bf16 v[48:51], a[76:79], a[112:115], v[48:51]\n"
"ds_read_b64_tr_b16 a[54:55], v137 offset:1024\n"
"v_mfma_f32_16x16x32_bf16 v[52:55], a[76:79], a[116:119], v[52:55]\n"
"buffer_load_dwordx4 v243, s[68:71], s81 offen offset:3072 lds\n"
"v_mfma_f32_16x16x32_bf16 v[56:59], a[76:79], a[120:123], v[56:59]\n"
"ds_read_b64_tr_b16 a[56:57], v138 offset:0\n"
"v_mfma_f32_16x16x32_bf16 v[60:63], a[76:79], a[124:127], v[60:63]\n"
"ds_write_b32 v161, v207 offset:2048\n"
"v_mfma_f32_16x16x32_bf16 v[64:67], a[80:83], a[112:115], v[64:67]\n"
"v_add_f32 v208, v208, v209\n"
"s_add_u32 m0, s57, 114688\n"
"v_mfma_f32_16x16x32_bf16 v[68:71], a[80:83], a[116:119], v[68:71]\n"
"buffer_load_dwordx4 v148, s[72:75], s81 offen offset:0 lds\n"
"v_add_f32 v210, v210, v211\n"
"v_mfma_f32_16x16x32_bf16 v[72:75], a[80:83], a[120:123], v[72:75]\n"
"ds_read_b64_tr_b16 a[58:59], v138 offset:1024\n"
"v_add_f32 v208, v208, v210\n"
"v_mfma_f32_16x16x32_bf16 v[76:79], a[80:83], a[124:127], v[76:79]\n"
"ds_read_b64_tr_b16 a[60:61], v139 offset:0\n"
"v_cvt_pk_bf16_f32 v208, v208, v208\n"
"v_mfma_f32_16x16x32_bf16 v[80:83], a[84:87], a[112:115], v[80:83]\n"
"v_and_b32 v208, 0xffff0000, v208\n"
"v_mfma_f32_16x16x32_bf16 v[84:87], a[84:87], a[116:119], v[84:87]\n"
"buffer_load_dwordx4 v149, s[72:75], s81 offen offset:1024 lds\n"
"v_add_f32 v209, 1.0, v208\n"
"v_mfma_f32_16x16x32_bf16 v[88:91], a[84:87], a[120:123], v[88:91]\n"
"ds_read_b64_tr_b16 a[62:63], v139 offset:1024\n"
"v_cvt_pk_bf16_f32 v210, v209, v209\n"
"v_mfma_f32_16x16x32_bf16 v[92:95], a[84:87], a[124:127], v[92:95]\n"
"v_and_b32 v209, 0xffff0000, v210\n"
"v_mfma_f32_16x16x32_bf16 v[96:99], a[88:91], a[112:115], v[96:99]\n"
"v_mul_f32 v211, 0x3e0293ee, v209\n"
"v_mfma_f32_16x16x32_bf16 v[100:103], a[88:91], a[116:119], v[100:103]\n"
"buffer_load_dwordx4 v150, s[72:75], s81 offen offset:2048 lds\n"
"v_mfma_f32_16x16x32_bf16 v[104:107], a[88:91], a[120:123], v[104:107]\n"
"ds_write_b32 v161, v211 offset:3072\n"
"v_mfma_f32_16x16x32_bf16 v[108:111], a[88:91], a[124:127], v[108:111]\n"
"v_add_f32 v212, v212, v213\n"
"v_add_f32 v214, v214, v215\n"
"v_mfma_f32_16x16x32_bf16 v[112:115], a[92:95], a[112:115], v[112:115]\n"
"v_add_f32 v212, v212, v214\n"
"v_mfma_f32_16x16x32_bf16 v[116:119], a[92:95], a[116:119], v[116:119]\n"
"buffer_load_dwordx4 v151, s[72:75], s81 offen offset:3072 lds\n"
"v_mfma_f32_16x16x32_bf16 v[120:123], a[92:95], a[120:123], v[120:123]\n"
"s_add_u32 s80, s80, 128\n"
"s_add_u32 s81, s81, 524288\n"
"v_mfma_f32_16x16x32_bf16 v[124:127], a[92:95], a[124:127], v[124:127]\n"
"s_waitcnt lgkmcnt(0)\n"
"v_mfma_f32_16x16x32_bf16 a[128:131], a[32:35], a[0:3], a[128:131]\n"
"buffer_load_dword v227, v240, s[88:91], s94 offen\n"
"s_add_u32 s94, s94, 524288\n"
"v_mfma_f32_16x16x32_bf16 a[132:135], a[32:35], a[4:7], a[132:135]\n"
"buffer_load_dword v227, v140, s[64:67], s95 offen\n"
"v_mfma_f32_16x16x32_bf16 a[136:139], a[32:35], a[8:11], a[136:139]\n"
"buffer_load_dword v227, v141, s[64:67], s95 offen offset:1024\n"
"v_mfma_f32_16x16x32_bf16 a[140:143], a[32:35], a[12:15], a[140:143]\n"
"buffer_load_dword v227, v240, s[76:79], s93 offen\n"
"s_add_u32 s95, s95, 128\n"
"v_mfma_f32_16x16x32_bf16 a[144:147], a[32:35], a[16:19], a[144:147]\n"
"s_add_u32 s93, s93, 32768\n"
"v_mfma_f32_16x16x32_bf16 a[148:151], a[32:35], a[20:23], a[148:151]\n"
"buffer_load_dword v227, v240, s[76:79], s93 offen\n"
"v_mfma_f32_16x16x32_bf16 a[152:155], a[32:35], a[24:27], a[152:155]\n"
"s_add_u32 s93, s93, 32768\n"
"v_mfma_f32_16x16x32_bf16 a[156:159], a[32:35], a[28:31], a[156:159]\n"
"ds_read_b64_tr_b16 a[96:97], v132 offset:8192\n"
"v_mfma_f32_16x16x32_bf16 a[160:163], a[36:39], a[0:3], a[160:163]\n"
"v_cvt_pk_bf16_f32 v212, v212, v212\n"
"v_mfma_f32_16x16x32_bf16 a[164:167], a[36:39], a[4:7], a[164:167]\n"
"ds_read_b64_tr_b16 a[98:99], v132 offset:9216\n"
"v_mfma_f32_16x16x32_bf16 a[168:171], a[36:39], a[8:11], a[168:171]\n"
"v_and_b32 v212, 0xffff0000, v212\n"
"v_mfma_f32_16x16x32_bf16 a[172:175], a[36:39], a[12:15], a[172:175]\n"
"ds_read_b128 a[64:67], v129 offset:32768\n"
"v_mfma_f32_16x16x32_bf16 a[176:179], a[36:39], a[16:19], a[176:179]\n"
"v_add_f32 v213, 1.0, v212\n"
"v_mfma_f32_16x16x32_bf16 a[180:183], a[36:39], a[20:23], a[180:183]\n"
"ds_read_b128 a[68:71], v129 offset:33792\n"
"v_mfma_f32_16x16x32_bf16 a[184:187], a[36:39], a[24:27], a[184:187]\n"
"v_mfma_f32_16x16x32_bf16 a[188:191], a[36:39], a[28:31], a[188:191]\n"
"ds_read_b128 a[72:75], v129 offset:36864\n"
"v_cvt_pk_bf16_f32 v214, v213, v213\n"
"v_mfma_f32_16x16x32_bf16 a[192:195], a[40:43], a[0:3], a[192:195]\n"
"v_mfma_f32_16x16x32_bf16 a[196:199], a[40:43], a[4:7], a[196:199]\n"
"ds_read_b128 a[76:79], v129 offset:37888\n"
"v_mfma_f32_16x16x32_bf16 a[200:203], a[40:43], a[8:11], a[200:203]\n"
"v_and_b32 v213, 0xffff0000, v214\n"
"v_mfma_f32_16x16x32_bf16 a[204:207], a[40:43], a[12:15], a[204:207]\n"
"ds_read_b128 a[80:83], v129 offset:40960\n"
"v_mfma_f32_16x16x32_bf16 a[208:211], a[40:43], a[16:19], a[208:211]\n"
"v_mul_f32 v215, 0x3e0293ee, v213\n"
"v_mfma_f32_16x16x32_bf16 a[212:215], a[40:43], a[20:23], a[212:215]\n"
"ds_read_b128 a[84:87], v129 offset:41984\n"
"v_mfma_f32_16x16x32_bf16 a[216:219], a[40:43], a[24:27], a[216:219]\n"
"ds_write_b32 v161, v215 offset:4096\n"
"v_mfma_f32_16x16x32_bf16 a[220:223], a[40:43], a[28:31], a[220:223]\n"
"ds_read_b128 a[88:91], v129 offset:45056\n"
"v_mfma_f32_16x16x32_bf16 a[224:227], a[44:47], a[0:3], a[224:227]\n"
"v_mfma_f32_16x16x32_bf16 a[228:231], a[44:47], a[4:7], a[228:231]\n"
"ds_read_b128 a[92:95], v129 offset:46080\n"
"v_mfma_f32_16x16x32_bf16 a[232:235], a[44:47], a[8:11], a[232:235]\n"
"v_mfma_f32_16x16x32_bf16 a[236:239], a[44:47], a[12:15], a[236:239]\n"
"ds_read_b64_tr_b16 a[100:101], v133 offset:8192\n"
"v_mfma_f32_16x16x32_bf16 a[240:243], a[44:47], a[16:19], a[240:243]\n"
"v_add_f32 v216, v216, v217\n"
"v_mfma_f32_16x16x32_bf16 a[244:247], a[44:47], a[20:23], a[244:247]\n"
"ds_read_b64_tr_b16 a[102:103], v133 offset:9216\n"
"v_mfma_f32_16x16x32_bf16 a[248:251], a[44:47], a[24:27], a[248:251]\n"
"v_add_f32 v218, v218, v219\n"
"v_mfma_f32_16x16x32_bf16 a[252:255], a[44:47], a[28:31], a[252:255]\n"
"ds_read_b64_tr_b16 a[104:105], v134 offset:8192\n"
"v_mfma_f32_16x16x32_bf16 v[0:3], a[0:3], a[48:51], v[0:3]\n"
"v_add_f32 v216, v216, v218\n"
"v_mfma_f32_16x16x32_bf16 v[4:7], a[0:3], a[52:55], v[4:7]\n"
"ds_read_b64_tr_b16 a[106:107], v134 offset:9216\n"
"v_mfma_f32_16x16x32_bf16 v[8:11], a[0:3], a[56:59], v[8:11]\n"
"v_mfma_f32_16x16x32_bf16 v[12:15], a[0:3], a[60:63], v[12:15]\n"
"ds_read_b64_tr_b16 a[108:109], v135 offset:8192\n"
"v_cvt_pk_bf16_f32 v216, v216, v216\n"
"v_mfma_f32_16x16x32_bf16 v[16:19], a[4:7], a[48:51], v[16:19]\n"
"v_mfma_f32_16x16x32_bf16 v[20:23], a[4:7], a[52:55], v[20:23]\n"
"ds_read_b64_tr_b16 a[110:111], v135 offset:9216\n"
"v_mfma_f32_16x16x32_bf16 v[24:27], a[4:7], a[56:59], v[24:27]\n"
"v_and_b32 v216, 0xffff0000, v216\n"
"v_mfma_f32_16x16x32_bf16 v[28:31], a[4:7], a[60:63], v[28:31]\n"
"ds_read_b64_tr_b16 a[112:113], v136 offset:8192\n"
"v_mfma_f32_16x16x32_bf16 v[32:35], a[8:11], a[48:51], v[32:35]\n"
"v_add_f32 v217, s43, v216\n"
"v_mfma_f32_16x16x32_bf16 v[36:39], a[8:11], a[52:55], v[36:39]\n"
"ds_read_b64_tr_b16 a[114:115], v136 offset:9216\n"
"v_mfma_f32_16x16x32_bf16 v[40:43], a[8:11], a[56:59], v[40:43]\n"
"v_cvt_pk_bf16_f32 v218, v217, v217\n"
"v_mfma_f32_16x16x32_bf16 v[44:47], a[8:11], a[60:63], v[44:47]\n"
"ds_read_b64_tr_b16 a[116:117], v137 offset:8192\n"
"v_mfma_f32_16x16x32_bf16 v[48:51], a[12:15], a[48:51], v[48:51]\n"
"v_mfma_f32_16x16x32_bf16 v[52:55], a[12:15], a[52:55], v[52:55]\n"
"ds_read_b64_tr_b16 a[118:119], v137 offset:9216\n"
"v_and_b32 v217, 0xffff0000, v218\n"
"v_mfma_f32_16x16x32_bf16 v[56:59], a[12:15], a[56:59], v[56:59]\n"
"v_mfma_f32_16x16x32_bf16 v[60:63], a[12:15], a[60:63], v[60:63]\n"
"ds_read_b64_tr_b16 a[120:121], v138 offset:8192\n"
"v_mfma_f32_16x16x32_bf16 v[64:67], a[16:19], a[48:51], v[64:67]\n"
"v_lshl_add_u32 v212, v252, 2, v162\n"
"v_mfma_f32_16x16x32_bf16 v[68:71], a[16:19], a[52:55], v[68:71]\n"
"ds_read_b64_tr_b16 a[122:123], v138 offset:9216\n"
"v_mfma_f32_16x16x32_bf16 v[72:75], a[16:19], a[56:59], v[72:75]\n"
"v_lshl_add_u32 v213, v252, 1, v162\n"
"v_mfma_f32_16x16x32_bf16 v[76:79], a[16:19], a[60:63], v[76:79]\n"
"ds_read_b64_tr_b16 a[124:125], v139 offset:8192\n"
"v_mfma_f32_16x16x32_bf16 v[80:83], a[20:23], a[48:51], v[80:83]\n"
"v_mov_b32 v219, 0x3f80\n"
"v_mfma_f32_16x16x32_bf16 v[84:87], a[20:23], a[52:55], v[84:87]\n"
"ds_read_b64_tr_b16 a[126:127], v139 offset:9216\n"
"v_mfma_f32_16x16x32_bf16 v[88:91], a[20:23], a[56:59], v[88:91]\n"
"v_mfma_f32_16x16x32_bf16 v[92:95], a[20:23], a[60:63], v[92:95]\n"
"PG3_%=:\n"
"s_cbranch_execz RPG3_%=\n"
"v_lshlrev_b32 v221, 5, v221\n"
"v_mfma_f32_16x16x32_bf16 v[96:99], a[24:27], a[48:51], v[96:99]\n"
"v_mfma_f32_16x16x32_bf16 v[100:103], a[24:27], a[52:55], v[100:103]\n"
"v_add_u32 v221, s60, v221\n"
"v_mfma_f32_16x16x32_bf16 v[104:107], a[24:27], a[56:59], v[104:107]\n"
"v_mfma_f32_16x16x32_bf16 v[108:111], a[24:27], a[60:63], v[108:111]\n"
"v_mfma_f32_16x16x32_bf16 v[112:115], a[28:31], a[48:51], v[112:115]\n"
"s_waitcnt lgkmcnt(0)\n"
"s_waitcnt vmcnt(5)\n"
"s_barrier\n"
"v_mfma_f32_16x16x32_bf16 v[116:119], a[28:31], a[52:55], v[116:119]\n"
"v_mfma_f32_16x16x32_bf16 v[120:123], a[28:31], a[56:59], v[120:123]\n"
"v_mfma_f32_16x16x32_bf16 v[124:127], a[28:31], a[60:63], v[124:127]\n"
"v_mfma_f32_16x16x32_bf16 a[128:131], a[96:99], a[64:67], a[128:131]\n"
"s_add_u32 m0, s82, 32768\n"
"s_add_u32 s60, %3, 131072\n"
"v_mfma_f32_16x16x32_bf16 a[132:135], a[96:99], a[68:71], a[132:135]\n"
"buffer_load_dwordx4 v140, s[64:67], s80 offen offset:0 lds\n"
"v_and_b32 v224, 31, v252\n"
"v_mfma_f32_16x16x32_bf16 a[136:139], a[96:99], a[72:75], a[136:139]\n"
"ds_read_b64_tr_b16 a[32:33], v132 offset:32768\n"
"v_add_u32 v220, v220, v224\n"
"v_mfma_f32_16x16x32_bf16 a[140:143], a[96:99], a[76:79], a[140:143]\n"
"ds_read_b64_tr_b16 a[34:35], v132 offset:33792\n"
"v_lshlrev_b32 v224, 7, v224\n"
"v_mfma_f32_16x16x32_bf16 a[144:147], a[96:99], a[80:83], a[144:147]\n"
"v_and_b32 v220, 7, v220\n"
"v_mfma_f32_16x16x32_bf16 a[148:151], a[96:99], a[84:87], a[148:151]\n"
"buffer_load_dwordx4 v141, s[64:67], s80 offen offset:1024 lds\n"
"v_lshl_add_u32 v224, v220, 4, v224\n"
"v_mfma_f32_16x16x32_bf16 a[152:155], a[96:99], a[88:91], a[152:155]\n"
"ds_read_b128 a[0:3], v128 offset:0\n"
"v_add_u32 v224, s60, v224\n"
"v_mfma_f32_16x16x32_bf16 a[156:159], a[96:99], a[92:95], a[156:159]\n"
"ds_read_b128 a[4:7], v128 offset:1024\n"
"v_mfma_f32_16x16x32_bf16 a[160:163], a[100:103], a[64:67], a[160:163]\n"
"ds_read_b128 v[196:199], v221 offset:2560\n"
"ds_read_b128 v[200:203], v221 offset:2576\n"
"v_mfma_f32_16x16x32_bf16 a[164:167], a[100:103], a[68:71], a[164:167]\n"
"ds_read_b128 v[204:207], v221 offset:0\n"
"buffer_load_dwordx4 v142, s[64:67], s80 offen offset:2048 lds\n"
"v_mfma_f32_16x16x32_bf16 a[168:171], a[100:103], a[72:75], a[168:171]\n"
"ds_read_b128 a[8:11], v128 offset:4096\n"
"v_mfma_f32_16x16x32_bf16 a[172:175], a[100:103], a[76:79], a[172:175]\n"
"ds_read_b128 a[12:15], v128 offset:5120\n"
"ds_read_b128 v[208:211], v221 offset:16\n"
"v_mfma_f32_16x16x32_bf16 a[176:179], a[100:103], a[80:83], a[176:179]\n"
"s_waitcnt lgkmcnt(3)\n"
"v_fma_f32 v172, v172, v196, v204\n"
"v_fma_f32 v173, v173, v197, v205\n"
"v_mfma_f32_16x16x32_bf16 a[180:183], a[100:103], a[84:87], a[180:183]\n"
"buffer_load_dwordx4 v143, s[64:67], s80 offen offset:3072 lds\n"
"v_mfma_f32_16x16x32_bf16 a[184:187], a[100:103], a[88:91], a[184:187]\n"
"ds_read_b128 a[16:19], v128 offset:8192\n"
"v_fma_f32 v174, v174, v198, v206\n"
"v_mfma_f32_16x16x32_bf16 a[188:191], a[100:103], a[92:95], a[188:191]\n"
"v_fma_f32 v175, v175, v199, v207\n"
"s_waitcnt lgkmcnt(1)\n"
"v_fma_f32 v176, v176, v200, v208\n"
"v_mfma_f32_16x16x32_bf16 a[192:195], a[104:107], a[64:67], a[192:195]\n"
"v_fma_f32 v177, v177, v201, v209\n"
"s_add_u32 m0, s82, 36864\n"
"v_mfma_f32_16x16x32_bf16 a[196:199], a[104:107], a[68:71], a[196:199]\n"
"buffer_load_dwordx4 v144, s[64:67], s80 offen offset:0 lds\n"
"v_mfma_f32_16x16x32_bf16 a[200:203], a[104:107], a[72:75], a[200:203]\n"
"ds_read_b128 a[20:23], v128 offset:9216\n"
"v_fma_f32 v178, v178, v202, v210\n"
"v_mfma_f32_16x16x32_bf16 a[204:207], a[104:107], a[76:79], a[204:207]\n"
"ds_read_b128 a[24:27], v128 offset:12288\n"
"v_fma_f32 v179, v179, v203, v211\n"
"v_mfma_f32_16x16x32_bf16 a[208:211], a[104:107], a[80:83], a[208:211]\n"
"v_cvt_pk_bf16_f32 v160, v172, v173\n"
"v_mfma_f32_16x16x32_bf16 a[212:215], a[104:107], a[84:87], a[212:215]\n"
"buffer_load_dwordx4 v145, s[64:67], s80 offen offset:1024 lds\n"
"v_cvt_pk_bf16_f32 v161, v174, v175\n"
"v_mfma_f32_16x16x32_bf16 a[216:219], a[104:107], a[88:91], a[216:219]\n"
"ds_read_b128 a[28:31], v128 offset:13312\n"
"v_cvt_pk_bf16_f32 v162, v176, v177\n"
"v_mfma_f32_16x16x32_bf16 a[220:223], a[104:107], a[92:95], a[220:223]\n"
"ds_read_b64_tr_b16 a[36:37], v133 offset:32768\n"
"v_cvt_pk_bf16_f32 v163, v178, v179\n"
"v_mfma_f32_16x16x32_bf16 a[224:227], a[108:111], a[64:67], a[224:227]\n"
"ds_write_b128 v224, v[160:163] offset:0\n"
"v_mfma_f32_16x16x32_bf16 a[228:231], a[108:111], a[68:71], a[228:231]\n"
"buffer_load_dwordx4 v146, s[64:67], s80 offen offset:2048 lds\n"
"v_mfma_f32_16x16x32_bf16 a[232:235], a[108:111], a[72:75], a[232:235]\n"
"ds_read_b64_tr_b16 a[38:39], v133 offset:33792\n"
"ds_read_b128 v[196:199], v221 offset:3584\n"
"v_mfma_f32_16x16x32_bf16 a[236:239], a[108:111], a[76:79], a[236:239]\n"
"ds_read_b128 v[200:203], v221 offset:3600\n"
"v_mfma_f32_16x16x32_bf16 a[240:243], a[108:111], a[80:83], a[240:243]\n"
"ds_read_b64_tr_b16 a[40:41], v134 offset:32768\n"
"ds_read_b128 v[204:207], v221 offset:1024\n"
"v_mfma_f32_16x16x32_bf16 a[244:247], a[108:111], a[84:87], a[244:247]\n"
"ds_read_b128 v[208:211], v221 offset:1040\n"
"buffer_load_dwordx4 v147, s[64:67], s80 offen offset:3072 lds\n"
"v_mfma_f32_16x16x32_bf16 a[248:251], a[108:111], a[88:91], a[248:251]\n"
"ds_read_b64_tr_b16 a[42:43], v134 offset:33792\n"
"v_mfma_f32_16x16x32_bf16 a[252:255], a[108:111], a[92:95], a[252:255]\n"
"s_waitcnt lgkmcnt(2)\n"
"v_fma_f32 v180, v180, v196, v204\n"
"v_fma_f32 v181, v181, v197, v205\n"
"v_mfma_f32_16x16x32_bf16 v[0:3], a[64:67], a[112:115], v[0:3]\n"
"v_fma_f32 v182, v182, v198, v206\n"
"s_add_u32 m0, s57, 65536\n"
"v_mfma_f32_16x16x32_bf16 v[4:7], a[64:67], a[116:119], v[4:7]\n"
"buffer_load_dwordx4 v240, s[68:71], s81 offen offset:0 lds\n"
"v_fma_f32 v183, v183, v199, v207\n"
"v_mfma_f32_16x16x32_bf16 v[8:11], a[64:67], a[120:123], v[8:11]\n"
"ds_read_b64_tr_b16 a[44:45], v135 offset:32768\n"
"v_mfma_f32_16x16x32_bf16 v[12:15], a[64:67], a[124:127], v[12:15]\n"
"ds_read_b64_tr_b16 a[46:47], v135 offset:33792\n"
"s_waitcnt lgkmcnt(3)\n"
"v_fma_f32 v184, v184, v200, v208\n"
"v_mfma_f32_16x16x32_bf16 v[16:19], a[68:71], a[112:115], v[16:19]\n"
"v_fma_f32 v185, v185, v201, v209\n"
"v_fma_f32 v186, v186, v202, v210\n"
"v_mfma_f32_16x16x32_bf16 v[20:23], a[68:71], a[116:119], v[20:23]\n"
"buffer_load_dwordx4 v241, s[68:71], s81 offen offset:1024 lds\n"
"v_mfma_f32_16x16x32_bf16 v[24:27], a[68:71], a[120:123], v[24:27]\n"
"ds_read_b64_tr_b16 a[48:49], v136 offset:32768\n"
"v_fma_f32 v187, v187, v203, v211\n"
"v_mfma_f32_16x16x32_bf16 v[28:31], a[68:71], a[124:127], v[28:31]\n"
"ds_read_b64_tr_b16 a[50:51], v136 offset:33792\n"
"v_cvt_pk_bf16_f32 v164, v180, v181\n"
"v_mfma_f32_16x16x32_bf16 v[32:35], a[72:75], a[112:115], v[32:35]\n"
"v_cvt_pk_bf16_f32 v165, v182, v183\n"
"v_mfma_f32_16x16x32_bf16 v[36:39], a[72:75], a[116:119], v[36:39]\n"
"buffer_load_dwordx4 v242, s[68:71], s81 offen offset:2048 lds\n"
"v_cvt_pk_bf16_f32 v166, v184, v185\n"
"v_mfma_f32_16x16x32_bf16 v[40:43], a[72:75], a[120:123], v[40:43]\n"
"ds_read_b64_tr_b16 a[52:53], v137 offset:32768\n"
"v_cvt_pk_bf16_f32 v167, v186, v187\n"
"v_mfma_f32_16x16x32_bf16 v[44:47], a[72:75], a[124:127], v[44:47]\n"
"ds_write_b128 v224, v[164:167] offset:4096\n"
"v_mfma_f32_16x16x32_bf16 v[48:51], a[76:79], a[112:115], v[48:51]\n"
"ds_read_b64_tr_b16 a[54:55], v137 offset:33792\n"
"v_mfma_f32_16x16x32_bf16 v[52:55], a[76:79], a[116:119], v[52:55]\n"
"ds_read_b128 v[196:199], v221 offset:4608\n"
"buffer_load_dwordx4 v243, s[68:71], s81 offen offset:3072 lds\n"
"v_mfma_f32_16x16x32_bf16 v[56:59], a[76:79], a[120:123], v[56:59]\n"
"ds_read_b64_tr_b16 a[56:57], v138 offset:32768\n"
"ds_read_b128 v[200:203], v221 offset:4624\n"
"v_mfma_f32_16x16x32_bf16 v[60:63], a[76:79], a[124:127], v[60:63]\n"
"ds_read_b128 v[204:207], v221 offset:2048\n"
"v_mfma_f32_16x16x32_bf16 v[64:67], a[80:83], a[112:115], v[64:67]\n"
"ds_read_b128 v[208:211], v221 offset:2064\n"
"s_add_u32 m0, s57, 81920\n"
"v_mfma_f32_16x16x32_bf16 v[68:71], a[80:83], a[116:119], v[68:71]\n"
"buffer_load_dwordx4 v148, s[72:75], s81 offen offset:0 lds\n"
"s_waitcnt lgkmcnt(1)\n"
"v_fma_f32 v188, v188, v196, v204\n"
"v_mfma_f32_16x16x32_bf16 v[72:75], a[80:83], a[120:123], v[72:75]\n"
"ds_read_b64_tr_b16 a[58:59], v138 offset:33792\n"
"v_fma_f32 v189, v189, v197, v205\n"
"v_mfma_f32_16x16x32_bf16 v[76:79], a[80:83], a[124:127], v[76:79]\n"
"ds_read_b64_tr_b16 a[60:61], v139 offset:32768\n"
"v_mfma_f32_16x16x32_bf16 v[80:83], a[84:87], a[112:115], v[80:83]\n"
"v_fma_f32 v190, v190, v198, v206\n"
"v_fma_f32 v191, v191, v199, v207\n"
"v_mfma_f32_16x16x32_bf16 v[84:87], a[84:87], a[116:119], v[84:87]\n"
"buffer_load_dwordx4 v149, s[72:75], s81 offen offset:1024 lds\n"
"s_waitcnt lgkmcnt(2)\n"
"v_fma_f32 v192, v192, v200, v208\n"
"v_mfma_f32_16x16x32_bf16 v[88:91], a[84:87], a[120:123], v[88:91]\n"
"ds_read_b64_tr_b16 a[62:63], v139 offset:33792\n"
"v_mfma_f32_16x16x32_bf16 v[92:95], a[84:87], a[124:127], v[92:95]\n"
"v_fma_f32 v193, v193, v201, v209\n"
"v_fma_f32 v194, v194, v202, v210\n"
"v_mfma_f32_16x16x32_bf16 v[96:99], a[88:91], a[112:115], v[96:99]\n"
"v_fma_f32 v195, v195, v203, v211\n"
"v_mfma_f32_16x16x32_bf16 v[100:103], a[88:91], a[116:119], v[100:103]\n"
"buffer_load_dwordx4 v150, s[72:75], s81 offen offset:2048 lds\n"
"v_cvt_pk_bf16_f32 v168, v188, v189\n"
"v_mfma_f32_16x16x32_bf16 v[104:107], a[88:91], a[120:123], v[104:107]\n"
"v_cvt_pk_bf16_f32 v169, v190, v191\n"
"v_cvt_pk_bf16_f32 v170, v192, v193\n"
"v_mfma_f32_16x16x32_bf16 v[108:111], a[88:91], a[124:127], v[108:111]\n"
"v_cvt_pk_bf16_f32 v171, v194, v195\n"
"v_mfma_f32_16x16x32_bf16 v[112:115], a[92:95], a[112:115], v[112:115]\n"
"ds_write_b128 v224, v[168:171] offset:8192\n"
"v_mfma_f32_16x16x32_bf16 v[116:119], a[92:95], a[116:119], v[116:119]\n"
"buffer_load_dwordx4 v151, s[72:75], s81 offen offset:3072 lds\n"
"v_mfma_f32_16x16x32_bf16 v[120:123], a[92:95], a[120:123], v[120:123]\n"
"s_add_u32 s80, s80, 128\n"
"s_add_u32 s81, s81, 524288\n"
"v_mfma_f32_16x16x32_bf16 v[124:127], a[92:95], a[124:127], v[124:127]\n"
"s_waitcnt lgkmcnt(0)\n"
"v_mfma_f32_16x16x32_bf16 a[128:131], a[32:35], a[0:3], a[128:131]\n"
"buffer_load_dword v227, v240, s[88:91], s94 offen\n"
"s_add_u32 s94, s94, 524288\n"
"v_mfma_f32_16x16x32_bf16 a[132:135], a[32:35], a[4:7], a[132:135]\n"
"buffer_load_dword v227, v140, s[64:67], s95 offen\n"
"buffer_load_dword v227, v141, s[64:67], s95 offen offset:1024\n"
"v_mfma_f32_16x16x32_bf16 a[136:139], a[32:35], a[8:11], a[136:139]\n"
"buffer_load_dword v227, v240, s[76:79], s93 offen\n"
"s_add_u32 s95, s95, 128\n"
"v_mfma_f32_16x16x32_bf16 a[140:143], a[32:35], a[12:15], a[140:143]\n"
"s_add_u32 s93, s93, 32768\n"
"buffer_load_dword v227, v240, s[76:79], s93 offen\n"
"v_mfma_f32_16x16x32_bf16 a[144:147], a[32:35], a[16:19], a[144:147]\n"
"buffer_store_dword v216, v212, s[96:99], s48 offen sc1\n"
"s_add_u32 s93, s93, 32768\n"
"v_mfma_f32_16x16x32_bf16 a[148:151], a[32:35], a[20:23], a[148:151]\n"
"buffer_store_dword v217, v212, s[96:99], s49 offen sc1\n"
"buffer_store_short v218, v213, s[96:99], s54 offen sc1\n"
"v_mfma_f32_16x16x32_bf16 a[152:155], a[32:35], a[24:27], a[152:155]\n"
"buffer_store_short v219, v213, s[96:99], s55 offen sc1\n"
"v_mfma_f32_16x16x32_bf16 a[156:159], a[32:35], a[28:31], a[156:159]\n"
"ds_read_b64_tr_b16 a[96:97], v132 offset:40960\n"
"v_mfma_f32_16x16x32_bf16 a[160:163], a[36:39], a[0:3], a[160:163]\n"
"v_mfma_f32_16x16x32_bf16 a[164:167], a[36:39], a[4:7], a[164:167]\n"
"ds_read_b64_tr_b16 a[98:99], v132 offset:41984\n"
"v_mfma_f32_16x16x32_bf16 a[168:171], a[36:39], a[8:11], a[168:171]\n"
"s_and_b32 s86, %2, 1\n"
"s_lshl_b32 s86, s86, 2\n"
"v_mfma_f32_16x16x32_bf16 a[172:175], a[36:39], a[12:15], a[172:175]\n"
"ds_read_b128 a[64:67], v129 offset:0\n"
"s_add_u32 s8, s8, s86\n"
"v_mfma_f32_16x16x32_bf16 a[176:179], a[36:39], a[16:19], a[176:179]\n"
"s_add_u32 s86, %2, 1\n"
"s_and_b32 s86, s86, 2\n"
"v_mfma_f32_16x16x32_bf16 a[180:183], a[36:39], a[20:23], a[180:183]\n"
"ds_read_b128 a[68:71], v129 offset:1024\n"
"s_lshl_b32 s86, s86, 1\n"
"v_mfma_f32_16x16x32_bf16 a[184:187], a[36:39], a[24:27], a[184:187]\n"
"s_sub_u32 s86, 4, s86\n"
"s_add_u32 s10, s10, s86\n"
"v_mfma_f32_16x16x32_bf16 a[188:191], a[36:39], a[28:31], a[188:191]\n"
"ds_read_b128 a[72:75], v129 offset:4096\n"
"s_lshl_b32 s62, s25, 1\n"
"v_mfma_f32_16x16x32_bf16 a[192:195], a[40:43], a[0:3], a[192:195]\n"
"s_add_u32 s85, s62, %2\n"
"s_lshl_b32 s26, 7, s85\n"
"v_mfma_f32_16x16x32_bf16 a[196:199], a[40:43], a[4:7], a[196:199]\n"
"ds_read_b128 a[76:79], v129 offset:5120\n"
"s_add_u32 s27, s85, 1\n"
"v_mfma_f32_16x16x32_bf16 a[200:203], a[40:43], a[8:11], a[200:203]\n"
"s_add_u32 s86, s62, 4\n"
"s_cmp_lt_u32 s25, 2\n"
"s_cselect_b32 s86, s86, 1\n"
"v_mfma_f32_16x16x32_bf16 a[204:207], a[40:43], a[12:15], a[204:207]\n"
"ds_read_b128 a[80:83], v129 offset:8192\n"
"s_cmp_lt_u32 %2, 2\n"
"s_cselect_b32 s27, s27, 99\n"
"s_cselect_b32 s26, s26, 0\n"
"s_cselect_b32 s86, s86, 1\n"
"v_mfma_f32_16x16x32_bf16 a[208:211], a[40:43], a[16:19], a[208:211]\n"
"s_lshl_b32 s11, 3, s86\n"
"s_or_b32 s26, s26, s11\n"
"v_mfma_f32_16x16x32_bf16 a[212:215], a[40:43], a[20:23], a[212:215]\n"
"ds_read_b128 a[84:87], v129 offset:9216\n"
"s_cmp_eq_u32 %2, 2\n"
"s_cselect_b32 s85, 0x30000, 0\n"
"v_mfma_f32_16x16x32_bf16 a[216:219], a[40:43], a[24:27], a[216:219]\n"
"s_or_b32 s26, s26, s85\n"
"s_or_b32 s26, s26, 0x20000\n"
"v_mfma_f32_16x16x32_bf16 a[220:223], a[40:43], a[28:31], a[220:223]\n"
"ds_read_b128 a[88:91], v129 offset:12288\n"
"s_cmp_eq_u32 %2, 3\n"
"s_cselect_b32 s12, 14, 99\n"
"v_mfma_f32_16x16x32_bf16 a[224:227], a[44:47], a[0:3], a[224:227]\n"
"s_add_u32 s13, s27, 1\n"
"s_mul_i32 s58, s8, 20480\n"
"v_mfma_f32_16x16x32_bf16 a[228:231], a[44:47], a[4:7], a[228:231]\n"
"ds_read_b128 a[92:95], v129 offset:13312\n"
"s_lshl_b32 s59, %2, 14\n"
"v_mfma_f32_16x16x32_bf16 a[232:235], a[44:47], a[8:11], a[232:235]\n"
"s_add_u32 s59, s59, %3\n"
"s_add_u32 s59, s59, 65536\n"
"v_mfma_f32_16x16x32_bf16 a[236:239], a[44:47], a[12:15], a[236:239]\n"
"ds_read_b64_tr_b16 a[100:101], v133 offset:40960\n"
"v_add_u32 v250, s59, v250\n"
"v_mfma_f32_16x16x32_bf16 a[240:243], a[44:47], a[16:19], a[240:243]\n"
"v_min_u32 v231, 7, v252\n"
"s_lshl_b32 s85, s8, 1\n"
"v_mfma_f32_16x16x32_bf16 a[244:247], a[44:47], a[20:23], a[244:247]\n"
"ds_read_b64_tr_b16 a[102:103], v133 offset:41984\n"
"v_add_lshl_u32 v231, s85, v231, 3\n"
"v_mfma_f32_16x16x32_bf16 a[248:251], a[44:47], a[24:27], a[248:251]\n"
"v_mfma_f32_16x16x32_bf16 a[252:255], a[44:47], a[28:31], a[252:255]\n"
"ds_read_b64_tr_b16 a[104:105], v134 offset:40960\n"
"v_mfma_f32_16x16x32_bf16 v[0:3], a[0:3], a[48:51], v[0:3]\n"
"v_mfma_f32_16x16x32_bf16 v[4:7], a[0:3], a[52:55], v[4:7]\n"
"ds_read_b64_tr_b16 a[106:107], v134 offset:41984\n"
"v_mfma_f32_16x16x32_bf16 v[8:11], a[0:3], a[56:59], v[8:11]\n"
"v_mfma_f32_16x16x32_bf16 v[12:15], a[0:3], a[60:63], v[12:15]\n"
"ds_read_b64_tr_b16 a[108:109], v135 offset:40960\n"
"v_mfma_f32_16x16x32_bf16 v[16:19], a[4:7], a[48:51], v[16:19]\n"
"v_mfma_f32_16x16x32_bf16 v[20:23], a[4:7], a[52:55], v[20:23]\n"
"ds_read_b64_tr_b16 a[110:111], v135 offset:41984\n"
"v_mfma_f32_16x16x32_bf16 v[24:27], a[4:7], a[56:59], v[24:27]\n"
"v_mfma_f32_16x16x32_bf16 v[28:31], a[4:7], a[60:63], v[28:31]\n"
"ds_read_b64_tr_b16 a[112:113], v136 offset:40960\n"
"v_mfma_f32_16x16x32_bf16 v[32:35], a[8:11], a[48:51], v[32:35]\n"
"v_mfma_f32_16x16x32_bf16 v[36:39], a[8:11], a[52:55], v[36:39]\n"
"ds_read_b64_tr_b16 a[114:115], v136 offset:41984\n"
"v_mfma_f32_16x16x32_bf16 v[40:43], a[8:11], a[56:59], v[40:43]\n"
"v_mfma_f32_16x16x32_bf16 v[44:47], a[8:11], a[60:63], v[44:47]\n"
"ds_read_b64_tr_b16 a[116:117], v137 offset:40960\n"
"v_mfma_f32_16x16x32_bf16 v[48:51], a[12:15], a[48:51], v[48:51]\n"
"v_mfma_f32_16x16x32_bf16 v[52:55], a[12:15], a[52:55], v[52:55]\n"
"ds_read_b64_tr_b16 a[118:119], v137 offset:41984\n"
"v_mfma_f32_16x16x32_bf16 v[56:59], a[12:15], a[56:59], v[56:59]\n"
"v_mfma_f32_16x16x32_bf16 v[60:63], a[12:15], a[60:63], v[60:63]\n"
"ds_read_b64_tr_b16 a[120:121], v138 offset:40960\n"
"v_mfma_f32_16x16x32_bf16 v[64:67], a[16:19], a[48:51], v[64:67]\n"
"v_mfma_f32_16x16x32_bf16 v[68:71], a[16:19], a[52:55], v[68:71]\n"
"ds_read_b64_tr_b16 a[122:123], v138 offset:41984\n"
"v_mfma_f32_16x16x32_bf16 v[72:75], a[16:19], a[56:59], v[72:75]\n"
"v_mfma_f32_16x16x32_bf16 v[76:79], a[16:19], a[60:63], v[76:79]\n"
"ds_read_b64_tr_b16 a[124:125], v139 offset:40960\n"
"v_mfma_f32_16x16x32_bf16 v[80:83], a[20:23], a[48:51], v[80:83]\n"
"v_mfma_f32_16x16x32_bf16 v[84:87], a[20:23], a[52:55], v[84:87]\n"
"ds_read_b64_tr_b16 a[126:127], v139 offset:41984\n"
"v_mfma_f32_16x16x32_bf16 v[88:91], a[20:23], a[56:59], v[88:91]\n"
"v_mfma_f32_16x16x32_bf16 v[92:95], a[20:23], a[60:63], v[92:95]\n"
"v_mfma_f32_16x16x32_bf16 v[96:99], a[24:27], a[48:51], v[96:99]\n"
"v_mfma_f32_16x16x32_bf16 v[100:103], a[24:27], a[52:55], v[100:103]\n"
"v_mfma_f32_16x16x32_bf16 v[104:107], a[24:27], a[56:59], v[104:107]\n"
"v_mfma_f32_16x16x32_bf16 v[108:111], a[24:27], a[60:63], v[108:111]\n"
"v_mfma_f32_16x16x32_bf16 v[112:115], a[28:31], a[48:51], v[112:115]\n"
"s_waitcnt lgkmcnt(0)\n"
"s_waitcnt vmcnt(9)\n"
"s_barrier\n"
"v_mfma_f32_16x16x32_bf16 v[116:119], a[28:31], a[52:55], v[116:119]\n"
"v_mfma_f32_16x16x32_bf16 v[120:123], a[28:31], a[56:59], v[120:123]\n"
"v_mfma_f32_16x16x32_bf16 v[124:127], a[28:31], a[60:63], v[124:127]\n"
"v_mfma_f32_16x16x32_bf16 a[128:131], a[96:99], a[64:67], a[128:131]\n"
"s_add_u32 m0, s82, 0\n"
"v_mfma_f32_16x16x32_bf16 a[132:135], a[96:99], a[68:71], a[132:135]\n"
"buffer_load_dwordx4 v140, s[64:67], s80 offen offset:0 lds\n"
"v_mfma_f32_16x16x32_bf16 a[136:139], a[96:99], a[72:75], a[136:139]\n"
"ds_read_b64_tr_b16 a[32:33], v132 offset:0\n"
"v_mfma_f32_16x16x32_bf16 a[140:143], a[96:99], a[76:79], a[140:143]\n"
"ds_read_b64_tr_b16 a[34:35], v132 offset:1024\n"
"v_mfma_f32_16x16x32_bf16 a[144:147], a[96:99], a[80:83], a[144:147]\n"
"v_mfma_f32_16x16x32_bf16 a[148:151], a[96:99], a[84:87], a[148:151]\n"
"buffer_load_dwordx4 v141, s[64:67], s80 offen offset:1024 lds\n"
"v_mfma_f32_16x16x32_bf16 a[152:155], a[96:99], a[88:91], a[152:155]\n"
"ds_read_b128 a[0:3], v128 offset:32768\n"
"v_mfma_f32_16x16x32_bf16 a[156:159], a[96:99], a[92:95], a[156:159]\n"
"ds_read_b128 a[4:7], v128 offset:33792\n"
"v_mfma_f32_16x16x32_bf16 a[160:163], a[100:103], a[64:67], a[160:163]\n"
"v_mfma_f32_16x16x32_bf16 a[164:167], a[100:103], a[68:71], a[164:167]\n"
"buffer_load_dwordx4 v142, s[64:67], s80 offen offset:2048 lds\n"
"v_mfma_f32_16x16x32_bf16 a[168:171], a[100:103], a[72:75], a[168:171]\n"
"ds_read_b128 a[8:11], v128 offset:36864\n"
"v_mfma_f32_16x16x32_bf16 a[172:175], a[100:103], a[76:79], a[172:175]\n"
"ds_read_b128 a[12:15], v128 offset:37888\n"
"v_mfma_f32_16x16x32_bf16 a[176:179], a[100:103], a[80:83], a[176:179]\n"
"v_mfma_f32_16x16x32_bf16 a[180:183], a[100:103], a[84:87], a[180:183]\n"
"buffer_load_dwordx4 v143, s[64:67], s80 offen offset:3072 lds\n"
"v_mfma_f32_16x16x32_bf16 a[184:187], a[100:103], a[88:91], a[184:187]\n"
"ds_read_b128 a[16:19], v128 offset:40960\n"
"v_mfma_f32_16x16x32_bf16 a[188:191], a[100:103], a[92:95], a[188:191]\n"
"v_mfma_f32_16x16x32_bf16 a[192:195], a[104:107], a[64:67], a[192:195]\n"
"s_add_u32 m0, s82, 4096\n"
"v_mfma_f32_16x16x32_bf16 a[196:199], a[104:107], a[68:71], a[196:199]\n"
"buffer_load_dwordx4 v144, s[64:67], s80 offen offset:0 lds\n"
"v_mfma_f32_16x16x32_bf16 a[200:203], a[104:107], a[72:75], a[200:203]\n"
"ds_read_b128 a[20:23], v128 offset:41984\n"
"v_mfma_f32_16x16x32_bf16 a[204:207], a[104:107], a[76:79], a[204:207]\n"
"ds_read_b128 a[24:27], v128 offset:45056\n"
"v_mfma_f32_16x16x32_bf16 a[208:211], a[104:107], a[80:83], a[208:211]\n"
"v_mfma_f32_16x16x32_bf16 a[212:215], a[104:107], a[84:87], a[212:215]\n"
"buffer_load_dwordx4 v145, s[64:67], s80 offen offset:1024 lds\n"
"v_mfma_f32_16x16x32_bf16 a[216:219], a[104:107], a[88:91], a[216:219]\n"
"ds_read_b128 a[28:31], v128 offset:46080\n"
"v_mfma_f32_16x16x32_bf16 a[220:223], a[104:107], a[92:95], a[220:223]\n"
"ds_read_b64_tr_b16 a[36:37], v133 offset:0\n"
"v_mfma_f32_16x16x32_bf16 a[224:227], a[108:111], a[64:67], a[224:227]\n"
"v_mfma_f32_16x16x32_bf16 a[228:231], a[108:111], a[68:71], a[228:231]\n"
"buffer_load_dwordx4 v146, s[64:67], s80 offen offset:2048 lds\n"
"v_mfma_f32_16x16x32_bf16 a[232:235], a[108:111], a[72:75], a[232:235]\n"
"ds_read_b64_tr_b16 a[38:39], v133 offset:1024\n"
"v_mfma_f32_16x16x32_bf16 a[236:239], a[108:111], a[76:79], a[236:239]\n"
"v_mfma_f32_16x16x32_bf16 a[240:243], a[108:111], a[80:83], a[240:243]\n"
"ds_read_b64_tr_b16 a[40:41], v134 offset:0\n"
"v_mfma_f32_16x16x32_bf16 a[244:247], a[108:111], a[84:87], a[244:247]\n"
"buffer_load_dwordx4 v147, s[64:67], s80 offen offset:3072 lds\n"
"v_mfma_f32_16x16x32_bf16 a[248:251], a[108:111], a[88:91], a[248:251]\n"
"ds_read_b64_tr_b16 a[42:43], v134 offset:1024\n"
"s_mul_i32 s60, %2, 2560\n"
"v_mfma_f32_16x16x32_bf16 a[252:255], a[108:111], a[92:95], a[252:255]\n"
"s_add_u32 s60, s60, %3\n"
"s_add_u32 s60, s60, 131072\n"
"v_mfma_f32_16x16x32_bf16 v[0:3], a[64:67], a[112:115], v[0:3]\n"
"v_add_u32 v224, s60, v251\n"
"s_add_u32 m0, s57, 98304\n"
"v_mfma_f32_16x16x32_bf16 v[4:7], a[64:67], a[116:119], v[4:7]\n"
"ds_read_b128 v[160:163], v224 offset:0\n"
"buffer_load_dwordx4 v240, s[68:71], s81 offen offset:0 lds\n"
"v_mfma_f32_16x16x32_bf16 v[8:11], a[64:67], a[120:123], v[8:11]\n"
"ds_read_b64_tr_b16 a[44:45], v135 offset:0\n"
"ds_read_b128 v[164:167], v224 offset:1024\n"
"v_mfma_f32_16x16x32_bf16 v[12:15], a[64:67], a[124:127], v[12:15]\n"
"ds_read_b64_tr_b16 a[46:47], v135 offset:1024\n"
"ds_read_b128 v[168:171], v224 offset:2048\n"
"v_mfma_f32_16x16x32_bf16 v[16:19], a[68:71], a[112:115], v[16:19]\n"
"s_lshr_b32 s60, %1, 1\n"
"s_mul_i32 s60, s60, 20480\n"
"v_mfma_f32_16x16x32_bf16 v[20:23], a[68:71], a[116:119], v[20:23]\n"
"buffer_load_dwordx4 v241, s[68:71], s81 offen offset:1024 lds\n"
"s_mul_i32 s61, %2, 5120\n"
"v_mfma_f32_16x16x32_bf16 v[24:27], a[68:71], a[120:123], v[24:27]\n"
"ds_read_b64_tr_b16 a[48:49], v136 offset:0\n"
"s_add_u32 s60, s60, s61\n"
"v_mfma_f32_16x16x32_bf16 v[28:31], a[68:71], a[124:127], v[28:31]\n"
"ds_read_b64_tr_b16 a[50:51], v136 offset:1024\n"
"s_and_b32 s61, %1, 1\n"
"v_mfma_f32_16x16x32_bf16 v[32:35], a[72:75], a[112:115], v[32:35]\n"
"s_lshl_b32 s61, s61, 7\n"
"s_add_u32 s60, s60, s61\n"
"v_mfma_f32_16x16x32_bf16 v[36:39], a[72:75], a[116:119], v[36:39]\n"
"buffer_load_dwordx4 v242, s[68:71], s81 offen offset:2048 lds\n"
"v_lshrrev_b32 v225, 3, v252\n"
"v_mfma_f32_16x16x32_bf16 v[40:43], a[72:75], a[120:123], v[40:43]\n"
"ds_read_b64_tr_b16 a[52:53], v137 offset:0\n"
"v_sub_u32 v200, v252, v225\n"
"v_mfma_f32_16x16x32_bf16 v[44:47], a[72:75], a[124:127], v[44:47]\n"
"s_lshl_b32 s61, %2, 2\n"
"v_subrev_u32 v200, s61, v200\n"
"v_mfma_f32_16x16x32_bf16 v[48:51], a[76:79], a[112:115], v[48:51]\n"
"ds_read_b64_tr_b16 a[54:55], v137 offset:1024\n"
"v_lshlrev_b32 v225, 8, v225\n"
"v_mfma_f32_16x16x32_bf16 v[52:55], a[76:79], a[116:119], v[52:55]\n"
"buffer_load_dwordx4 v243, s[68:71], s81 offen offset:3072 lds\n"
"v_and_b32 v200, 7, v200\n"
"v_mfma_f32_16x16x32_bf16 v[56:59], a[76:79], a[120:123], v[56:59]\n"
"ds_read_b64_tr_b16 a[56:57], v138 offset:0\n"
"v_lshl_add_u32 v225, v200, 4, v225\n"
"v_mfma_f32_16x16x32_bf16 v[60:63], a[76:79], a[124:127], v[60:63]\n"
"v_lshrrev_b32 v200, 5, v252\n"
"v_lshl_add_u32 v200, v200, 30, v225\n"
"v_mfma_f32_16x16x32_bf16 v[64:67], a[80:83], a[112:115], v[64:67]\n"
"s_add_u32 m0, s57, 114688\n"
"s_add_u32 s61, s60, 4096\n"
"v_mfma_f32_16x16x32_bf16 v[68:71], a[80:83], a[116:119], v[68:71]\n"
"buffer_load_dwordx4 v148, s[72:75], s81 offen offset:0 lds\n"
"v_mfma_f32_16x16x32_bf16 v[72:75], a[80:83], a[120:123], v[72:75]\n"
"ds_read_b64_tr_b16 a[58:59], v138 offset:1024\n"
"v_mfma_f32_16x16x32_bf16 v[76:79], a[80:83], a[124:127], v[76:79]\n"
"ds_read_b64_tr_b16 a[60:61], v139 offset:0\n"
"v_mfma_f32_16x16x32_bf16 v[80:83], a[84:87], a[112:115], v[80:83]\n"
"v_mfma_f32_16x16x32_bf16 v[84:87], a[84:87], a[116:119], v[84:87]\n"
"buffer_load_dwordx4 v149, s[72:75], s81 offen offset:1024 lds\n"
"v_mfma_f32_16x16x32_bf16 v[88:91], a[84:87], a[120:123], v[88:91]\n"
"ds_read_b64_tr_b16 a[62:63], v139 offset:1024\n"
"v_mfma_f32_16x16x32_bf16 v[92:95], a[84:87], a[124:127], v[92:95]\n"
"v_mfma_f32_16x16x32_bf16 v[96:99], a[88:91], a[112:115], v[96:99]\n"
"v_mfma_f32_16x16x32_bf16 v[100:103], a[88:91], a[116:119], v[100:103]\n"
"buffer_load_dwordx4 v150, s[72:75], s81 offen offset:2048 lds\n"
"v_mfma_f32_16x16x32_bf16 v[104:107], a[88:91], a[120:123], v[104:107]\n"
"v_mfma_f32_16x16x32_bf16 v[108:111], a[88:91], a[124:127], v[108:111]\n"
"v_mfma_f32_16x16x32_bf16 v[112:115], a[92:95], a[112:115], v[112:115]\n"
"v_mfma_f32_16x16x32_bf16 v[116:119], a[92:95], a[116:119], v[116:119]\n"
"buffer_load_dwordx4 v151, s[72:75], s81 offen offset:3072 lds\n"
"v_mfma_f32_16x16x32_bf16 v[120:123], a[92:95], a[120:123], v[120:123]\n"
"s_add_u32 s80, s80, 128\n"
"s_add_u32 s81, s81, 524288\n"
"v_mfma_f32_16x16x32_bf16 v[124:127], a[92:95], a[124:127], v[124:127]\n"
"s_waitcnt lgkmcnt(1)\n"
"buffer_store_dwordx4 v[160:163], v225, s[96:99], s60 offen sc1\n"
"buffer_store_dwordx4 v[164:167], v225, s[96:99], s60 offen offset:2048 sc1\n"
"buffer_store_dwordx4 v[168:171], v200, s[96:99], s61 offen sc1\n"
"s_waitcnt lgkmcnt(0)\n"
"v_mfma_f32_16x16x32_bf16 a[128:131], a[32:35], a[0:3], a[128:131]\n"
"ds_read_b64_tr_b16 a[96:97], v132 offset:8192\n"
"v_mfma_f32_16x16x32_bf16 a[132:135], a[32:35], a[4:7], a[132:135]\n"
"v_mfma_f32_16x16x32_bf16 a[136:139], a[32:35], a[8:11], a[136:139]\n"
"ds_read_b64_tr_b16 a[98:99], v132 offset:9216\n"
"v_mfma_f32_16x16x32_bf16 a[140:143], a[32:35], a[12:15], a[140:143]\n"
"v_mfma_f32_16x16x32_bf16 a[144:147], a[32:35], a[16:19], a[144:147]\n"
"ds_read_b128 a[64:67], v129 offset:32768\n"
"v_mfma_f32_16x16x32_bf16 a[148:151], a[32:35], a[20:23], a[148:151]\n"
"v_mfma_f32_16x16x32_bf16 a[152:155], a[32:35], a[24:27], a[152:155]\n"
"v_mfma_f32_16x16x32_bf16 a[156:159], a[32:35], a[28:31], a[156:159]\n"
"ds_read_b128 a[68:71], v129 offset:33792\n"
"v_mfma_f32_16x16x32_bf16 a[160:163], a[36:39], a[0:3], a[160:163]\n"
"v_mfma_f32_16x16x32_bf16 a[164:167], a[36:39], a[4:7], a[164:167]\n"
"ds_read_b128 a[72:75], v129 offset:36864\n"
"v_mfma_f32_16x16x32_bf16 a[168:171], a[36:39], a[8:11], a[168:171]\n"
"v_mfma_f32_16x16x32_bf16 a[172:175], a[36:39], a[12:15], a[172:175]\n"
"ds_read_b128 a[76:79], v129 offset:37888\n"
"v_mfma_f32_16x16x32_bf16 a[176:179], a[36:39], a[16:19], a[176:179]\n"
"v_mfma_f32_16x16x32_bf16 a[180:183], a[36:39], a[20:23], a[180:183]\n"
"v_mfma_f32_16x16x32_bf16 a[184:187], a[36:39], a[24:27], a[184:187]\n"
"ds_read_b128 a[80:83], v129 offset:40960\n"
"v_mfma_f32_16x16x32_bf16 a[188:191], a[36:39], a[28:31], a[188:191]\n"
"v_mfma_f32_16x16x32_bf16 a[192:195], a[40:43], a[0:3], a[192:195]\n"
"ds_read_b128 a[84:87], v129 offset:41984\n"
"v_mfma_f32_16x16x32_bf16 a[196:199], a[40:43], a[4:7], a[196:199]\n"
"v_mfma_f32_16x16x32_bf16 a[200:203], a[40:43], a[8:11], a[200:203]\n"
"ds_read_b128 a[88:91], v129 offset:45056\n"
"v_mfma_f32_16x16x32_bf16 a[204:207], a[40:43], a[12:15], a[204:207]\n"
"v_mfma_f32_16x16x32_bf16 a[208:211], a[40:43], a[16:19], a[208:211]\n"
"v_mfma_f32_16x16x32_bf16 a[212:215], a[40:43], a[20:23], a[212:215]\n"
"ds_read_b128 a[92:95], v129 offset:46080\n"
"v_mfma_f32_16x16x32_bf16 a[216:219], a[40:43], a[24:27], a[216:219]\n"
"v_mfma_f32_16x16x32_bf16 a[220:223], a[40:43], a[28:31], a[220:223]\n"
"ds_read_b64_tr_b16 a[100:101], v133 offset:8192\n"
"v_mfma_f32_16x16x32_bf16 a[224:227], a[44:47], a[0:3], a[224:227]\n"
"v_mfma_f32_16x16x32_bf16 a[228:231], a[44:47], a[4:7], a[228:231]\n"
"ds_read_b64_tr_b16 a[102:103], v133 offset:9216\n"
"v_mfma_f32_16x16x32_bf16 a[232:235], a[44:47], a[8:11], a[232:235]\n"
"v_mfma_f32_16x16x32_bf16 a[236:239], a[44:47], a[12:15], a[236:239]\n"
"v_mfma_f32_16x16x32_bf16 a[240:243], a[44:47], a[16:19], a[240:243]\n"
"ds_read_b64_tr_b16 a[104:105], v134 offset:8192\n"
"v_mfma_f32_16x16x32_bf16 a[244:247], a[44:47], a[20:23], a[244:247]\n"
"v_mfma_f32_16x16x32_bf16 a[248:251], a[44:47], a[24:27], a[248:251]\n"
"ds_read_b64_tr_b16 a[106:107], v134 offset:9216\n"
"v_mfma_f32_16x16x32_bf16 a[252:255], a[44:47], a[28:31], a[252:255]\n"
"v_mfma_f32_16x16x32_bf16 v[0:3], a[0:3], a[48:51], v[0:3]\n"
"ds_read_b64_tr_b16 a[108:109], v135 offset:8192\n"
"v_mfma_f32_16x16x32_bf16 v[4:7], a[0:3], a[52:55], v[4:7]\n"
"v_mfma_f32_16x16x32_bf16 v[8:11], a[0:3], a[56:59], v[8:11]\n"
"v_mfma_f32_16x16x32_bf16 v[12:15], a[0:3], a[60:63], v[12:15]\n"
"ds_read_b64_tr_b16 a[110:111], v135 offset:9216\n"
"v_mfma_f32_16x16x32_bf16 v[16:19], a[4:7], a[48:51], v[16:19]\n"
"v_mfma_f32_16x16x32_bf16 v[20:23], a[4:7], a[52:55], v[20:23]\n"
"ds_read_b64_tr_b16 a[112:113], v136 offset:8192\n"
"v_mfma_f32_16x16x32_bf16 v[24:27], a[4:7], a[56:59], v[24:27]\n"
"v_mfma_f32_16x16x32_bf16 v[28:31], a[4:7], a[60:63], v[28:31]\n"
"ds_read_b64_tr_b16 a[114:115], v136 offset:9216\n"
"v_mfma_f32_16x16x32_bf16 v[32:35], a[8:11], a[48:51], v[32:35]\n"
"v_mfma_f32_16x16x32_bf16 v[36:39], a[8:11], a[52:55], v[36:39]\n"
"v_mfma_f32_16x16x32_bf16 v[40:43], a[8:11], a[56:59], v[40:43]\n"
"ds_read_b64_tr_b16 a[116:117], v137 offset:8192\n"
"v_mfma_f32_16x16x32_bf16 v[44:47], a[8:11], a[60:63], v[44:47]\n"
"v_mfma_f32_16x16x32_bf16 v[48:51], a[12:15], a[48:51], v[48:51]\n"
"ds_read_b64_tr_b16 a[118:119], v137 offset:9216\n"
"v_mfma_f32_16x16x32_bf16 v[52:55], a[12:15], a[52:55], v[52:55]\n"
"v_mfma_f32_16x16x32_bf16 v[56:59], a[12:15], a[56:59], v[56:59]\n"
"ds_read_b64_tr_b16 a[120:121], v138 offset:8192\n"
"v_mfma_f32_16x16x32_bf16 v[60:63], a[12:15], a[60:63], v[60:63]\n"
"v_mfma_f32_16x16x32_bf16 v[64:67], a[16:19], a[48:51], v[64:67]\n"
"v_mfma_f32_16x16x32_bf16 v[68:71], a[16:19], a[52:55], v[68:71]\n"
"ds_read_b64_tr_b16 a[122:123], v138 offset:9216\n"
"v_mfma_f32_16x16x32_bf16 v[72:75], a[16:19], a[56:59], v[72:75]\n"
"v_mfma_f32_16x16x32_bf16 v[76:79], a[16:19], a[60:63], v[76:79]\n"
"ds_read_b64_tr_b16 a[124:125], v139 offset:8192\n"
"v_mfma_f32_16x16x32_bf16 v[80:83], a[20:23], a[48:51], v[80:83]\n"
"v_mfma_f32_16x16x32_bf16 v[84:87], a[20:23], a[52:55], v[84:87]\n"
"PG4_%=:\n"
"s_cbranch_execz RPG4_%=\n"
"ds_read_b64_tr_b16 a[126:127], v139 offset:9216\n"
"v_mfma_f32_16x16x32_bf16 v[88:91], a[20:23], a[56:59], v[88:91]\n"
"v_mfma_f32_16x16x32_bf16 v[92:95], a[20:23], a[60:63], v[92:95]\n"
"v_mfma_f32_16x16x32_bf16 v[96:99], a[24:27], a[48:51], v[96:99]\n"
"v_mfma_f32_16x16x32_bf16 v[100:103], a[24:27], a[52:55], v[100:103]\n"
"v_mfma_f32_16x16x32_bf16 v[104:107], a[24:27], a[56:59], v[104:107]\n"
"v_mfma_f32_16x16x32_bf16 v[108:111], a[24:27], a[60:63], v[108:111]\n"
"v_mfma_f32_16x16x32_bf16 v[112:115], a[28:31], a[48:51], v[112:115]\n"
"s_waitcnt lgkmcnt(0)\n"
"s_waitcnt vmcnt(0)\n"
"s_barrier\n"
"s_cmp_lg_u32 %2, 0\n"
"s_cbranch_scc1 PUBH_%=\n"
"v_min_u32 v130, 1, v252\n"
"v_lshlrev_b32 v130, 30, v130\n"
"v_mov_b32 v222, s14\n"
"v_mov_b32 v223, s15\n"
"s_lshl_b32 s62, %1, 3\n"
"s_add_u32 s62, s62, 105918464\n"
"buffer_store_dwordx2 v[222:223], v130, s[96:99], s62 offen sc1\n"
"PUBH_%=:\n"
"v_mfma_f32_16x16x32_bf16 v[116:119], a[28:31], a[52:55], v[116:119]\n"
"v_mfma_f32_16x16x32_bf16 v[120:123], a[28:31], a[56:59], v[120:123]\n"
"v_mfma_f32_16x16x32_bf16 v[124:127], a[28:31], a[60:63], v[124:127]\n"
"v_mfma_f32_16x16x32_bf16 a[128:131], a[96:99], a[64:67], a[128:131]\n"
"s_add_u32 m0, s57, 32768\n"
"v_mfma_f32_16x16x32_bf16 a[132:135], a[96:99], a[68:71], a[132:135]\n"
"buffer_load_dwordx4 v240, s[76:79], s63 offen offset:0 lds\n"
"v_mfma_f32_16x16x32_bf16 a[136:139], a[96:99], a[72:75], a[136:139]\n"
"ds_read_b64_tr_b16 a[32:33], v132 offset:32768\n"
"v_mfma_f32_16x16x32_bf16 a[140:143], a[96:99], a[76:79], a[140:143]\n"
"ds_read_b64_tr_b16 a[34:35], v132 offset:33792\n"
"v_mfma_f32_16x16x32_bf16 a[144:147], a[96:99], a[80:83], a[144:147]\n"
"ds_read_b128 a[0:3], v128 offset:0\n"
"v_mfma_f32_16x16x32_bf16 a[148:151], a[96:99], a[84:87], a[148:151]\n"
"buffer_load_dwordx4 v241, s[76:79], s63 offen offset:1024 lds\n"
"v_mfma_f32_16x16x32_bf16 a[152:155], a[96:99], a[88:91], a[152:155]\n"
"v_mfma_f32_16x16x32_bf16 a[156:159], a[96:99], a[92:95], a[156:159]\n"
"ds_read_b128 a[4:7], v128 offset:1024\n"
"v_mfma_f32_16x16x32_bf16 a[160:163], a[100:103], a[64:67], a[160:163]\n"
"ds_read_b128 a[8:11], v128 offset:4096\n"
"v_mfma_f32_16x16x32_bf16 a[164:167], a[100:103], a[68:71], a[164:167]\n"
"buffer_load_dwordx4 v242, s[76:79], s63 offen offset:2048 lds\n"
"v_mfma_f32_16x16x32_bf16 a[168:171], a[100:103], a[72:75], a[168:171]\n"
"v_mfma_f32_16x16x32_bf16 a[172:175], a[100:103], a[76:79], a[172:175]\n"
"ds_read_b128 a[12:15], v128 offset:5120\n"
"v_mfma_f32_16x16x32_bf16 a[176:179], a[100:103], a[80:83], a[176:179]\n"
"ds_read_b128 a[16:19], v128 offset:8192\n"
"v_mfma_f32_16x16x32_bf16 a[180:183], a[100:103], a[84:87], a[180:183]\n"
"buffer_load_dwordx4 v243, s[76:79], s63 offen offset:3072 lds\n"
"v_mfma_f32_16x16x32_bf16 a[184:187], a[100:103], a[88:91], a[184:187]\n"
"s_add_u32 s63, s63, 524288\n"
"v_mfma_f32_16x16x32_bf16 a[188:191], a[100:103], a[92:95], a[188:191]\n"
"v_mfma_f32_16x16x32_bf16 a[192:195], a[104:107], a[64:67], a[192:195]\n"
"s_add_u32 m0, s57, 49152\n"
"v_mfma_f32_16x16x32_bf16 a[196:199], a[104:107], a[68:71], a[196:199]\n"
"buffer_load_dwordx4 v240, s[76:79], s63 offen offset:0 lds\n"
"v_mfma_f32_16x16x32_bf16 a[200:203], a[104:107], a[72:75], a[200:203]\n"
"ds_read_b128 a[20:23], v128 offset:9216\n"
"v_mfma_f32_16x16x32_bf16 a[204:207], a[104:107], a[76:79], a[204:207]\n"
"ds_read_b128 a[24:27], v128 offset:12288\n"
"v_mfma_f32_16x16x32_bf16 a[208:211], a[104:107], a[80:83], a[208:211]\n"
"v_mfma_f32_16x16x32_bf16 a[212:215], a[104:107], a[84:87], a[212:215]\n"
"buffer_load_dwordx4 v241, s[76:79], s63 offen offset:1024 lds\n"
"v_mfma_f32_16x16x32_bf16 a[216:219], a[104:107], a[88:91], a[216:219]\n"
"ds_read_b128 a[28:31], v128 offset:13312\n"
"v_mfma_f32_16x16x32_bf16 a[220:223], a[104:107], a[92:95], a[220:223]\n"
"ds_read_b64_tr_b16 a[36:37], v133 offset:32768\n"
"v_mfma_f32_16x16x32_bf16 a[224:227], a[108:111], a[64:67], a[224:227]\n"
"v_mfma_f32_16x16x32_bf16 a[228:231], a[108:111], a[68:71], a[228:231]\n"
"buffer_load_dwordx4 v242, s[76:79], s63 offen offset:2048 lds\n"
"v_mfma_f32_16x16x32_bf16 a[232:235], a[108:111], a[72:75], a[232:235]\n"
"ds_read_b64_tr_b16 a[38:39], v133 offset:33792\n"
"v_mfma_f32_16x16x32_bf16 a[236:239], a[108:111], a[76:79], a[236:239]\n"
"ds_read_b64_tr_b16 a[40:41], v134 offset:32768\n"
"v_mfma_f32_16x16x32_bf16 a[240:243], a[108:111], a[80:83], a[240:243]\n"
"ds_read_b64_tr_b16 a[42:43], v134 offset:33792\n"
"v_mfma_f32_16x16x32_bf16 a[244:247], a[108:111], a[84:87], a[244:247]\n"
"buffer_load_dwordx4 v243, s[76:79], s63 offen offset:3072 lds\n"
"v_mfma_f32_16x16x32_bf16 a[248:251], a[108:111], a[88:91], a[248:251]\n"
"s_add_u32 s63, s63, 524288\n"
"v_mfma_f32_16x16x32_bf16 a[252:255], a[108:111], a[92:95], a[252:255]\n"
"v_mfma_f32_16x16x32_bf16 v[0:3], a[64:67], a[112:115], v[0:3]\n"
"s_add_u32 m0, s57, 65536\n"
"v_mfma_f32_16x16x32_bf16 v[4:7], a[64:67], a[116:119], v[4:7]\n"
"buffer_load_dwordx4 v240, s[76:79], s63 offen offset:0 lds\n"
"v_mfma_f32_16x16x32_bf16 v[8:11], a[64:67], a[120:123], v[8:11]\n"
"ds_read_b64_tr_b16 a[44:45], v135 offset:32768\n"
"v_mfma_f32_16x16x32_bf16 v[12:15], a[64:67], a[124:127], v[12:15]\n"
"ds_read_b64_tr_b16 a[46:47], v135 offset:33792\n"
"v_mfma_f32_16x16x32_bf16 v[16:19], a[68:71], a[112:115], v[16:19]\n"
"v_mfma_f32_16x16x32_bf16 v[20:23], a[68:71], a[116:119], v[20:23]\n"
"buffer_load_dwordx4 v241, s[76:79], s63 offen offset:1024 lds\n"
"v_mfma_f32_16x16x32_bf16 v[24:27], a[68:71], a[120:123], v[24:27]\n"
"ds_read_b64_tr_b16 a[48:49], v136 offset:32768\n"
"v_mfma_f32_16x16x32_bf16 v[28:31], a[68:71], a[124:127], v[28:31]\n"
"ds_read_b64_tr_b16 a[50:51], v136 offset:33792\n"
"v_mfma_f32_16x16x32_bf16 v[32:35], a[72:75], a[112:115], v[32:35]\n"
"v_mfma_f32_16x16x32_bf16 v[36:39], a[72:75], a[116:119], v[36:39]\n"
"buffer_load_dwordx4 v242, s[76:79], s63 offen offset:2048 lds\n"
"v_mfma_f32_16x16x32_bf16 v[40:43], a[72:75], a[120:123], v[40:43]\n"
"ds_read_b64_tr_b16 a[52:53], v137 offset:32768\n"
"v_mfma_f32_16x16x32_bf16 v[44:47], a[72:75], a[124:127], v[44:47]\n"
"ds_read_b64_tr_b16 a[54:55], v137 offset:33792\n"
"v_mfma_f32_16x16x32_bf16 v[48:51], a[76:79], a[112:115], v[48:51]\n"
"v_mfma_f32_16x16x32_bf16 v[52:55], a[76:79], a[116:119], v[52:55]\n"
"buffer_load_dwordx4 v243, s[76:79], s63 offen offset:3072 lds\n"
"v_mfma_f32_16x16x32_bf16 v[56:59], a[76:79], a[120:123], v[56:59]\n"
"s_add_u32 s63, s63, 524288\n"
"v_mfma_f32_16x16x32_bf16 v[60:63], a[76:79], a[124:127], v[60:63]\n"
"ds_read_b64_tr_b16 a[56:57], v138 offset:32768\n"
"v_mfma_f32_16x16x32_bf16 v[64:67], a[80:83], a[112:115], v[64:67]\n"
"s_add_u32 m0, s57, 81920\n"
"v_mfma_f32_16x16x32_bf16 v[68:71], a[80:83], a[116:119], v[68:71]\n"
"buffer_load_dwordx4 v240, s[76:79], s63 offen offset:0 lds\n"
"v_mfma_f32_16x16x32_bf16 v[72:75], a[80:83], a[120:123], v[72:75]\n"
"ds_read_b64_tr_b16 a[58:59], v138 offset:33792\n"
"v_mfma_f32_16x16x32_bf16 v[76:79], a[80:83], a[124:127], v[76:79]\n"
"v_mfma_f32_16x16x32_bf16 v[80:83], a[84:87], a[112:115], v[80:83]\n"
"ds_read_b64_tr_b16 a[60:61], v139 offset:32768\n"
"v_mfma_f32_16x16x32_bf16 v[84:87], a[84:87], a[116:119], v[84:87]\n"
"buffer_load_dwordx4 v241, s[76:79], s63 offen offset:1024 lds\n"
"v_mfma_f32_16x16x32_bf16 v[88:91], a[84:87], a[120:123], v[88:91]\n"
"ds_read_b64_tr_b16 a[62:63], v139 offset:33792\n"
"v_mfma_f32_16x16x32_bf16 v[92:95], a[84:87], a[124:127], v[92:95]\n"
"v_mfma_f32_16x16x32_bf16 v[96:99], a[88:91], a[112:115], v[96:99]\n"
"v_mfma_f32_16x16x32_bf16 v[100:103], a[88:91], a[116:119], v[100:103]\n"
"buffer_load_dwordx4 v242, s[76:79], s63 offen offset:2048 lds\n"
"v_mfma_f32_16x16x32_bf16 v[104:107], a[88:91], a[120:123], v[104:107]\n"
"v_mfma_f32_16x16x32_bf16 v[108:111], a[88:91], a[124:127], v[108:111]\n"
"v_mfma_f32_16x16x32_bf16 v[112:115], a[92:95], a[112:115], v[112:115]\n"
"v_mfma_f32_16x16x32_bf16 v[116:119], a[92:95], a[116:119], v[116:119]\n"
"buffer_load_dwordx4 v243, s[76:79], s63 offen offset:3072 lds\n"
"v_mfma_f32_16x16x32_bf16 v[120:123], a[92:95], a[120:123], v[120:123]\n"
"s_add_u32 s63, s63, 524288\n"
"v_mfma_f32_16x16x32_bf16 v[124:127], a[92:95], a[124:127], v[124:127]\n"
"s_waitcnt lgkmcnt(0)\n"
"v_mfma_f32_16x16x32_bf16 a[128:131], a[32:35], a[0:3], a[128:131]\n"
"ds_read_b64_tr_b16 a[96:97], v132 offset:40960\n"
"v_mfma_f32_16x16x32_bf16 a[132:135], a[32:35], a[4:7], a[132:135]\n"
"v_mfma_f32_16x16x32_bf16 a[136:139], a[32:35], a[8:11], a[136:139]\n"
"ds_read_b64_tr_b16 a[98:99], v132 offset:41984\n"
"v_mfma_f32_16x16x32_bf16 a[140:143], a[32:35], a[12:15], a[140:143]\n"
"v_mfma_f32_16x16x32_bf16 a[144:147], a[32:35], a[16:19], a[144:147]\n"
"ds_read_b128 a[64:67], v129 offset:0\n"
"v_mfma_f32_16x16x32_bf16 a[148:151], a[32:35], a[20:23], a[148:151]\n"
"v_mfma_f32_16x16x32_bf16 a[152:155], a[32:35], a[24:27], a[152:155]\n"
"v_mfma_f32_16x16x32_bf16 a[156:159], a[32:35], a[28:31], a[156:159]\n"
"ds_read_b128 a[68:71], v129 offset:1024\n"
"v_mfma_f32_16x16x32_bf16 a[160:163], a[36:39], a[0:3], a[160:163]\n"
"v_mfma_f32_16x16x32_bf16 a[164:167], a[36:39], a[4:7], a[164:167]\n"
"ds_read_b128 a[72:75], v129 offset:4096\n"
"v_mfma_f32_16x16x32_bf16 a[168:171], a[36:39], a[8:11], a[168:171]\n"
"v_mfma_f32_16x16x32_bf16 a[172:175], a[36:39], a[12:15], a[172:175]\n"
"ds_read_b128 a[76:79], v129 offset:5120\n"
"v_mfma_f32_16x16x32_bf16 a[176:179], a[36:39], a[16:19], a[176:179]\n"
"v_mfma_f32_16x16x32_bf16 a[180:183], a[36:39], a[20:23], a[180:183]\n"
"v_mfma_f32_16x16x32_bf16 a[184:187], a[36:39], a[24:27], a[184:187]\n"
"ds_read_b128 a[80:83], v129 offset:8192\n"
"v_mfma_f32_16x16x32_bf16 a[188:191], a[36:39], a[28:31], a[188:191]\n"
"v_mfma_f32_16x16x32_bf16 a[192:195], a[40:43], a[0:3], a[192:195]\n"
"ds_read_b128 a[84:87], v129 offset:9216\n"
"v_mfma_f32_16x16x32_bf16 a[196:199], a[40:43], a[4:7], a[196:199]\n"
"v_mfma_f32_16x16x32_bf16 a[200:203], a[40:43], a[8:11], a[200:203]\n"
"ds_read_b128 a[88:91], v129 offset:12288\n"
"v_mfma_f32_16x16x32_bf16 a[204:207], a[40:43], a[12:15], a[204:207]\n"
"v_mfma_f32_16x16x32_bf16 a[208:211], a[40:43], a[16:19], a[208:211]\n"
"v_mfma_f32_16x16x32_bf16 a[212:215], a[40:43], a[20:23], a[212:215]\n"
"ds_read_b128 a[92:95], v129 offset:13312\n"
"v_mfma_f32_16x16x32_bf16 a[216:219], a[40:43], a[24:27], a[216:219]\n"
"v_mfma_f32_16x16x32_bf16 a[220:223], a[40:43], a[28:31], a[220:223]\n"
"ds_read_b64_tr_b16 a[100:101], v133 offset:40960\n"
"v_mfma_f32_16x16x32_bf16 a[224:227], a[44:47], a[0:3], a[224:227]\n"
"v_mfma_f32_16x16x32_bf16 a[228:231], a[44:47], a[4:7], a[228:231]\n"
"ds_read_b64_tr_b16 a[102:103], v133 offset:41984\n"
"v_mfma_f32_16x16x32_bf16 a[232:235], a[44:47], a[8:11], a[232:235]\n"
"v_mfma_f32_16x16x32_bf16 a[236:239], a[44:47], a[12:15], a[236:239]\n"
"v_mfma_f32_16x16x32_bf16 a[240:243], a[44:47], a[16:19], a[240:243]\n"
"ds_read_b64_tr_b16 a[104:105], v134 offset:40960\n"
"v_mfma_f32_16x16x32_bf16 a[244:247], a[44:47], a[20:23], a[244:247]\n"
"v_mfma_f32_16x16x32_bf16 a[248:251], a[44:47], a[24:27], a[248:251]\n"
"ds_read_b64_tr_b16 a[106:107], v134 offset:41984\n"
"v_mfma_f32_16x16x32_bf16 a[252:255], a[44:47], a[28:31], a[252:255]\n"
"v_mfma_f32_16x16x32_bf16 v[0:3], a[0:3], a[48:51], v[0:3]\n"
"ds_read_b64_tr_b16 a[108:109], v135 offset:40960\n"
"v_mfma_f32_16x16x32_bf16 v[4:7], a[0:3], a[52:55], v[4:7]\n"
"v_mfma_f32_16x16x32_bf16 v[8:11], a[0:3], a[56:59], v[8:11]\n"
"v_mfma_f32_16x16x32_bf16 v[12:15], a[0:3], a[60:63], v[12:15]\n"
"ds_read_b64_tr_b16 a[110:111], v135 offset:41984\n"
"v_mfma_f32_16x16x32_bf16 v[16:19], a[4:7], a[48:51], v[16:19]\n"
"s_mov_b32 s86, 105918464\n"
"v_mfma_f32_16x16x32_bf16 v[20:23], a[4:7], a[52:55], v[20:23]\n"
"ds_read_b64_tr_b16 a[112:113], v136 offset:40960\n"
"v_mfma_f32_16x16x32_bf16 v[24:27], a[4:7], a[56:59], v[24:27]\n"
"buffer_load_dwordx2 v[248:249], v231, s[96:99], s86 offen sc1\n"
"v_mfma_f32_16x16x32_bf16 v[28:31], a[4:7], a[60:63], v[28:31]\n"
"ds_read_b64_tr_b16 a[114:115], v136 offset:41984\n"
"v_mfma_f32_16x16x32_bf16 v[32:35], a[8:11], a[48:51], v[32:35]\n"
"v_mfma_f32_16x16x32_bf16 v[36:39], a[8:11], a[52:55], v[36:39]\n"
"v_mfma_f32_16x16x32_bf16 v[40:43], a[8:11], a[56:59], v[40:43]\n"
"ds_read_b64_tr_b16 a[116:117], v137 offset:40960\n"
"v_mfma_f32_16x16x32_bf16 v[44:47], a[8:11], a[60:63], v[44:47]\n"
"v_mfma_f32_16x16x32_bf16 v[48:51], a[12:15], a[48:51], v[48:51]\n"
"ds_read_b64_tr_b16 a[118:119], v137 offset:41984\n"
"v_mfma_f32_16x16x32_bf16 v[52:55], a[12:15], a[52:55], v[52:55]\n"
"v_mfma_f32_16x16x32_bf16 v[56:59], a[12:15], a[56:59], v[56:59]\n"
"ds_read_b64_tr_b16 a[120:121], v138 offset:40960\n"
"v_mfma_f32_16x16x32_bf16 v[60:63], a[12:15], a[60:63], v[60:63]\n"
"v_mfma_f32_16x16x32_bf16 v[64:67], a[16:19], a[48:51], v[64:67]\n"
"v_mfma_f32_16x16x32_bf16 v[68:71], a[16:19], a[52:55], v[68:71]\n"
"ds_read_b64_tr_b16 a[122:123], v138 offset:41984\n"
"v_mfma_f32_16x16x32_bf16 v[72:75], a[16:19], a[56:59], v[72:75]\n"
"v_mfma_f32_16x16x32_bf16 v[76:79], a[16:19], a[60:63], v[76:79]\n"
"ds_read_b64_tr_b16 a[124:125], v139 offset:40960\n"
"v_mfma_f32_16x16x32_bf16 v[80:83], a[20:23], a[48:51], v[80:83]\n"
"v_mfma_f32_16x16x32_bf16 v[84:87], a[20:23], a[52:55], v[84:87]\n"
"ds_read_b64_tr_b16 a[126:127], v139 offset:41984\n"
"v_mfma_f32_16x16x32_bf16 v[88:91], a[20:23], a[56:59], v[88:91]\n"
"v_mfma_f32_16x16x32_bf16 v[92:95], a[20:23], a[60:63], v[92:95]\n"
"v_mfma_f32_16x16x32_bf16 v[96:99], a[24:27], a[48:51], v[96:99]\n"
"v_mfma_f32_16x16x32_bf16 v[100:103], a[24:27], a[52:55], v[100:103]\n"
"v_mfma_f32_16x16x32_bf16 v[104:107], a[24:27], a[56:59], v[104:107]\n"
"s_waitcnt vmcnt(0)\n"
"v_cmp_ne_u64 vcc, s[14:15], v[248:249]\n"
"s_nop 0\n"
"s_cbranch_vccnz SLOW_H_%=\n"
"OK_H_%=:\n"
"v_mfma_f32_16x16x32_bf16 v[108:111], a[24:27], a[60:63], v[108:111]\n"
"v_mfma_f32_16x16x32_bf16 v[112:115], a[28:31], a[48:51], v[112:115]\n"
"s_waitcnt lgkmcnt(0)\n"
"s_barrier\n"
"v_mfma_f32_16x16x32_bf16 v[116:119], a[28:31], a[52:55], v[116:119]\n"
"v_mfma_f32_16x16x32_bf16 v[120:123], a[28:31], a[56:59], v[120:123]\n"
"v_mfma_f32_16x16x32_bf16 v[124:127], a[28:31], a[60:63], v[124:127]\n"
"v_mfma_f32_16x16x32_bf16 a[128:131], a[96:99], a[64:67], a[128:131]\n"
"v_mfma_f32_16x16x32_bf16 a[132:135], a[96:99], a[68:71], a[132:135]\n"
"buffer_load_dwordx4 a[32:35], v244, s[96:99], s58 offen offset:0 sc1\n"
"v_mfma_f32_16x16x32_bf16 a[136:139], a[96:99], a[72:75], a[136:139]\n"
"ds_read_b64_tr_b16 a[0:1], v232 offset:0\n"
"v_mfma_f32_16x16x32_bf16 a[140:143], a[96:99], a[76:79], a[140:143]\n"
"ds_read_b64_tr_b16 a[2:3], v232 offset:1024\n"
"v_mfma_f32_16x16x32_bf16 a[144:147], a[96:99], a[80:83], a[144:147]\n"
"v_mfma_f32_16x16x32_bf16 a[148:151], a[96:99], a[84:87], a[148:151]\n"
"buffer_load_dwordx4 a[36:39], v245, s[96:99], s58 offen offset:0 sc1\n"
"v_mfma_f32_16x16x32_bf16 a[152:155], a[96:99], a[88:91], a[152:155]\n"
"ds_read_b64_tr_b16 a[4:5], v233 offset:0\n"
"v_mfma_f32_16x16x32_bf16 a[156:159], a[96:99], a[92:95], a[156:159]\n"
"v_mfma_f32_16x16x32_bf16 a[160:163], a[100:103], a[64:67], a[160:163]\n"
"ds_read_b64_tr_b16 a[6:7], v233 offset:1024\n"
"v_mfma_f32_16x16x32_bf16 a[164:167], a[100:103], a[68:71], a[164:167]\n"
"buffer_load_dwordx4 a[40:43], v246, s[96:99], s58 offen offset:0 sc1\n"
"v_mfma_f32_16x16x32_bf16 a[168:171], a[100:103], a[72:75], a[168:171]\n"
"v_mfma_f32_16x16x32_bf16 a[172:175], a[100:103], a[76:79], a[172:175]\n"
"ds_read_b64_tr_b16 a[8:9], v234 offset:0\n"
"v_mfma_f32_16x16x32_bf16 a[176:179], a[100:103], a[80:83], a[176:179]\n"
"v_mfma_f32_16x16x32_bf16 a[180:183], a[100:103], a[84:87], a[180:183]\n"
"buffer_load_dwordx4 a[44:47], v247, s[96:99], s58 offen offset:0 sc1\n"
"v_mfma_f32_16x16x32_bf16 a[184:187], a[100:103], a[88:91], a[184:187]\n"
"ds_read_b64_tr_b16 a[10:11], v234 offset:1024\n"
"v_mfma_f32_16x16x32_bf16 a[188:191], a[100:103], a[92:95], a[188:191]\n"
"ds_read_b64_tr_b16 a[12:13], v235 offset:0\n"
"v_mfma_f32_16x16x32_bf16 a[192:195], a[104:107], a[64:67], a[192:195]\n"
"v_mfma_f32_16x16x32_bf16 a[196:199], a[104:107], a[68:71], a[196:199]\n"
"buffer_load_dwordx4 a[48:51], v244, s[96:99], s58 offen offset:1024 sc1\n"
"v_mfma_f32_16x16x32_bf16 a[200:203], a[104:107], a[72:75], a[200:203]\n"
"ds_read_b64_tr_b16 a[14:15], v235 offset:1024\n"
"v_mfma_f32_16x16x32_bf16 a[204:207], a[104:107], a[76:79], a[204:207]\n"
"v_mfma_f32_16x16x32_bf16 a[208:211], a[104:107], a[80:83], a[208:211]\n"
"ds_read_b64_tr_b16 a[16:17], v236 offset:0\n"
"v_mfma_f32_16x16x32_bf16 a[212:215], a[104:107], a[84:87], a[212:215]\n"
"buffer_load_dwordx4 a[52:55], v245, s[96:99], s58 offen offset:1024 sc1\n"
"v_mfma_f32_16x16x32_bf16 a[216:219], a[104:107], a[88:91], a[216:219]\n"
"v_mfma_f32_16x16x32_bf16 a[220:223], a[104:107], a[92:95], a[220:223]\n"
"ds_read_b64_tr_b16 a[18:19], v236 offset:1024\n"
"v_mfma_f32_16x16x32_bf16 a[224:227], a[108:111], a[64:67], a[224:227]\n"
"v_mfma_f32_16x16x32_bf16 a[228:231], a[108:111], a[68:71], a[228:231]\n"
"buffer_load_dwordx4 a[56:59], v246, s[96:99], s58 offen offset:1024 sc1\n"
"v_mfma_f32_16x16x32_bf16 a[232:235], a[108:111], a[72:75], a[232:235]\n"
"ds_read_b64_tr_b16 a[20:21], v237 offset:0\n"
"v_mfma_f32_16x16x32_bf16 a[236:239], a[108:111], a[76:79], a[236:239]\n"
"ds_read_b64_tr_b16 a[22:23], v237 offset:1024\n"
"v_mfma_f32_16x16x32_bf16 a[240:243], a[108:111], a[80:83], a[240:243]\n"
"v_mfma_f32_16x16x32_bf16 a[244:247], a[108:111], a[84:87], a[244:247]\n"
"buffer_load_dwordx4 a[60:63], v247, s[96:99], s58 offen offset:1024 sc1\n"
"v_mfma_f32_16x16x32_bf16 a[248:251], a[108:111], a[88:91], a[248:251]\n"
"ds_read_b64_tr_b16 a[24:25], v238 offset:0\n"
"v_mfma_f32_16x16x32_bf16 a[252:255], a[108:111], a[92:95], a[252:255]\n"
"v_mfma_f32_16x16x32_bf16 v[0:3], a[64:67], a[112:115], v[0:3]\n"
"ds_read_b64_tr_b16 a[26:27], v238 offset:1024\n"
"v_mfma_f32_16x16x32_bf16 v[4:7], a[64:67], a[116:119], v[4:7]\n"
"v_mfma_f32_16x16x32_bf16 v[8:11], a[64:67], a[120:123], v[8:11]\n"
"buffer_load_dwordx4 a[96:99], v244, s[96:99], s58 offen offset:2048 sc1\n"
"v_mfma_f32_16x16x32_bf16 v[12:15], a[64:67], a[124:127], v[12:15]\n"
"ds_read_b64_tr_b16 a[28:29], v239 offset:0\n"
"v_mfma_f32_16x16x32_bf16 v[16:19], a[68:71], a[112:115], v[16:19]\n"
"v_mfma_f32_16x16x32_bf16 v[20:23], a[68:71], a[116:119], v[20:23]\n"
"ds_read_b64_tr_b16 a[30:31], v239 offset:1024\n"
"v_mfma_f32_16x16x32_bf16 v[24:27], a[68:71], a[120:123], v[24:27]\n"
"v_mfma_f32_16x16x32_bf16 v[28:31], a[68:71], a[124:127], v[28:31]\n"
"buffer_load_dwordx4 a[100:103], v245, s[96:99], s58 offen offset:2048 sc1\n"
"v_mfma_f32_16x16x32_bf16 v[32:35], a[72:75], a[112:115], v[32:35]\n"
"v_mfma_f32_16x16x32_bf16 v[36:39], a[72:75], a[116:119], v[36:39]\n"
"v_mfma_f32_16x16x32_bf16 v[40:43], a[72:75], a[120:123], v[40:43]\n"
"v_cvt_pk_bf16_f32 v160, v0, v1\n"
"v_cvt_pk_bf16_f32 v161, v2, v3\n"
"v_cvt_pk_bf16_f32 v162, v16, v17\n"
"v_mfma_f32_16x16x32_bf16 v[44:47], a[72:75], a[124:127], v[44:47]\n"
"v_cvt_pk_bf16_f32 v163, v18, v19\n"
"v_cvt_pk_bf16_f32 v164, v4, v5\n"
"v_mfma_f32_16x16x32_bf16 v[48:51], a[76:79], a[112:115], v[48:51]\n"
"buffer_load_dwordx4 a[104:107], v246, s[96:99], s58 offen offset:2048 sc1\n"
"v_cvt_pk_bf16_f32 v165, v6, v7\n"
"v_mfma_f32_16x16x32_bf16 v[52:55], a[76:79], a[116:119], v[52:55]\n"
"v_cvt_pk_bf16_f32 v166, v20, v21\n"
"v_cvt_pk_bf16_f32 v167, v22, v23\n"
"v_mfma_f32_16x16x32_bf16 v[56:59], a[76:79], a[120:123], v[56:59]\n"
"v_cvt_pk_bf16_f32 v168, v8, v9\n"
"v_cvt_pk_bf16_f32 v169, v10, v11\n"
"v_cvt_pk_bf16_f32 v170, v24, v25\n"
"v_mfma_f32_16x16x32_bf16 v[60:63], a[76:79], a[124:127], v[60:63]\n"
"v_cvt_pk_bf16_f32 v171, v26, v27\n"
"v_cvt_pk_bf16_f32 v172, v12, v13\n"
"v_mfma_f32_16x16x32_bf16 v[64:67], a[80:83], a[112:115], v[64:67]\n"
"v_cvt_pk_bf16_f32 v173, v14, v15\n"
"v_cvt_pk_bf16_f32 v174, v28, v29\n"
"v_mfma_f32_16x16x32_bf16 v[68:71], a[80:83], a[116:119], v[68:71]\n"
"buffer_load_dwordx4 a[108:111], v247, s[96:99], s58 offen offset:2048 sc1\n"
"v_cvt_pk_bf16_f32 v175, v30, v31\n"
"v_mfma_f32_16x16x32_bf16 v[72:75], a[80:83], a[120:123], v[72:75]\n"
"v_cvt_pk_bf16_f32 v176, v32, v33\n"
"v_cvt_pk_bf16_f32 v177, v34, v35\n"
"v_mfma_f32_16x16x32_bf16 v[76:79], a[80:83], a[124:127], v[76:79]\n"
"v_cvt_pk_bf16_f32 v178, v48, v49\n"
"v_cvt_pk_bf16_f32 v179, v50, v51\n"
"v_mfma_f32_16x16x32_bf16 v[80:83], a[84:87], a[112:115], v[80:83]\n"
"v_cvt_pk_bf16_f32 v180, v36, v37\n"
"v_cvt_pk_bf16_f32 v181, v38, v39\n"
"v_mfma_f32_16x16x32_bf16 v[84:87], a[84:87], a[116:119], v[84:87]\n"
"v_cvt_pk_bf16_f32 v182, v52, v53\n"
"v_cvt_pk_bf16_f32 v183, v54, v55\n"
"v_mfma_f32_16x16x32_bf16 v[88:91], a[84:87], a[120:123], v[88:91]\n"
"v_cvt_pk_bf16_f32 v184, v40, v41\n"
"v_cvt_pk_bf16_f32 v185, v42, v43\n"
"v_mfma_f32_16x16x32_bf16 v[92:95], a[84:87], a[124:127], v[92:95]\n"
"v_cvt_pk_bf16_f32 v186, v56, v57\n"
"v_cvt_pk_bf16_f32 v187, v58, v59\n"
"v_mfma_f32_16x16x32_bf16 v[96:99], a[88:91], a[112:115], v[96:99]\n"
"v_cvt_pk_bf16_f32 v188, v44, v45\n"
"v_cvt_pk_bf16_f32 v189, v46, v47\n"
"v_mfma_f32_16x16x32_bf16 v[100:103], a[88:91], a[116:119], v[100:103]\n"
"v_cvt_pk_bf16_f32 v190, v60, v61\n"
"v_cvt_pk_bf16_f32 v191, v62, v63\n"
"v_mfma_f32_16x16x32_bf16 v[104:107], a[88:91], a[120:123], v[104:107]\n"
"v_cvt_pk_bf16_f32 v192, v64, v65\n"
"v_cvt_pk_bf16_f32 v193, v66, v67\n"
"v_mfma_f32_16x16x32_bf16 v[108:111], a[88:91], a[124:127], v[108:111]\n"
"v_cvt_pk_bf16_f32 v194, v80, v81\n"
"v_cvt_pk_bf16_f32 v195, v82, v83\n"
"v_mfma_f32_16x16x32_bf16 v[112:115], a[92:95], a[112:115], v[112:115]\n"
"v_cvt_pk_bf16_f32 v196, v68, v69\n"
"v_cvt_pk_bf16_f32 v197, v70, v71\n"
"v_mfma_f32_16x16x32_bf16 v[116:119], a[92:95], a[116:119], v[116:119]\n"
"v_cvt_pk_bf16_f32 v198, v84, v85\n"
"v_cvt_pk_bf16_f32 v199, v86, v87\n"
"v_mfma_f32_16x16x32_bf16 v[120:123], a[92:95], a[120:123], v[120:123]\n"
"v_cvt_pk_bf16_f32 v200, v72, v73\n"
"v_cvt_pk_bf16_f32 v201, v74, v75\n"
"v_mfma_f32_16x16x32_bf16 v[124:127], a[92:95], a[124:127], v[124:127]\n"
"v_cvt_pk_bf16_f32 v202, v88, v89\n"
"v_cvt_pk_bf16_f32 v203, v90, v91\n"
"s_waitcnt vmcnt(11) lgkmcnt(0)\n"
"v_mfma_f32_16x16x32_bf16 v[0:3], a[0:3], a[32:35], 0\n"
"ds_read_b64_tr_b16 a[64:65], v232 offset:8192\n"
"v_cvt_pk_bf16_f32 v204, v76, v77\n"
"v_cvt_pk_bf16_f32 v205, v78, v79\n"
"s_waitcnt vmcnt(10)\n"
"v_mfma_f32_16x16x32_bf16 v[4:7], a[0:3], a[36:39], 0\n"
"buffer_load_dwordx4 a[112:115], v244, s[96:99], s58 offen offset:3072 sc1\n"
"v_cvt_pk_bf16_f32 v206, v92, v93\n"
"v_cvt_pk_bf16_f32 v207, v94, v95\n"
"s_waitcnt vmcnt(10)\n"
"v_mfma_f32_16x16x32_bf16 v[8:11], a[0:3], a[40:43], 0\n"
"ds_read_b64_tr_b16 a[66:67], v232 offset:9216\n"
"v_cvt_pk_bf16_f32 v208, v96, v97\n"
"s_waitcnt vmcnt(9)\n"
"v_mfma_f32_16x16x32_bf16 v[12:15], a[0:3], a[44:47], 0\n"
"ds_read_b64_tr_b16 a[68:69], v233 offset:8192\n"
"v_cvt_pk_bf16_f32 v209, v98, v99\n"
"v_cvt_pk_bf16_f32 v210, v112, v113\n"
"v_mfma_f32_16x16x32_bf16 v[16:19], a[4:7], a[32:35], 0\n"
"ds_read_b64_tr_b16 a[70:71], v233 offset:9216\n"
"v_cvt_pk_bf16_f32 v211, v114, v115\n"
"v_mfma_f32_16x16x32_bf16 v[20:23], a[4:7], a[36:39], 0\n"
"v_cvt_pk_bf16_f32 v212, v100, v101\n"
"v_cvt_pk_bf16_f32 v213, v102, v103\n"
"v_mfma_f32_16x16x32_bf16 v[24:27], a[4:7], a[40:43], 0\n"
"buffer_store_dwordx4 v[160:163], v153, s[96:99], s84 offen offset:0 sc1\n"
"v_cvt_pk_bf16_f32 v214, v116, v117\n"
"v_cvt_pk_bf16_f32 v215, v118, v119\n"
"v_mfma_f32_16x16x32_bf16 v[28:31], a[4:7], a[44:47], 0\n"
"ds_read_b64_tr_b16 a[72:73], v234 offset:8192\n"
"v_cvt_pk_bf16_f32 v216, v104, v105\n"
"v_cvt_pk_bf16_f32 v217, v106, v107\n"
"v_mfma_f32_16x16x32_bf16 v[32:35], a[8:11], a[32:35], 0\n"
"ds_read_b64_tr_b16 a[74:75], v234 offset:9216\n"
"v_cvt_pk_bf16_f32 v218, v120, v121\n"
"v_mfma_f32_16x16x32_bf16 v[36:39], a[8:11], a[36:39], 0\n"
"ds_read_b64_tr_b16 a[76:77], v235 offset:8192\n"
"v_cvt_pk_bf16_f32 v219, v122, v123\n"
"v_cvt_pk_bf16_f32 v220, v108, v109\n"
"v_mfma_f32_16x16x32_bf16 v[40:43], a[8:11], a[40:43], 0\n"
"v_cvt_pk_bf16_f32 v221, v110, v111\n"
"v_cvt_pk_bf16_f32 v222, v124, v125\n"
"v_mfma_f32_16x16x32_bf16 v[44:47], a[8:11], a[44:47], 0\n"
"buffer_load_dwordx4 a[116:119], v245, s[96:99], s58 offen offset:3072 sc1\n"
"v_cvt_pk_bf16_f32 v223, v126, v127\n"
"v_mfma_f32_16x16x32_bf16 v[48:51], a[12:15], a[32:35], 0\n"
"ds_read_b64_tr_b16 a[78:79], v235 offset:9216\n"
"v_accvgpr_read_b32 v128, a128\n"
"v_accvgpr_read_b32 v129, a129\n"
"v_mfma_f32_16x16x32_bf16 v[52:55], a[12:15], a[36:39], 0\n"
"ds_read_b64_tr_b16 a[80:81], v236 offset:8192\n"
"v_accvgpr_read_b32 v130, a130\n"
"v_mfma_f32_16x16x32_bf16 v[56:59], a[12:15], a[40:43], 0\n"
"ds_read_b64_tr_b16 a[82:83], v236 offset:9216\n"
"v_accvgpr_read_b32 v131, a131\n"
"v_accvgpr_read_b32 v132, a160\n"
"v_mfma_f32_16x16x32_bf16 v[60:63], a[12:15], a[44:47], 0\n"
"v_accvgpr_read_b32 v133, a161\n"
"v_accvgpr_read_b32 v134, a162\n"
"v_mfma_f32_16x16x32_bf16 v[64:67], a[16:19], a[32:35], 0\n"
"buffer_load_dwordx4 a[120:123], v246, s[96:99], s58 offen offset:3072 sc1\n"
"v_accvgpr_read_b32 v135, a163\n"
"v_mfma_f32_16x16x32_bf16 v[68:71], a[16:19], a[36:39], 0\n"
"ds_read_b64_tr_b16 a[84:85], v237 offset:8192\n"
"v_cvt_pk_bf16_f32 v136, v128, v129\n"
"v_cvt_pk_bf16_f32 v137, v130, v131\n"
"v_mfma_f32_16x16x32_bf16 v[72:75], a[16:19], a[40:43], 0\n"
"ds_read_b64_tr_b16 a[86:87], v237 offset:9216\n"
"v_cvt_pk_bf16_f32 v138, v132, v133\n"
"v_mfma_f32_16x16x32_bf16 v[76:79], a[16:19], a[44:47], 0\n"
"ds_read_b64_tr_b16 a[88:89], v238 offset:8192\n"
"v_cvt_pk_bf16_f32 v139, v134, v135\n"
"v_mfma_f32_16x16x32_bf16 v[80:83], a[20:23], a[32:35], 0\n"
"buffer_store_dwordx4 v[136:139], v152, s[96:99], s83 offen offset:0 sc1\n"
"v_mfma_f32_16x16x32_bf16 v[84:87], a[20:23], a[36:39], 0\n"
"buffer_load_dwordx4 a[124:127], v247, s[96:99], s58 offen offset:3072 sc1\n"
"v_accvgpr_read_b32 v140, a192\n"
"v_accvgpr_read_b32 v141, a193\n"
"v_mfma_f32_16x16x32_bf16 v[88:91], a[20:23], a[40:43], 0\n"
"ds_read_b64_tr_b16 a[90:91], v238 offset:9216\n"
"v_accvgpr_read_b32 v142, a194\n"
"v_mfma_f32_16x16x32_bf16 v[92:95], a[20:23], a[44:47], 0\n"
"ds_read_b64_tr_b16 a[92:93], v239 offset:8192\n"
"v_accvgpr_read_b32 v143, a195\n"
"v_mfma_f32_16x16x32_bf16 v[96:99], a[24:27], a[32:35], 0\n"
"ds_read_b64_tr_b16 a[94:95], v239 offset:9216\n"
"v_accvgpr_read_b32 v144, a224\n"
"v_accvgpr_read_b32 v145, a225\n"
"v_mfma_f32_16x16x32_bf16 v[100:103], a[24:27], a[36:39], 0\n"
"v_accvgpr_read_b32 v146, a226\n"
"v_mfma_f32_16x16x32_bf16 v[104:107], a[24:27], a[40:43], 0\n"
"buffer_store_dwordx4 v[164:167], v153, s[96:99], s84 offen offset:256 sc1\n"
"v_accvgpr_read_b32 v147, a227\n"
"v_cvt_pk_bf16_f32 v148, v140, v141\n"
"v_mfma_f32_16x16x32_bf16 v[108:111], a[24:27], a[44:47], 0\n"
"v_cvt_pk_bf16_f32 v149, v142, v143\n"
"v_cvt_pk_bf16_f32 v150, v144, v145\n"
"v_mfma_f32_16x16x32_bf16 v[112:115], a[28:31], a[32:35], 0\n"
"v_cvt_pk_bf16_f32 v151, v146, v147\n"
"s_waitcnt lgkmcnt(0)\n"
"s_barrier\n"
"v_mfma_f32_16x16x32_bf16 v[116:119], a[28:31], a[36:39], 0\n"
"buffer_store_dwordx4 v[148:151], v152, s[96:99], s83 offen offset:2048 sc1\n"
"v_mfma_f32_16x16x32_bf16 v[120:123], a[28:31], a[40:43], 0\n"
"v_mfma_f32_16x16x32_bf16 v[124:127], a[28:31], a[44:47], 0\n"
"s_add_u32 s58, s58, 4096\n"
"s_waitcnt vmcnt(15)\n"
"v_mfma_f32_16x16x32_bf16 v[0:3], a[64:67], a[48:51], v[0:3]\n"
"ds_read_b64_tr_b16 a[0:1], v232 offset:16384\n"
"v_accvgpr_read_b32 v128, a132\n"
"s_waitcnt vmcnt(14)\n"
"v_mfma_f32_16x16x32_bf16 v[4:7], a[64:67], a[52:55], v[4:7]\n"
"buffer_load_dwordx4 a[32:35], v244, s[96:99], s58 offen offset:0 sc1\n"
"s_waitcnt vmcnt(14)\n"
"v_mfma_f32_16x16x32_bf16 v[8:11], a[64:67], a[56:59], v[8:11]\n"
"ds_read_b64_tr_b16 a[2:3], v232 offset:17408\n"
"s_waitcnt vmcnt(13)\n"
"v_mfma_f32_16x16x32_bf16 v[12:15], a[64:67], a[60:63], v[12:15]\n"
"ds_read_b64_tr_b16 a[4:5], v233 offset:16384\n"
"v_accvgpr_read_b32 v129, a133\n"
"v_mfma_f32_16x16x32_bf16 v[16:19], a[68:71], a[48:51], v[16:19]\n"
"ds_read_b64_tr_b16 a[6:7], v233 offset:17408\n"
"v_mfma_f32_16x16x32_bf16 v[20:23], a[68:71], a[52:55], v[20:23]\n"
"v_accvgpr_read_b32 v130, a134\n"
"v_mfma_f32_16x16x32_bf16 v[24:27], a[68:71], a[56:59], v[24:27]\n"
"ds_read_b64_tr_b16 a[8:9], v234 offset:16384\n"
"v_mfma_f32_16x16x32_bf16 v[28:31], a[68:71], a[60:63], v[28:31]\n"
"buffer_store_dwordx4 v[168:171], v153, s[96:99], s84 offen offset:1024 sc1\n"
"v_mfma_f32_16x16x32_bf16 v[32:35], a[72:75], a[48:51], v[32:35]\n"
"ds_read_b64_tr_b16 a[10:11], v234 offset:17408\n"
"v_accvgpr_read_b32 v131, a135\n"
"v_mfma_f32_16x16x32_bf16 v[36:39], a[72:75], a[52:55], v[36:39]\n"
"ds_read_b64_tr_b16 a[12:13], v235 offset:16384\n"
"v_mfma_f32_16x16x32_bf16 v[40:43], a[72:75], a[56:59], v[40:43]\n"
"v_accvgpr_read_b32 v132, a164\n"
"v_mfma_f32_16x16x32_bf16 v[44:47], a[72:75], a[60:63], v[44:47]\n"
"ds_read_b64_tr_b16 a[14:15], v235 offset:17408\n"
"v_mfma_f32_16x16x32_bf16 v[48:51], a[76:79], a[48:51], v[48:51]\n"
"ds_read_b64_tr_b16 a[16:17], v236 offset:16384\n"
"v_mfma_f32_16x16x32_bf16 v[52:55], a[76:79], a[52:55], v[52:55]\n"
"buffer_load_dwordx4 a[36:39], v245, s[96:99], s58 offen offset:0 sc1\n"
"v_accvgpr_read_b32 v133, a165\n"
"v_mfma_f32_16x16x32_bf16 v[56:59], a[76:79], a[56:59], v[56:59]\n"
"ds_read_b64_tr_b16 a[18:19], v236 offset:17408\n"
"v_mfma_f32_16x16x32_bf16 v[60:63], a[76:79], a[60:63], v[60:63]\n"
"v_accvgpr_read_b32 v134, a166\n"
"v_mfma_f32_16x16x32_bf16 v[64:67], a[80:83], a[48:51], v[64:67]\n"
"ds_read_b64_tr_b16 a[20:21], v237 offset:16384\n"
"v_mfma_f32_16x16x32_bf16 v[68:71], a[80:83], a[52:55], v[68:71]\n"
"ds_read_b64_tr_b16 a[22:23], v237 offset:17408\n"
"v_mfma_f32_16x16x32_bf16 v[72:75], a[80:83], a[56:59], v[72:75]\n"
"ds_read_b64_tr_b16 a[24:25], v238 offset:16384\n"
"v_accvgpr_read_b32 v135, a167\n"
"v_mfma_f32_16x16x32_bf16 v[76:79], a[80:83], a[60:63], v[76:79]\n"
"buffer_load_dwordx4 a[40:43], v246, s[96:99], s58 offen offset:0 sc1\n"
"v_mfma_f32_16x16x32_bf16 v[80:83], a[84:87], a[48:51], v[80:83]\n"
"PG5_%=:\n"
"s_cbranch_execz RPG5_%=\n"
"v_cvt_pk_bf16_f32 v136, v128, v129\n"
"v_mfma_f32_16x16x32_bf16 v[84:87], a[84:87], a[52:55], v[84:87]\n"
"ds_read_b64_tr_b16 a[26:27], v238 offset:17408\n"
"v_mfma_f32_16x16x32_bf16 v[88:91], a[84:87], a[56:59], v[88:91]\n"
"ds_read_b64_tr_b16 a[28:29], v239 offset:16384\n"
"v_mfma_f32_16x16x32_bf16 v[92:95], a[84:87], a[60:63], v[92:95]\n"
"ds_read_b64_tr_b16 a[30:31], v239 offset:17408\n"
"v_cvt_pk_bf16_f32 v137, v130, v131\n"
"v_mfma_f32_16x16x32_bf16 v[96:99], a[88:91], a[48:51], v[96:99]\n"
"v_mfma_f32_16x16x32_bf16 v[100:103], a[88:91], a[52:55], v[100:103]\n"
"buffer_load_dwordx4 a[44:47], v247, s[96:99], s58 offen offset:0 sc1\n"
"v_cvt_pk_bf16_f32 v138, v132, v133\n"
"v_mfma_f32_16x16x32_bf16 v[104:107], a[88:91], a[56:59], v[104:107]\n"
"v_mfma_f32_16x16x32_bf16 v[108:111], a[88:91], a[60:63], v[108:111]\n"
"v_cvt_pk_bf16_f32 v139, v134, v135\n"
"v_mfma_f32_16x16x32_bf16 v[112:115], a[92:95], a[48:51], v[112:115]\n"
"v_mfma_f32_16x16x32_bf16 v[116:119], a[92:95], a[52:55], v[116:119]\n"
"buffer_store_dwordx4 v[136:139], v152, s[96:99], s83 offen offset:128 sc1\n"
"v_mfma_f32_16x16x32_bf16 v[120:123], a[92:95], a[56:59], v[120:123]\n"
"v_mfma_f32_16x16x32_bf16 v[124:127], a[92:95], a[60:63], v[124:127]\n"
"s_waitcnt lgkmcnt(0)\n"
"s_add_u32 m0, s57, 32768\n"
"s_waitcnt vmcnt(14)\n"
"v_mfma_f32_16x16x32_bf16 v[0:3], a[0:3], a[96:99], v[0:3]\n"
"ds_read_b64_tr_b16 a[64:65], v232 offset:24576\n"
"v_accvgpr_read_b32 v140, a196\n"
"v_mfma_f32_16x16x32_bf16 v[4:7], a[0:3], a[100:103], v[4:7]\n"
"buffer_load_dwordx4 a[48:51], v244, s[96:99], s58 offen offset:1024 sc1\n"
"v_mfma_f32_16x16x32_bf16 v[8:11], a[0:3], a[104:107], v[8:11]\n"
"ds_read_b64_tr_b16 a[66:67], v232 offset:25600\n"
"v_accvgpr_read_b32 v141, a197\n"
"v_mfma_f32_16x16x32_bf16 v[12:15], a[0:3], a[108:111], v[12:15]\n"
"ds_read_b64_tr_b16 a[68:69], v233 offset:24576\n"
"v_mfma_f32_16x16x32_bf16 v[16:19], a[4:7], a[96:99], v[16:19]\n"
"ds_read_b64_tr_b16 a[70:71], v233 offset:25600\n"
"v_mfma_f32_16x16x32_bf16 v[20:23], a[4:7], a[100:103], v[20:23]\n"
"buffer_store_dwordx4 v[172:175], v153, s[96:99], s84 offen offset:1280 sc1\n"
"s_add_u32 s84, s84, 8192\n"
"v_mfma_f32_16x16x32_bf16 v[24:27], a[4:7], a[104:107], v[24:27]\n"
"v_accvgpr_read_b32 v142, a198\n"
"v_mfma_f32_16x16x32_bf16 v[28:31], a[4:7], a[108:111], v[28:31]\n"
"ds_read_b64_tr_b16 a[72:73], v234 offset:24576\n"
"v_mfma_f32_16x16x32_bf16 v[32:35], a[8:11], a[96:99], v[32:35]\n"
"ds_read_b64_tr_b16 a[74:75], v234 offset:25600\n"
"v_accvgpr_read_b32 v143, a199\n"
"v_mfma_f32_16x16x32_bf16 v[36:39], a[8:11], a[100:103], v[36:39]\n"
"buffer_load_dwordx4 a[52:55], v245, s[96:99], s58 offen offset:1024 sc1\n"
"v_mfma_f32_16x16x32_bf16 v[40:43], a[8:11], a[104:107], v[40:43]\n"
"ds_read_b64_tr_b16 a[76:77], v235 offset:24576\n"
"v_mfma_f32_16x16x32_bf16 v[44:47], a[8:11], a[108:111], v[44:47]\n"
"ds_read_b64_tr_b16 a[78:79], v235 offset:25600\n"
"v_accvgpr_read_b32 v144, a228\n"
"v_mfma_f32_16x16x32_bf16 v[48:51], a[12:15], a[96:99], v[48:51]\n"
"v_mfma_f32_16x16x32_bf16 v[52:55], a[12:15], a[100:103], v[52:55]\n"
"buffer_load_dwordx4 v240, s[76:79], s63 offen offset:0 lds\n"
"v_accvgpr_read_b32 v145, a229\n"
"v_mfma_f32_16x16x32_bf16 v[56:59], a[12:15], a[104:107], v[56:59]\n"
"ds_read_b64_tr_b16 a[80:81], v236 offset:24576\n"
"v_mfma_f32_16x16x32_bf16 v[60:63], a[12:15], a[108:111], v[60:63]\n"
"ds_read_b64_tr_b16 a[82:83], v236 offset:25600\n"
"v_mfma_f32_16x16x32_bf16 v[64:67], a[16:19], a[96:99], v[64:67]\n"
"ds_read_b64_tr_b16 a[84:85], v237 offset:24576\n"
"v_accvgpr_read_b32 v146, a230\n"
"v_mfma_f32_16x16x32_bf16 v[68:71], a[16:19], a[100:103], v[68:71]\n"
"ds_read_b64_tr_b16 a[86:87], v237 offset:25600\n"
"v_mfma_f32_16x16x32_bf16 v[72:75], a[16:19], a[104:107], v[72:75]\n"
"buffer_load_dwordx4 a[56:59], v246, s[96:99], s58 offen offset:1024 sc1\n"
"v_accvgpr_read_b32 v147, a231\n"
"v_mfma_f32_16x16x32_bf16 v[76:79], a[16:19], a[108:111], v[76:79]\n"
"v_mfma_f32_16x16x32_bf16 v[80:83], a[20:23], a[96:99], v[80:83]\n"
"ds_read_b64_tr_b16 a[88:89], v238 offset:24576\n"
"v_cvt_pk_bf16_f32 v148, v140, v141\n"
"v_mfma_f32_16x16x32_bf16 v[84:87], a[20:23], a[100:103], v[84:87]\n"
"ds_read_b64_tr_b16 a[90:91], v238 offset:25600\n"
"v_mfma_f32_16x16x32_bf16 v[88:91], a[20:23], a[104:107], v[88:91]\n"
"buffer_load_dwordx4 a[60:63], v247, s[96:99], s58 offen offset:1024 sc1\n"
"v_mfma_f32_16x16x32_bf16 v[92:95], a[20:23], a[108:111], v[92:95]\n"
"ds_read_b64_tr_b16 a[92:93], v239 offset:24576\n"
"v_cvt_pk_bf16_f32 v149, v142, v143\n"
"v_mfma_f32_16x16x32_bf16 v[96:99], a[24:27], a[96:99], v[96:99]\n"
"ds_read_b64_tr_b16 a[94:95], v239 offset:25600\n"
"v_mfma_f32_16x16x32_bf16 v[100:103], a[24:27], a[100:103], v[100:103]\n"
"v_cvt_pk_bf16_f32 v150, v144, v145\n"
"v_mfma_f32_16x16x32_bf16 v[104:107], a[24:27], a[104:107], v[104:107]\n"
"buffer_load_dwordx4 v241, s[76:79], s63 offen offset:1024 lds\n"
"v_mfma_f32_16x16x32_bf16 v[108:111], a[24:27], a[108:111], v[108:111]\n"
"v_cvt_pk_bf16_f32 v151, v146, v147\n"
"v_mfma_f32_16x16x32_bf16 v[112:115], a[28:31], a[96:99], v[112:115]\n"
"s_waitcnt lgkmcnt(0)\n"
"s_barrier\n"
"v_mfma_f32_16x16x32_bf16 v[116:119], a[28:31], a[100:103], v[116:119]\n"
"buffer_store_dwordx4 v[148:151], v152, s[96:99], s83 offen offset:2176 sc1\n"
"s_add_u32 s83, s83, 8192\n"
"v_mfma_f32_16x16x32_bf16 v[120:123], a[28:31], a[104:107], v[120:123]\n"
"v_mfma_f32_16x16x32_bf16 v[124:127], a[28:31], a[108:111], v[124:127]\n"
"s_waitcnt vmcnt(19)\n"
"v_mfma_f32_16x16x32_bf16 v[0:3], a[64:67], a[112:115], v[0:3]\n"
"ds_read_b64_tr_b16 a[0:1], v232 offset:32768\n"
"v_accvgpr_read_b32 v128, a136\n"
"v_mfma_f32_16x16x32_bf16 v[4:7], a[64:67], a[116:119], v[4:7]\n"
"buffer_load_dwordx4 a[96:99], v244, s[96:99], s58 offen offset:2048 sc1\n"
"v_accvgpr_read_b32 v129, a137\n"
"s_waitcnt vmcnt(19)\n"
"v_mfma_f32_16x16x32_bf16 v[8:11], a[64:67], a[120:123], v[8:11]\n"
"ds_read_b64_tr_b16 a[2:3], v232 offset:33792\n"
"v_accvgpr_read_b32 v130, a138\n"
"s_waitcnt vmcnt(17)\n"
"v_mfma_f32_16x16x32_bf16 v[12:15], a[64:67], a[124:127], v[12:15]\n"
"ds_read_b64_tr_b16 a[4:5], v233 offset:32768\n"
"v_accvgpr_read_b32 v131, a139\n"
"v_mfma_f32_16x16x32_bf16 v[16:19], a[68:71], a[112:115], v[16:19]\n"
"buffer_store_dwordx4 v[176:179], v153, s[96:99], s84 offen offset:0 sc1\n"
"v_mfma_f32_16x16x32_bf16 v[20:23], a[68:71], a[116:119], v[20:23]\n"
"ds_read_b64_tr_b16 a[6:7], v233 offset:33792\n"
"v_accvgpr_read_b32 v132, a168\n"
"v_mfma_f32_16x16x32_bf16 v[24:27], a[68:71], a[120:123], v[24:27]\n"
"ds_read_b64_tr_b16 a[8:9], v234 offset:32768\n"
"v_accvgpr_read_b32 v133, a169\n"
"v_mfma_f32_16x16x32_bf16 v[28:31], a[68:71], a[124:127], v[28:31]\n"
"ds_read_b64_tr_b16 a[10:11], v234 offset:33792\n"
"v_accvgpr_read_b32 v134, a170\n"
"v_mfma_f32_16x16x32_bf16 v[32:35], a[72:75], a[112:115], v[32:35]\n"
"buffer_load_dwordx4 a[100:103], v245, s[96:99], s58 offen offset:2048 sc1\n"
"v_accvgpr_read_b32 v135, a171\n"
"v_mfma_f32_16x16x32_bf16 v[36:39], a[72:75], a[116:119], v[36:39]\n"
"v_cvt_pk_bf16_f32 v136, v128, v129\n"
"v_mfma_f32_16x16x32_bf16 v[40:43], a[72:75], a[120:123], v[40:43]\n"
"ds_read_b64_tr_b16 a[12:13], v235 offset:32768\n"
"v_cvt_pk_bf16_f32 v137, v130, v131\n"
"v_mfma_f32_16x16x32_bf16 v[44:47], a[72:75], a[124:127], v[44:47]\n"
"ds_read_b64_tr_b16 a[14:15], v235 offset:33792\n"
"v_cvt_pk_bf16_f32 v138, v132, v133\n"
"v_mfma_f32_16x16x32_bf16 v[48:51], a[76:79], a[112:115], v[48:51]\n"
"buffer_load_dwordx4 v242, s[76:79], s63 offen offset:2048 lds\n"
"v_cvt_pk_bf16_f32 v139, v134, v135\n"
"v_mfma_f32_16x16x32_bf16 v[52:55], a[76:79], a[116:119], v[52:55]\n"
"ds_read_b64_tr_b16 a[16:17], v236 offset:32768\n"
"v_mfma_f32_16x16x32_bf16 v[56:59], a[76:79], a[120:123], v[56:59]\n"
"ds_read_b64_tr_b16 a[18:19], v236 offset:33792\n"
"buffer_store_dwordx4 v[136:139], v152, s[96:99], s83 offen offset:0 sc1\n"
"v_mfma_f32_16x16x32_bf16 v[60:63], a[76:79], a[124:127], v[60:63]\n"
"ds_read_b64_tr_b16 a[20:21], v237 offset:32768\n"
"v_accvgpr_read_b32 v140, a200\n"
"v_mfma_f32_16x16x32_bf16 v[64:67], a[80:83], a[112:115], v[64:67]\n"
"buffer_load_dwordx4 a[104:107], v246, s[96:99], s58 offen offset:2048 sc1\n"
"v_accvgpr_read_b32 v141, a201\n"
"v_mfma_f32_16x16x32_bf16 v[68:71], a[80:83], a[116:119], v[68:71]\n"
"v_accvgpr_read_b32 v142, a202\n"
"v_mfma_f32_16x16x32_bf16 v[72:75], a[80:83], a[120:123], v[72:75]\n"
"ds_read_b64_tr_b16 a[22:23], v237 offset:33792\n"
"v_accvgpr_read_b32 v143, a203\n"
"v_mfma_f32_16x16x32_bf16 v[76:79], a[80:83], a[124:127], v[76:79]\n"
"buffer_store_dwordx4 v[180:183], v153, s[96:99], s84 offen offset:256 sc1\n"
"v_mfma_f32_16x16x32_bf16 v[80:83], a[84:87], a[112:115], v[80:83]\n"
"ds_read_b64_tr_b16 a[24:25], v238 offset:32768\n"
"v_accvgpr_read_b32 v144, a232\n"
"v_mfma_f32_16x16x32_bf16 v[84:87], a[84:87], a[116:119], v[84:87]\n"
"ds_read_b64_tr_b16 a[26:27], v238 offset:33792\n"
"v_accvgpr_read_b32 v145, a233\n"
"v_mfma_f32_16x16x32_bf16 v[88:91], a[84:87], a[120:123], v[88:91]\n"
"ds_read_b64_tr_b16 a[28:29], v239 offset:32768\n"
"v_mfma_f32_16x16x32_bf16 v[92:95], a[84:87], a[124:127], v[92:95]\n"
"buffer_load_dwordx4 a[108:111], v247, s[96:99], s58 offen offset:2048 sc1\n"
"v_accvgpr_read_b32 v146, a234\n"
"v_mfma_f32_16x16x32_bf16 v[96:99], a[88:91], a[112:115], v[96:99]\n"
"ds_read_b64_tr_b16 a[30:31], v239 offset:33792\n"
"v_accvgpr_read_b32 v147, a235\n"
"v_mfma_f32_16x16x32_bf16 v[100:103], a[88:91], a[116:119], v[100:103]\n"
"v_cvt_pk_bf16_f32 v148, v140, v141\n"
"v_mfma_f32_16x16x32_bf16 v[104:107], a[88:91], a[120:123], v[104:107]\n"
"v_cvt_pk_bf16_f32 v149, v142, v143\n"
"v_mfma_f32_16x16x32_bf16 v[108:111], a[88:91], a[124:127], v[108:111]\n"
"buffer_load_dwordx4 v243, s[76:79], s63 offen offset:3072 lds\n"
"v_cvt_pk_bf16_f32 v150, v144, v145\n"
"v_mfma_f32_16x16x32_bf16 v[112:115], a[92:95], a[112:115], v[112:115]\n"
"v_cvt_pk_bf16_f32 v151, v146, v147\n"
"v_mfma_f32_16x16x32_bf16 v[116:119], a[92:95], a[116:119], v[116:119]\n"
"buffer_store_dwordx4 v[148:151], v152, s[96:99], s83 offen offset:2048 sc1\n"
"v_mfma_f32_16x16x32_bf16 v[120:123], a[92:95], a[120:123], v[120:123]\n"
"v_mfma_f32_16x16x32_bf16 v[124:127], a[92:95], a[124:127], v[124:127]\n"
"s_add_u32 s63, s63, 524288\n"
"s_waitcnt lgkmcnt(0)\n"
"s_add_u32 m0, s57, 49152\n"
"s_waitcnt vmcnt(21)\n"
"v_mfma_f32_16x16x32_bf16 v[0:3], a[0:3], a[32:35], v[0:3]\n"
"ds_read_b64_tr_b16 a[64:65], v232 offset:40960\n"
"v_accvgpr_read_b32 v128, a140\n"
"v_mfma_f32_16x16x32_bf16 v[4:7], a[0:3], a[36:39], v[4:7]\n"
"buffer_load_dwordx4 a[112:115], v244, s[96:99], s58 offen offset:3072 sc1\n"
"s_waitcnt vmcnt(21)\n"
"v_mfma_f32_16x16x32_bf16 v[8:11], a[0:3], a[40:43], v[8:11]\n"
"ds_read_b64_tr_b16 a[66:67], v232 offset:41984\n"
"v_accvgpr_read_b32 v129, a141\n"
"s_waitcnt vmcnt(20)\n"
"v_mfma_f32_16x16x32_bf16 v[12:15], a[0:3], a[44:47], v[12:15]\n"
"ds_read_b64_tr_b16 a[68:69], v233 offset:40960\n"
"v_mfma_f32_16x16x32_bf16 v[16:19], a[4:7], a[32:35], v[16:19]\n"
"ds_read_b64_tr_b16 a[70:71], v233 offset:41984\n"
"v_mfma_f32_16x16x32_bf16 v[20:23], a[4:7], a[36:39], v[20:23]\n"
"buffer_store_dwordx4 v[184:187], v153, s[96:99], s84 offen offset:1024 sc1\n"
"v_accvgpr_read_b32 v130, a142\n"
"v_mfma_f32_16x16x32_bf16 v[24:27], a[4:7], a[40:43], v[24:27]\n"
"v_mfma_f32_16x16x32_bf16 v[28:31], a[4:7], a[44:47], v[28:31]\n"
"ds_read_b64_tr_b16 a[72:73], v234 offset:40960\n"
"v_accvgpr_read_b32 v131, a143\n"
"v_mfma_f32_16x16x32_bf16 v[32:35], a[8:11], a[32:35], v[32:35]\n"
"ds_read_b64_tr_b16 a[74:75], v234 offset:41984\n"
"v_mfma_f32_16x16x32_bf16 v[36:39], a[8:11], a[36:39], v[36:39]\n"
"buffer_load_dwordx4 a[116:119], v245, s[96:99], s58 offen offset:3072 sc1\n"
"v_mfma_f32_16x16x32_bf16 v[40:43], a[8:11], a[40:43], v[40:43]\n"
"ds_read_b64_tr_b16 a[76:77], v235 offset:40960\n"
"v_accvgpr_read_b32 v132, a172\n"
"v_mfma_f32_16x16x32_bf16 v[44:47], a[8:11], a[44:47], v[44:47]\n"
"ds_read_b64_tr_b16 a[78:79], v235 offset:41984\n"
"v_mfma_f32_16x16x32_bf16 v[48:51], a[12:15], a[32:35], v[48:51]\n"
"v_accvgpr_read_b32 v133, a173\n"
"v_mfma_f32_16x16x32_bf16 v[52:55], a[12:15], a[36:39], v[52:55]\n"
"buffer_load_dwordx4 v240, s[76:79], s63 offen offset:0 lds\n"
"v_mfma_f32_16x16x32_bf16 v[56:59], a[12:15], a[40:43], v[56:59]\n"
"ds_read_b64_tr_b16 a[80:81], v236 offset:40960\n"
"v_mfma_f32_16x16x32_bf16 v[60:63], a[12:15], a[44:47], v[60:63]\n"
"ds_read_b64_tr_b16 a[82:83], v236 offset:41984\n"
"v_accvgpr_read_b32 v134, a174\n"
"v_mfma_f32_16x16x32_bf16 v[64:67], a[16:19], a[32:35], v[64:67]\n"
"ds_read_b64_tr_b16 a[84:85], v237 offset:40960\n"
"v_mfma_f32_16x16x32_bf16 v[68:71], a[16:19], a[36:39], v[68:71]\n"
"ds_read_b64_tr_b16 a[86:87], v237 offset:41984\n"
"v_mfma_f32_16x16x32_bf16 v[72:75], a[16:19], a[40:43], v[72:75]\n"
"buffer_load_dwordx4 a[120:123], v246, s[96:99], s58 offen offset:3072 sc1\n"
"v_accvgpr_read_b32 v135, a175\n"
"v_mfma_f32_16x16x32_bf16 v[76:79], a[16:19], a[44:47], v[76:79]\n"
"v_mfma_f32_16x16x32_bf16 v[80:83], a[20:23], a[32:35], v[80:83]\n"
"ds_read_b64_tr_b16 a[88:89], v238 offset:40960\n"
"v_cvt_pk_bf16_f32 v136, v128, v129\n"
"v_mfma_f32_16x16x32_bf16 v[84:87], a[20:23], a[36:39], v[84:87]\n"
"ds_read_b64_tr_b16 a[90:91], v238 offset:41984\n"
"v_mfma_f32_16x16x32_bf16 v[88:91], a[20:23], a[40:43], v[88:91]\n"
"buffer_load_dwordx4 a[124:127], v247, s[96:99], s58 offen offset:3072 sc1\n"
"v_mfma_f32_16x16x32_bf16 v[92:95], a[20:23], a[44:47], v[92:95]\n"
"ds_read_b64_tr_b16 a[92:93], v239 offset:40960\n"
"v_cvt_pk_bf16_f32 v137, v130, v131\n"
"v_mfma_f32_16x16x32_bf16 v[96:99], a[24:27], a[32:35], v[96:99]\n"
"ds_read_b64_tr_b16 a[94:95], v239 offset:41984\n"
"v_mfma_f32_16x16x32_bf16 v[100:103], a[24:27], a[36:39], v[100:103]\n"
"v_cvt_pk_bf16_f32 v138, v132, v133\n"
"v_mfma_f32_16x16x32_bf16 v[104:107], a[24:27], a[40:43], v[104:107]\n"
"buffer_load_dwordx4 v241, s[76:79], s63 offen offset:1024 lds\n"
"v_mfma_f32_16x16x32_bf16 v[108:111], a[24:27], a[44:47], v[108:111]\n"
"v_cvt_pk_bf16_f32 v139, v134, v135\n"
"v_mfma_f32_16x16x32_bf16 v[112:115], a[28:31], a[32:35], v[112:115]\n"
"s_waitcnt lgkmcnt(0)\n"
"s_barrier\n"
"v_mfma_f32_16x16x32_bf16 v[116:119], a[28:31], a[36:39], v[116:119]\n"
"buffer_store_dwordx4 v[136:139], v152, s[96:99], s83 offen offset:128 sc1\n"
"v_mfma_f32_16x16x32_bf16 v[120:123], a[28:31], a[40:43], v[120:123]\n"
"v_mfma_f32_16x16x32_bf16 v[124:127], a[28:31], a[44:47], v[124:127]\n"
"s_add_u32 s58, s58, 4096\n"
"s_waitcnt vmcnt(22)\n"
"v_mfma_f32_16x16x32_bf16 v[0:3], a[64:67], a[48:51], v[0:3]\n"
"ds_read_b64_tr_b16 a[0:1], v232 offset:49152\n"
"v_accvgpr_read_b32 v140, a204\n"
"v_mfma_f32_16x16x32_bf16 v[4:7], a[64:67], a[52:55], v[4:7]\n"
"buffer_load_dwordx4 a[32:35], v244, s[96:99], s58 offen offset:0 sc1\n"
"s_waitcnt vmcnt(22)\n"
"v_mfma_f32_16x16x32_bf16 v[8:11], a[64:67], a[56:59], v[8:11]\n"
"ds_read_b64_tr_b16 a[2:3], v232 offset:50176\n"
"v_accvgpr_read_b32 v141, a205\n"
"s_waitcnt vmcnt(21)\n"
"v_mfma_f32_16x16x32_bf16 v[12:15], a[64:67], a[60:63], v[12:15]\n"
"ds_read_b64_tr_b16 a[4:5], v233 offset:49152\n"
"v_mfma_f32_16x16x32_bf16 v[16:19], a[68:71], a[48:51], v[16:19]\n"
"ds_read_b64_tr_b16 a[6:7], v233 offset:50176\n"
"v_mfma_f32_16x16x32_bf16 v[20:23], a[68:71], a[52:55], v[20:23]\n"
"buffer_store_dwordx4 v[188:191], v153, s[96:99], s84 offen offset:1280 sc1\n"
"s_add_u32 s84, s84, 8192\n"
"v_mfma_f32_16x16x32_bf16 v[24:27], a[68:71], a[56:59], v[24:27]\n"
"v_accvgpr_read_b32 v142, a206\n"
"v_mfma_f32_16x16x32_bf16 v[28:31], a[68:71], a[60:63], v[28:31]\n"
"ds_read_b64_tr_b16 a[8:9], v234 offset:49152\n"
"v_mfma_f32_16x16x32_bf16 v[32:35], a[72:75], a[48:51], v[32:35]\n"
"ds_read_b64_tr_b16 a[10:11], v234 offset:50176\n"
"v_accvgpr_read_b32 v143, a207\n"
"v_mfma_f32_16x16x32_bf16 v[36:39], a[72:75], a[52:55], v[36:39]\n"
"buffer_load_dwordx4 a[36:39], v245, s[96:99], s58 offen offset:0 sc1\n"
"v_mfma_f32_16x16x32_bf16 v[40:43], a[72:75], a[56:59], v[40:43]\n"
"ds_read_b64_tr_b16 a[12:13], v235 offset:49152\n"
"v_mfma_f32_16x16x32_bf16 v[44:47], a[72:75], a[60:63], v[44:47]\n"
"ds_read_b64_tr_b16 a[14:15], v235 offset:50176\n"
"v_accvgpr_read_b32 v144, a236\n"
"v_mfma_f32_16x16x32_bf16 v[48:51], a[76:79], a[48:51], v[48:51]\n"
"v_mfma_f32_16x16x32_bf16 v[52:55], a[76:79], a[52:55], v[52:55]\n"
"buffer_load_dwordx4 v242, s[76:79], s63 offen offset:2048 lds\n"
"v_accvgpr_read_b32 v145, a237\n"
"v_mfma_f32_16x16x32_bf16 v[56:59], a[76:79], a[56:59], v[56:59]\n"
"ds_read_b64_tr_b16 a[16:17], v236 offset:49152\n"
"v_mfma_f32_16x16x32_bf16 v[60:63], a[76:79], a[60:63], v[60:63]\n"
"ds_read_b64_tr_b16 a[18:19], v236 offset:50176\n"
"v_mfma_f32_16x16x32_bf16 v[64:67], a[80:83], a[48:51], v[64:67]\n"
"ds_read_b64_tr_b16 a[20:21], v237 offset:49152\n"
"v_accvgpr_read_b32 v146, a238\n"
"v_mfma_f32_16x16x32_bf16 v[68:71], a[80:83], a[52:55], v[68:71]\n"
"ds_read_b64_tr_b16 a[22:23], v237 offset:50176\n"
"v_mfma_f32_16x16x32_bf16 v[72:75], a[80:83], a[56:59], v[72:75]\n"
"buffer_load_dwordx4 a[40:43], v246, s[96:99], s58 offen offset:0 sc1\n"
"v_accvgpr_read_b32 v147, a239\n"
"v_mfma_f32_16x16x32_bf16 v[76:79], a[80:83], a[60:63], v[76:79]\n"
"v_mfma_f32_16x16x32_bf16 v[80:83], a[84:87], a[48:51], v[80:83]\n"
"ds_read_b64_tr_b16 a[24:25], v238 offset:49152\n"
"v_cvt_pk_bf16_f32 v148, v140, v141\n"
"v_mfma_f32_16x16x32_bf16 v[84:87], a[84:87], a[52:55], v[84:87]\n"
"ds_read_b64_tr_b16 a[26:27], v238 offset:50176\n"
"v_mfma_f32_16x16x32_bf16 v[88:91], a[84:87], a[56:59], v[88:91]\n"
"buffer_load_dwordx4 a[44:47], v247, s[96:99], s58 offen offset:0 sc1\n"
"v_mfma_f32_16x16x32_bf16 v[92:95], a[84:87], a[60:63], v[92:95]\n"
"ds_read_b64_tr_b16 a[28:29], v239 offset:49152\n"
"v_cvt_pk_bf16_f32 v149, v142, v143\n"
"v_mfma_f32_16x16x32_bf16 v[96:99], a[88:91], a[48:51], v[96:99]\n"
"ds_read_b64_tr_b16 a[30:31], v239 offset:50176\n"
"v_mfma_f32_16x16x32_bf16 v[100:103], a[88:91], a[52:55], v[100:103]\n"
"v_cvt_pk_bf16_f32 v150, v144, v145\n"
"v_mfma_f32_16x16x32_bf16 v[104:107], a[88:91], a[56:59], v[104:107]\n"
"buffer_load_dwordx4 v243, s[76:79], s63 offen offset:3072 lds\n"
"v_mfma_f32_16x16x32_bf16 v[108:111], a[88:91], a[60:63], v[108:111]\n"
"v_cvt_pk_bf16_f32 v151, v146, v147\n"
"v_mfma_f32_16x16x32_bf16 v[112:115], a[92:95], a[48:51], v[112:115]\n"
"v_mfma_f32_16x16x32_bf16 v[116:119], a[92:95], a[52:55], v[116:119]\n"
"buffer_store_dwordx4 v[148:151], v152, s[96:99], s83 offen offset:2176 sc1\n"
"s_add_u32 s83, s83, 8192\n"
"v_mfma_f32_16x16x32_bf16 v[120:123], a[92:95], a[56:59], v[120:123]\n"
"v_mfma_f32_16x16x32_bf16 v[124:127], a[92:95], a[60:63], v[124:127]\n"
"s_add_u32 s63, s63, 524288\n"
"s_waitcnt lgkmcnt(0)\n"
"s_add_u32 m0, s57, 65536\n"
"s_waitcnt vmcnt(21)\n"
"v_mfma_f32_16x16x32_bf16 v[0:3], a[0:3], a[96:99], v[0:3]\n"
"ds_read_b64_tr_b16 a[64:65], v232 offset:57344\n"
"v_accvgpr_read_b32 v128, a144\n"
"v_mfma_f32_16x16x32_bf16 v[4:7], a[0:3], a[100:103], v[4:7]\n"
"buffer_load_dwordx4 a[48:51], v244, s[96:99], s58 offen offset:1024 sc1\n"
"v_accvgpr_read_b32 v129, a145\n"
"s_waitcnt vmcnt(21)\n"
"v_mfma_f32_16x16x32_bf16 v[8:11], a[0:3], a[104:107], v[8:11]\n"
"ds_read_b64_tr_b16 a[66:67], v232 offset:58368\n"
"v_accvgpr_read_b32 v130, a146\n"
"s_waitcnt vmcnt(19)\n"
"v_mfma_f32_16x16x32_bf16 v[12:15], a[0:3], a[108:111], v[12:15]\n"
"ds_read_b64_tr_b16 a[68:69], v233 offset:57344\n"
"v_accvgpr_read_b32 v131, a147\n"
"v_mfma_f32_16x16x32_bf16 v[16:19], a[4:7], a[96:99], v[16:19]\n"
"buffer_store_dwordx4 v[192:195], v153, s[96:99], s84 offen offset:0 sc1\n"
"v_mfma_f32_16x16x32_bf16 v[20:23], a[4:7], a[100:103], v[20:23]\n"
"ds_read_b64_tr_b16 a[70:71], v233 offset:58368\n"
"v_accvgpr_read_b32 v132, a176\n"
"v_mfma_f32_16x16x32_bf16 v[24:27], a[4:7], a[104:107], v[24:27]\n"
"ds_read_b64_tr_b16 a[72:73], v234 offset:57344\n"
"v_accvgpr_read_b32 v133, a177\n"
"v_mfma_f32_16x16x32_bf16 v[28:31], a[4:7], a[108:111], v[28:31]\n"
"ds_read_b64_tr_b16 a[74:75], v234 offset:58368\n"
"v_accvgpr_read_b32 v134, a178\n"
"v_mfma_f32_16x16x32_bf16 v[32:35], a[8:11], a[96:99], v[32:35]\n"
"buffer_load_dwordx4 a[52:55], v245, s[96:99], s58 offen offset:1024 sc1\n"
"v_accvgpr_read_b32 v135, a179\n"
"v_mfma_f32_16x16x32_bf16 v[36:39], a[8:11], a[100:103], v[36:39]\n"
"v_cvt_pk_bf16_f32 v136, v128, v129\n"
"v_mfma_f32_16x16x32_bf16 v[40:43], a[8:11], a[104:107], v[40:43]\n"
"ds_read_b64_tr_b16 a[76:77], v235 offset:57344\n"
"v_cvt_pk_bf16_f32 v137, v130, v131\n"
"v_mfma_f32_16x16x32_bf16 v[44:47], a[8:11], a[108:111], v[44:47]\n"
"ds_read_b64_tr_b16 a[78:79], v235 offset:58368\n"
"v_cvt_pk_bf16_f32 v138, v132, v133\n"
"v_mfma_f32_16x16x32_bf16 v[48:51], a[12:15], a[96:99], v[48:51]\n"
"buffer_load_dwordx4 v240, s[76:79], s63 offen offset:0 lds\n"
"v_cvt_pk_bf16_f32 v139, v134, v135\n"
"v_mfma_f32_16x16x32_bf16 v[52:55], a[12:15], a[100:103], v[52:55]\n"
"ds_read_b64_tr_b16 a[80:81], v236 offset:57344\n"
"v_mfma_f32_16x16x32_bf16 v[56:59], a[12:15], a[104:107], v[56:59]\n"
"ds_read_b64_tr_b16 a[82:83], v236 offset:58368\n"
"buffer_store_dwordx4 v[136:139], v152, s[96:99], s83 offen offset:0 sc1\n"
"v_mfma_f32_16x16x32_bf16 v[60:63], a[12:15], a[108:111], v[60:63]\n"
"ds_read_b64_tr_b16 a[84:85], v237 offset:57344\n"
"v_accvgpr_read_b32 v140, a208\n"
"v_mfma_f32_16x16x32_bf16 v[64:67], a[16:19], a[96:99], v[64:67]\n"
"buffer_load_dwordx4 a[56:59], v246, s[96:99], s58 offen offset:1024 sc1\n"
"v_accvgpr_read_b32 v141, a209\n"
"v_mfma_f32_16x16x32_bf16 v[68:71], a[16:19], a[100:103], v[68:71]\n"
"v_accvgpr_read_b32 v142, a210\n"
"v_mfma_f32_16x16x32_bf16 v[72:75], a[16:19], a[104:107], v[72:75]\n"
"ds_read_b64_tr_b16 a[86:87], v237 offset:58368\n"
"v_accvgpr_read_b32 v143, a211\n"
"v_mfma_f32_16x16x32_bf16 v[76:79], a[16:19], a[108:111], v[76:79]\n"
"buffer_store_dwordx4 v[196:199], v153, s[96:99], s84 offen offset:256 sc1\n"
"v_mfma_f32_16x16x32_bf16 v[80:83], a[20:23], a[96:99], v[80:83]\n"
"ds_read_b64_tr_b16 a[88:89], v238 offset:57344\n"
"v_accvgpr_read_b32 v144, a240\n"
"v_mfma_f32_16x16x32_bf16 v[84:87], a[20:23], a[100:103], v[84:87]\n"
"ds_read_b64_tr_b16 a[90:91], v238 offset:58368\n"
"v_accvgpr_read_b32 v145, a241\n"
"v_mfma_f32_16x16x32_bf16 v[88:91], a[20:23], a[104:107], v[88:91]\n"
"ds_read_b64_tr_b16 a[92:93], v239 offset:57344\n"
"v_mfma_f32_16x16x32_bf16 v[92:95], a[20:23], a[108:111], v[92:95]\n"
"buffer_load_dwordx4 a[60:63], v247, s[96:99], s58 offen offset:1024 sc1\n"
"v_accvgpr_read_b32 v146, a242\n"
"v_mfma_f32_16x16x32_bf16 v[96:99], a[24:27], a[96:99], v[96:99]\n"
"ds_read_b64_tr_b16 a[94:95], v239 offset:58368\n"
"v_accvgpr_read_b32 v147, a243\n"
"v_mfma_f32_16x16x32_bf16 v[100:103], a[24:27], a[100:103], v[100:103]\n"
"v_cvt_pk_bf16_f32 v148, v140, v141\n"
"v_mfma_f32_16x16x32_bf16 v[104:107], a[24:27], a[104:107], v[104:107]\n"
"v_cvt_pk_bf16_f32 v149, v142, v143\n"
"v_mfma_f32_16x16x32_bf16 v[108:111], a[24:27], a[108:111], v[108:111]\n"
"buffer_load_dwordx4 v241, s[76:79], s63 offen offset:1024 lds\n"
"v_cvt_pk_bf16_f32 v150, v144, v145\n"
"v_mfma_f32_16x16x32_bf16 v[112:115], a[28:31], a[96:99], v[112:115]\n"
"v_cvt_pk_bf16_f32 v151, v146, v147\n"
"s_waitcnt lgkmcnt(0)\n"
"s_waitcnt vmcnt(22)\n"
"s_barrier\n"
"v_mfma_f32_16x16x32_bf16 v[116:119], a[28:31], a[100:103], v[116:119]\n"
"buffer_store_dwordx4 v[148:151], v152, s[96:99], s83 offen offset:2048 sc1\n"
"v_mfma_f32_16x16x32_bf16 v[120:123], a[28:31], a[104:107], v[120:123]\n"
"v_mfma_f32_16x16x32_bf16 v[124:127], a[28:31], a[108:111], v[124:127]\n"
"v_mfma_f32_16x16x32_bf16 v[0:3], a[64:67], a[112:115], v[0:3]\n"
"ds_read_b64_tr_b16 a[0:1], v232 offset:0\n"
"v_accvgpr_read_b32 v128, a148\n"
"v_mfma_f32_16x16x32_bf16 v[4:7], a[64:67], a[116:119], v[4:7]\n"
"buffer_load_dwordx4 a[96:99], v244, s[96:99], s58 offen offset:2048 sc1\n"
"s_waitcnt vmcnt(22)\n"
"v_mfma_f32_16x16x32_bf16 v[8:11], a[64:67], a[120:123], v[8:11]\n"
"ds_read_b64_tr_b16 a[2:3], v232 offset:1024\n"
"v_accvgpr_read_b32 v129, a149\n"
"s_waitcnt vmcnt(21)\n"
"v_mfma_f32_16x16x32_bf16 v[12:15], a[64:67], a[124:127], v[12:15]\n"
"ds_read_b64_tr_b16 a[4:5], v233 offset:0\n"
"v_mfma_f32_16x16x32_bf16 v[16:19], a[68:71], a[112:115], v[16:19]\n"
"ds_read_b64_tr_b16 a[6:7], v233 offset:1024\n"
"v_mfma_f32_16x16x32_bf16 v[20:23], a[68:71], a[116:119], v[20:23]\n"
"buffer_store_dwordx4 v[200:203], v153, s[96:99], s84 offen offset:1024 sc1\n"
"v_accvgpr_read_b32 v130, a150\n"
"v_mfma_f32_16x16x32_bf16 v[24:27], a[68:71], a[120:123], v[24:27]\n"
"v_mfma_f32_16x16x32_bf16 v[28:31], a[68:71], a[124:127], v[28:31]\n"
"ds_read_b64_tr_b16 a[8:9], v234 offset:0\n"
"v_accvgpr_read_b32 v131, a151\n"
"v_mfma_f32_16x16x32_bf16 v[32:35], a[72:75], a[112:115], v[32:35]\n"
"ds_read_b64_tr_b16 a[10:11], v234 offset:1024\n"
"v_mfma_f32_16x16x32_bf16 v[36:39], a[72:75], a[116:119], v[36:39]\n"
"buffer_load_dwordx4 a[100:103], v245, s[96:99], s58 offen offset:2048 sc1\n"
"v_mfma_f32_16x16x32_bf16 v[40:43], a[72:75], a[120:123], v[40:43]\n"
"ds_read_b64_tr_b16 a[12:13], v235 offset:0\n"
"v_accvgpr_read_b32 v132, a180\n"
"v_mfma_f32_16x16x32_bf16 v[44:47], a[72:75], a[124:127], v[44:47]\n"
"ds_read_b64_tr_b16 a[14:15], v235 offset:1024\n"
"v_mfma_f32_16x16x32_bf16 v[48:51], a[76:79], a[112:115], v[48:51]\n"
"v_accvgpr_read_b32 v133, a181\n"
"v_mfma_f32_16x16x32_bf16 v[52:55], a[76:79], a[116:119], v[52:55]\n"
"buffer_load_dwordx4 v242, s[76:79], s63 offen offset:2048 lds\n"
"v_mfma_f32_16x16x32_bf16 v[56:59], a[76:79], a[120:123], v[56:59]\n"
"ds_read_b64_tr_b16 a[16:17], v236 offset:0\n"
"v_mfma_f32_16x16x32_bf16 v[60:63], a[76:79], a[124:127], v[60:63]\n"
"ds_read_b64_tr_b16 a[18:19], v236 offset:1024\n"
"v_accvgpr_read_b32 v134, a182\n"
"v_mfma_f32_16x16x32_bf16 v[64:67], a[80:83], a[112:115], v[64:67]\n"
"ds_read_b64_tr_b16 a[20:21], v237 offset:0\n"
"v_mfma_f32_16x16x32_bf16 v[68:71], a[80:83], a[116:119], v[68:71]\n"
"ds_read_b64_tr_b16 a[22:23], v237 offset:1024\n"
"v_mfma_f32_16x16x32_bf16 v[72:75], a[80:83], a[120:123], v[72:75]\n"
"buffer_load_dwordx4 a[104:107], v246, s[96:99], s58 offen offset:2048 sc1\n"
"v_accvgpr_read_b32 v135, a183\n"
"v_mfma_f32_16x16x32_bf16 v[76:79], a[80:83], a[124:127], v[76:79]\n"
"v_mfma_f32_16x16x32_bf16 v[80:83], a[84:87], a[112:115], v[80:83]\n"
"ds_read_b64_tr_b16 a[24:25], v238 offset:0\n"
"v_cvt_pk_bf16_f32 v136, v128, v129\n"
"v_mfma_f32_16x16x32_bf16 v[84:87], a[84:87], a[116:119], v[84:87]\n"
"ds_read_b64_tr_b16 a[26:27], v238 offset:1024\n"
"v_mfma_f32_16x16x32_bf16 v[88:91], a[84:87], a[120:123], v[88:91]\n"
"buffer_load_dwordx4 a[108:111], v247, s[96:99], s58 offen offset:2048 sc1\n"
"v_mfma_f32_16x16x32_bf16 v[92:95], a[84:87], a[124:127], v[92:95]\n"
"ds_read_b64_tr_b16 a[28:29], v239 offset:0\n"
"v_cvt_pk_bf16_f32 v137, v130, v131\n"
"v_mfma_f32_16x16x32_bf16 v[96:99], a[88:91], a[112:115], v[96:99]\n"
"ds_read_b64_tr_b16 a[30:31], v239 offset:1024\n"
"v_mfma_f32_16x16x32_bf16 v[100:103], a[88:91], a[116:119], v[100:103]\n"
"v_cvt_pk_bf16_f32 v138, v132, v133\n"
"v_mfma_f32_16x16x32_bf16 v[104:107], a[88:91], a[120:123], v[104:107]\n"
"buffer_load_dwordx4 v243, s[76:79], s63 offen offset:3072 lds\n"
"v_mfma_f32_16x16x32_bf16 v[108:111], a[88:91], a[124:127], v[108:111]\n"
"v_cvt_pk_bf16_f32 v139, v134, v135\n"
"v_mfma_f32_16x16x32_bf16 v[112:115], a[92:95], a[112:115], v[112:115]\n"
"v_mfma_f32_16x16x32_bf16 v[116:119], a[92:95], a[116:119], v[116:119]\n"
"buffer_store_dwordx4 v[136:139], v152, s[96:99], s83 offen offset:128 sc1\n"
"v_mfma_f32_16x16x32_bf16 v[120:123], a[92:95], a[120:123], v[120:123]\n"
"v_mfma_f32_16x16x32_bf16 v[124:127], a[92:95], a[124:127], v[124:127]\n"
"s_add_u32 s63, s63, 524288\n"
"s_waitcnt lgkmcnt(0)\n"
"s_add_u32 m0, s57, 81920\n"
"s_waitcnt vmcnt(22)\n"
"v_mfma_f32_16x16x32_bf16 v[0:3], a[0:3], a[32:35], v[0:3]\n"
"ds_read_b64_tr_b16 a[64:65], v232 offset:8192\n"
"v_accvgpr_read_b32 v140, a212\n"
"v_mfma_f32_16x16x32_bf16 v[4:7], a[0:3], a[36:39], v[4:7]\n"
"buffer_load_dwordx4 a[112:115], v244, s[96:99], s58 offen offset:3072 sc1\n"
"s_waitcnt vmcnt(22)\n"
"v_mfma_f32_16x16x32_bf16 v[8:11], a[0:3], a[40:43], v[8:11]\n"
"ds_read_b64_tr_b16 a[66:67], v232 offset:9216\n"
"v_accvgpr_read_b32 v141, a213\n"
"s_waitcnt vmcnt(21)\n"
"v_mfma_f32_16x16x32_bf16 v[12:15], a[0:3], a[44:47], v[12:15]\n"
"ds_read_b64_tr_b16 a[68:69], v233 offset:8192\n"
"v_mfma_f32_16x16x32_bf16 v[16:19], a[4:7], a[32:35], v[16:19]\n"
"ds_read_b64_tr_b16 a[70:71], v233 offset:9216\n"
"v_mfma_f32_16x16x32_bf16 v[20:23], a[4:7], a[36:39], v[20:23]\n"
"buffer_store_dwordx4 v[204:207], v153, s[96:99], s84 offen offset:1280 sc1\n"
"s_add_u32 s84, s84, 8192\n"
"v_mfma_f32_16x16x32_bf16 v[24:27], a[4:7], a[40:43], v[24:27]\n"
"v_accvgpr_read_b32 v142, a214\n"
"v_mfma_f32_16x16x32_bf16 v[28:31], a[4:7], a[44:47], v[28:31]\n"
"ds_read_b64_tr_b16 a[72:73], v234 offset:8192\n"
"v_mfma_f32_16x16x32_bf16 v[32:35], a[8:11], a[32:35], v[32:35]\n"
"ds_read_b64_tr_b16 a[74:75], v234 offset:9216\n"
"v_accvgpr_read_b32 v143, a215\n"
"v_mfma_f32_16x16x32_bf16 v[36:39], a[8:11], a[36:39], v[36:39]\n"
"buffer_load_dwordx4 a[116:119], v245, s[96:99], s58 offen offset:3072 sc1\n"
"v_mfma_f32_16x16x32_bf16 v[40:43], a[8:11], a[40:43], v[40:43]\n"
"ds_read_b64_tr_b16 a[76:77], v235 offset:8192\n"
"v_mfma_f32_16x16x32_bf16 v[44:47], a[8:11], a[44:47], v[44:47]\n"
"ds_read_b64_tr_b16 a[78:79], v235 offset:9216\n"
"v_accvgpr_read_b32 v144, a244\n"
"PG6_%=:\n"
"s_cbranch_execz RPG6_%=\n"
"v_mfma_f32_16x16x32_bf16 v[48:51], a[12:15], a[32:35], v[48:51]\n"
"v_mfma_f32_16x16x32_bf16 v[52:55], a[12:15], a[36:39], v[52:55]\n"
"buffer_load_dwordx4 v240, s[76:79], s63 offen offset:0 lds\n"
"v_accvgpr_read_b32 v145, a245\n"
"v_mfma_f32_16x16x32_bf16 v[56:59], a[12:15], a[40:43], v[56:59]\n"
"ds_read_b64_tr_b16 a[80:81], v236 offset:8192\n"
"v_mfma_f32_16x16x32_bf16 v[60:63], a[12:15], a[44:47], v[60:63]\n"
"ds_read_b64_tr_b16 a[82:83], v236 offset:9216\n"
"v_mfma_f32_16x16x32_bf16 v[64:67], a[16:19], a[32:35], v[64:67]\n"
"ds_read_b64_tr_b16 a[84:85], v237 offset:8192\n"
"v_accvgpr_read_b32 v146, a246\n"
"v_mfma_f32_16x16x32_bf16 v[68:71], a[16:19], a[36:39], v[68:71]\n"
"ds_read_b64_tr_b16 a[86:87], v237 offset:9216\n"
"v_mfma_f32_16x16x32_bf16 v[72:75], a[16:19], a[40:43], v[72:75]\n"
"buffer_load_dwordx4 a[120:123], v246, s[96:99], s58 offen offset:3072 sc1\n"
"v_accvgpr_read_b32 v147, a247\n"
"v_mfma_f32_16x16x32_bf16 v[76:79], a[16:19], a[44:47], v[76:79]\n"
"v_mfma_f32_16x16x32_bf16 v[80:83], a[20:23], a[32:35], v[80:83]\n"
"ds_read_b64_tr_b16 a[88:89], v238 offset:8192\n"
"v_cvt_pk_bf16_f32 v148, v140, v141\n"
"v_mfma_f32_16x16x32_bf16 v[84:87], a[20:23], a[36:39], v[84:87]\n"
"ds_read_b64_tr_b16 a[90:91], v238 offset:9216\n"
"v_mfma_f32_16x16x32_bf16 v[88:91], a[20:23], a[40:43], v[88:91]\n"
"buffer_load_dwordx4 a[124:127], v247, s[96:99], s58 offen offset:3072 sc1\n"
"v_mfma_f32_16x16x32_bf16 v[92:95], a[20:23], a[44:47], v[92:95]\n"
"ds_read_b64_tr_b16 a[92:93], v239 offset:8192\n"
"v_cvt_pk_bf16_f32 v149, v142, v143\n"
"v_mfma_f32_16x16x32_bf16 v[96:99], a[24:27], a[32:35], v[96:99]\n"
"ds_read_b64_tr_b16 a[94:95], v239 offset:9216\n"
"v_mfma_f32_16x16x32_bf16 v[100:103], a[24:27], a[36:39], v[100:103]\n"
"v_cvt_pk_bf16_f32 v150, v144, v145\n"
"v_mfma_f32_16x16x32_bf16 v[104:107], a[24:27], a[40:43], v[104:107]\n"
"buffer_load_dwordx4 v241, s[76:79], s63 offen offset:1024 lds\n"
"v_mfma_f32_16x16x32_bf16 v[108:111], a[24:27], a[44:47], v[108:111]\n"
"v_cvt_pk_bf16_f32 v151, v146, v147\n"
"v_mfma_f32_16x16x32_bf16 v[112:115], a[28:31], a[32:35], v[112:115]\n"
"s_waitcnt lgkmcnt(0)\n"
"s_waitcnt vmcnt(22)\n"
"s_barrier\n"
"v_mfma_f32_16x16x32_bf16 v[116:119], a[28:31], a[36:39], v[116:119]\n"
"buffer_store_dwordx4 v[148:151], v152, s[96:99], s83 offen offset:2176 sc1\n"
"s_add_u32 s83, s83, 8192\n"
"v_mfma_f32_16x16x32_bf16 v[120:123], a[28:31], a[40:43], v[120:123]\n"
"v_mfma_f32_16x16x32_bf16 v[124:127], a[28:31], a[44:47], v[124:127]\n"
"s_add_u32 s58, s58, 4096\n"
"v_mfma_f32_16x16x32_bf16 v[0:3], a[64:67], a[48:51], v[0:3]\n"
"ds_read_b64_tr_b16 a[0:1], v232 offset:16384\n"
"v_accvgpr_read_b32 v128, a152\n"
"v_mfma_f32_16x16x32_bf16 v[4:7], a[64:67], a[52:55], v[4:7]\n"
"buffer_load_dwordx4 a[32:35], v244, s[96:99], s58 offen offset:0 sc1\n"
"v_accvgpr_read_b32 v129, a153\n"
"s_waitcnt vmcnt(21)\n"
"v_mfma_f32_16x16x32_bf16 v[8:11], a[64:67], a[56:59], v[8:11]\n"
"ds_read_b64_tr_b16 a[2:3], v232 offset:17408\n"
"v_accvgpr_read_b32 v130, a154\n"
"s_waitcnt vmcnt(19)\n"
"v_mfma_f32_16x16x32_bf16 v[12:15], a[64:67], a[60:63], v[12:15]\n"
"ds_read_b64_tr_b16 a[4:5], v233 offset:16384\n"
"v_accvgpr_read_b32 v131, a155\n"
"v_mfma_f32_16x16x32_bf16 v[16:19], a[68:71], a[48:51], v[16:19]\n"
"buffer_store_dwordx4 v[208:211], v153, s[96:99], s84 offen offset:0 sc1\n"
"v_mfma_f32_16x16x32_bf16 v[20:23], a[68:71], a[52:55], v[20:23]\n"
"ds_read_b64_tr_b16 a[6:7], v233 offset:17408\n"
"v_accvgpr_read_b32 v132, a184\n"
"v_mfma_f32_16x16x32_bf16 v[24:27], a[68:71], a[56:59], v[24:27]\n"
"ds_read_b64_tr_b16 a[8:9], v234 offset:16384\n"
"v_accvgpr_read_b32 v133, a185\n"
"v_mfma_f32_16x16x32_bf16 v[28:31], a[68:71], a[60:63], v[28:31]\n"
"ds_read_b64_tr_b16 a[10:11], v234 offset:17408\n"
"v_accvgpr_read_b32 v134, a186\n"
"v_mfma_f32_16x16x32_bf16 v[32:35], a[72:75], a[48:51], v[32:35]\n"
"buffer_load_dwordx4 a[36:39], v245, s[96:99], s58 offen offset:0 sc1\n"
"v_accvgpr_read_b32 v135, a187\n"
"v_mfma_f32_16x16x32_bf16 v[36:39], a[72:75], a[52:55], v[36:39]\n"
"v_cvt_pk_bf16_f32 v136, v128, v129\n"
"v_mfma_f32_16x16x32_bf16 v[40:43], a[72:75], a[56:59], v[40:43]\n"
"ds_read_b64_tr_b16 a[12:13], v235 offset:16384\n"
"v_cvt_pk_bf16_f32 v137, v130, v131\n"
"v_mfma_f32_16x16x32_bf16 v[44:47], a[72:75], a[60:63], v[44:47]\n"
"ds_read_b64_tr_b16 a[14:15], v235 offset:17408\n"
"v_cvt_pk_bf16_f32 v138, v132, v133\n"
"v_mfma_f32_16x16x32_bf16 v[48:51], a[76:79], a[48:51], v[48:51]\n"
"buffer_load_dwordx4 v242, s[76:79], s63 offen offset:2048 lds\n"
"v_cvt_pk_bf16_f32 v139, v134, v135\n"
"v_mfma_f32_16x16x32_bf16 v[52:55], a[76:79], a[52:55], v[52:55]\n"
"ds_read_b64_tr_b16 a[16:17], v236 offset:16384\n"
"v_mfma_f32_16x16x32_bf16 v[56:59], a[76:79], a[56:59], v[56:59]\n"
"ds_read_b64_tr_b16 a[18:19], v236 offset:17408\n"
"buffer_store_dwordx4 v[136:139], v152, s[96:99], s83 offen offset:0 sc1\n"
"v_mfma_f32_16x16x32_bf16 v[60:63], a[76:79], a[60:63], v[60:63]\n"
"ds_read_b64_tr_b16 a[20:21], v237 offset:16384\n"
"v_accvgpr_read_b32 v140, a216\n"
"v_mfma_f32_16x16x32_bf16 v[64:67], a[80:83], a[48:51], v[64:67]\n"
"buffer_load_dwordx4 a[40:43], v246, s[96:99], s58 offen offset:0 sc1\n"
"v_accvgpr_read_b32 v141, a217\n"
"v_mfma_f32_16x16x32_bf16 v[68:71], a[80:83], a[52:55], v[68:71]\n"
"v_accvgpr_read_b32 v142, a218\n"
"v_mfma_f32_16x16x32_bf16 v[72:75], a[80:83], a[56:59], v[72:75]\n"
"ds_read_b64_tr_b16 a[22:23], v237 offset:17408\n"
"v_accvgpr_read_b32 v143, a219\n"
"v_mfma_f32_16x16x32_bf16 v[76:79], a[80:83], a[60:63], v[76:79]\n"
"buffer_store_dwordx4 v[212:215], v153, s[96:99], s84 offen offset:256 sc1\n"
"v_mfma_f32_16x16x32_bf16 v[80:83], a[84:87], a[48:51], v[80:83]\n"
"ds_read_b64_tr_b16 a[24:25], v238 offset:16384\n"
"v_accvgpr_read_b32 v144, a248\n"
"v_mfma_f32_16x16x32_bf16 v[84:87], a[84:87], a[52:55], v[84:87]\n"
"ds_read_b64_tr_b16 a[26:27], v238 offset:17408\n"
"v_accvgpr_read_b32 v145, a249\n"
"v_mfma_f32_16x16x32_bf16 v[88:91], a[84:87], a[56:59], v[88:91]\n"
"ds_read_b64_tr_b16 a[28:29], v239 offset:16384\n"
"v_mfma_f32_16x16x32_bf16 v[92:95], a[84:87], a[60:63], v[92:95]\n"
"buffer_load_dwordx4 a[44:47], v247, s[96:99], s58 offen offset:0 sc1\n"
"v_accvgpr_read_b32 v146, a250\n"
"v_mfma_f32_16x16x32_bf16 v[96:99], a[88:91], a[48:51], v[96:99]\n"
"ds_read_b64_tr_b16 a[30:31], v239 offset:17408\n"
"v_accvgpr_read_b32 v147, a251\n"
"v_mfma_f32_16x16x32_bf16 v[100:103], a[88:91], a[52:55], v[100:103]\n"
"v_cvt_pk_bf16_f32 v148, v140, v141\n"
"v_mfma_f32_16x16x32_bf16 v[104:107], a[88:91], a[56:59], v[104:107]\n"
"v_cvt_pk_bf16_f32 v149, v142, v143\n"
"v_mfma_f32_16x16x32_bf16 v[108:111], a[88:91], a[60:63], v[108:111]\n"
"buffer_load_dwordx4 v243, s[76:79], s63 offen offset:3072 lds\n"
"v_cvt_pk_bf16_f32 v150, v144, v145\n"
"v_mfma_f32_16x16x32_bf16 v[112:115], a[92:95], a[48:51], v[112:115]\n"
"v_cvt_pk_bf16_f32 v151, v146, v147\n"
"v_mfma_f32_16x16x32_bf16 v[116:119], a[92:95], a[52:55], v[116:119]\n"
"buffer_store_dwordx4 v[148:151], v152, s[96:99], s83 offen offset:2048 sc1\n"
"v_mfma_f32_16x16x32_bf16 v[120:123], a[92:95], a[56:59], v[120:123]\n"
"v_mfma_f32_16x16x32_bf16 v[124:127], a[92:95], a[60:63], v[124:127]\n"
"s_add_u32 s63, s63, 524288\n"
"s_waitcnt lgkmcnt(0)\n"
"s_add_u32 m0, s57, 32768\n"
"s_waitcnt vmcnt(22)\n"
"v_mfma_f32_16x16x32_bf16 v[0:3], a[0:3], a[96:99], v[0:3]\n"
"ds_read_b64_tr_b16 a[64:65], v232 offset:24576\n"
"v_accvgpr_read_b32 v128, a156\n"
"v_mfma_f32_16x16x32_bf16 v[4:7], a[0:3], a[100:103], v[4:7]\n"
"buffer_load_dwordx4 a[48:51], v244, s[96:99], s58 offen offset:1024 sc1\n"
"s_waitcnt vmcnt(22)\n"
"v_mfma_f32_16x16x32_bf16 v[8:11], a[0:3], a[104:107], v[8:11]\n"
"ds_read_b64_tr_b16 a[66:67], v232 offset:25600\n"
"v_accvgpr_read_b32 v129, a157\n"
"s_waitcnt vmcnt(21)\n"
"v_mfma_f32_16x16x32_bf16 v[12:15], a[0:3], a[108:111], v[12:15]\n"
"ds_read_b64_tr_b16 a[68:69], v233 offset:24576\n"
"v_mfma_f32_16x16x32_bf16 v[16:19], a[4:7], a[96:99], v[16:19]\n"
"ds_read_b64_tr_b16 a[70:71], v233 offset:25600\n"
"v_mfma_f32_16x16x32_bf16 v[20:23], a[4:7], a[100:103], v[20:23]\n"
"buffer_store_dwordx4 v[216:219], v153, s[96:99], s84 offen offset:1024 sc1\n"
"v_accvgpr_read_b32 v130, a158\n"
"v_mfma_f32_16x16x32_bf16 v[24:27], a[4:7], a[104:107], v[24:27]\n"
"v_mfma_f32_16x16x32_bf16 v[28:31], a[4:7], a[108:111], v[28:31]\n"
"ds_read_b64_tr_b16 a[72:73], v234 offset:24576\n"
"v_accvgpr_read_b32 v131, a159\n"
"v_mfma_f32_16x16x32_bf16 v[32:35], a[8:11], a[96:99], v[32:35]\n"
"ds_read_b64_tr_b16 a[74:75], v234 offset:25600\n"
"v_mfma_f32_16x16x32_bf16 v[36:39], a[8:11], a[100:103], v[36:39]\n"
"buffer_load_dwordx4 a[52:55], v245, s[96:99], s58 offen offset:1024 sc1\n"
"v_mfma_f32_16x16x32_bf16 v[40:43], a[8:11], a[104:107], v[40:43]\n"
"ds_read_b64_tr_b16 a[76:77], v235 offset:24576\n"
"v_accvgpr_read_b32 v132, a188\n"
"v_mfma_f32_16x16x32_bf16 v[44:47], a[8:11], a[108:111], v[44:47]\n"
"ds_read_b64_tr_b16 a[78:79], v235 offset:25600\n"
"v_mfma_f32_16x16x32_bf16 v[48:51], a[12:15], a[96:99], v[48:51]\n"
"v_accvgpr_read_b32 v133, a189\n"
"v_mfma_f32_16x16x32_bf16 v[52:55], a[12:15], a[100:103], v[52:55]\n"
"buffer_load_dwordx4 v240, s[76:79], s63 offen offset:0 lds\n"
"v_mfma_f32_16x16x32_bf16 v[56:59], a[12:15], a[104:107], v[56:59]\n"
"ds_read_b64_tr_b16 a[80:81], v236 offset:24576\n"
"v_mfma_f32_16x16x32_bf16 v[60:63], a[12:15], a[108:111], v[60:63]\n"
"ds_read_b64_tr_b16 a[82:83], v236 offset:25600\n"
"v_accvgpr_read_b32 v134, a190\n"
"v_mfma_f32_16x16x32_bf16 v[64:67], a[16:19], a[96:99], v[64:67]\n"
"ds_read_b64_tr_b16 a[84:85], v237 offset:24576\n"
"v_mfma_f32_16x16x32_bf16 v[68:71], a[16:19], a[100:103], v[68:71]\n"
"ds_read_b64_tr_b16 a[86:87], v237 offset:25600\n"
"v_mfma_f32_16x16x32_bf16 v[72:75], a[16:19], a[104:107], v[72:75]\n"
"buffer_load_dwordx4 a[56:59], v246, s[96:99], s58 offen offset:1024 sc1\n"
"v_accvgpr_read_b32 v135, a191\n"
"v_mfma_f32_16x16x32_bf16 v[76:79], a[16:19], a[108:111], v[76:79]\n"
"v_mfma_f32_16x16x32_bf16 v[80:83], a[20:23], a[96:99], v[80:83]\n"
"ds_read_b64_tr_b16 a[88:89], v238 offset:24576\n"
"v_cvt_pk_bf16_f32 v136, v128, v129\n"
"v_mfma_f32_16x16x32_bf16 v[84:87], a[20:23], a[100:103], v[84:87]\n"
"ds_read_b64_tr_b16 a[90:91], v238 offset:25600\n"
"v_mfma_f32_16x16x32_bf16 v[88:91], a[20:23], a[104:107], v[88:91]\n"
"buffer_load_dwordx4 a[60:63], v247, s[96:99], s58 offen offset:1024 sc1\n"
"v_mfma_f32_16x16x32_bf16 v[92:95], a[20:23], a[108:111], v[92:95]\n"
"ds_read_b64_tr_b16 a[92:93], v239 offset:24576\n"
"v_cvt_pk_bf16_f32 v137, v130, v131\n"
"v_mfma_f32_16x16x32_bf16 v[96:99], a[24:27], a[96:99], v[96:99]\n"
"ds_read_b64_tr_b16 a[94:95], v239 offset:25600\n"
"v_mfma_f32_16x16x32_bf16 v[100:103], a[24:27], a[100:103], v[100:103]\n"
"v_cvt_pk_bf16_f32 v138, v132, v133\n"
"v_mfma_f32_16x16x32_bf16 v[104:107], a[24:27], a[104:107], v[104:107]\n"
"buffer_load_dwordx4 v241, s[76:79], s63 offen offset:1024 lds\n"
"v_mfma_f32_16x16x32_bf16 v[108:111], a[24:27], a[108:111], v[108:111]\n"
"v_cvt_pk_bf16_f32 v139, v134, v135\n"
"v_mfma_f32_16x16x32_bf16 v[112:115], a[28:31], a[96:99], v[112:115]\n"
"s_waitcnt lgkmcnt(0)\n"
"s_waitcnt vmcnt(22)\n"
"s_barrier\n"
"v_mfma_f32_16x16x32_bf16 v[116:119], a[28:31], a[100:103], v[116:119]\n"
"buffer_store_dwordx4 v[136:139], v152, s[96:99], s83 offen offset:128 sc1\n"
"v_mfma_f32_16x16x32_bf16 v[120:123], a[28:31], a[104:107], v[120:123]\n"
"v_mfma_f32_16x16x32_bf16 v[124:127], a[28:31], a[108:111], v[124:127]\n"
"v_mfma_f32_16x16x32_bf16 v[0:3], a[64:67], a[112:115], v[0:3]\n"
"ds_read_b64_tr_b16 a[0:1], v232 offset:32768\n"
"v_accvgpr_read_b32 v140, a220\n"
"v_mfma_f32_16x16x32_bf16 v[4:7], a[64:67], a[116:119], v[4:7]\n"
"buffer_load_dwordx4 a[96:99], v244, s[96:99], s58 offen offset:2048 sc1\n"
"s_waitcnt vmcnt(22)\n"
"v_mfma_f32_16x16x32_bf16 v[8:11], a[64:67], a[120:123], v[8:11]\n"
"ds_read_b64_tr_b16 a[2:3], v232 offset:33792\n"
"v_accvgpr_read_b32 v141, a221\n"
"s_waitcnt vmcnt(21)\n"
"v_mfma_f32_16x16x32_bf16 v[12:15], a[64:67], a[124:127], v[12:15]\n"
"ds_read_b64_tr_b16 a[4:5], v233 offset:32768\n"
"v_mfma_f32_16x16x32_bf16 v[16:19], a[68:71], a[112:115], v[16:19]\n"
"ds_read_b64_tr_b16 a[6:7], v233 offset:33792\n"
"v_mfma_f32_16x16x32_bf16 v[20:23], a[68:71], a[116:119], v[20:23]\n"
"buffer_store_dwordx4 v[220:223], v153, s[96:99], s84 offen offset:1280 sc1\n"
"s_add_u32 s84, s84, 8192\n"
"v_mfma_f32_16x16x32_bf16 v[24:27], a[68:71], a[120:123], v[24:27]\n"
"v_accvgpr_read_b32 v142, a222\n"
"v_mfma_f32_16x16x32_bf16 v[28:31], a[68:71], a[124:127], v[28:31]\n"
"ds_read_b64_tr_b16 a[8:9], v234 offset:32768\n"
"v_mfma_f32_16x16x32_bf16 v[32:35], a[72:75], a[112:115], v[32:35]\n"
"ds_read_b64_tr_b16 a[10:11], v234 offset:33792\n"
"v_accvgpr_read_b32 v143, a223\n"
"v_mfma_f32_16x16x32_bf16 v[36:39], a[72:75], a[116:119], v[36:39]\n"
"buffer_load_dwordx4 a[100:103], v245, s[96:99], s58 offen offset:2048 sc1\n"
"v_mfma_f32_16x16x32_bf16 v[40:43], a[72:75], a[120:123], v[40:43]\n"
"ds_read_b64_tr_b16 a[12:13], v235 offset:32768\n"
"v_mfma_f32_16x16x32_bf16 v[44:47], a[72:75], a[124:127], v[44:47]\n"
"ds_read_b64_tr_b16 a[14:15], v235 offset:33792\n"
"v_accvgpr_read_b32 v144, a252\n"
"v_mfma_f32_16x16x32_bf16 v[48:51], a[76:79], a[112:115], v[48:51]\n"
"v_mfma_f32_16x16x32_bf16 v[52:55], a[76:79], a[116:119], v[52:55]\n"
"buffer_load_dwordx4 v242, s[76:79], s63 offen offset:2048 lds\n"
"v_accvgpr_read_b32 v145, a253\n"
"v_mfma_f32_16x16x32_bf16 v[56:59], a[76:79], a[120:123], v[56:59]\n"
"ds_read_b64_tr_b16 a[16:17], v236 offset:32768\n"
"v_mfma_f32_16x16x32_bf16 v[60:63], a[76:79], a[124:127], v[60:63]\n"
"ds_read_b64_tr_b16 a[18:19], v236 offset:33792\n"
"v_mfma_f32_16x16x32_bf16 v[64:67], a[80:83], a[112:115], v[64:67]\n"
"ds_read_b64_tr_b16 a[20:21], v237 offset:32768\n"
"v_accvgpr_read_b32 v146, a254\n"
"v_mfma_f32_16x16x32_bf16 v[68:71], a[80:83], a[116:119], v[68:71]\n"
"ds_read_b64_tr_b16 a[22:23], v237 offset:33792\n"
"v_mfma_f32_16x16x32_bf16 v[72:75], a[80:83], a[120:123], v[72:75]\n"
"buffer_load_dwordx4 a[104:107], v246, s[96:99], s58 offen offset:2048 sc1\n"
"v_accvgpr_read_b32 v147, a255\n"
"v_mfma_f32_16x16x32_bf16 v[76:79], a[80:83], a[124:127], v[76:79]\n"
"v_mfma_f32_16x16x32_bf16 v[80:83], a[84:87], a[112:115], v[80:83]\n"
"ds_read_b64_tr_b16 a[24:25], v238 offset:32768\n"
"v_cvt_pk_bf16_f32 v148, v140, v141\n"
"v_mfma_f32_16x16x32_bf16 v[84:87], a[84:87], a[116:119], v[84:87]\n"
"ds_read_b64_tr_b16 a[26:27], v238 offset:33792\n"
"v_mfma_f32_16x16x32_bf16 v[88:91], a[84:87], a[120:123], v[88:91]\n"
"buffer_load_dwordx4 a[108:111], v247, s[96:99], s58 offen offset:2048 sc1\n"
"v_mfma_f32_16x16x32_bf16 v[92:95], a[84:87], a[124:127], v[92:95]\n"
"ds_read_b64_tr_b16 a[28:29], v239 offset:32768\n"
"v_cvt_pk_bf16_f32 v149, v142, v143\n"
"v_mfma_f32_16x16x32_bf16 v[96:99], a[88:91], a[112:115], v[96:99]\n"
"ds_read_b64_tr_b16 a[30:31], v239 offset:33792\n"
"v_mfma_f32_16x16x32_bf16 v[100:103], a[88:91], a[116:119], v[100:103]\n"
"v_cvt_pk_bf16_f32 v150, v144, v145\n"
"v_mfma_f32_16x16x32_bf16 v[104:107], a[88:91], a[120:123], v[104:107]\n"
"buffer_load_dwordx4 v243, s[76:79], s63 offen offset:3072 lds\n"
"v_mfma_f32_16x16x32_bf16 v[108:111], a[88:91], a[124:127], v[108:111]\n"
"v_cvt_pk_bf16_f32 v151, v146, v147\n"
"v_mfma_f32_16x16x32_bf16 v[112:115], a[92:95], a[112:115], v[112:115]\n"
"v_mfma_f32_16x16x32_bf16 v[116:119], a[92:95], a[116:119], v[116:119]\n"
"buffer_store_dwordx4 v[148:151], v152, s[96:99], s83 offen offset:2176 sc1\n"
"s_add_u32 s83, s83, 8192\n"
"v_mfma_f32_16x16x32_bf16 v[120:123], a[92:95], a[120:123], v[120:123]\n"
"v_mfma_f32_16x16x32_bf16 v[124:127], a[92:95], a[124:127], v[124:127]\n"
"s_add_u32 s63, s63, 524288\n"
"s_waitcnt lgkmcnt(0)\n"
"s_add_u32 m0, s57, 49152\n"
"s_waitcnt vmcnt(21)\n"
"v_mfma_f32_16x16x32_bf16 v[0:3], a[0:3], a[32:35], v[0:3]\n"
"ds_read_b64_tr_b16 a[64:65], v232 offset:40960\n"
"v_mfma_f32_16x16x32_bf16 v[4:7], a[0:3], a[36:39], v[4:7]\n"
"buffer_load_dwordx4 a[112:115], v244, s[96:99], s58 offen offset:3072 sc1\n"
"s_waitcnt vmcnt(21)\n"
"v_mfma_f32_16x16x32_bf16 v[8:11], a[0:3], a[40:43], v[8:11]\n"
"ds_read_b64_tr_b16 a[66:67], v232 offset:41984\n"
"s_waitcnt vmcnt(19)\n"
"v_mfma_f32_16x16x32_bf16 v[12:15], a[0:3], a[44:47], v[12:15]\n"
"ds_read_b64_tr_b16 a[68:69], v233 offset:40960\n"
"v_mfma_f32_16x16x32_bf16 v[16:19], a[4:7], a[32:35], v[16:19]\n"
"ds_read_b64_tr_b16 a[70:71], v233 offset:41984\n"
"v_mfma_f32_16x16x32_bf16 v[20:23], a[4:7], a[36:39], v[20:23]\n"
"v_mfma_f32_16x16x32_bf16 v[24:27], a[4:7], a[40:43], v[24:27]\n"
"buffer_load_dwordx4 a[116:119], v245, s[96:99], s58 offen offset:3072 sc1\n"
"v_mfma_f32_16x16x32_bf16 v[28:31], a[4:7], a[44:47], v[28:31]\n"
"ds_read_b64_tr_b16 a[72:73], v234 offset:40960\n"
"v_mfma_f32_16x16x32_bf16 v[32:35], a[8:11], a[32:35], v[32:35]\n"
"ds_read_b64_tr_b16 a[74:75], v234 offset:41984\n"
"v_mfma_f32_16x16x32_bf16 v[36:39], a[8:11], a[36:39], v[36:39]\n"
"ds_read_b64_tr_b16 a[76:77], v235 offset:40960\n"
"v_mfma_f32_16x16x32_bf16 v[40:43], a[8:11], a[40:43], v[40:43]\n"
"v_mfma_f32_16x16x32_bf16 v[44:47], a[8:11], a[44:47], v[44:47]\n"
"buffer_load_dwordx4 v240, s[76:79], s63 offen offset:0 lds\n"
"v_mfma_f32_16x16x32_bf16 v[48:51], a[12:15], a[32:35], v[48:51]\n"
"ds_read_b64_tr_b16 a[78:79], v235 offset:41984\n"
"v_mfma_f32_16x16x32_bf16 v[52:55], a[12:15], a[36:39], v[52:55]\n"
"ds_read_b64_tr_b16 a[80:81], v236 offset:40960\n"
"v_mfma_f32_16x16x32_bf16 v[56:59], a[12:15], a[40:43], v[56:59]\n"
"ds_read_b64_tr_b16 a[82:83], v236 offset:41984\n"
"v_mfma_f32_16x16x32_bf16 v[60:63], a[12:15], a[44:47], v[60:63]\n"
"v_mfma_f32_16x16x32_bf16 v[64:67], a[16:19], a[32:35], v[64:67]\n"
"buffer_load_dwordx4 a[120:123], v246, s[96:99], s58 offen offset:3072 sc1\n"
"v_mfma_f32_16x16x32_bf16 v[68:71], a[16:19], a[36:39], v[68:71]\n"
"ds_read_b64_tr_b16 a[84:85], v237 offset:40960\n"
"v_mfma_f32_16x16x32_bf16 v[72:75], a[16:19], a[40:43], v[72:75]\n"
"ds_read_b64_tr_b16 a[86:87], v237 offset:41984\n"
"v_mfma_f32_16x16x32_bf16 v[76:79], a[16:19], a[44:47], v[76:79]\n"
"ds_read_b64_tr_b16 a[88:89], v238 offset:40960\n"
"v_mfma_f32_16x16x32_bf16 v[80:83], a[20:23], a[32:35], v[80:83]\n"
"v_mfma_f32_16x16x32_bf16 v[84:87], a[20:23], a[36:39], v[84:87]\n"
"buffer_load_dwordx4 a[124:127], v247, s[96:99], s58 offen offset:3072 sc1\n"
"v_mfma_f32_16x16x32_bf16 v[88:91], a[20:23], a[40:43], v[88:91]\n"
"ds_read_b64_tr_b16 a[90:91], v238 offset:41984\n"
"v_mfma_f32_16x16x32_bf16 v[92:95], a[20:23], a[44:47], v[92:95]\n"
"ds_read_b64_tr_b16 a[92:93], v239 offset:40960\n"
"v_mfma_f32_16x16x32_bf16 v[96:99], a[24:27], a[32:35], v[96:99]\n"
"ds_read_b64_tr_b16 a[94:95], v239 offset:41984\n"
"v_mfma_f32_16x16x32_bf16 v[100:103], a[24:27], a[36:39], v[100:103]\n"
"v_mfma_f32_16x16x32_bf16 v[104:107], a[24:27], a[40:43], v[104:107]\n"
"buffer_load_dwordx4 v241, s[76:79], s63 offen offset:1024 lds\n"
"v_mfma_f32_16x16x32_bf16 v[108:111], a[24:27], a[44:47], v[108:111]\n"
"v_mfma_f32_16x16x32_bf16 v[112:115], a[28:31], a[32:35], v[112:115]\n"
"s_waitcnt lgkmcnt(0)\n"
"s_waitcnt vmcnt(19)\n"
"s_barrier\n"
"v_mfma_f32_16x16x32_bf16 v[116:119], a[28:31], a[36:39], v[116:119]\n"
"v_mfma_f32_16x16x32_bf16 v[120:123], a[28:31], a[40:43], v[120:123]\n"
"v_mfma_f32_16x16x32_bf16 v[124:127], a[28:31], a[44:47], v[124:127]\n"
"s_add_u32 s58, s58, 4096\n"
"v_mfma_f32_16x16x32_bf16 v[0:3], a[64:67], a[48:51], v[0:3]\n"
"ds_read_b64_tr_b16 a[0:1], v232 offset:49152\n"
"v_mfma_f32_16x16x32_bf16 v[4:7], a[64:67], a[52:55], v[4:7]\n"
"buffer_load_dwordx4 a[32:35], v244, s[96:99], s58 offen offset:0 sc1\n"
"s_waitcnt vmcnt(18)\n"
"v_mfma_f32_16x16x32_bf16 v[8:11], a[64:67], a[56:59], v[8:11]\n"
"ds_read_b64_tr_b16 a[2:3], v232 offset:50176\n"
"s_waitcnt vmcnt(17)\n"
"v_mfma_f32_16x16x32_bf16 v[12:15], a[64:67], a[60:63], v[12:15]\n"
"ds_read_b64_tr_b16 a[4:5], v233 offset:49152\n"
"v_mfma_f32_16x16x32_bf16 v[16:19], a[68:71], a[48:51], v[16:19]\n"
"ds_read_b64_tr_b16 a[6:7], v233 offset:50176\n"
"v_mfma_f32_16x16x32_bf16 v[20:23], a[68:71], a[52:55], v[20:23]\n"
"v_mfma_f32_16x16x32_bf16 v[24:27], a[68:71], a[56:59], v[24:27]\n"
"buffer_load_dwordx4 a[36:39], v245, s[96:99], s58 offen offset:0 sc1\n"
"v_mfma_f32_16x16x32_bf16 v[28:31], a[68:71], a[60:63], v[28:31]\n"
"ds_read_b64_tr_b16 a[8:9], v234 offset:49152\n"
"v_mfma_f32_16x16x32_bf16 v[32:35], a[72:75], a[48:51], v[32:35]\n"
"ds_read_b64_tr_b16 a[10:11], v234 offset:50176\n"
"v_mfma_f32_16x16x32_bf16 v[36:39], a[72:75], a[52:55], v[36:39]\n"
"ds_read_b64_tr_b16 a[12:13], v235 offset:49152\n"
"v_mfma_f32_16x16x32_bf16 v[40:43], a[72:75], a[56:59], v[40:43]\n"
"v_mfma_f32_16x16x32_bf16 v[44:47], a[72:75], a[60:63], v[44:47]\n"
"buffer_load_dwordx4 v242, s[76:79], s63 offen offset:2048 lds\n"
"v_mfma_f32_16x16x32_bf16 v[48:51], a[76:79], a[48:51], v[48:51]\n"
"ds_read_b64_tr_b16 a[14:15], v235 offset:50176\n"
"v_mfma_f32_16x16x32_bf16 v[52:55], a[76:79], a[52:55], v[52:55]\n"
"ds_read_b64_tr_b16 a[16:17], v236 offset:49152\n"
"v_mfma_f32_16x16x32_bf16 v[56:59], a[76:79], a[56:59], v[56:59]\n"
"ds_read_b64_tr_b16 a[18:19], v236 offset:50176\n"
"v_mfma_f32_16x16x32_bf16 v[60:63], a[76:79], a[60:63], v[60:63]\n"
"v_mfma_f32_16x16x32_bf16 v[64:67], a[80:83], a[48:51], v[64:67]\n"
"buffer_load_dwordx4 a[40:43], v246, s[96:99], s58 offen offset:0 sc1\n"
"v_mfma_f32_16x16x32_bf16 v[68:71], a[80:83], a[52:55], v[68:71]\n"
"ds_read_b64_tr_b16 a[20:21], v237 offset:49152\n"
"v_mfma_f32_16x16x32_bf16 v[72:75], a[80:83], a[56:59], v[72:75]\n"
"ds_read_b64_tr_b16 a[22:23], v237 offset:50176\n"
"v_mfma_f32_16x16x32_bf16 v[76:79], a[80:83], a[60:63], v[76:79]\n"
"ds_read_b64_tr_b16 a[24:25], v238 offset:49152\n"
"v_mfma_f32_16x16x32_bf16 v[80:83], a[84:87], a[48:51], v[80:83]\n"
"v_mfma_f32_16x16x32_bf16 v[84:87], a[84:87], a[52:55], v[84:87]\n"
"buffer_load_dwordx4 a[44:47], v247, s[96:99], s58 offen offset:0 sc1\n"
"v_mfma_f32_16x16x32_bf16 v[88:91], a[84:87], a[56:59], v[88:91]\n"
"ds_read_b64_tr_b16 a[26:27], v238 offset:50176\n"
"v_mfma_f32_16x16x32_bf16 v[92:95], a[84:87], a[60:63], v[92:95]\n"
"ds_read_b64_tr_b16 a[28:29], v239 offset:49152\n"
"v_mfma_f32_16x16x32_bf16 v[96:99], a[88:91], a[48:51], v[96:99]\n"
"ds_read_b64_tr_b16 a[30:31], v239 offset:50176\n"
"v_mfma_f32_16x16x32_bf16 v[100:103], a[88:91], a[52:55], v[100:103]\n"
"v_mfma_f32_16x16x32_bf16 v[104:107], a[88:91], a[56:59], v[104:107]\n"
"buffer_load_dwordx4 v243, s[76:79], s63 offen offset:3072 lds\n"
"v_mfma_f32_16x16x32_bf16 v[108:111], a[88:91], a[60:63], v[108:111]\n"
"v_mfma_f32_16x16x32_bf16 v[112:115], a[92:95], a[48:51], v[112:115]\n"
"v_mfma_f32_16x16x32_bf16 v[116:119], a[92:95], a[52:55], v[116:119]\n"
"v_mfma_f32_16x16x32_bf16 v[120:123], a[92:95], a[56:59], v[120:123]\n"
"v_mfma_f32_16x16x32_bf16 v[124:127], a[92:95], a[60:63], v[124:127]\n"
"s_add_u32 s63, s63, 524288\n"
"s_waitcnt lgkmcnt(0)\n"
"s_waitcnt vmcnt(16)\n"
"v_mfma_f32_16x16x32_bf16 v[0:3], a[0:3], a[96:99], v[0:3]\n"
"ds_read_b64_tr_b16 a[64:65], v232 offset:57344\n"
"v_mfma_f32_16x16x32_bf16 v[4:7], a[0:3], a[100:103], v[4:7]\n"
"buffer_load_dwordx4 a[48:51], v244, s[96:99], s58 offen offset:1024 sc1\n"
"s_waitcnt vmcnt(16)\n"
"v_mfma_f32_16x16x32_bf16 v[8:11], a[0:3], a[104:107], v[8:11]\n"
"ds_read_b64_tr_b16 a[66:67], v232 offset:58368\n"
"s_waitcnt vmcnt(15)\n"
"v_mfma_f32_16x16x32_bf16 v[12:15], a[0:3], a[108:111], v[12:15]\n"
"ds_read_b64_tr_b16 a[68:69], v233 offset:57344\n"
"v_mfma_f32_16x16x32_bf16 v[16:19], a[4:7], a[96:99], v[16:19]\n"
"v_mfma_f32_16x16x32_bf16 v[20:23], a[4:7], a[100:103], v[20:23]\n"
"ds_read_b64_tr_b16 a[70:71], v233 offset:58368\n"
"v_mfma_f32_16x16x32_bf16 v[24:27], a[4:7], a[104:107], v[24:27]\n"
"ds_read_b64_tr_b16 a[72:73], v234 offset:57344\n"
"v_mfma_f32_16x16x32_bf16 v[28:31], a[4:7], a[108:111], v[28:31]\n"
"ds_read_b64_tr_b16 a[74:75], v234 offset:58368\n"
"v_mfma_f32_16x16x32_bf16 v[32:35], a[8:11], a[96:99], v[32:35]\n"
"buffer_load_dwordx4 a[52:55], v245, s[96:99], s58 offen offset:1024 sc1\n"
"v_mfma_f32_16x16x32_bf16 v[36:39], a[8:11], a[100:103], v[36:39]\n"
"v_mfma_f32_16x16x32_bf16 v[40:43], a[8:11], a[104:107], v[40:43]\n"
"ds_read_b64_tr_b16 a[76:77], v235 offset:57344\n"
"v_mfma_f32_16x16x32_bf16 v[44:47], a[8:11], a[108:111], v[44:47]\n"
"ds_read_b64_tr_b16 a[78:79], v235 offset:58368\n"
"v_mfma_f32_16x16x32_bf16 v[48:51], a[12:15], a[96:99], v[48:51]\n"
"v_mfma_f32_16x16x32_bf16 v[52:55], a[12:15], a[100:103], v[52:55]\n"
"ds_read_b64_tr_b16 a[80:81], v236 offset:57344\n"
"v_mfma_f32_16x16x32_bf16 v[56:59], a[12:15], a[104:107], v[56:59]\n"
"ds_read_b64_tr_b16 a[82:83], v236 offset:58368\n"
"v_mfma_f32_16x16x32_bf16 v[60:63], a[12:15], a[108:111], v[60:63]\n"
"ds_read_b64_tr_b16 a[84:85], v237 offset:57344\n"
"v_mfma_f32_16x16x32_bf16 v[64:67], a[16:19], a[96:99], v[64:67]\n"
"buffer_load_dwordx4 a[56:59], v246, s[96:99], s58 offen offset:1024 sc1\n"
"v_mfma_f32_16x16x32_bf16 v[68:71], a[16:19], a[100:103], v[68:71]\n"
"v_mfma_f32_16x16x32_bf16 v[72:75], a[16:19], a[104:107], v[72:75]\n"
"ds_read_b64_tr_b16 a[86:87], v237 offset:58368\n"
"v_mfma_f32_16x16x32_bf16 v[76:79], a[16:19], a[108:111], v[76:79]\n"
"ds_read_b64_tr_b16 a[88:89], v238 offset:57344\n"
"v_mfma_f32_16x16x32_bf16 v[80:83], a[20:23], a[96:99], v[80:83]\n"
"ds_read_b64_tr_b16 a[90:91], v238 offset:58368\n"
"v_mfma_f32_16x16x32_bf16 v[84:87], a[20:23], a[100:103], v[84:87]\n"
"v_mfma_f32_16x16x32_bf16 v[88:91], a[20:23], a[104:107], v[88:91]\n"
"ds_read_b64_tr_b16 a[92:93], v239 offset:57344\n"
"v_mfma_f32_16x16x32_bf16 v[92:95], a[20:23], a[108:111], v[92:95]\n"
"buffer_load_dwordx4 a[60:63], v247, s[96:99], s58 offen offset:1024 sc1\n"
"v_mfma_f32_16x16x32_bf16 v[96:99], a[24:27], a[96:99], v[96:99]\n"
"ds_read_b64_tr_b16 a[94:95], v239 offset:58368\n"
"v_mfma_f32_16x16x32_bf16 v[100:103], a[24:27], a[100:103], v[100:103]\n"
"v_mfma_f32_16x16x32_bf16 v[104:107], a[24:27], a[104:107], v[104:107]\n"
"v_mfma_f32_16x16x32_bf16 v[108:111], a[24:27], a[108:111], v[108:111]\n"
"v_mfma_f32_16x16x32_bf16 v[112:115], a[28:31], a[96:99], v[112:115]\n"
"s_waitcnt lgkmcnt(0)\n"
"s_waitcnt vmcnt(13)\n"
"s_barrier\n"
"s_cmp_lg_u32 %2, 0\n"
"s_cbranch_scc1 PUBKV_%=\n"
"v_min_u32 v155, 1, v252\n"
"v_lshlrev_b32 v155, 30, v155\n"
"v_mov_b32 v156, s14\n"
"v_mov_b32 v157, s15\n"
"s_lshl_b32 s85, s24, 3\n"
"s_add_u32 s85, s85, s25\n"
"s_lshl_b32 s85, s85, 3\n"
"s_add_u32 s85, s85, 105916416\n"
"buffer_store_dwordx2 v[156:157], v155, s[96:99], s85 offen sc1\n"
"PUBKV_%=:\n"
"v_mfma_f32_16x16x32_bf16 v[116:119], a[28:31], a[100:103], v[116:119]\n"
"v_mfma_f32_16x16x32_bf16 v[120:123], a[28:31], a[104:107], v[120:123]\n"
"v_mfma_f32_16x16x32_bf16 v[124:127], a[28:31], a[108:111], v[124:127]\n"
"v_mfma_f32_16x16x32_bf16 v[0:3], a[64:67], a[112:115], v[0:3]\n"
"ds_read_b64_tr_b16 a[0:1], v232 offset:0\n"
"v_mfma_f32_16x16x32_bf16 v[4:7], a[64:67], a[116:119], v[4:7]\n"
"buffer_load_dwordx4 a[96:99], v244, s[96:99], s58 offen offset:2048 sc1\n"
"s_waitcnt vmcnt(13)\n"
"v_mfma_f32_16x16x32_bf16 v[8:11], a[64:67], a[120:123], v[8:11]\n"
"ds_read_b64_tr_b16 a[2:3], v232 offset:1024\n"
"s_waitcnt vmcnt(12)\n"
"v_mfma_f32_16x16x32_bf16 v[12:15], a[64:67], a[124:127], v[12:15]\n"
"ds_read_b64_tr_b16 a[4:5], v233 offset:0\n"
"v_mfma_f32_16x16x32_bf16 v[16:19], a[68:71], a[112:115], v[16:19]\n"
"v_mfma_f32_16x16x32_bf16 v[20:23], a[68:71], a[116:119], v[20:23]\n"
"ds_read_b64_tr_b16 a[6:7], v233 offset:1024\n"
"v_mfma_f32_16x16x32_bf16 v[24:27], a[68:71], a[120:123], v[24:27]\n"
"ds_read_b64_tr_b16 a[8:9], v234 offset:0\n"
"v_mfma_f32_16x16x32_bf16 v[28:31], a[68:71], a[124:127], v[28:31]\n"
"ds_read_b64_tr_b16 a[10:11], v234 offset:1024\n"
"v_mfma_f32_16x16x32_bf16 v[32:35], a[72:75], a[112:115], v[32:35]\n"
"buffer_load_dwordx4 a[100:103], v245, s[96:99], s58 offen offset:2048 sc1\n"
"v_mfma_f32_16x16x32_bf16 v[36:39], a[72:75], a[116:119], v[36:39]\n"
"v_mfma_f32_16x16x32_bf16 v[40:43], a[72:75], a[120:123], v[40:43]\n"
"ds_read_b64_tr_b16 a[12:13], v235 offset:0\n"
"v_mfma_f32_16x16x32_bf16 v[44:47], a[72:75], a[124:127], v[44:47]\n"
"ds_read_b64_tr_b16 a[14:15], v235 offset:1024\n"
"v_mfma_f32_16x16x32_bf16 v[48:51], a[76:79], a[112:115], v[48:51]\n"
"v_mfma_f32_16x16x32_bf16 v[52:55], a[76:79], a[116:119], v[52:55]\n"
"ds_read_b64_tr_b16 a[16:17], v236 offset:0\n"
"v_mfma_f32_16x16x32_bf16 v[56:59], a[76:79], a[120:123], v[56:59]\n"
"ds_read_b64_tr_b16 a[18:19], v236 offset:1024\n"
"v_mfma_f32_16x16x32_bf16 v[60:63], a[76:79], a[124:127], v[60:63]\n"
"ds_read_b64_tr_b16 a[20:21], v237 offset:0\n"
"v_mfma_f32_16x16x32_bf16 v[64:67], a[80:83], a[112:115], v[64:67]\n"
"buffer_load_dwordx4 a[104:107], v246, s[96:99], s58 offen offset:2048 sc1\n"
"v_mfma_f32_16x16x32_bf16 v[68:71], a[80:83], a[116:119], v[68:71]\n"
"v_mfma_f32_16x16x32_bf16 v[72:75], a[80:83], a[120:123], v[72:75]\n"
"ds_read_b64_tr_b16 a[22:23], v237 offset:1024\n"
"v_mfma_f32_16x16x32_bf16 v[76:79], a[80:83], a[124:127], v[76:79]\n"
"ds_read_b64_tr_b16 a[24:25], v238 offset:0\n"
"v_mfma_f32_16x16x32_bf16 v[80:83], a[84:87], a[112:115], v[80:83]\n"
"ds_read_b64_tr_b16 a[26:27], v238 offset:1024\n"
"v_mfma_f32_16x16x32_bf16 v[84:87], a[84:87], a[116:119], v[84:87]\n"
"v_mfma_f32_16x16x32_bf16 v[88:91], a[84:87], a[120:123], v[88:91]\n"
"ds_read_b64_tr_b16 a[28:29], v239 offset:0\n"
"v_mfma_f32_16x16x32_bf16 v[92:95], a[84:87], a[124:127], v[92:95]\n"
"buffer_load_dwordx4 a[108:111], v247, s[96:99], s58 offen offset:2048 sc1\n"
"v_mfma_f32_16x16x32_bf16 v[96:99], a[88:91], a[112:115], v[96:99]\n"
"ds_read_b64_tr_b16 a[30:31], v239 offset:1024\n"
"v_mfma_f32_16x16x32_bf16 v[100:103], a[88:91], a[116:119], v[100:103]\n"
"v_mfma_f32_16x16x32_bf16 v[104:107], a[88:91], a[120:123], v[104:107]\n"
"v_mfma_f32_16x16x32_bf16 v[108:111], a[88:91], a[124:127], v[108:111]\n"
"v_mfma_f32_16x16x32_bf16 v[112:115], a[92:95], a[112:115], v[112:115]\n"
"v_mfma_f32_16x16x32_bf16 v[116:119], a[92:95], a[116:119], v[116:119]\n"
"v_mfma_f32_16x16x32_bf16 v[120:123], a[92:95], a[120:123], v[120:123]\n"
"v_mfma_f32_16x16x32_bf16 v[124:127], a[92:95], a[124:127], v[124:127]\n"
"s_waitcnt lgkmcnt(0)\n"
"v_min_u32 v231, 7, v252\n"
"s_waitcnt vmcnt(10)\n"
"v_mfma_f32_16x16x32_bf16 v[0:3], a[0:3], a[32:35], v[0:3]\n"
"ds_read_b64_tr_b16 a[64:65], v232 offset:8192\n"
"s_lshl_b32 s85, s24, 3\n"
"v_mfma_f32_16x16x32_bf16 v[4:7], a[0:3], a[36:39], v[4:7]\n"
"buffer_load_dwordx4 a[112:115], v244, s[96:99], s58 offen offset:3072 sc1\n"
"v_mfma_f32_16x16x32_bf16 v[8:11], a[0:3], a[40:43], v[8:11]\n"
"ds_read_b64_tr_b16 a[66:67], v232 offset:9216\n"
"v_add_lshl_u32 v231, s85, v231, 3\n"
"PG7_%=:\n"
"s_cbranch_execz RPG7_%=\n"
"s_waitcnt vmcnt(10)\n"
"v_mfma_f32_16x16x32_bf16 v[12:15], a[0:3], a[44:47], v[12:15]\n"
"ds_read_b64_tr_b16 a[68:69], v233 offset:8192\n"
"v_mfma_f32_16x16x32_bf16 v[16:19], a[4:7], a[32:35], v[16:19]\n"
"v_mfma_f32_16x16x32_bf16 v[20:23], a[4:7], a[36:39], v[20:23]\n"
"ds_read_b64_tr_b16 a[70:71], v233 offset:9216\n"
"v_mfma_f32_16x16x32_bf16 v[24:27], a[4:7], a[40:43], v[24:27]\n"
"ds_read_b64_tr_b16 a[72:73], v234 offset:8192\n"
"v_mfma_f32_16x16x32_bf16 v[28:31], a[4:7], a[44:47], v[28:31]\n"
"ds_read_b64_tr_b16 a[74:75], v234 offset:9216\n"
"v_mfma_f32_16x16x32_bf16 v[32:35], a[8:11], a[32:35], v[32:35]\n"
"buffer_load_dwordx4 a[116:119], v245, s[96:99], s58 offen offset:3072 sc1\n"
"v_mfma_f32_16x16x32_bf16 v[36:39], a[8:11], a[36:39], v[36:39]\n"
"v_mfma_f32_16x16x32_bf16 v[40:43], a[8:11], a[40:43], v[40:43]\n"
"ds_read_b64_tr_b16 a[76:77], v235 offset:8192\n"
"v_mfma_f32_16x16x32_bf16 v[44:47], a[8:11], a[44:47], v[44:47]\n"
"ds_read_b64_tr_b16 a[78:79], v235 offset:9216\n"
"v_mfma_f32_16x16x32_bf16 v[48:51], a[12:15], a[32:35], v[48:51]\n"
"v_mfma_f32_16x16x32_bf16 v[52:55], a[12:15], a[36:39], v[52:55]\n"
"ds_read_b64_tr_b16 a[80:81], v236 offset:8192\n"
"v_mfma_f32_16x16x32_bf16 v[56:59], a[12:15], a[40:43], v[56:59]\n"
"ds_read_b64_tr_b16 a[82:83], v236 offset:9216\n"
"v_mfma_f32_16x16x32_bf16 v[60:63], a[12:15], a[44:47], v[60:63]\n"
"ds_read_b64_tr_b16 a[84:85], v237 offset:8192\n"
"v_mfma_f32_16x16x32_bf16 v[64:67], a[16:19], a[32:35], v[64:67]\n"
"buffer_load_dwordx4 a[120:123], v246, s[96:99], s58 offen offset:3072 sc1\n"
"v_mfma_f32_16x16x32_bf16 v[68:71], a[16:19], a[36:39], v[68:71]\n"
"v_mfma_f32_16x16x32_bf16 v[72:75], a[16:19], a[40:43], v[72:75]\n"
"ds_read_b64_tr_b16 a[86:87], v237 offset:9216\n"
"v_mfma_f32_16x16x32_bf16 v[76:79], a[16:19], a[44:47], v[76:79]\n"
"ds_read_b64_tr_b16 a[88:89], v238 offset:8192\n"
"v_mfma_f32_16x16x32_bf16 v[80:83], a[20:23], a[32:35], v[80:83]\n"
"ds_read_b64_tr_b16 a[90:91], v238 offset:9216\n"
"v_mfma_f32_16x16x32_bf16 v[84:87], a[20:23], a[36:39], v[84:87]\n"
"v_mfma_f32_16x16x32_bf16 v[88:91], a[20:23], a[40:43], v[88:91]\n"
"ds_read_b64_tr_b16 a[92:93], v239 offset:8192\n"
"v_mfma_f32_16x16x32_bf16 v[92:95], a[20:23], a[44:47], v[92:95]\n"
"buffer_load_dwordx4 a[124:127], v247, s[96:99], s58 offen offset:3072 sc1\n"
"v_mfma_f32_16x16x32_bf16 v[96:99], a[24:27], a[32:35], v[96:99]\n"
"ds_read_b64_tr_b16 a[94:95], v239 offset:9216\n"
"v_mfma_f32_16x16x32_bf16 v[100:103], a[24:27], a[36:39], v[100:103]\n"
"v_mfma_f32_16x16x32_bf16 v[104:107], a[24:27], a[40:43], v[104:107]\n"
"v_mfma_f32_16x16x32_bf16 v[108:111], a[24:27], a[44:47], v[108:111]\n"
"v_mfma_f32_16x16x32_bf16 v[112:115], a[28:31], a[32:35], v[112:115]\n"
"s_waitcnt lgkmcnt(0)\n"
"s_waitcnt vmcnt(10)\n"
"s_barrier\n"
"v_mfma_f32_16x16x32_bf16 v[116:119], a[28:31], a[36:39], v[116:119]\n"
"v_mfma_f32_16x16x32_bf16 v[120:123], a[28:31], a[40:43], v[120:123]\n"
"v_mfma_f32_16x16x32_bf16 v[124:127], a[28:31], a[44:47], v[124:127]\n"
"s_add_u32 s58, s58, 4096\n"
"v_mfma_f32_16x16x32_bf16 v[0:3], a[64:67], a[48:51], v[0:3]\n"
"ds_read_b64_tr_b16 a[0:1], v232 offset:16384\n"
"v_mfma_f32_16x16x32_bf16 v[4:7], a[64:67], a[52:55], v[4:7]\n"
"ds_read_b64_tr_b16 a[2:3], v232 offset:17408\n"
"s_waitcnt vmcnt(9)\n"
"v_mfma_f32_16x16x32_bf16 v[8:11], a[64:67], a[56:59], v[8:11]\n"
"s_mov_b32 s86, 105916416\n"
"s_waitcnt vmcnt(8)\n"
"v_mfma_f32_16x16x32_bf16 v[12:15], a[64:67], a[60:63], v[12:15]\n"
"ds_read_b64_tr_b16 a[4:5], v233 offset:16384\n"
"v_mfma_f32_16x16x32_bf16 v[16:19], a[68:71], a[48:51], v[16:19]\n"
"ds_read_b64_tr_b16 a[6:7], v233 offset:17408\n"
"buffer_load_dwordx2 v[248:249], v231, s[96:99], s86 offen sc1\n"
"v_mfma_f32_16x16x32_bf16 v[20:23], a[68:71], a[52:55], v[20:23]\n"
"s_mov_b32 s42, 0x49800000\n"
"s_lshl_b32 s38, s8, 17\n"
"v_mfma_f32_16x16x32_bf16 v[24:27], a[68:71], a[56:59], v[24:27]\n"
"ds_read_b64_tr_b16 a[8:9], v234 offset:16384\n"
"v_readlane_b32 s45, v254, 18\n"
"v_mfma_f32_16x16x32_bf16 v[28:31], a[68:71], a[60:63], v[28:31]\n"
"v_lshrrev_b32 v128, 3, v252\n"
"s_lshl_b32 s39, s24, 12\n"
"v_mfma_f32_16x16x32_bf16 v[32:35], a[72:75], a[48:51], v[32:35]\n"
"ds_read_b64_tr_b16 a[10:11], v234 offset:17408\n"
"v_readlane_b32 s28, v254, 2\n"
"v_mfma_f32_16x16x32_bf16 v[36:39], a[72:75], a[52:55], v[36:39]\n"
"ds_read_b64_tr_b16 a[12:13], v235 offset:16384\n"
"s_add_u32 s38, s38, s39\n"
"v_mfma_f32_16x16x32_bf16 v[40:43], a[72:75], a[56:59], v[40:43]\n"
"v_and_b32 v129, 7, v252\n"
"s_add_u32 s38, s38, 50331648\n"
"v_mfma_f32_16x16x32_bf16 v[44:47], a[72:75], a[60:63], v[44:47]\n"
"ds_read_b64_tr_b16 a[14:15], v235 offset:17408\n"
"v_readlane_b32 s29, v254, 6\n"
"v_mfma_f32_16x16x32_bf16 v[48:51], a[76:79], a[48:51], v[48:51]\n"
"v_lshlrev_b32 v129, 4, v129\n"
"s_mov_b32 s32, 0\n"
"v_mfma_f32_16x16x32_bf16 v[52:55], a[76:79], a[52:55], v[52:55]\n"
"ds_read_b64_tr_b16 a[16:17], v236 offset:16384\n"
"s_mov_b32 s33, 32\n"
"v_mfma_f32_16x16x32_bf16 v[56:59], a[76:79], a[56:59], v[56:59]\n"
"ds_read_b64_tr_b16 a[18:19], v236 offset:17408\n"
"v_readlane_b32 s30, v254, 10\n"
"v_mfma_f32_16x16x32_bf16 v[60:63], a[76:79], a[60:63], v[60:63]\n"
"v_mad_u32_u24 v128, v128, s45, v129\n"
"s_lshl_b32 s50, %1, 2\n"
"v_mfma_f32_16x16x32_bf16 v[64:67], a[80:83], a[48:51], v[64:67]\n"
"ds_read_b64_tr_b16 a[20:21], v237 offset:16384\n"
"s_add_u32 s50, s50, %2\n"
"v_mfma_f32_16x16x32_bf16 v[68:71], a[80:83], a[52:55], v[68:71]\n"
"ds_read_b64_tr_b16 a[22:23], v237 offset:17408\n"
"v_readlane_b32 s44, v254, 14\n"
"v_mfma_f32_16x16x32_bf16 v[72:75], a[80:83], a[56:59], v[72:75]\n"
"s_lshl_b32 s48, s45, 3\n"
"s_mul_i32 s50, s50, 24576\n"
"v_mfma_f32_16x16x32_bf16 v[76:79], a[80:83], a[60:63], v[76:79]\n"
"ds_read_b64_tr_b16 a[24:25], v238 offset:16384\n"
"s_add_u32 s50, s50, 106954752\n"
"v_mfma_f32_16x16x32_bf16 v[80:83], a[84:87], a[48:51], v[80:83]\n"
"s_sub_u32 s48, s48, 1024\n"
"s_mov_b32 s31, 0x20000\n"
"v_mfma_f32_16x16x32_bf16 v[84:87], a[84:87], a[52:55], v[84:87]\n"
"ds_read_b64_tr_b16 a[26:27], v238 offset:17408\n"
"s_add_u32 s51, s50, 24576\n"
"v_mfma_f32_16x16x32_bf16 v[88:91], a[84:87], a[56:59], v[88:91]\n"
"ds_read_b64_tr_b16 a[28:29], v239 offset:16384\n"
"s_add_u32 s40, s57, 32768\n"
"v_mfma_f32_16x16x32_bf16 v[92:95], a[84:87], a[60:63], v[92:95]\n"
"s_mov_b32 m0, s40\n"
"s_sub_u32 s50, s50, 2048\n"
"v_mfma_f32_16x16x32_bf16 v[96:99], a[88:91], a[48:51], v[96:99]\n"
"ds_read_b64_tr_b16 a[30:31], v239 offset:17408\n"
"s_mov_b32 s64, 0xffffffff\n"
"v_mfma_f32_16x16x32_bf16 v[100:103], a[88:91], a[52:55], v[100:103]\n"
"buffer_load_dwordx4 v128, s[28:31], s44 offen offset:0 nt lds\n"
"s_mov_b32 s65, 0xfffffff0\n"
"v_mfma_f32_16x16x32_bf16 v[104:107], a[88:91], a[56:59], v[104:107]\n"
"s_add_u32 s44, s44, s48\n"
"v_mfma_f32_16x16x32_bf16 v[108:111], a[88:91], a[60:63], v[108:111]\n"
"buffer_load_dwordx4 v128, s[28:31], s44 offen offset:1024 nt lds\n"
"v_mfma_f32_16x16x32_bf16 v[112:115], a[92:95], a[48:51], v[112:115]\n"
"s_add_u32 s44, s44, s48\n"
"v_mfma_f32_16x16x32_bf16 v[116:119], a[92:95], a[52:55], v[116:119]\n"
"buffer_load_dwordx4 v128, s[28:31], s44 offen offset:2048 nt lds\n"
"v_mfma_f32_16x16x32_bf16 v[120:123], a[92:95], a[56:59], v[120:123]\n"
"s_add_u32 s44, s44, s48\n"
"v_mfma_f32_16x16x32_bf16 v[124:127], a[92:95], a[60:63], v[124:127]\n"
"buffer_load_dwordx4 v128, s[28:31], s44 offen offset:3072 nt lds\n"
"s_waitcnt lgkmcnt(0)\n"
"v_readlane_b32 s36, v229, 0\n"
"v_readlane_b32 s37, v229, 1\n"
"s_waitcnt vmcnt(11)\n"
"v_mfma_f32_16x16x32_bf16 v[0:3], a[0:3], a[96:99], v[0:3]\n"
"ds_read_b64_tr_b16 a[64:65], v232 offset:24576\n"
"s_sub_u32 s36, s36, 16777216\n"
"v_mfma_f32_16x16x32_bf16 v[4:7], a[0:3], a[100:103], v[4:7]\n"
"ds_read_b64_tr_b16 a[66:67], v232 offset:25600\n"
"s_sub_u32 s37, s37, 16777216\n"
"s_waitcnt vmcnt(10)\n"
"v_mfma_f32_16x16x32_bf16 v[8:11], a[0:3], a[104:107], v[8:11]\n"
"s_waitcnt vmcnt(4)\n"
"v_cmp_ne_u64 vcc, s[14:15], v[248:249]\n"
"s_nop 0\n"
"s_cbranch_vccnz SLOW_KV_%=\n"
"OK_KV_%=:\n"
"v_mfma_f32_16x16x32_bf16 v[12:15], a[0:3], a[108:111], v[12:15]\n"
"ds_read_b64_tr_b16 a[68:69], v233 offset:24576\n"
"s_add_u32 m0, s57, 0\n"
"v_mfma_f32_16x16x32_bf16 v[16:19], a[4:7], a[96:99], v[16:19]\n"
"ds_read_b64_tr_b16 a[70:71], v233 offset:25600\n"
"buffer_load_dwordx4 v251, s[96:99], s36 offen offset:0 sc1 lds\n"
"v_mfma_f32_16x16x32_bf16 v[20:23], a[4:7], a[100:103], v[20:23]\n"
"s_mov_b32 s66, 0xfffffffe\n"
"s_mov_b32 s67, 0xffffffe0\n"
"v_mfma_f32_16x16x32_bf16 v[24:27], a[4:7], a[104:107], v[24:27]\n"
"ds_read_b64_tr_b16 a[72:73], v234 offset:24576\n"
"s_mov_b32 s68, 0xfffffffc\n"
"v_mfma_f32_16x16x32_bf16 v[28:31], a[4:7], a[108:111], v[28:31]\n"
"buffer_load_dwordx4 v251, s[96:99], s36 offen offset:1024 sc1 lds\n"
"s_mov_b32 s69, 0xffffffc0\n"
"s_mov_b32 s70, 0xfffffff8\n"
"v_mfma_f32_16x16x32_bf16 v[32:35], a[8:11], a[96:99], v[32:35]\n"
"ds_read_b64_tr_b16 a[74:75], v234 offset:25600\n"
"s_mov_b32 s71, 0xffffff80\n"
"v_mfma_f32_16x16x32_bf16 v[36:39], a[8:11], a[100:103], v[36:39]\n"
"ds_read_b64_tr_b16 a[76:77], v235 offset:24576\n"
"s_mov_b32 s72, 0xffffff00\n"
"v_mfma_f32_16x16x32_bf16 v[40:43], a[8:11], a[104:107], v[40:43]\n"
"buffer_load_dwordx4 v251, s[96:99], s36 offen offset:2048 sc1 lds\n"
"s_mov_b32 s73, 0xfffff000\n"
"s_mov_b32 s74, 0xfffffe00\n"
"v_mfma_f32_16x16x32_bf16 v[44:47], a[8:11], a[108:111], v[44:47]\n"
"ds_read_b64_tr_b16 a[78:79], v235 offset:25600\n"
"s_mov_b32 s75, 0xffffe000\n"
"v_mfma_f32_16x16x32_bf16 v[48:51], a[12:15], a[96:99], v[48:51]\n"
"s_mov_b32 s76, 0xfffffc00\n"
"s_mov_b32 s77, 0xffffc000\n"
"v_mfma_f32_16x16x32_bf16 v[52:55], a[12:15], a[100:103], v[52:55]\n"
"ds_read_b64_tr_b16 a[80:81], v236 offset:24576\n"
"buffer_load_dwordx4 v251, s[96:99], s36 offen offset:3072 sc1 lds\n"
"s_mov_b32 s78, 0xfffff800\n"
"v_mfma_f32_16x16x32_bf16 v[56:59], a[12:15], a[104:107], v[56:59]\n"
"ds_read_b64_tr_b16 a[82:83], v236 offset:25600\n"
"s_mov_b32 s79, 0xffff8000\n"
"v_mfma_f32_16x16x32_bf16 v[60:63], a[12:15], a[108:111], v[60:63]\n"
"s_mov_b32 s80, 0xffff0000\n"
"v_mfma_f32_16x16x32_bf16 v[64:67], a[16:19], a[96:99], v[64:67]\n"
"ds_read_b64_tr_b16 a[84:85], v237 offset:24576\n"
"v_mfma_f32_16x16x32_bf16 v[68:71], a[16:19], a[100:103], v[68:71]\n"
"ds_read_b64_tr_b16 a[86:87], v237 offset:25600\n"
"s_add_u32 m0, s57, 16384\n"
"v_mfma_f32_16x16x32_bf16 v[72:75], a[16:19], a[104:107], v[72:75]\n"
"buffer_load_dwordx4 v251, s[96:99], s37 offen offset:0 sc1 lds\n"
"v_mfma_f32_16x16x32_bf16 v[76:79], a[16:19], a[108:111], v[76:79]\n"
"ds_read_b64_tr_b16 a[88:89], v238 offset:24576\n"
"v_mfma_f32_16x16x32_bf16 v[80:83], a[20:23], a[96:99], v[80:83]\n"
"v_mfma_f32_16x16x32_bf16 v[84:87], a[20:23], a[100:103], v[84:87]\n"
"ds_read_b64_tr_b16 a[90:91], v238 offset:25600\n"
"buffer_load_dwordx4 v251, s[96:99], s37 offen offset:1024 sc1 lds\n"
"v_mfma_f32_16x16x32_bf16 v[88:91], a[20:23], a[104:107], v[88:91]\n"
"ds_read_b64_tr_b16 a[92:93], v239 offset:24576\n"
"v_mfma_f32_16x16x32_bf16 v[92:95], a[20:23], a[108:111], v[92:95]\n"
"v_mfma_f32_16x16x32_bf16 v[96:99], a[24:27], a[96:99], v[96:99]\n"
"ds_read_b64_tr_b16 a[94:95], v239 offset:25600\n"
"buffer_load_dwordx4 v251, s[96:99], s37 offen offset:2048 sc1 lds\n"
"v_mfma_f32_16x16x32_bf16 v[100:103], a[24:27], a[100:103], v[100:103]\n"
"v_mfma_f32_16x16x32_bf16 v[104:107], a[24:27], a[104:107], v[104:107]\n"
"v_mfma_f32_16x16x32_bf16 v[108:111], a[24:27], a[108:111], v[108:111]\n"
"buffer_load_dwordx4 v251, s[96:99], s37 offen offset:3072 sc1 lds\n"
"v_mfma_f32_16x16x32_bf16 v[112:115], a[28:31], a[96:99], v[112:115]\n"
"v_readlane_b32 s45, v254, 16\n"
"v_lshrrev_b32 v128, 3, v252\n"
"v_readlane_b32 s28, v254, 0\n"
"v_and_b32 v129, 7, v252\n"
"v_readlane_b32 s29, v254, 4\n"
"v_lshlrev_b32 v129, 4, v129\n"
"v_mfma_f32_16x16x32_bf16 v[116:119], a[28:31], a[100:103], v[116:119]\n"
"v_readlane_b32 s30, v254, 8\n"
"v_mad_u32_u24 v128, v128, s45, v129\n"
"v_readlane_b32 s44, v254, 12\n"
"s_lshl_b32 s48, s45, 3\n"
"s_sub_u32 s48, s48, 1024\n"
"s_mov_b32 s31, 0x20000\n"
"v_mfma_f32_16x16x32_bf16 v[120:123], a[28:31], a[104:107], v[120:123]\n"
"s_lshl_b32 s40, %2, 12\n"
"s_add_u32 s40, s40, s57\n"
"s_add_u32 s40, s40, 131072\n"
"s_mov_b32 m0, s40\n"
"s_nop 0\n"
"buffer_load_dwordx4 v128, s[28:31], s44 offen offset:0 nt lds\n"
"s_add_u32 s44, s44, s48\n"
"v_mfma_f32_16x16x32_bf16 v[124:127], a[28:31], a[108:111], v[124:127]\n"
"buffer_load_dwordx4 v128, s[28:31], s44 offen offset:1024 nt lds\n"
"s_add_u32 s44, s44, s48\n"
"buffer_load_dwordx4 v128, s[28:31], s44 offen offset:2048 nt lds\n"
"s_add_u32 s44, s44, s48\n"
"buffer_load_dwordx4 v128, s[28:31], s44 offen offset:3072 nt lds\n"
"s_waitcnt lgkmcnt(0)\n"
"v_mfma_f32_16x16x32_bf16 v[0:3], a[64:67], a[112:115], v[0:3]\n"
"v_mfma_f32_16x16x32_bf16 v[4:7], a[64:67], a[116:119], v[4:7]\n"
"v_mfma_f32_16x16x32_bf16 v[8:11], a[64:67], a[120:123], v[8:11]\n"
"v_mfma_f32_16x16x32_bf16 v[12:15], a[64:67], a[124:127], v[12:15]\n"
"v_mfma_f32_16x16x32_bf16 v[16:19], a[68:71], a[112:115], v[16:19]\n"
"v_mfma_f32_16x16x32_bf16 v[20:23], a[68:71], a[116:119], v[20:23]\n"
"s_mov_b32 s81, 0xfff00000\n"
"s_mov_b32 s82, 0xfffe0000\n"
"v_mfma_f32_16x16x32_bf16 v[24:27], a[68:71], a[120:123], v[24:27]\n"
"v_readlane_b32 s45, v254, 17\n"
"s_mov_b32 s83, 0xffe00000\n"
"v_mfma_f32_16x16x32_bf16 v[28:31], a[68:71], a[124:127], v[28:31]\n"
"v_lshrrev_b32 v128, 3, v252\n"
"s_mov_b32 s84, 0xfffc0000\n"
"v_mfma_f32_16x16x32_bf16 v[32:35], a[72:75], a[112:115], v[32:35]\n"
"v_readlane_b32 s28, v254, 1\n"
"s_mov_b32 s85, 0xffc00000\n"
"v_mfma_f32_16x16x32_bf16 v[36:39], a[72:75], a[116:119], v[36:39]\n"
"v_and_b32 v129, 7, v252\n"
"s_mov_b32 s86, 0xfff80000\n"
"v_mfma_f32_16x16x32_bf16 v[40:43], a[72:75], a[120:123], v[40:43]\n"
"v_readlane_b32 s29, v254, 5\n"
"s_mov_b32 s87, 0xff800000\n"
"v_mfma_f32_16x16x32_bf16 v[44:47], a[72:75], a[124:127], v[44:47]\n"
"v_lshlrev_b32 v129, 4, v129\n"
"s_mov_b32 s88, 0xff000000\n"
"v_mfma_f32_16x16x32_bf16 v[48:51], a[76:79], a[112:115], v[48:51]\n"
"v_readlane_b32 s30, v254, 9\n"
"s_mov_b32 s89, 0xf0000000\n"
"v_mfma_f32_16x16x32_bf16 v[52:55], a[76:79], a[116:119], v[52:55]\n"
"v_mad_u32_u24 v128, v128, s45, v129\n"
"s_mov_b32 s90, 0xfe000000\n"
"v_mfma_f32_16x16x32_bf16 v[56:59], a[76:79], a[120:123], v[56:59]\n"
"s_mov_b32 s91, 0xe0000000\n"
"s_mov_b32 s92, 0xfc000000\n"
"v_mfma_f32_16x16x32_bf16 v[60:63], a[76:79], a[124:127], v[60:63]\n"
"v_readlane_b32 s44, v254, 13\n"
"s_mov_b32 s93, 0xc0000000\n"
"v_mfma_f32_16x16x32_bf16 v[64:67], a[80:83], a[112:115], v[64:67]\n"
"s_lshl_b32 s48, s45, 3\n"
"s_mov_b32 s94, 0xf8000000\n"
"v_mfma_f32_16x16x32_bf16 v[68:71], a[80:83], a[116:119], v[68:71]\n"
"s_sub_u32 s48, s48, 1024\n"
"s_mov_b32 s95, 0x80000000\n"
"v_mfma_f32_16x16x32_bf16 v[72:75], a[80:83], a[120:123], v[72:75]\n"
"s_mov_b32 s31, 0x20000\n"
"v_mfma_f32_16x16x32_bf16 v[76:79], a[80:83], a[124:127], v[76:79]\n"
"s_lshl_b32 s40, %2, 12\n"
"v_mfma_f32_16x16x32_bf16 v[80:83], a[84:87], a[112:115], v[80:83]\n"
"s_add_u32 s40, s40, s57\n"
"v_mfma_f32_16x16x32_bf16 v[84:87], a[84:87], a[116:119], v[84:87]\n"
"s_add_u32 s40, s40, 135168\n"
"v_mfma_f32_16x16x32_bf16 v[88:91], a[84:87], a[120:123], v[88:91]\n"
"s_mov_b32 m0, s40\n"
"v_mfma_f32_16x16x32_bf16 v[92:95], a[84:87], a[124:127], v[92:95]\n"
"v_mfma_f32_16x16x32_bf16 v[96:99], a[88:91], a[112:115], v[96:99]\n"
"buffer_load_dwordx4 v128, s[28:31], s44 offen offset:0 nt lds\n"
"v_mfma_f32_16x16x32_bf16 v[100:103], a[88:91], a[116:119], v[100:103]\n"
"s_add_u32 s44, s44, s48\n"
"v_mfma_f32_16x16x32_bf16 v[104:107], a[88:91], a[120:123], v[104:107]\n"
"buffer_load_dwordx4 v128, s[28:31], s44 offen offset:1024 nt lds\n"
"v_mfma_f32_16x16x32_bf16 v[108:111], a[88:91], a[124:127], v[108:111]\n"
"s_add_u32 s44, s44, s48\n"
"v_mfma_f32_16x16x32_bf16 v[112:115], a[92:95], a[112:115], v[112:115]\n"
"buffer_load_dwordx4 v128, s[28:31], s44 offen offset:2048 nt lds\n"
"v_mfma_f32_16x16x32_bf16 v[116:119], a[92:95], a[116:119], v[116:119]\n"
"s_add_u32 s44, s44, s48\n"
"v_mfma_f32_16x16x32_bf16 v[120:123], a[92:95], a[120:123], v[120:123]\n"
"buffer_load_dwordx4 v128, s[28:31], s44 offen offset:3072 nt lds\n"
"v_mfma_f32_16x16x32_bf16 v[124:127], a[92:95], a[124:127], v[124:127]\n"
"s_waitcnt lgkmcnt(0)\n"
"v_cvt_pk_bf16_f32 v160, v0, v1\n"
"v_cvt_pk_bf16_f32 v161, v2, v3\n"
"v_cvt_pk_bf16_f32 v162, v16, v17\n"
"v_cvt_pk_bf16_f32 v163, v18, v19\n"
"ds_write_b128 v250, v[160:163] offset:0\n"
"v_cvt_pk_bf16_f32 v164, v32, v33\n"
"v_cvt_pk_bf16_f32 v165, v34, v35\n"
"v_cvt_pk_bf16_f32 v166, v48, v49\n"
"v_cvt_pk_bf16_f32 v167, v50, v51\n"
"ds_write_b128 v250, v[164:167] offset:2048\n"
"v_cvt_pk_bf16_f32 v168, v64, v65\n"
"v_cvt_pk_bf16_f32 v169, v66, v67\n"
"v_cvt_pk_bf16_f32 v170, v80, v81\n"
"v_cvt_pk_bf16_f32 v171, v82, v83\n"
"ds_write_b128 v250, v[168:171] offset:4096\n"
"v_cvt_pk_bf16_f32 v172, v96, v97\n"
"v_cvt_pk_bf16_f32 v173, v98, v99\n"
"v_cvt_pk_bf16_f32 v174, v112, v113\n"
"v_cvt_pk_bf16_f32 v175, v114, v115\n"
"ds_write_b128 v250, v[172:175] offset:6144\n"
"v_cvt_pk_bf16_f32 v176, v4, v5\n"
"v_cvt_pk_bf16_f32 v177, v6, v7\n"
"v_cvt_pk_bf16_f32 v178, v20, v21\n"
"v_cvt_pk_bf16_f32 v179, v22, v23\n"
"ds_write_b128 v250, v[176:179] offset:256\n"
"v_cvt_pk_bf16_f32 v180, v36, v37\n"
"v_cvt_pk_bf16_f32 v181, v38, v39\n"
"v_cvt_pk_bf16_f32 v182, v52, v53\n"
"v_cvt_pk_bf16_f32 v183, v54, v55\n"
"ds_write_b128 v250, v[180:183] offset:2304\n"
"v_cvt_pk_bf16_f32 v184, v68, v69\n"
"v_cvt_pk_bf16_f32 v185, v70, v71\n"
"v_cvt_pk_bf16_f32 v186, v84, v85\n"
"v_cvt_pk_bf16_f32 v187, v86, v87\n"
"ds_write_b128 v250, v[184:187] offset:4352\n"
"v_cvt_pk_bf16_f32 v188, v100, v101\n"
"v_cvt_pk_bf16_f32 v189, v102, v103\n"
"v_cvt_pk_bf16_f32 v190, v116, v117\n"
"v_cvt_pk_bf16_f32 v191, v118, v119\n"
"ds_write_b128 v250, v[188:191] offset:6400\n"
"v_cvt_pk_bf16_f32 v160, v8, v9\n"
"v_cvt_pk_bf16_f32 v161, v10, v11\n"
"v_cvt_pk_bf16_f32 v162, v24, v25\n"
"v_cvt_pk_bf16_f32 v163, v26, v27\n"
"ds_write_b128 v250, v[160:163] offset:8192\n"
"v_cvt_pk_bf16_f32 v164, v40, v41\n"
"v_cvt_pk_bf16_f32 v165, v42, v43\n"
"v_cvt_pk_bf16_f32 v166, v56, v57\n"
"v_cvt_pk_bf16_f32 v167, v58, v59\n"
"ds_write_b128 v250, v[164:167] offset:10240\n"
"v_cvt_pk_bf16_f32 v168, v72, v73\n"
"v_cvt_pk_bf16_f32 v169, v74, v75\n"
"v_cvt_pk_bf16_f32 v170, v88, v89\n"
"v_cvt_pk_bf16_f32 v171, v90, v91\n"
"ds_write_b128 v250, v[168:171] offset:12288\n"
"v_cvt_pk_bf16_f32 v172, v104, v105\n"
"v_cvt_pk_bf16_f32 v173, v106, v107\n"
"v_cvt_pk_bf16_f32 v174, v120, v121\n"
"v_cvt_pk_bf16_f32 v175, v122, v123\n"
"ds_write_b128 v250, v[172:175] offset:14336\n"
"v_cvt_pk_bf16_f32 v176, v12, v13\n"
"v_cvt_pk_bf16_f32 v177, v14, v15\n"
"v_cvt_pk_bf16_f32 v178, v28, v29\n"
"v_cvt_pk_bf16_f32 v179, v30, v31\n"
"ds_write_b128 v250, v[176:179] offset:8448\n"
"v_cvt_pk_bf16_f32 v180, v44, v45\n"
"v_cvt_pk_bf16_f32 v181, v46, v47\n"
"v_cvt_pk_bf16_f32 v182, v60, v61\n"
"v_cvt_pk_bf16_f32 v183, v62, v63\n"
"ds_write_b128 v250, v[180:183] offset:10496\n"
"v_cvt_pk_bf16_f32 v184, v76, v77\n"
"v_cvt_pk_bf16_f32 v185, v78, v79\n"
"v_cvt_pk_bf16_f32 v186, v92, v93\n"
"v_cvt_pk_bf16_f32 v187, v94, v95\n"
"ds_write_b128 v250, v[184:187] offset:12544\n"
"v_cvt_pk_bf16_f32 v188, v108, v109\n"
"v_cvt_pk_bf16_f32 v189, v110, v111\n"
"v_cvt_pk_bf16_f32 v190, v124, v125\n"
"v_cvt_pk_bf16_f32 v191, v126, v127\n"
"ds_write_b128 v250, v[188:191] offset:14592\n"
"v_add_u32 v154, s59, v251\n"
"s_waitcnt lgkmcnt(0)\n"
"ds_read_b128 a[128:131], v154 offset:0\n"
"ds_read_b128 a[132:135], v154 offset:1024\n"
"ds_read_b128 a[136:139], v154 offset:2048\n"
"ds_read_b128 a[140:143], v154 offset:3072\n"
"ds_read_b128 a[144:147], v154 offset:4096\n"
"ds_read_b128 a[148:151], v154 offset:5120\n"
"ds_read_b128 a[152:155], v154 offset:6144\n"
"ds_read_b128 a[156:159], v154 offset:7168\n"
"ds_read_b128 a[160:163], v154 offset:8192\n"
"ds_read_b128 a[164:167], v154 offset:9216\n"
"ds_read_b128 a[168:171], v154 offset:10240\n"
"ds_read_b128 a[172:175], v154 offset:11264\n"
"ds_read_b128 a[176:179], v154 offset:12288\n"
"ds_read_b128 a[180:183], v154 offset:13312\n"
"ds_read_b128 a[184:187], v154 offset:14336\n"
"ds_read_b128 a[188:191], v154 offset:15360\n"
"s_lshl_b32 s60, %1, 15\n"
"s_add_u32 s60, s60, 4194304\n"
"s_xor_b32 s61, %2, 1\n"
"s_lshl_b32 s61, s61, 14\n"
"s_add_u32 s43, s60, s61\n"
"s_and_b32 s61, %2, 1\n"
"s_lshl_b32 s61, s61, 14\n"
"s_add_u32 s60, s60, s61\n"
"s_cmp_lt_u32 %2, 2\n"
"s_cselect_b32 s40, 1073741824, s60\n"
"v_add_u32 v224, %3, v251\n"
"v_add_u32 v225, 65536, v224\n"
"v_mov_b32 v227, 0xff800000\n"
"v_and_b32 v237, 15, v252\n"
"v_lshrrev_b32 v239, 5, v252\n"
"v_bfe_u32 v238, v252, 4, 1\n"
"v_lshl_or_b32 v237, v239, 4, v237\n"
"v_lshlrev_b32 v237, 4, v237\n"
"v_lshl_or_b32 v228, v238, 17, v237\n"
"v_mov_b32 v232, -1\n"
"v_mov_b32 v233, -1\n"
"v_mov_b32 v234, -1\n"
"v_mov_b32 v235, -1\n"
"QEND_%=:\n"
"s_mov_b64 exec, 0\n"
"s_cmp_lg_u32 %2, 0\n"
"s_cbranch_scc1 TWB0_%=\n"
"s_branch PG8_%=\n"
"RPG8_%=:\n"
"s_branch PG12_%=\n"
"RPG12_%=:\n"
"s_branch PG16_%=\n"
"RPG16_%=:\n"
"s_nop 0\n"
"s_nop 0\n"
"s_nop 0\n"
"TWB0_%=:\n"
"s_cmp_lg_u32 %2, 1\n"
"s_cbranch_scc1 TWB1_%=\n"
"s_branch PG9_%=\n"
"RPG9_%=:\n"
"s_branch PG13_%=\n"
"RPG13_%=:\n"
"s_nop 0\n"
"s_nop 0\n"
"s_nop 0\n"
"s_nop 0\n"
"TWB1_%=:\n"
"s_cmp_lg_u32 %2, 2\n"
"s_cbranch_scc1 TWB2_%=\n"
"s_branch PG10_%=\n"
"RPG10_%=:\n"
"s_branch PG14_%=\n"
"RPG14_%=:\n"
"s_nop 0\n"
"s_nop 0\n"
"s_nop 0\n"
"s_nop 0\n"
"TWB2_%=:\n"
"s_cmp_lg_u32 %2, 3\n"
"s_cbranch_scc1 TWB3_%=\n"
"s_branch PG11_%=\n"
"RPG11_%=:\n"
"s_branch PG15_%=\n"
"RPG15_%=:\n"
"s_nop 0\n"
"s_nop 0\n"
"s_nop 0\n"
"s_nop 0\n"
"TWB3_%=:\n"
"s_mov_b64 exec, -1\n"
"s_waitcnt vmcnt(8)\n"
"s_barrier\n"
"ds_read_b128 a[192:195], v224 offset:0\n"
"ds_read_b128 a[196:199], v224 offset:1024\n"
"ds_read_b128 a[200:203], v224 offset:2048\n"
"ds_read_b128 a[204:207], v224 offset:3072\n"
"ds_read_b128 a[208:211], v224 offset:4096\n"
"ds_read_b128 a[212:215], v224 offset:5120\n"
"ds_read_b128 a[216:219], v224 offset:6144\n"
"s_waitcnt lgkmcnt(15)\n"
"buffer_store_dwordx4 a[128:131], v251, s[96:99], s40 offen offset:0 sc1\n"
"buffer_store_dwordx4 a[132:135], v251, s[96:99], s40 offen offset:1024 sc1\n"
"buffer_store_dwordx4 a[136:139], v251, s[96:99], s40 offen offset:2048 sc1\n"
"buffer_store_dwordx4 a[140:143], v251, s[96:99], s40 offen offset:3072 sc1\n"
"s_add_u32 s40, s40, 4096\n"
"buffer_store_dwordx4 a[144:147], v251, s[96:99], s40 offen offset:0 sc1\n"
"buffer_store_dwordx4 a[148:151], v251, s[96:99], s40 offen offset:1024 sc1\n"
"buffer_store_dwordx4 a[152:155], v251, s[96:99], s40 offen offset:2048 sc1\n"
"buffer_store_dwordx4 a[156:159], v251, s[96:99], s40 offen offset:3072 sc1\n"
"s_add_u32 s40, s40, 4096\n"
"s_waitcnt lgkmcnt(7)\n"
"buffer_store_dwordx4 a[160:163], v251, s[96:99], s40 offen offset:0 sc1\n"
"buffer_store_dwordx4 a[164:167], v251, s[96:99], s40 offen offset:1024 sc1\n"
"buffer_store_dwordx4 a[168:171], v251, s[96:99], s40 offen offset:2048 sc1\n"
"buffer_store_dwordx4 a[172:175], v251, s[96:99], s40 offen offset:3072 sc1\n"
"s_add_u32 s40, s40, 4096\n"
"buffer_store_dwordx4 a[176:179], v251, s[96:99], s40 offen offset:0 sc1\n"
"buffer_store_dwordx4 a[180:183], v251, s[96:99], s40 offen offset:1024 sc1\n"
"buffer_store_dwordx4 a[184:187], v251, s[96:99], s40 offen offset:2048 sc1\n"
"buffer_store_dwordx4 a[188:191], v251, s[96:99], s40 offen offset:3072 sc1\n"
"s_add_u32 s40, s40, 4096\n"
"s_bitcmp1_b32 s26, 0\n"
"s_cbranch_scc1 FIRSTM_0_%=\n"
"s_branch FIRST_0_%=\n"
".p2align 6\n"
"MID_0_%=:\n"
"v_exp_f32 v208, v96\n"
"v_exp_f32 v209, v97\n"
"s_waitcnt lgkmcnt(6)\n"
"v_mfma_f32_32x32x16_bf16 v[0:15], a[192:195], a[128:131], v[128:143]\n"
"v_exp_f32 v210, v98\n"
"v_exp_f32 v211, v99\n"
"v_add_f32 v214, v214, v208\n"
"v_mfma_f32_32x32x16_bf16 v[16:31], a[192:195], a[160:163], v[144:159]\n"
"ds_read_b128 a[220:223], v224 offset:7168\n"
"v_exp_f32 v212, v100\n"
"v_add_f32 v215, v215, v209\n"
"v_cvt_pk_bf16_f32 v192, v208, v209\n"
"s_waitcnt lgkmcnt(4)\n"
"v_mfma_f32_32x32x16_bf16 v[0:15], a[196:199], a[132:135], v[0:15]\n"
"v_exp_f32 v213, v101\n"
"v_add_f32 v214, v214, v210\n"
"v_add_f32 v215, v215, v211\n"
"v_mfma_f32_32x32x16_bf16 v[16:31], a[196:199], a[164:167], v[16:31]\n"
"ds_read_b128 a[224:227], v224 offset:8192\n"
"v_exp_f32 v208, v102\n"
"v_cvt_pk_bf16_f32 v193, v210, v211\n"
"v_mfma_f32_32x32x16_bf16 v[0:15], a[200:203], a[136:139], v[0:15]\n"
"v_exp_f32 v209, v103\n"
"v_add_f32 v214, v214, v212\n"
"v_add_f32 v215, v215, v213\n"
"v_cvt_pk_bf16_f32 v194, v212, v213\n"
"v_mfma_f32_32x32x16_bf16 v[16:31], a[200:203], a[168:171], v[16:31]\n"
"ds_read_b128 a[228:231], v224 offset:9216\n"
"v_exp_f32 v210, v112\n"
"v_add_f32 v214, v214, v208\n"
"v_mfma_f32_32x32x16_bf16 v[0:15], a[204:207], a[140:143], v[0:15]\n"
"v_exp_f32 v211, v113\n"
"v_add_f32 v215, v215, v209\n"
"v_cvt_pk_bf16_f32 v195, v208, v209\n"
"v_mfma_f32_32x32x16_bf16 v[16:31], a[204:207], a[172:175], v[16:31]\n"
"ds_read_b128 a[232:235], v224 offset:10240\n"
"v_exp_f32 v212, v114\n"
"v_add_f32 v216, v216, v210\n"
"s_waitcnt lgkmcnt(3)\n"
"v_mfma_f32_32x32x16_bf16 v[0:15], a[208:211], a[144:147], v[0:15]\n"
"v_exp_f32 v213, v115\n"
"v_add_f32 v217, v217, v211\n"
"v_cvt_pk_bf16_f32 v196, v210, v211\n"
"v_mfma_f32_32x32x16_bf16 v[16:31], a[208:211], a[176:179], v[16:31]\n"
"ds_read_b128 a[236:239], v224 offset:11264\n"
"v_exp_f32 v208, v116\n"
"v_add_f32 v216, v216, v212\n"
"v_mfma_f32_32x32x16_bf16 v[0:15], a[212:215], a[148:151], v[0:15]\n"
"v_exp_f32 v209, v117\n"
"v_add_f32 v217, v217, v213\n"
"v_cvt_pk_bf16_f32 v197, v212, v213\n"
"v_mfma_f32_32x32x16_bf16 v[16:31], a[212:215], a[180:183], v[16:31]\n"
"ds_read_b128 a[240:243], v224 offset:12288\n"
"v_exp_f32 v210, v118\n"
"v_add_f32 v216, v216, v208\n"
"v_mfma_f32_32x32x16_bf16 v[0:15], a[216:219], a[152:155], v[0:15]\n"
"v_exp_f32 v211, v119\n"
"v_add_f32 v217, v217, v209\n"
"v_cvt_pk_bf16_f32 v198, v208, v209\n"
"v_mfma_f32_32x32x16_bf16 v[16:31], a[216:219], a[184:187], v[16:31]\n"
"ds_read_b128 a[244:247], v224 offset:13312\n"
"v_exp_f32 v212, v104\n"
"v_add_f32 v216, v216, v210\n"
"v_mfma_f32_32x32x16_bf16 v[0:15], a[220:223], a[156:159], v[0:15]\n"
"v_exp_f32 v213, v105\n"
"v_add_f32 v217, v217, v211\n"
"v_cvt_pk_bf16_f32 v199, v210, v211\n"
"v_mfma_f32_32x32x16_bf16 v[16:31], a[220:223], a[188:191], v[16:31]\n"
"ds_read_b128 a[248:251], v224 offset:14336\n"
"v_exp_f32 v208, v106\n"
"v_add_f32 v214, v214, v212\n"
"s_waitcnt lgkmcnt(3)\n"
"v_mfma_f32_32x32x16_bf16 v[32:47], a[224:227], a[128:131], v[128:143]\n"
"v_exp_f32 v209, v107\n"
"v_add_f32 v215, v215, v213\n"
"v_cvt_pk_bf16_f32 v200, v212, v213\n"
"v_mfma_f32_32x32x16_bf16 v[48:63], a[224:227], a[160:163], v[144:159]\n"
"PG8_%=:\n"
"s_cbranch_execz RPG8_%=\n"
"ds_read_b128 a[252:255], v224 offset:15360\n"
"v_exp_f32 v210, v108\n"
"v_add_f32 v214, v214, v208\n"
"v_mfma_f32_32x32x16_bf16 v[32:47], a[228:231], a[132:135], v[32:47]\n"
"v_exp_f32 v211, v109\n"
"v_readlane_b32 s34, v229, s32\n"
"v_readlane_b32 s35, v229, s33\n"
"v_add_f32 v215, v215, v209\n"
"v_mfma_f32_32x32x16_bf16 v[48:63], a[228:231], a[164:167], v[48:63]\n"
"v_exp_f32 v212, v110\n"
"v_readlane_b32 s36, v230, s32\n"
"v_readlane_b32 s37, v230, s33\n"
"v_cvt_pk_bf16_f32 v201, v208, v209\n"
"v_mfma_f32_32x32x16_bf16 v[32:47], a[232:235], a[136:139], v[32:47]\n"
"v_exp_f32 v213, v111\n"
"v_add_f32 v214, v214, v210\n"
"v_add_f32 v215, v215, v211\n"
"s_add_u32 s50, s50, 2048\n"
"v_mfma_f32_32x32x16_bf16 v[48:63], a[232:235], a[168:171], v[48:63]\n"
"v_exp_f32 v208, v120\n"
"v_cvt_pk_bf16_f32 v202, v210, v211\n"
"s_cmp_ge_u32 s50, s51\n"
"s_cselect_b32 s50, 1073741824, s50\n"
"v_mfma_f32_32x32x16_bf16 v[32:47], a[236:239], a[140:143], v[32:47]\n"
"v_exp_f32 v209, v121\n"
"v_add_f32 v214, v214, v212\n"
"s_waitcnt vmcnt(0) lgkmcnt(0)\n"
"s_barrier\n"
"s_cmp_eq_u32 s32, s12\n"
"s_cbranch_scc1 PUBRT_%=\n"
"PUBRT_RET_%=:\n"
"v_mfma_f32_32x32x16_bf16 v[48:63], a[236:239], a[172:175], v[48:63]\n"
"v_exp_f32 v210, v122\n"
"v_add_f32 v215, v215, v213\n"
"v_cvt_pk_bf16_f32 v203, v212, v213\n"
"v_add_f32 v216, v216, v208\n"
"v_mfma_f32_32x32x16_bf16 v[32:47], a[240:243], a[144:147], v[32:47]\n"
"ds_read_b128 a[192:195], v224 offset:49152\n"
"v_exp_f32 v211, v123\n"
"v_add_f32 v217, v217, v209\n"
"s_add_u32 m0, s57, 32768\n"
"v_mfma_f32_32x32x16_bf16 v[48:63], a[240:243], a[176:179], v[48:63]\n"
"buffer_load_dwordx4 v251, s[96:99], s34 offen offset:0 sc1 lds\n"
"v_exp_f32 v212, v124\n"
"v_cvt_pk_bf16_f32 v204, v208, v209\n"
"v_add_f32 v216, v216, v210\n"
"v_mfma_f32_32x32x16_bf16 v[32:47], a[244:247], a[148:151], v[32:47]\n"
"ds_read_b128 a[196:199], v224 offset:50176\n"
"v_exp_f32 v213, v125\n"
"v_add_f32 v217, v217, v211\n"
"v_mfma_f32_32x32x16_bf16 v[48:63], a[244:247], a[180:183], v[48:63]\n"
"buffer_load_dwordx4 v251, s[96:99], s34 offen offset:1024 sc1 lds\n"
"v_exp_f32 v208, v126\n"
"v_cvt_pk_bf16_f32 v205, v210, v211\n"
"v_add_f32 v216, v216, v212\n"
"v_mfma_f32_32x32x16_bf16 v[32:47], a[248:251], a[152:155], v[32:47]\n"
"ds_read_b128 a[200:203], v224 offset:51200\n"
"v_exp_f32 v209, v127\n"
"v_add_f32 v217, v217, v213\n"
"v_mfma_f32_32x32x16_bf16 v[48:63], a[248:251], a[184:187], v[48:63]\n"
"v_cvt_pk_bf16_f32 v206, v212, v213\n"
"v_add_f32 v216, v216, v208\n"
"v_add_f32 v217, v217, v209\n"
"v_cvt_pk_bf16_f32 v207, v208, v209\n"
"v_mfma_f32_32x32x16_bf16 v[32:47], a[252:255], a[156:159], v[32:47]\n"
"ds_read_b128 a[204:207], v224 offset:52224\n"
"v_add_f32 v218, v214, v215\n"
"v_add_f32 v219, v216, v217\n"
"v_mfma_f32_32x32x16_bf16 v[48:63], a[252:255], a[188:191], v[48:63]\n"
"buffer_load_dwordx4 v251, s[96:99], s34 offen offset:2048 sc1 lds\n"
"v_max_f32 v236, v218, v219\n"
"v_cmp_nge_f32 vcc, s42, v236\n"
"s_nop 0\n"
"s_cbranch_vccnz STUB_MID_0_%=\n"
"RET_MID_0_%=:\n"
"s_waitcnt lgkmcnt(2)\n"
"v_mfma_f32_32x32x16_bf16 a[0:15], a[192:195], v[176:179], a[0:15]\n"
"ds_read_b128 a[208:211], v224 offset:53248\n"
"v_exp_f32 v208, v0\n"
"v_exp_f32 v209, v1\n"
"v_add_f32 v220, v220, v218\n"
"v_mfma_f32_32x32x16_bf16 a[16:31], a[192:195], v[180:183], a[16:31]\n"
"buffer_load_dwordx4 v251, s[96:99], s34 offen offset:3072 sc1 lds\n"
"v_exp_f32 v210, v2\n"
"v_exp_f32 v211, v3\n"
"v_mov_b32 v214, v208\n"
"v_add_f32 v221, v221, v219\n"
"v_mfma_f32_32x32x16_bf16 a[32:47], a[196:199], v[176:179], a[32:47]\n"
"ds_read_b128 a[212:215], v224 offset:54272\n"
"v_mov_b32 v215, v209\n"
"v_cvt_pk_bf16_f32 v160, v208, v209\n"
"s_add_u32 m0, s57, 98304\n"
"v_mfma_f32_32x32x16_bf16 a[48:63], a[196:199], v[180:183], a[48:63]\n"
"buffer_load_dwordx4 v251, s[96:99], s35 offen offset:0 sc1 lds\n"
"v_exp_f32 v212, v4\n"
"v_add_f32 v214, v214, v210\n"
"s_waitcnt lgkmcnt(2)\n"
"v_mfma_f32_32x32x16_bf16 a[64:79], a[200:203], v[176:179], a[64:79]\n"
"ds_read_b128 a[216:219], v224 offset:55296\n"
"v_exp_f32 v213, v5\n"
"v_add_f32 v215, v215, v211\n"
"v_cvt_pk_bf16_f32 v161, v210, v211\n"
"v_mfma_f32_32x32x16_bf16 a[80:95], a[200:203], v[180:183], a[80:95]\n"
"v_exp_f32 v208, v6\n"
"v_exp_f32 v209, v7\n"
"v_add_f32 v214, v214, v212\n"
"v_mfma_f32_32x32x16_bf16 a[96:111], a[204:207], v[176:179], a[96:111]\n"
"ds_read_b128 a[220:223], v224 offset:56320\n"
"v_exp_f32 v210, v16\n"
"v_add_f32 v215, v215, v213\n"
"v_cvt_pk_bf16_f32 v162, v212, v213\n"
"v_mfma_f32_32x32x16_bf16 a[112:127], a[204:207], v[180:183], a[112:127]\n"
"buffer_load_dwordx4 v251, s[96:99], s35 offen offset:1024 sc1 lds\n"
"v_exp_f32 v211, v17\n"
"v_add_f32 v214, v214, v208\n"
"s_waitcnt lgkmcnt(2)\n"
"v_mfma_f32_32x32x16_bf16 a[0:15], a[208:211], v[184:187], a[0:15]\n"
"ds_read_b128 a[224:227], v224 offset:57344\n"
"v_exp_f32 v212, v18\n"
"v_add_f32 v215, v215, v209\n"
"v_cvt_pk_bf16_f32 v163, v208, v209\n"
"v_mfma_f32_32x32x16_bf16 a[16:31], a[208:211], v[188:191], a[16:31]\n"
"buffer_load_dwordx4 v251, s[96:99], s35 offen offset:2048 sc1 lds\n"
"v_exp_f32 v213, v19\n"
"v_mov_b32 v216, v210\n"
"v_mov_b32 v217, v211\n"
"v_mfma_f32_32x32x16_bf16 a[32:47], a[212:215], v[184:187], a[32:47]\n"
"ds_read_b128 a[228:231], v224 offset:58368\n"
"v_exp_f32 v208, v20\n"
"v_cvt_pk_bf16_f32 v164, v210, v211\n"
"v_add_f32 v216, v216, v212\n"
"v_mfma_f32_32x32x16_bf16 a[48:63], a[212:215], v[188:191], a[48:63]\n"
"buffer_load_dwordx4 v251, s[96:99], s35 offen offset:3072 sc1 lds\n"
"v_exp_f32 v209, v21\n"
"v_add_f32 v217, v217, v213\n"
"s_waitcnt lgkmcnt(2)\n"
"v_mfma_f32_32x32x16_bf16 a[64:79], a[216:219], v[184:187], a[64:79]\n"
"ds_read_b128 a[232:235], v224 offset:59392\n"
"v_exp_f32 v210, v22\n"
"v_cvt_pk_bf16_f32 v165, v212, v213\n"
"s_add_u32 m0, s57, 0\n"
"v_mfma_f32_32x32x16_bf16 a[80:95], a[216:219], v[188:191], a[80:95]\n"
"buffer_load_dwordx4 v251, s[96:99], s36 offen offset:0 sc1 lds\n"
"v_exp_f32 v211, v23\n"
"v_add_f32 v216, v216, v208\n"
"v_add_f32 v217, v217, v209\n"
"v_mfma_f32_32x32x16_bf16 a[96:111], a[220:223], v[184:187], a[96:111]\n"
"ds_read_b128 a[236:239], v224 offset:60416\n"
"v_exp_f32 v212, v8\n"
"v_cvt_pk_bf16_f32 v166, v208, v209\n"
"v_add_f32 v216, v216, v210\n"
"v_mfma_f32_32x32x16_bf16 a[112:127], a[220:223], v[188:191], a[112:127]\n"
"v_exp_f32 v213, v9\n"
"v_add_f32 v217, v217, v211\n"
"v_cvt_pk_bf16_f32 v167, v210, v211\n"
"s_waitcnt lgkmcnt(2)\n"
"v_mfma_f32_32x32x16_bf16 a[0:15], a[224:227], v[192:195], a[0:15]\n"
"ds_read_b128 a[240:243], v224 offset:61440\n"
"v_exp_f32 v208, v10\n"
"v_add_f32 v214, v214, v212\n"
"v_mfma_f32_32x32x16_bf16 a[16:31], a[224:227], v[196:199], a[16:31]\n"
"buffer_load_dwordx4 v251, s[96:99], s36 offen offset:1024 sc1 lds\n"
"v_exp_f32 v209, v11\n"
"v_add_f32 v215, v215, v213\n"
"v_cvt_pk_bf16_f32 v168, v212, v213\n"
"v_mfma_f32_32x32x16_bf16 a[32:47], a[228:231], v[192:195], a[32:47]\n"
"ds_read_b128 a[244:247], v224 offset:62464\n"
"v_exp_f32 v210, v12\n"
"v_exp_f32 v211, v13\n"
"v_add_f32 v214, v214, v208\n"
"v_mfma_f32_32x32x16_bf16 a[48:63], a[228:231], v[196:199], a[48:63]\n"
"ds_read_b128 a[192:195], v224 offset:16384\n"
"v_add_f32 v215, v215, v209\n"
"v_cvt_pk_bf16_f32 v169, v208, v209\n"
"s_waitcnt lgkmcnt(3)\n"
"v_mfma_f32_32x32x16_bf16 a[64:79], a[232:235], v[192:195], a[64:79]\n"
"ds_read_b128 a[248:251], v224 offset:63488\n"
"buffer_load_dwordx4 v251, s[96:99], s36 offen offset:2048 sc1 lds\n"
"v_exp_f32 v212, v14\n"
"v_add_f32 v214, v214, v210\n"
"v_mfma_f32_32x32x16_bf16 a[80:95], a[232:235], v[196:199], a[80:95]\n"
"ds_read_b128 a[196:199], v224 offset:17408\n"
"v_exp_f32 v213, v15\n"
"v_add_f32 v215, v215, v211\n"
"v_cvt_pk_bf16_f32 v170, v210, v211\n"
"v_mfma_f32_32x32x16_bf16 a[96:111], a[236:239], v[192:195], a[96:111]\n"
"ds_read_b128 a[252:255], v224 offset:64512\n"
"buffer_load_dwordx4 v251, s[96:99], s36 offen offset:3072 sc1 lds\n"
"v_exp_f32 v208, v24\n"
"v_add_f32 v214, v214, v212\n"
"v_mfma_f32_32x32x16_bf16 a[112:127], a[236:239], v[196:199], a[112:127]\n"
"ds_read_b128 a[200:203], v224 offset:18432\n"
"v_exp_f32 v209, v25\n"
"v_add_f32 v215, v215, v213\n"
"s_add_u32 m0, s57, 65536\n"
"s_waitcnt lgkmcnt(5)\n"
"v_mfma_f32_32x32x16_bf16 a[0:15], a[240:243], v[200:203], a[0:15]\n"
"buffer_load_dwordx4 v251, s[96:99], s37 offen offset:0 sc1 lds\n"
"v_exp_f32 v210, v26\n"
"v_cvt_pk_bf16_f32 v171, v212, v213\n"
"v_add_f32 v216, v216, v208\n"
"v_mfma_f32_32x32x16_bf16 a[16:31], a[240:243], v[204:207], a[16:31]\n"
"ds_read_b128 a[204:207], v224 offset:19456\n"
"v_exp_f32 v211, v27\n"
"v_add_f32 v217, v217, v209\n"
"v_cvt_pk_bf16_f32 v172, v208, v209\n"
"v_mfma_f32_32x32x16_bf16 a[32:47], a[244:247], v[200:203], a[32:47]\n"
"buffer_load_dwordx4 v251, s[96:99], s37 offen offset:1024 sc1 lds\n"
"v_exp_f32 v212, v28\n"
"v_add_f32 v216, v216, v210\n"
"v_mfma_f32_32x32x16_bf16 a[48:63], a[244:247], v[204:207], a[48:63]\n"
"ds_read_b128 a[208:211], v224 offset:20480\n"
"v_exp_f32 v213, v29\n"
"v_add_f32 v217, v217, v211\n"
"s_add_u32 s33, s33, 1\n"
"s_waitcnt lgkmcnt(3)\n"
"v_mfma_f32_32x32x16_bf16 a[64:79], a[248:251], v[200:203], a[64:79]\n"
"buffer_load_dwordx4 v251, s[96:99], s37 offen offset:2048 sc1 lds\n"
"v_exp_f32 v208, v30\n"
"v_cvt_pk_bf16_f32 v173, v210, v211\n"
"v_add_f32 v216, v216, v212\n"
"v_mfma_f32_32x32x16_bf16 a[80:95], a[248:251], v[204:207], a[80:95]\n"
"ds_read_b128 a[212:215], v224 offset:21504\n"
"v_exp_f32 v209, v31\n"
"v_add_f32 v217, v217, v213\n"
"s_add_u32 s32, s32, 1\n"
"v_mfma_f32_32x32x16_bf16 a[96:111], a[252:255], v[200:203], a[96:111]\n"
"buffer_load_dwordx4 v251, s[96:99], s37 offen offset:3072 sc1 lds\n"
"buffer_store_dwordx4 v[232:235], v251, s[96:99], s50 offen sc1\n"
"v_cvt_pk_bf16_f32 v174, v212, v213\n"
"v_add_f32 v216, v216, v208\n"
"v_add_f32 v217, v217, v209\n"
"v_mfma_f32_32x32x16_bf16 a[112:127], a[252:255], v[204:207], a[112:127]\n"
"ds_read_b128 a[216:219], v224 offset:22528\n"
"buffer_store_dwordx4 v[232:235], v251, s[96:99], s50 offen offset:1024 sc1\n"
"v_cvt_pk_bf16_f32 v175, v208, v209\n"
"s_bitcmp1_b32 s26, s32\n"
"s_cbranch_scc1 DISP_1_%=\n"
"MID_1_%=:\n"
"v_exp_f32 v208, v32\n"
"v_exp_f32 v209, v33\n"
"s_waitcnt lgkmcnt(6)\n"
"v_mfma_f32_32x32x16_bf16 v[64:79], a[192:195], a[128:131], v[128:143]\n"
"v_exp_f32 v210, v34\n"
"v_exp_f32 v211, v35\n"
"v_add_f32 v214, v214, v208\n"
"v_mfma_f32_32x32x16_bf16 v[80:95], a[192:195], a[160:163], v[144:159]\n"
"ds_read_b128 a[220:223], v224 offset:23552\n"
"v_exp_f32 v212, v36\n"
"v_add_f32 v215, v215, v209\n"
"v_cvt_pk_bf16_f32 v192, v208, v209\n"
"s_waitcnt lgkmcnt(4)\n"
"v_mfma_f32_32x32x16_bf16 v[64:79], a[196:199], a[132:135], v[64:79]\n"
"v_exp_f32 v213, v37\n"
"v_add_f32 v214, v214, v210\n"
"v_add_f32 v215, v215, v211\n"
"v_mfma_f32_32x32x16_bf16 v[80:95], a[196:199], a[164:167], v[80:95]\n"
"ds_read_b128 a[224:227], v224 offset:24576\n"
"v_exp_f32 v208, v38\n"
"v_cvt_pk_bf16_f32 v193, v210, v211\n"
"v_mfma_f32_32x32x16_bf16 v[64:79], a[200:203], a[136:139], v[64:79]\n"
"v_exp_f32 v209, v39\n"
"v_add_f32 v214, v214, v212\n"
"v_add_f32 v215, v215, v213\n"
"v_cvt_pk_bf16_f32 v194, v212, v213\n"
"v_mfma_f32_32x32x16_bf16 v[80:95], a[200:203], a[168:171], v[80:95]\n"
"ds_read_b128 a[228:231], v224 offset:25600\n"
"v_exp_f32 v210, v48\n"
"v_add_f32 v214, v214, v208\n"
"v_mfma_f32_32x32x16_bf16 v[64:79], a[204:207], a[140:143], v[64:79]\n"
"v_exp_f32 v211, v49\n"
"v_add_f32 v215, v215, v209\n"
"v_cvt_pk_bf16_f32 v195, v208, v209\n"
"v_mfma_f32_32x32x16_bf16 v[80:95], a[204:207], a[172:175], v[80:95]\n"
"ds_read_b128 a[232:235], v224 offset:26624\n"
"v_exp_f32 v212, v50\n"
"v_add_f32 v216, v216, v210\n"
"s_waitcnt lgkmcnt(3)\n"
"v_mfma_f32_32x32x16_bf16 v[64:79], a[208:211], a[144:147], v[64:79]\n"
"v_exp_f32 v213, v51\n"
"v_add_f32 v217, v217, v211\n"
"v_cvt_pk_bf16_f32 v196, v210, v211\n"
"v_mfma_f32_32x32x16_bf16 v[80:95], a[208:211], a[176:179], v[80:95]\n"
"ds_read_b128 a[236:239], v224 offset:27648\n"
"v_exp_f32 v208, v52\n"
"v_add_f32 v216, v216, v212\n"
"v_mfma_f32_32x32x16_bf16 v[64:79], a[212:215], a[148:151], v[64:79]\n"
"v_exp_f32 v209, v53\n"
"v_add_f32 v217, v217, v213\n"
"v_cvt_pk_bf16_f32 v197, v212, v213\n"
"v_mfma_f32_32x32x16_bf16 v[80:95], a[212:215], a[180:183], v[80:95]\n"
"ds_read_b128 a[240:243], v224 offset:28672\n"
"v_exp_f32 v210, v54\n"
"v_add_f32 v216, v216, v208\n"
"v_mfma_f32_32x32x16_bf16 v[64:79], a[216:219], a[152:155], v[64:79]\n"
"v_exp_f32 v211, v55\n"
"v_add_f32 v217, v217, v209\n"
"v_cvt_pk_bf16_f32 v198, v208, v209\n"
"v_mfma_f32_32x32x16_bf16 v[80:95], a[216:219], a[184:187], v[80:95]\n"
"ds_read_b128 a[244:247], v224 offset:29696\n"
"v_exp_f32 v212, v40\n"
"v_add_f32 v216, v216, v210\n"
"v_mfma_f32_32x32x16_bf16 v[64:79], a[220:223], a[156:159], v[64:79]\n"
"v_exp_f32 v213, v41\n"
"v_add_f32 v217, v217, v211\n"
"v_cvt_pk_bf16_f32 v199, v210, v211\n"
"v_mfma_f32_32x32x16_bf16 v[80:95], a[220:223], a[188:191], v[80:95]\n"
"ds_read_b128 a[248:251], v224 offset:30720\n"
"v_exp_f32 v208, v42\n"
"v_add_f32 v214, v214, v212\n"
"s_waitcnt lgkmcnt(3)\n"
"v_mfma_f32_32x32x16_bf16 v[96:111], a[224:227], a[128:131], v[128:143]\n"
"v_exp_f32 v209, v43\n"
"v_add_f32 v215, v215, v213\n"
"v_cvt_pk_bf16_f32 v200, v212, v213\n"
"v_mfma_f32_32x32x16_bf16 v[112:127], a[224:227], a[160:163], v[144:159]\n"
"ds_read_b128 a[252:255], v224 offset:31744\n"
"v_exp_f32 v210, v44\n"
"v_add_f32 v214, v214, v208\n"
"v_mfma_f32_32x32x16_bf16 v[96:111], a[228:231], a[132:135], v[96:111]\n"
"v_exp_f32 v211, v45\n"
"v_readlane_b32 s34, v229, s32\n"
"v_readlane_b32 s35, v229, s33\n"
"v_add_f32 v215, v215, v209\n"
"v_mfma_f32_32x32x16_bf16 v[112:127], a[228:231], a[164:167], v[112:127]\n"
"v_exp_f32 v212, v46\n"
"v_readlane_b32 s36, v230, s32\n"
"v_readlane_b32 s37, v230, s33\n"
"v_cvt_pk_bf16_f32 v201, v208, v209\n"
"v_mfma_f32_32x32x16_bf16 v[96:111], a[232:235], a[136:139], v[96:111]\n"
"v_exp_f32 v213, v47\n"
"v_add_f32 v214, v214, v210\n"
"v_add_f32 v215, v215, v211\n"
"s_add_u32 s50, s50, 2048\n"
"v_mfma_f32_32x32x16_bf16 v[112:127], a[232:235], a[168:171], v[112:127]\n"
"v_exp_f32 v208, v56\n"
"v_cvt_pk_bf16_f32 v202, v210, v211\n"
"s_cmp_ge_u32 s50, s51\n"
"s_cselect_b32 s50, 1073741824, s50\n"
"v_mfma_f32_32x32x16_bf16 v[96:111], a[236:239], a[140:143], v[96:111]\n"
"v_exp_f32 v209, v57\n"
"v_add_f32 v214, v214, v212\n"
"s_waitcnt vmcnt(0) lgkmcnt(0)\n"
"s_barrier\n"
"v_mfma_f32_32x32x16_bf16 v[112:127], a[236:239], a[172:175], v[112:127]\n"
"v_exp_f32 v210, v58\n"
"v_add_f32 v215, v215, v213\n"
"v_cvt_pk_bf16_f32 v203, v212, v213\n"
"v_add_f32 v216, v216, v208\n"
"v_mfma_f32_32x32x16_bf16 v[96:111], a[240:243], a[144:147], v[96:111]\n"
"ds_read_b128 a[192:195], v224 offset:32768\n"
"v_exp_f32 v211, v59\n"
"v_add_f32 v217, v217, v209\n"
"s_add_u32 m0, s57, 49152\n"
"v_mfma_f32_32x32x16_bf16 v[112:127], a[240:243], a[176:179], v[112:127]\n"
"buffer_load_dwordx4 v251, s[96:99], s34 offen offset:0 sc1 lds\n"
"v_exp_f32 v212, v60\n"
"v_cvt_pk_bf16_f32 v204, v208, v209\n"
"v_add_f32 v216, v216, v210\n"
"v_mfma_f32_32x32x16_bf16 v[96:111], a[244:247], a[148:151], v[96:111]\n"
"ds_read_b128 a[196:199], v224 offset:33792\n"
"v_exp_f32 v213, v61\n"
"v_add_f32 v217, v217, v211\n"
"v_mfma_f32_32x32x16_bf16 v[112:127], a[244:247], a[180:183], v[112:127]\n"
"buffer_load_dwordx4 v251, s[96:99], s34 offen offset:1024 sc1 lds\n"
"v_exp_f32 v208, v62\n"
"v_cvt_pk_bf16_f32 v205, v210, v211\n"
"v_add_f32 v216, v216, v212\n"
"v_mfma_f32_32x32x16_bf16 v[96:111], a[248:251], a[152:155], v[96:111]\n"
"ds_read_b128 a[200:203], v224 offset:34816\n"
"v_exp_f32 v209, v63\n"
"v_add_f32 v217, v217, v213\n"
"v_mfma_f32_32x32x16_bf16 v[112:127], a[248:251], a[184:187], v[112:127]\n"
"v_cvt_pk_bf16_f32 v206, v212, v213\n"
"v_add_f32 v216, v216, v208\n"
"v_add_f32 v217, v217, v209\n"
"v_cvt_pk_bf16_f32 v207, v208, v209\n"
"v_mfma_f32_32x32x16_bf16 v[96:111], a[252:255], a[156:159], v[96:111]\n"
"ds_read_b128 a[204:207], v224 offset:35840\n"
"v_add_f32 v218, v214, v215\n"
"v_add_f32 v219, v216, v217\n"
"v_mfma_f32_32x32x16_bf16 v[112:127], a[252:255], a[188:191], v[112:127]\n"
"buffer_load_dwordx4 v251, s[96:99], s34 offen offset:2048 sc1 lds\n"
"v_max_f32 v236, v218, v219\n"
"v_cmp_nge_f32 vcc, s42, v236\n"
"s_nop 0\n"
"s_cbranch_vccnz STUB_MID_1_%=\n"
"RET_MID_1_%=:\n"
"s_waitcnt lgkmcnt(2)\n"
"v_mfma_f32_32x32x16_bf16 a[0:15], a[192:195], v[160:163], a[0:15]\n"
"ds_read_b128 a[208:211], v224 offset:36864\n"
"v_exp_f32 v208, v64\n"
"v_exp_f32 v209, v65\n"
"v_add_f32 v220, v220, v218\n"
"v_mfma_f32_32x32x16_bf16 a[16:31], a[192:195], v[164:167], a[16:31]\n"
"buffer_load_dwordx4 v251, s[96:99], s34 offen offset:3072 sc1 lds\n"
"v_exp_f32 v210, v66\n"
"v_exp_f32 v211, v67\n"
"v_mov_b32 v214, v208\n"
"v_add_f32 v221, v221, v219\n"
"v_mfma_f32_32x32x16_bf16 a[32:47], a[196:199], v[160:163], a[32:47]\n"
"ds_read_b128 a[212:215], v224 offset:37888\n"
"v_mov_b32 v215, v209\n"
"v_cvt_pk_bf16_f32 v176, v208, v209\n"
"s_add_u32 m0, s57, 114688\n"
"v_mfma_f32_32x32x16_bf16 a[48:63], a[196:199], v[164:167], a[48:63]\n"
"buffer_load_dwordx4 v251, s[96:99], s35 offen offset:0 sc1 lds\n"
"v_exp_f32 v212, v68\n"
"v_add_f32 v214, v214, v210\n"
"s_waitcnt lgkmcnt(2)\n"
"v_mfma_f32_32x32x16_bf16 a[64:79], a[200:203], v[160:163], a[64:79]\n"
"ds_read_b128 a[216:219], v224 offset:38912\n"
"v_exp_f32 v213, v69\n"
"v_add_f32 v215, v215, v211\n"
"v_cvt_pk_bf16_f32 v177, v210, v211\n"
"v_mfma_f32_32x32x16_bf16 a[80:95], a[200:203], v[164:167], a[80:95]\n"
"v_exp_f32 v208, v70\n"
"v_exp_f32 v209, v71\n"
"v_add_f32 v214, v214, v212\n"
"v_mfma_f32_32x32x16_bf16 a[96:111], a[204:207], v[160:163], a[96:111]\n"
"ds_read_b128 a[220:223], v224 offset:39936\n"
"v_exp_f32 v210, v80\n"
"v_add_f32 v215, v215, v213\n"
"v_cvt_pk_bf16_f32 v178, v212, v213\n"
"v_mfma_f32_32x32x16_bf16 a[112:127], a[204:207], v[164:167], a[112:127]\n"
"buffer_load_dwordx4 v251, s[96:99], s35 offen offset:1024 sc1 lds\n"
"v_exp_f32 v211, v81\n"
"v_add_f32 v214, v214, v208\n"
"s_waitcnt lgkmcnt(2)\n"
"v_mfma_f32_32x32x16_bf16 a[0:15], a[208:211], v[168:171], a[0:15]\n"
"ds_read_b128 a[224:227], v224 offset:40960\n"
"v_exp_f32 v212, v82\n"
"v_add_f32 v215, v215, v209\n"
"v_cvt_pk_bf16_f32 v179, v208, v209\n"
"v_mfma_f32_32x32x16_bf16 a[16:31], a[208:211], v[172:175], a[16:31]\n"
"buffer_load_dwordx4 v251, s[96:99], s35 offen offset:2048 sc1 lds\n"
"v_exp_f32 v213, v83\n"
"v_mov_b32 v216, v210\n"
"v_mov_b32 v217, v211\n"
"v_mfma_f32_32x32x16_bf16 a[32:47], a[212:215], v[168:171], a[32:47]\n"
"ds_read_b128 a[228:231], v224 offset:41984\n"
"v_exp_f32 v208, v84\n"
"v_cvt_pk_bf16_f32 v180, v210, v211\n"
"v_add_f32 v216, v216, v212\n"
"v_mfma_f32_32x32x16_bf16 a[48:63], a[212:215], v[172:175], a[48:63]\n"
"buffer_load_dwordx4 v251, s[96:99], s35 offen offset:3072 sc1 lds\n"
"v_exp_f32 v209, v85\n"
"v_add_f32 v217, v217, v213\n"
"s_waitcnt lgkmcnt(2)\n"
"v_mfma_f32_32x32x16_bf16 a[64:79], a[216:219], v[168:171], a[64:79]\n"
"ds_read_b128 a[232:235], v224 offset:43008\n"
"v_exp_f32 v210, v86\n"
"v_cvt_pk_bf16_f32 v181, v212, v213\n"
"s_add_u32 m0, s57, 16384\n"
"v_mfma_f32_32x32x16_bf16 a[80:95], a[216:219], v[172:175], a[80:95]\n"
"buffer_load_dwordx4 v251, s[96:99], s36 offen offset:0 sc1 lds\n"
"v_exp_f32 v211, v87\n"
"v_add_f32 v216, v216, v208\n"
"v_add_f32 v217, v217, v209\n"
"v_mfma_f32_32x32x16_bf16 a[96:111], a[220:223], v[168:171], a[96:111]\n"
"ds_read_b128 a[236:239], v224 offset:44032\n"
"v_exp_f32 v212, v72\n"
"v_cvt_pk_bf16_f32 v182, v208, v209\n"
"v_add_f32 v216, v216, v210\n"
"v_mfma_f32_32x32x16_bf16 a[112:127], a[220:223], v[172:175], a[112:127]\n"
"v_exp_f32 v213, v73\n"
"v_add_f32 v217, v217, v211\n"
"v_cvt_pk_bf16_f32 v183, v210, v211\n"
"s_waitcnt lgkmcnt(2)\n"
"v_mfma_f32_32x32x16_bf16 a[0:15], a[224:227], v[192:195], a[0:15]\n"
"ds_read_b128 a[240:243], v224 offset:45056\n"
"v_exp_f32 v208, v74\n"
"v_add_f32 v214, v214, v212\n"
"v_mfma_f32_32x32x16_bf16 a[16:31], a[224:227], v[196:199], a[16:31]\n"
"buffer_load_dwordx4 v251, s[96:99], s36 offen offset:1024 sc1 lds\n"
"v_exp_f32 v209, v75\n"
"v_add_f32 v215, v215, v213\n"
"v_cvt_pk_bf16_f32 v184, v212, v213\n"
"v_mfma_f32_32x32x16_bf16 a[32:47], a[228:231], v[192:195], a[32:47]\n"
"ds_read_b128 a[244:247], v224 offset:46080\n"
"v_exp_f32 v210, v76\n"
"v_exp_f32 v211, v77\n"
"v_add_f32 v214, v214, v208\n"
"v_mfma_f32_32x32x16_bf16 a[48:63], a[228:231], v[196:199], a[48:63]\n"
"ds_read_b128 a[192:195], v224 offset:0\n"
"v_add_f32 v215, v215, v209\n"
"v_cvt_pk_bf16_f32 v185, v208, v209\n"
"s_waitcnt lgkmcnt(3)\n"
"v_mfma_f32_32x32x16_bf16 a[64:79], a[232:235], v[192:195], a[64:79]\n"
"ds_read_b128 a[248:251], v224 offset:47104\n"
"buffer_load_dwordx4 v251, s[96:99], s36 offen offset:2048 sc1 lds\n"
"v_exp_f32 v212, v78\n"
"v_add_f32 v214, v214, v210\n"
"v_mfma_f32_32x32x16_bf16 a[80:95], a[232:235], v[196:199], a[80:95]\n"
"ds_read_b128 a[196:199], v224 offset:1024\n"
"v_exp_f32 v213, v79\n"
"v_add_f32 v215, v215, v211\n"
"v_cvt_pk_bf16_f32 v186, v210, v211\n"
"v_mfma_f32_32x32x16_bf16 a[96:111], a[236:239], v[192:195], a[96:111]\n"
"ds_read_b128 a[252:255], v224 offset:48128\n"
"buffer_load_dwordx4 v251, s[96:99], s36 offen offset:3072 sc1 lds\n"
"v_exp_f32 v208, v88\n"
"v_add_f32 v214, v214, v212\n"
"v_mfma_f32_32x32x16_bf16 a[112:127], a[236:239], v[196:199], a[112:127]\n"
"ds_read_b128 a[200:203], v224 offset:2048\n"
"v_exp_f32 v209, v89\n"
"v_add_f32 v215, v215, v213\n"
"s_add_u32 m0, s57, 81920\n"
"s_waitcnt lgkmcnt(5)\n"
"v_mfma_f32_32x32x16_bf16 a[0:15], a[240:243], v[200:203], a[0:15]\n"
"buffer_load_dwordx4 v251, s[96:99], s37 offen offset:0 sc1 lds\n"
"v_exp_f32 v210, v90\n"
"v_cvt_pk_bf16_f32 v187, v212, v213\n"
"v_add_f32 v216, v216, v208\n"
"v_mfma_f32_32x32x16_bf16 a[16:31], a[240:243], v[204:207], a[16:31]\n"
"ds_read_b128 a[204:207], v224 offset:3072\n"
"v_exp_f32 v211, v91\n"
"v_add_f32 v217, v217, v209\n"
"v_cvt_pk_bf16_f32 v188, v208, v209\n"
"v_mfma_f32_32x32x16_bf16 a[32:47], a[244:247], v[200:203], a[32:47]\n"
"buffer_load_dwordx4 v251, s[96:99], s37 offen offset:1024 sc1 lds\n"
"v_exp_f32 v212, v92\n"
"v_add_f32 v216, v216, v210\n"
"v_mfma_f32_32x32x16_bf16 a[48:63], a[244:247], v[204:207], a[48:63]\n"
"ds_read_b128 a[208:211], v224 offset:4096\n"
"v_exp_f32 v213, v93\n"
"v_add_f32 v217, v217, v211\n"
"s_add_u32 s33, s33, 1\n"
"s_waitcnt lgkmcnt(3)\n"
"v_mfma_f32_32x32x16_bf16 a[64:79], a[248:251], v[200:203], a[64:79]\n"
"buffer_load_dwordx4 v251, s[96:99], s37 offen offset:2048 sc1 lds\n"
"v_exp_f32 v208, v94\n"
"v_cvt_pk_bf16_f32 v189, v210, v211\n"
"v_add_f32 v216, v216, v212\n"
"v_mfma_f32_32x32x16_bf16 a[80:95], a[248:251], v[204:207], a[80:95]\n"
"ds_read_b128 a[212:215], v224 offset:5120\n"
"v_exp_f32 v209, v95\n"
"v_add_f32 v217, v217, v213\n"
"s_add_u32 s32, s32, 1\n"
"v_mfma_f32_32x32x16_bf16 a[96:111], a[252:255], v[200:203], a[96:111]\n"
"buffer_load_dwordx4 v251, s[96:99], s37 offen offset:3072 sc1 lds\n"
"buffer_store_dwordx4 v[232:235], v251, s[96:99], s50 offen sc1\n"
"v_cvt_pk_bf16_f32 v190, v212, v213\n"
"v_add_f32 v216, v216, v208\n"
"v_add_f32 v217, v217, v209\n"
"v_mfma_f32_32x32x16_bf16 a[112:127], a[252:255], v[204:207], a[112:127]\n"
"ds_read_b128 a[216:219], v224 offset:6144\n"
"buffer_store_dwordx4 v[232:235], v251, s[96:99], s50 offen offset:1024 sc1\n"
"v_cvt_pk_bf16_f32 v191, v208, v209\n"
"s_bitcmp1_b32 s26, s32\n"
"s_cbranch_scc1 DISP_0_%=\n"
"s_branch MID_0_%=\n"
"DISP_0_%=:\n"
"s_bitcmp1_b32 s11, s32\n"
"s_cbranch_scc1 MIDR_0_%=\n"
"s_cmp_eq_u32 s32, s27\n"
"s_cbranch_scc1 DRAINA_0_%=\n"
"s_cmp_eq_u32 s32, s13\n"
"s_cbranch_scc1 FIRST2M_0_%=\n"
"s_branch MIDM_0_%=\n"
"DISP_1_%=:\n"
"s_bitcmp1_b32 s11, s32\n"
"s_cbranch_scc1 MIDR_1_%=\n"
"s_cmp_eq_u32 s32, 17\n"
"s_cbranch_scc0 DISP_1B_%=\n"
"s_cmp_eq_u32 s13, 17\n"
"s_cbranch_scc1 FINALZ_%=\n"
"s_branch FINAL_1_%=\n"
"DISP_1B_%=:\n"
"s_cmp_eq_u32 s32, s27\n"
"s_cbranch_scc1 DRAINA_1_%=\n"
"s_cmp_eq_u32 s32, s13\n"
"s_cbranch_scc1 FIRST2_1_%=\n"
"s_branch MIDM_1_%=\n"
"PUBRT_%=:\n"
"s_lshl_b32 s39, %1, 3\n"
"s_add_u32 s39, s39, 105920512\n"
"v_min_u32 v237, 1, v252\n"
"v_lshlrev_b32 v237, 30, v237\n"
"v_mov_b32 v238, s14\n"
"v_mov_b32 v239, s15\n"
"buffer_store_dwordx2 v[238:239], v237, s[96:99], s39 offen sc1\n"
"s_branch PUBRT_RET_%=\n"
"MIDR_0_%=:\n"
"v_exp_f32 v208, v96\n"
"v_exp_f32 v209, v97\n"
"s_waitcnt lgkmcnt(6)\n"
"v_mfma_f32_32x32x16_bf16 v[0:15], a[192:195], a[128:131], v[128:143]\n"
"v_exp_f32 v210, v98\n"
"v_exp_f32 v211, v99\n"
"v_add_f32 v214, v214, v208\n"
"v_mfma_f32_32x32x16_bf16 v[16:31], a[192:195], a[160:163], v[144:159]\n"
"ds_read_b128 a[220:223], v224 offset:7168\n"
"v_exp_f32 v212, v100\n"
"v_add_f32 v215, v215, v209\n"
"v_cvt_pk_bf16_f32 v192, v208, v209\n"
"s_waitcnt lgkmcnt(4)\n"
"v_mfma_f32_32x32x16_bf16 v[0:15], a[196:199], a[132:135], v[0:15]\n"
"v_exp_f32 v213, v101\n"
"v_add_f32 v214, v214, v210\n"
"v_add_f32 v215, v215, v211\n"
"v_mfma_f32_32x32x16_bf16 v[16:31], a[196:199], a[164:167], v[16:31]\n"
"ds_read_b128 a[224:227], v224 offset:8192\n"
"v_exp_f32 v208, v102\n"
"v_cvt_pk_bf16_f32 v193, v210, v211\n"
"v_add_f32 v214, v214, v212\n"
"v_mfma_f32_32x32x16_bf16 v[0:15], a[200:203], a[136:139], v[0:15]\n"
"v_exp_f32 v209, v103\n"
"v_add_f32 v215, v215, v213\n"
"v_cvt_pk_bf16_f32 v194, v212, v213\n"
"v_mfma_f32_32x32x16_bf16 v[16:31], a[200:203], a[168:171], v[16:31]\n"
"ds_read_b128 a[228:231], v224 offset:9216\n"
"v_exp_f32 v210, v112\n"
"v_readlane_b32 s46, v254, 20\n"
"v_add_f32 v214, v214, v208\n"
"v_mfma_f32_32x32x16_bf16 v[0:15], a[204:207], a[140:143], v[0:15]\n"
"v_exp_f32 v211, v113\n"
"v_add_f32 v215, v215, v209\n"
"v_cvt_pk_bf16_f32 v195, v208, v209\n"
"v_mfma_f32_32x32x16_bf16 v[16:31], a[204:207], a[172:175], v[16:31]\n"
"ds_read_b128 a[232:235], v224 offset:10240\n"
"v_exp_f32 v212, v114\n"
"v_readlane_b32 s47, v254, 24\n"
"s_waitcnt lgkmcnt(3)\n"
"v_mfma_f32_32x32x16_bf16 v[0:15], a[208:211], a[144:147], v[0:15]\n"
"v_exp_f32 v213, v115\n"
"v_add_f32 v216, v216, v210\n"
"v_add_f32 v217, v217, v211\n"
"v_cvt_pk_bf16_f32 v196, v210, v211\n"
"v_mfma_f32_32x32x16_bf16 v[16:31], a[208:211], a[176:179], v[16:31]\n"
"ds_read_b128 a[236:239], v224 offset:11264\n"
"v_exp_f32 v208, v116\n"
"v_add_f32 v216, v216, v212\n"
"v_mfma_f32_32x32x16_bf16 v[0:15], a[212:215], a[148:151], v[0:15]\n"
"v_exp_f32 v209, v117\n"
"v_mov_b32_dpp v254, v254 row_shl:1 row_mask:0xf bank_mask:0xf\n"
"v_add_f32 v217, v217, v213\n"
"v_cvt_pk_bf16_f32 v197, v212, v213\n"
"v_mfma_f32_32x32x16_bf16 v[16:31], a[212:215], a[180:183], v[16:31]\n"
"ds_read_b128 a[240:243], v224 offset:12288\n"
"v_exp_f32 v210, v118\n"
"v_add_f32 v216, v216, v208\n"
"v_mfma_f32_32x32x16_bf16 v[0:15], a[216:219], a[152:155], v[0:15]\n"
"v_exp_f32 v211, v119\n"
"v_add_f32 v217, v217, v209\n"
"v_cvt_pk_bf16_f32 v198, v208, v209\n"
"v_mfma_f32_32x32x16_bf16 v[16:31], a[216:219], a[184:187], v[16:31]\n"
"ds_read_b128 a[244:247], v224 offset:13312\n"
"v_exp_f32 v212, v104\n"
"v_add_f32 v216, v216, v210\n"
"v_mfma_f32_32x32x16_bf16 v[0:15], a[220:223], a[156:159], v[0:15]\n"
"v_exp_f32 v213, v105\n"
"v_add_f32 v217, v217, v211\n"
"v_cvt_pk_bf16_f32 v199, v210, v211\n"
"v_mfma_f32_32x32x16_bf16 v[16:31], a[220:223], a[188:191], v[16:31]\n"
"ds_read_b128 a[248:251], v224 offset:14336\n"
"v_exp_f32 v208, v106\n"
"v_add_f32 v214, v214, v212\n"
"s_waitcnt lgkmcnt(3)\n"
"v_mfma_f32_32x32x16_bf16 v[32:47], a[224:227], a[128:131], v[128:143]\n"
"v_exp_f32 v209, v107\n"
"v_add_f32 v215, v215, v213\n"
"v_cvt_pk_bf16_f32 v200, v212, v213\n"
"v_mfma_f32_32x32x16_bf16 v[48:63], a[224:227], a[160:163], v[144:159]\n"
"ds_read_b128 a[252:255], v224 offset:15360\n"
"v_exp_f32 v210, v108\n"
"v_add_f32 v214, v214, v208\n"
"v_mfma_f32_32x32x16_bf16 v[32:47], a[228:231], a[132:135], v[32:47]\n"
"v_exp_f32 v211, v109\n"
"v_readlane_b32 s34, v229, s32\n"
"v_readlane_b32 s35, v229, s33\n"
"v_add_f32 v215, v215, v209\n"
"v_mfma_f32_32x32x16_bf16 v[48:63], a[228:231], a[164:167], v[48:63]\n"
"v_exp_f32 v212, v110\n"
"v_readlane_b32 s36, v230, s32\n"
"v_readlane_b32 s37, v230, s33\n"
"v_cvt_pk_bf16_f32 v201, v208, v209\n"
"v_mfma_f32_32x32x16_bf16 v[32:47], a[232:235], a[136:139], v[32:47]\n"
"v_exp_f32 v213, v111\n"
"v_add_f32 v214, v214, v210\n"
"v_add_f32 v215, v215, v211\n"
"s_add_u32 s50, s50, 2048\n"
"v_mfma_f32_32x32x16_bf16 v[48:63], a[232:235], a[168:171], v[48:63]\n"
"v_exp_f32 v208, v120\n"
"v_cvt_pk_bf16_f32 v202, v210, v211\n"
"s_cmp_ge_u32 s50, s51\n"
"s_cselect_b32 s50, 1073741824, s50\n"
"v_mfma_f32_32x32x16_bf16 v[32:47], a[236:239], a[140:143], v[32:47]\n"
"v_exp_f32 v209, v121\n"
"v_add_f32 v214, v214, v212\n"
"s_waitcnt vmcnt(0) lgkmcnt(0)\n"
"s_barrier\n"
"v_mfma_f32_32x32x16_bf16 v[48:63], a[236:239], a[172:175], v[48:63]\n"
"v_exp_f32 v210, v122\n"
"v_add_f32 v215, v215, v213\n"
"v_cvt_pk_bf16_f32 v203, v212, v213\n"
"v_add_f32 v216, v216, v208\n"
"v_mfma_f32_32x32x16_bf16 v[32:47], a[240:243], a[144:147], v[32:47]\n"
"ds_read_b128 a[192:195], v224 offset:49152\n"
"v_exp_f32 v211, v123\n"
"v_add_f32 v217, v217, v209\n"
"PG9_%=:\n"
"s_cbranch_execz RPG9_%=\n"
"s_add_u32 m0, s57, 32768\n"
"v_mfma_f32_32x32x16_bf16 v[48:63], a[240:243], a[176:179], v[48:63]\n"
"buffer_load_dwordx4 v251, s[96:99], s34 offen offset:0 sc1 lds\n"
"v_exp_f32 v212, v124\n"
"v_cvt_pk_bf16_f32 v204, v208, v209\n"
"v_mfma_f32_32x32x16_bf16 v[32:47], a[244:247], a[148:151], v[32:47]\n"
"ds_read_b128 a[196:199], v224 offset:50176\n"
"v_exp_f32 v213, v125\n"
"v_add_f32 v216, v216, v210\n"
"v_add_f32 v217, v217, v211\n"
"v_mfma_f32_32x32x16_bf16 v[48:63], a[244:247], a[180:183], v[48:63]\n"
"buffer_load_dwordx4 v251, s[96:99], s34 offen offset:1024 sc1 lds\n"
"v_exp_f32 v208, v126\n"
"v_cvt_pk_bf16_f32 v205, v210, v211\n"
"v_mfma_f32_32x32x16_bf16 v[32:47], a[248:251], a[152:155], v[32:47]\n"
"ds_read_b128 a[200:203], v224 offset:51200\n"
"v_exp_f32 v209, v127\n"
"v_add_f32 v216, v216, v212\n"
"v_add_f32 v217, v217, v213\n"
"v_mfma_f32_32x32x16_bf16 v[48:63], a[248:251], a[184:187], v[48:63]\n"
"v_cvt_pk_bf16_f32 v206, v212, v213\n"
"v_add_f32 v216, v216, v208\n"
"v_add_f32 v217, v217, v209\n"
"v_cvt_pk_bf16_f32 v207, v208, v209\n"
"v_mfma_f32_32x32x16_bf16 v[32:47], a[252:255], a[156:159], v[32:47]\n"
"ds_read_b128 a[204:207], v224 offset:52224\n"
"v_add_f32 v218, v214, v215\n"
"v_add_f32 v219, v216, v217\n"
"v_mfma_f32_32x32x16_bf16 v[48:63], a[252:255], a[188:191], v[48:63]\n"
"buffer_load_dwordx4 v251, s[96:99], s34 offen offset:2048 sc1 lds\n"
"v_max_f32 v236, v218, v219\n"
"v_cmp_nge_f32 vcc, s42, v236\n"
"s_nop 0\n"
"s_cbranch_vccnz STUB_MIDR_0_%=\n"
"RET_MIDR_0_%=:\n"
"s_waitcnt lgkmcnt(2)\n"
"v_mfma_f32_32x32x16_bf16 a[0:15], a[192:195], v[176:179], a[0:15]\n"
"ds_read_b128 a[208:211], v224 offset:53248\n"
"v_exp_f32 v208, v0\n"
"v_exp_f32 v209, v1\n"
"v_add_f32 v220, v220, v218\n"
"v_mfma_f32_32x32x16_bf16 a[16:31], a[192:195], v[180:183], a[16:31]\n"
"buffer_load_dwordx4 v251, s[96:99], s34 offen offset:3072 sc1 lds\n"
"v_exp_f32 v210, v2\n"
"v_exp_f32 v211, v3\n"
"v_mov_b32 v214, v208\n"
"v_add_f32 v221, v221, v219\n"
"v_mfma_f32_32x32x16_bf16 a[32:47], a[196:199], v[176:179], a[32:47]\n"
"ds_read_b128 a[212:215], v224 offset:54272\n"
"v_exp_f32 v212, v4\n"
"v_mov_b32 v215, v209\n"
"v_cvt_pk_bf16_f32 v160, v208, v209\n"
"s_add_u32 m0, s57, 98304\n"
"v_mfma_f32_32x32x16_bf16 a[48:63], a[196:199], v[180:183], a[48:63]\n"
"buffer_load_dwordx4 v251, s[96:99], s35 offen offset:0 sc1 lds\n"
"v_exp_f32 v213, v5\n"
"v_add_f32 v214, v214, v210\n"
"s_waitcnt lgkmcnt(2)\n"
"v_mfma_f32_32x32x16_bf16 a[64:79], a[200:203], v[176:179], a[64:79]\n"
"ds_read_b128 a[216:219], v224 offset:55296\n"
"v_exp_f32 v208, v6\n"
"v_add_f32 v215, v215, v211\n"
"v_cvt_pk_bf16_f32 v161, v210, v211\n"
"v_mfma_f32_32x32x16_bf16 a[80:95], a[200:203], v[180:183], a[80:95]\n"
"v_exp_f32 v209, v7\n"
"v_add_f32 v214, v214, v212\n"
"v_add_f32 v215, v215, v213\n"
"v_cvt_pk_bf16_f32 v162, v212, v213\n"
"v_mfma_f32_32x32x16_bf16 a[96:111], a[204:207], v[176:179], a[96:111]\n"
"ds_read_b128 a[220:223], v224 offset:56320\n"
"v_exp_f32 v210, v16\n"
"v_exp_f32 v211, v17\n"
"v_add_f32 v214, v214, v208\n"
"v_mfma_f32_32x32x16_bf16 a[112:127], a[204:207], v[180:183], a[112:127]\n"
"buffer_load_dwordx4 v251, s[96:99], s35 offen offset:1024 sc1 lds\n"
"v_exp_f32 v212, v18\n"
"v_add_f32 v215, v215, v209\n"
"v_cvt_pk_bf16_f32 v163, v208, v209\n"
"s_waitcnt lgkmcnt(2)\n"
"v_mfma_f32_32x32x16_bf16 a[0:15], a[208:211], v[184:187], a[0:15]\n"
"ds_read_b128 a[224:227], v224 offset:57344\n"
"v_exp_f32 v213, v19\n"
"v_mov_b32 v216, v210\n"
"v_mov_b32 v217, v211\n"
"v_mfma_f32_32x32x16_bf16 a[16:31], a[208:211], v[188:191], a[16:31]\n"
"buffer_load_dwordx4 v251, s[96:99], s35 offen offset:2048 sc1 lds\n"
"v_exp_f32 v208, v20\n"
"v_cvt_pk_bf16_f32 v164, v210, v211\n"
"v_add_f32 v216, v216, v212\n"
"v_mfma_f32_32x32x16_bf16 a[32:47], a[212:215], v[184:187], a[32:47]\n"
"ds_read_b128 a[228:231], v224 offset:58368\n"
"v_exp_f32 v209, v21\n"
"v_add_f32 v217, v217, v213\n"
"v_cvt_pk_bf16_f32 v165, v212, v213\n"
"v_mfma_f32_32x32x16_bf16 a[48:63], a[212:215], v[188:191], a[48:63]\n"
"buffer_load_dwordx4 v251, s[96:99], s35 offen offset:3072 sc1 lds\n"
"v_exp_f32 v210, v22\n"
"v_add_f32 v216, v216, v208\n"
"s_waitcnt lgkmcnt(2)\n"
"v_mfma_f32_32x32x16_bf16 a[64:79], a[216:219], v[184:187], a[64:79]\n"
"ds_read_b128 a[232:235], v224 offset:59392\n"
"v_exp_f32 v211, v23\n"
"v_add_f32 v217, v217, v209\n"
"v_cvt_pk_bf16_f32 v166, v208, v209\n"
"s_add_u32 m0, s57, 0\n"
"v_mfma_f32_32x32x16_bf16 a[80:95], a[216:219], v[188:191], a[80:95]\n"
"buffer_load_dwordx4 v251, s[96:99], s36 offen offset:0 sc1 lds\n"
"v_exp_f32 v212, v8\n"
"v_exp_f32 v213, v9\n"
"v_add_f32 v216, v216, v210\n"
"v_mfma_f32_32x32x16_bf16 a[96:111], a[220:223], v[184:187], a[96:111]\n"
"ds_read_b128 a[236:239], v224 offset:60416\n"
"v_exp_f32 v208, v10\n"
"v_add_f32 v217, v217, v211\n"
"v_cvt_pk_bf16_f32 v167, v210, v211\n"
"v_mfma_f32_32x32x16_bf16 a[112:127], a[220:223], v[188:191], a[112:127]\n"
"v_exp_f32 v209, v11\n"
"v_add_f32 v214, v214, v212\n"
"v_add_f32 v215, v215, v213\n"
"s_waitcnt lgkmcnt(2)\n"
"v_mfma_f32_32x32x16_bf16 a[0:15], a[224:227], v[192:195], a[0:15]\n"
"ds_read_b128 a[240:243], v224 offset:61440\n"
"ds_read_b64_tr_b16 v[176:177], v255 offset:0\n"
"v_exp_f32 v210, v12\n"
"v_cvt_pk_bf16_f32 v168, v212, v213\n"
"v_add_f32 v214, v214, v208\n"
"v_mfma_f32_32x32x16_bf16 a[16:31], a[224:227], v[196:199], a[16:31]\n"
"ds_read_b64_tr_b16 v[178:179], v255 offset:512\n"
"buffer_load_dwordx4 v251, s[96:99], s36 offen offset:1024 sc1 lds\n"
"v_exp_f32 v211, v13\n"
"v_add_f32 v215, v215, v209\n"
"v_mfma_f32_32x32x16_bf16 a[32:47], a[228:231], v[192:195], a[32:47]\n"
"ds_read_b128 a[244:247], v224 offset:62464\n"
"ds_read_b64_tr_b16 v[180:181], v255 offset:8\n"
"v_exp_f32 v212, v14\n"
"v_cvt_pk_bf16_f32 v169, v208, v209\n"
"v_add_f32 v214, v214, v210\n"
"v_mfma_f32_32x32x16_bf16 a[48:63], a[228:231], v[196:199], a[48:63]\n"
"ds_read_b128 a[192:195], v224 offset:16384\n"
"ds_read_b64_tr_b16 v[182:183], v255 offset:520\n"
"v_exp_f32 v213, v15\n"
"v_add_f32 v215, v215, v211\n"
"s_waitcnt lgkmcnt(7)\n"
"v_mfma_f32_32x32x16_bf16 a[64:79], a[232:235], v[192:195], a[64:79]\n"
"ds_read_b128 a[248:251], v224 offset:63488\n"
"ds_read_b64_tr_b16 v[184:185], v255 offset:64\n"
"buffer_load_dwordx4 v251, s[96:99], s36 offen offset:2048 sc1 lds\n"
"v_exp_f32 v208, v24\n"
"v_cvt_pk_bf16_f32 v170, v210, v211\n"
"v_mfma_f32_32x32x16_bf16 a[80:95], a[232:235], v[196:199], a[80:95]\n"
"ds_read_b128 a[196:199], v224 offset:17408\n"
"ds_read_b64_tr_b16 v[186:187], v255 offset:576\n"
"v_exp_f32 v209, v25\n"
"v_add_f32 v214, v214, v212\n"
"v_mfma_f32_32x32x16_bf16 a[96:111], a[236:239], v[192:195], a[96:111]\n"
"ds_read_b128 a[252:255], v224 offset:64512\n"
"ds_read_b64_tr_b16 v[188:189], v255 offset:72\n"
"buffer_load_dwordx4 v251, s[96:99], s36 offen offset:3072 sc1 lds\n"
"v_add_f32 v215, v215, v213\n"
"v_cvt_pk_bf16_f32 v171, v212, v213\n"
"v_mfma_f32_32x32x16_bf16 a[112:127], a[236:239], v[196:199], a[112:127]\n"
"ds_read_b128 a[200:203], v224 offset:18432\n"
"ds_read_b64_tr_b16 v[190:191], v255 offset:584\n"
"v_exp_f32 v210, v26\n"
"s_add_u32 m0, s57, 65536\n"
"s_waitcnt lgkmcnt(10)\n"
"v_mfma_f32_32x32x16_bf16 a[0:15], a[240:243], v[200:203], a[0:15]\n"
"buffer_load_dwordx4 v251, s[96:99], s37 offen offset:0 sc1 lds\n"
"v_exp_f32 v211, v27\n"
"v_add_f32 v216, v216, v208\n"
"v_add_f32 v217, v217, v209\n"
"v_mfma_f32_32x32x16_bf16 a[16:31], a[240:243], v[204:207], a[16:31]\n"
"ds_read_b128 a[204:207], v224 offset:19456\n"
"v_exp_f32 v212, v28\n"
"v_cvt_pk_bf16_f32 v172, v208, v209\n"
"v_add_f32 v216, v216, v210\n"
"v_mfma_f32_32x32x16_bf16 a[32:47], a[244:247], v[200:203], a[32:47]\n"
"buffer_load_dwordx4 v251, s[96:99], s37 offen offset:1024 sc1 lds\n"
"v_exp_f32 v213, v29\n"
"v_add_u32 v255, 4096, v255\n"
"v_add_f32 v217, v217, v211\n"
"v_cvt_pk_bf16_f32 v173, v210, v211\n"
"v_mfma_f32_32x32x16_bf16 a[48:63], a[244:247], v[204:207], a[48:63]\n"
"ds_read_b128 a[208:211], v224 offset:20480\n"
"v_exp_f32 v208, v30\n"
"v_add_f32 v216, v216, v212\n"
"s_add_u32 s33, s33, 1\n"
"s_waitcnt lgkmcnt(4)\n"
"v_mfma_f32_32x32x16_bf16 a[64:79], a[248:251], v[200:203], a[64:79]\n"
"buffer_load_dwordx4 v251, s[96:99], s37 offen offset:2048 sc1 lds\n"
"v_exp_f32 v209, v31\n"
"v_add_f32 v217, v217, v213\n"
"v_cvt_pk_bf16_f32 v174, v212, v213\n"
"v_mfma_f32_32x32x16_bf16 a[80:95], a[248:251], v[204:207], a[80:95]\n"
"ds_read_b128 a[212:215], v224 offset:21504\n"
"v_add_f32 v216, v216, v208\n"
"v_add_f32 v217, v217, v209\n"
"v_cvt_pk_bf16_f32 v175, v208, v209\n"
"s_add_u32 s32, s32, 1\n"
"v_mfma_f32_32x32x16_bf16 a[96:111], a[252:255], v[200:203], a[96:111]\n"
"buffer_load_dwordx4 v251, s[96:99], s37 offen offset:3072 sc1 lds\n"
"buffer_store_dwordx4 v[176:179], v251, s[96:99], s46 offen sc1\n"
"s_add_u32 s46, s46, s47\n"
"buffer_store_dwordx4 v[180:183], v251, s[96:99], s46 offen sc1\n"
"s_add_u32 s46, s46, s47\n"
"buffer_store_dwordx4 v[184:187], v251, s[96:99], s46 offen sc1\n"
"v_mfma_f32_32x32x16_bf16 a[112:127], a[252:255], v[204:207], a[112:127]\n"
"ds_read_b128 a[216:219], v224 offset:22528\n"
"s_add_u32 s46, s46, s47\n"
"s_waitcnt lgkmcnt(3)\n"
"buffer_store_dwordx4 v[188:191], v251, s[96:99], s46 offen sc1\n"
"buffer_store_dwordx4 v[232:235], v251, s[96:99], s50 offen sc1\n"
"buffer_store_dwordx4 v[232:235], v251, s[96:99], s50 offen offset:1024 sc1\n"
"s_bitcmp1_b32 s26, s32\n"
"s_cbranch_scc1 DISP_1_%=\n"
"s_branch MID_1_%=\n"
"MIDR_1_%=:\n"
"v_exp_f32 v208, v32\n"
"v_exp_f32 v209, v33\n"
"s_waitcnt lgkmcnt(6)\n"
"v_mfma_f32_32x32x16_bf16 v[64:79], a[192:195], a[128:131], v[128:143]\n"
"v_exp_f32 v210, v34\n"
"v_exp_f32 v211, v35\n"
"v_add_f32 v214, v214, v208\n"
"v_mfma_f32_32x32x16_bf16 v[80:95], a[192:195], a[160:163], v[144:159]\n"
"ds_read_b128 a[220:223], v224 offset:23552\n"
"v_exp_f32 v212, v36\n"
"v_add_f32 v215, v215, v209\n"
"v_cvt_pk_bf16_f32 v192, v208, v209\n"
"s_waitcnt lgkmcnt(4)\n"
"v_mfma_f32_32x32x16_bf16 v[64:79], a[196:199], a[132:135], v[64:79]\n"
"v_exp_f32 v213, v37\n"
"v_add_f32 v214, v214, v210\n"
"v_add_f32 v215, v215, v211\n"
"v_mfma_f32_32x32x16_bf16 v[80:95], a[196:199], a[164:167], v[80:95]\n"
"ds_read_b128 a[224:227], v224 offset:24576\n"
"v_exp_f32 v208, v38\n"
"v_cvt_pk_bf16_f32 v193, v210, v211\n"
"v_add_f32 v214, v214, v212\n"
"v_mfma_f32_32x32x16_bf16 v[64:79], a[200:203], a[136:139], v[64:79]\n"
"v_exp_f32 v209, v39\n"
"v_add_f32 v215, v215, v213\n"
"v_cvt_pk_bf16_f32 v194, v212, v213\n"
"v_mfma_f32_32x32x16_bf16 v[80:95], a[200:203], a[168:171], v[80:95]\n"
"ds_read_b128 a[228:231], v224 offset:25600\n"
"v_exp_f32 v210, v48\n"
"v_readlane_b32 s46, v254, 20\n"
"v_add_f32 v214, v214, v208\n"
"v_mfma_f32_32x32x16_bf16 v[64:79], a[204:207], a[140:143], v[64:79]\n"
"v_exp_f32 v211, v49\n"
"v_add_f32 v215, v215, v209\n"
"v_cvt_pk_bf16_f32 v195, v208, v209\n"
"v_mfma_f32_32x32x16_bf16 v[80:95], a[204:207], a[172:175], v[80:95]\n"
"ds_read_b128 a[232:235], v224 offset:26624\n"
"v_exp_f32 v212, v50\n"
"v_readlane_b32 s47, v254, 24\n"
"s_waitcnt lgkmcnt(3)\n"
"v_mfma_f32_32x32x16_bf16 v[64:79], a[208:211], a[144:147], v[64:79]\n"
"v_exp_f32 v213, v51\n"
"v_add_f32 v216, v216, v210\n"
"v_add_f32 v217, v217, v211\n"
"v_cvt_pk_bf16_f32 v196, v210, v211\n"
"v_mfma_f32_32x32x16_bf16 v[80:95], a[208:211], a[176:179], v[80:95]\n"
"ds_read_b128 a[236:239], v224 offset:27648\n"
"v_exp_f32 v208, v52\n"
"v_add_f32 v216, v216, v212\n"
"v_mfma_f32_32x32x16_bf16 v[64:79], a[212:215], a[148:151], v[64:79]\n"
"v_exp_f32 v209, v53\n"
"v_mov_b32_dpp v254, v254 row_shl:1 row_mask:0xf bank_mask:0xf\n"
"v_add_f32 v217, v217, v213\n"
"v_cvt_pk_bf16_f32 v197, v212, v213\n"
"v_mfma_f32_32x32x16_bf16 v[80:95], a[212:215], a[180:183], v[80:95]\n"
"ds_read_b128 a[240:243], v224 offset:28672\n"
"v_exp_f32 v210, v54\n"
"v_add_f32 v216, v216, v208\n"
"v_mfma_f32_32x32x16_bf16 v[64:79], a[216:219], a[152:155], v[64:79]\n"
"v_exp_f32 v211, v55\n"
"v_add_f32 v217, v217, v209\n"
"v_cvt_pk_bf16_f32 v198, v208, v209\n"
"v_mfma_f32_32x32x16_bf16 v[80:95], a[216:219], a[184:187], v[80:95]\n"
"ds_read_b128 a[244:247], v224 offset:29696\n"
"v_exp_f32 v212, v40\n"
"v_add_f32 v216, v216, v210\n"
"v_mfma_f32_32x32x16_bf16 v[64:79], a[220:223], a[156:159], v[64:79]\n"
"v_exp_f32 v213, v41\n"
"v_add_f32 v217, v217, v211\n"
"v_cvt_pk_bf16_f32 v199, v210, v211\n"
"v_mfma_f32_32x32x16_bf16 v[80:95], a[220:223], a[188:191], v[80:95]\n"
"ds_read_b128 a[248:251], v224 offset:30720\n"
"v_exp_f32 v208, v42\n"
"v_add_f32 v214, v214, v212\n"
"s_waitcnt lgkmcnt(3)\n"
"v_mfma_f32_32x32x16_bf16 v[96:111], a[224:227], a[128:131], v[128:143]\n"
"v_exp_f32 v209, v43\n"
"v_add_f32 v215, v215, v213\n"
"v_cvt_pk_bf16_f32 v200, v212, v213\n"
"v_mfma_f32_32x32x16_bf16 v[112:127], a[224:227], a[160:163], v[144:159]\n"
"ds_read_b128 a[252:255], v224 offset:31744\n"
"v_exp_f32 v210, v44\n"
"v_add_f32 v214, v214, v208\n"
"v_mfma_f32_32x32x16_bf16 v[96:111], a[228:231], a[132:135], v[96:111]\n"
"v_exp_f32 v211, v45\n"
"v_readlane_b32 s34, v229, s32\n"
"v_readlane_b32 s35, v229, s33\n"
"v_add_f32 v215, v215, v209\n"
"v_mfma_f32_32x32x16_bf16 v[112:127], a[228:231], a[164:167], v[112:127]\n"
"v_exp_f32 v212, v46\n"
"v_readlane_b32 s36, v230, s32\n"
"v_readlane_b32 s37, v230, s33\n"
"v_cvt_pk_bf16_f32 v201, v208, v209\n"
"v_mfma_f32_32x32x16_bf16 v[96:111], a[232:235], a[136:139], v[96:111]\n"
"v_exp_f32 v213, v47\n"
"v_add_f32 v214, v214, v210\n"
"v_add_f32 v215, v215, v211\n"
"s_add_u32 s50, s50, 2048\n"
"v_mfma_f32_32x32x16_bf16 v[112:127], a[232:235], a[168:171], v[112:127]\n"
"v_exp_f32 v208, v56\n"
"v_cvt_pk_bf16_f32 v202, v210, v211\n"
"s_cmp_ge_u32 s50, s51\n"
"s_cselect_b32 s50, 1073741824, s50\n"
"v_mfma_f32_32x32x16_bf16 v[96:111], a[236:239], a[140:143], v[96:111]\n"
"v_exp_f32 v209, v57\n"
"v_add_f32 v214, v214, v212\n"
"s_waitcnt vmcnt(0) lgkmcnt(0)\n"
"s_barrier\n"
"v_mfma_f32_32x32x16_bf16 v[112:127], a[236:239], a[172:175], v[112:127]\n"
"v_exp_f32 v210, v58\n"
"v_add_f32 v215, v215, v213\n"
"v_cvt_pk_bf16_f32 v203, v212, v213\n"
"v_add_f32 v216, v216, v208\n"
"v_mfma_f32_32x32x16_bf16 v[96:111], a[240:243], a[144:147], v[96:111]\n"
"ds_read_b128 a[192:195], v224 offset:32768\n"
"v_exp_f32 v211, v59\n"
"v_add_f32 v217, v217, v209\n"
"s_add_u32 m0, s57, 49152\n"
"v_mfma_f32_32x32x16_bf16 v[112:127], a[240:243], a[176:179], v[112:127]\n"
"buffer_load_dwordx4 v251, s[96:99], s34 offen offset:0 sc1 lds\n"
"v_exp_f32 v212, v60\n"
"v_cvt_pk_bf16_f32 v204, v208, v209\n"
"v_mfma_f32_32x32x16_bf16 v[96:111], a[244:247], a[148:151], v[96:111]\n"
"ds_read_b128 a[196:199], v224 offset:33792\n"
"v_exp_f32 v213, v61\n"
"v_add_f32 v216, v216, v210\n"
"v_add_f32 v217, v217, v211\n"
"v_mfma_f32_32x32x16_bf16 v[112:127], a[244:247], a[180:183], v[112:127]\n"
"buffer_load_dwordx4 v251, s[96:99], s34 offen offset:1024 sc1 lds\n"
"v_exp_f32 v208, v62\n"
"v_cvt_pk_bf16_f32 v205, v210, v211\n"
"v_mfma_f32_32x32x16_bf16 v[96:111], a[248:251], a[152:155], v[96:111]\n"
"ds_read_b128 a[200:203], v224 offset:34816\n"
"v_exp_f32 v209, v63\n"
"v_add_f32 v216, v216, v212\n"
"v_add_f32 v217, v217, v213\n"
"v_mfma_f32_32x32x16_bf16 v[112:127], a[248:251], a[184:187], v[112:127]\n"
"v_cvt_pk_bf16_f32 v206, v212, v213\n"
"v_add_f32 v216, v216, v208\n"
"v_add_f32 v217, v217, v209\n"
"v_cvt_pk_bf16_f32 v207, v208, v209\n"
"v_mfma_f32_32x32x16_bf16 v[96:111], a[252:255], a[156:159], v[96:111]\n"
"ds_read_b128 a[204:207], v224 offset:35840\n"
"v_add_f32 v218, v214, v215\n"
"v_add_f32 v219, v216, v217\n"
"v_mfma_f32_32x32x16_bf16 v[112:127], a[252:255], a[188:191], v[112:127]\n"
"buffer_load_dwordx4 v251, s[96:99], s34 offen offset:2048 sc1 lds\n"
"v_max_f32 v236, v218, v219\n"
"v_cmp_nge_f32 vcc, s42, v236\n"
"s_nop 0\n"
"s_cbranch_vccnz STUB_MIDR_1_%=\n"
"RET_MIDR_1_%=:\n"
"s_waitcnt lgkmcnt(2)\n"
"v_mfma_f32_32x32x16_bf16 a[0:15], a[192:195], v[160:163], a[0:15]\n"
"ds_read_b128 a[208:211], v224 offset:36864\n"
"v_exp_f32 v208, v64\n"
"v_exp_f32 v209, v65\n"
"v_add_f32 v220, v220, v218\n"
"v_mfma_f32_32x32x16_bf16 a[16:31], a[192:195], v[164:167], a[16:31]\n"
"buffer_load_dwordx4 v251, s[96:99], s34 offen offset:3072 sc1 lds\n"
"v_exp_f32 v210, v66\n"
"v_exp_f32 v211, v67\n"
"v_mov_b32 v214, v208\n"
"v_add_f32 v221, v221, v219\n"
"v_mfma_f32_32x32x16_bf16 a[32:47], a[196:199], v[160:163], a[32:47]\n"
"ds_read_b128 a[212:215], v224 offset:37888\n"
"v_exp_f32 v212, v68\n"
"v_mov_b32 v215, v209\n"
"v_cvt_pk_bf16_f32 v176, v208, v209\n"
"s_add_u32 m0, s57, 114688\n"
"v_mfma_f32_32x32x16_bf16 a[48:63], a[196:199], v[164:167], a[48:63]\n"
"buffer_load_dwordx4 v251, s[96:99], s35 offen offset:0 sc1 lds\n"
"v_exp_f32 v213, v69\n"
"v_add_f32 v214, v214, v210\n"
"s_waitcnt lgkmcnt(2)\n"
"v_mfma_f32_32x32x16_bf16 a[64:79], a[200:203], v[160:163], a[64:79]\n"
"ds_read_b128 a[216:219], v224 offset:38912\n"
"v_exp_f32 v208, v70\n"
"v_add_f32 v215, v215, v211\n"
"v_cvt_pk_bf16_f32 v177, v210, v211\n"
"v_mfma_f32_32x32x16_bf16 a[80:95], a[200:203], v[164:167], a[80:95]\n"
"v_exp_f32 v209, v71\n"
"v_add_f32 v214, v214, v212\n"
"v_add_f32 v215, v215, v213\n"
"v_cvt_pk_bf16_f32 v178, v212, v213\n"
"v_mfma_f32_32x32x16_bf16 a[96:111], a[204:207], v[160:163], a[96:111]\n"
"ds_read_b128 a[220:223], v224 offset:39936\n"
"v_exp_f32 v210, v80\n"
"v_exp_f32 v211, v81\n"
"v_add_f32 v214, v214, v208\n"
"v_mfma_f32_32x32x16_bf16 a[112:127], a[204:207], v[164:167], a[112:127]\n"
"buffer_load_dwordx4 v251, s[96:99], s35 offen offset:1024 sc1 lds\n"
"v_exp_f32 v212, v82\n"
"v_add_f32 v215, v215, v209\n"
"v_cvt_pk_bf16_f32 v179, v208, v209\n"
"s_waitcnt lgkmcnt(2)\n"
"v_mfma_f32_32x32x16_bf16 a[0:15], a[208:211], v[168:171], a[0:15]\n"
"ds_read_b128 a[224:227], v224 offset:40960\n"
"v_exp_f32 v213, v83\n"
"v_mov_b32 v216, v210\n"
"v_mov_b32 v217, v211\n"
"v_mfma_f32_32x32x16_bf16 a[16:31], a[208:211], v[172:175], a[16:31]\n"
"buffer_load_dwordx4 v251, s[96:99], s35 offen offset:2048 sc1 lds\n"
"v_exp_f32 v208, v84\n"
"v_cvt_pk_bf16_f32 v180, v210, v211\n"
"v_add_f32 v216, v216, v212\n"
"v_mfma_f32_32x32x16_bf16 a[32:47], a[212:215], v[168:171], a[32:47]\n"
"ds_read_b128 a[228:231], v224 offset:41984\n"
"v_exp_f32 v209, v85\n"
"v_add_f32 v217, v217, v213\n"
"v_cvt_pk_bf16_f32 v181, v212, v213\n"
"v_mfma_f32_32x32x16_bf16 a[48:63], a[212:215], v[172:175], a[48:63]\n"
"buffer_load_dwordx4 v251, s[96:99], s35 offen offset:3072 sc1 lds\n"
"v_exp_f32 v210, v86\n"
"v_add_f32 v216, v216, v208\n"
"s_waitcnt lgkmcnt(2)\n"
"v_mfma_f32_32x32x16_bf16 a[64:79], a[216:219], v[168:171], a[64:79]\n"
"ds_read_b128 a[232:235], v224 offset:43008\n"
"v_exp_f32 v211, v87\n"
"v_add_f32 v217, v217, v209\n"
"v_cvt_pk_bf16_f32 v182, v208, v209\n"
"s_add_u32 m0, s57, 16384\n"
"v_mfma_f32_32x32x16_bf16 a[80:95], a[216:219], v[172:175], a[80:95]\n"
"buffer_load_dwordx4 v251, s[96:99], s36 offen offset:0 sc1 lds\n"
"v_exp_f32 v212, v72\n"
"v_exp_f32 v213, v73\n"
"v_add_f32 v216, v216, v210\n"
"v_mfma_f32_32x32x16_bf16 a[96:111], a[220:223], v[168:171], a[96:111]\n"
"ds_read_b128 a[236:239], v224 offset:44032\n"
"v_exp_f32 v208, v74\n"
"v_add_f32 v217, v217, v211\n"
"v_cvt_pk_bf16_f32 v183, v210, v211\n"
"v_mfma_f32_32x32x16_bf16 a[112:127], a[220:223], v[172:175], a[112:127]\n"
"v_exp_f32 v209, v75\n"
"v_add_f32 v214, v214, v212\n"
"v_add_f32 v215, v215, v213\n"
"s_waitcnt lgkmcnt(2)\n"
"v_mfma_f32_32x32x16_bf16 a[0:15], a[224:227], v[192:195], a[0:15]\n"
"ds_read_b128 a[240:243], v224 offset:45056\n"
"ds_read_b64_tr_b16 v[160:161], v255 offset:0\n"
"v_exp_f32 v210, v76\n"
"v_cvt_pk_bf16_f32 v184, v212, v213\n"
"v_add_f32 v214, v214, v208\n"
"v_mfma_f32_32x32x16_bf16 a[16:31], a[224:227], v[196:199], a[16:31]\n"
"ds_read_b64_tr_b16 v[162:163], v255 offset:512\n"
"buffer_load_dwordx4 v251, s[96:99], s36 offen offset:1024 sc1 lds\n"
"v_exp_f32 v211, v77\n"
"v_add_f32 v215, v215, v209\n"
"v_mfma_f32_32x32x16_bf16 a[32:47], a[228:231], v[192:195], a[32:47]\n"
"ds_read_b128 a[244:247], v224 offset:46080\n"
"ds_read_b64_tr_b16 v[164:165], v255 offset:8\n"
"v_exp_f32 v212, v78\n"
"v_cvt_pk_bf16_f32 v185, v208, v209\n"
"v_add_f32 v214, v214, v210\n"
"v_mfma_f32_32x32x16_bf16 a[48:63], a[228:231], v[196:199], a[48:63]\n"
"ds_read_b128 a[192:195], v224 offset:0\n"
"ds_read_b64_tr_b16 v[166:167], v255 offset:520\n"
"v_exp_f32 v213, v79\n"
"v_add_f32 v215, v215, v211\n"
"s_waitcnt lgkmcnt(7)\n"
"v_mfma_f32_32x32x16_bf16 a[64:79], a[232:235], v[192:195], a[64:79]\n"
"ds_read_b128 a[248:251], v224 offset:47104\n"
"ds_read_b64_tr_b16 v[168:169], v255 offset:64\n"
"buffer_load_dwordx4 v251, s[96:99], s36 offen offset:2048 sc1 lds\n"
"v_exp_f32 v208, v88\n"
"v_cvt_pk_bf16_f32 v186, v210, v211\n"
"v_mfma_f32_32x32x16_bf16 a[80:95], a[232:235], v[196:199], a[80:95]\n"
"ds_read_b128 a[196:199], v224 offset:1024\n"
"ds_read_b64_tr_b16 v[170:171], v255 offset:576\n"
"v_exp_f32 v209, v89\n"
"v_add_f32 v214, v214, v212\n"
"v_mfma_f32_32x32x16_bf16 a[96:111], a[236:239], v[192:195], a[96:111]\n"
"ds_read_b128 a[252:255], v224 offset:48128\n"
"ds_read_b64_tr_b16 v[172:173], v255 offset:72\n"
"buffer_load_dwordx4 v251, s[96:99], s36 offen offset:3072 sc1 lds\n"
"v_add_f32 v215, v215, v213\n"
"v_cvt_pk_bf16_f32 v187, v212, v213\n"
"v_mfma_f32_32x32x16_bf16 a[112:127], a[236:239], v[196:199], a[112:127]\n"
"ds_read_b128 a[200:203], v224 offset:2048\n"
"ds_read_b64_tr_b16 v[174:175], v255 offset:584\n"
"v_exp_f32 v210, v90\n"
"s_add_u32 m0, s57, 81920\n"
"s_waitcnt lgkmcnt(10)\n"
"v_mfma_f32_32x32x16_bf16 a[0:15], a[240:243], v[200:203], a[0:15]\n"
"buffer_load_dwordx4 v251, s[96:99], s37 offen offset:0 sc1 lds\n"
"v_exp_f32 v211, v91\n"
"v_add_f32 v216, v216, v208\n"
"v_add_f32 v217, v217, v209\n"
"v_mfma_f32_32x32x16_bf16 a[16:31], a[240:243], v[204:207], a[16:31]\n"
"ds_read_b128 a[204:207], v224 offset:3072\n"
"v_exp_f32 v212, v92\n"
"v_cvt_pk_bf16_f32 v188, v208, v209\n"
"v_add_f32 v216, v216, v210\n"
"v_mfma_f32_32x32x16_bf16 a[32:47], a[244:247], v[200:203], a[32:47]\n"
"buffer_load_dwordx4 v251, s[96:99], s37 offen offset:1024 sc1 lds\n"
"v_exp_f32 v213, v93\n"
"v_add_u32 v255, 4096, v255\n"
"v_add_f32 v217, v217, v211\n"
"v_cvt_pk_bf16_f32 v189, v210, v211\n"
"v_mfma_f32_32x32x16_bf16 a[48:63], a[244:247], v[204:207], a[48:63]\n"
"ds_read_b128 a[208:211], v224 offset:4096\n"
"v_exp_f32 v208, v94\n"
"v_add_f32 v216, v216, v212\n"
"s_add_u32 s33, s33, 1\n"
"s_waitcnt lgkmcnt(4)\n"
"v_mfma_f32_32x32x16_bf16 a[64:79], a[248:251], v[200:203], a[64:79]\n"
"buffer_load_dwordx4 v251, s[96:99], s37 offen offset:2048 sc1 lds\n"
"v_exp_f32 v209, v95\n"
"v_add_f32 v217, v217, v213\n"
"v_cvt_pk_bf16_f32 v190, v212, v213\n"
"v_mfma_f32_32x32x16_bf16 a[80:95], a[248:251], v[204:207], a[80:95]\n"
"ds_read_b128 a[212:215], v224 offset:5120\n"
"v_add_f32 v216, v216, v208\n"
"v_add_f32 v217, v217, v209\n"
"v_cvt_pk_bf16_f32 v191, v208, v209\n"
"s_add_u32 s32, s32, 1\n"
"v_mfma_f32_32x32x16_bf16 a[96:111], a[252:255], v[200:203], a[96:111]\n"
"buffer_load_dwordx4 v251, s[96:99], s37 offen offset:3072 sc1 lds\n"
"buffer_store_dwordx4 v[160:163], v251, s[96:99], s46 offen sc1\n"
"s_add_u32 s46, s46, s47\n"
"buffer_store_dwordx4 v[164:167], v251, s[96:99], s46 offen sc1\n"
"s_add_u32 s46, s46, s47\n"
"buffer_store_dwordx4 v[168:171], v251, s[96:99], s46 offen sc1\n"
"v_mfma_f32_32x32x16_bf16 a[112:127], a[252:255], v[204:207], a[112:127]\n"
"ds_read_b128 a[216:219], v224 offset:6144\n"
"s_add_u32 s46, s46, s47\n"
"s_waitcnt lgkmcnt(3)\n"
"buffer_store_dwordx4 v[172:175], v251, s[96:99], s46 offen sc1\n"
"buffer_store_dwordx4 v[232:235], v251, s[96:99], s50 offen sc1\n"
"buffer_store_dwordx4 v[232:235], v251, s[96:99], s50 offen offset:1024 sc1\n"
"s_bitcmp1_b32 s26, s32\n"
"s_cbranch_scc1 DISP_0_%=\n"
"s_branch MID_0_%=\n"
"MIDM_0_%=:\n"
"v_exp_f32 v208, v96\n"
"v_exp_f32 v209, v97\n"
"v_cndmask_b32_e64 v0, v227, v128, s[64:65]\n"
"v_cndmask_b32_e64 v1, v227, v128, s[66:67]\n"
"v_cndmask_b32_e64 v2, v227, v128, s[68:69]\n"
"s_waitcnt lgkmcnt(6)\n"
"v_mfma_f32_32x32x16_bf16 v[16:31], a[192:195], a[160:163], v[144:159]\n"
"ds_read_b128 a[220:223], v224 offset:7168\n"
"v_exp_f32 v210, v98\n"
"v_exp_f32 v211, v99\n"
"v_cndmask_b32_e64 v3, v227, v128, s[70:71]\n"
"v_add_f32 v214, v214, v208\n"
"s_waitcnt lgkmcnt(6)\n"
"v_mfma_f32_32x32x16_bf16 v[16:31], a[196:199], a[164:167], v[16:31]\n"
"v_cndmask_b32_e64 v4, v227, v128, s[72:73]\n"
"v_cndmask_b32_e64 v5, v227, v128, s[74:75]\n"
"v_cndmask_b32_e64 v6, v227, v128, s[76:77]\n"
"v_add_f32 v215, v215, v209\n"
"v_cvt_pk_bf16_f32 v192, v208, v209\n"
"s_waitcnt lgkmcnt(4)\n"
"v_mfma_f32_32x32x16_bf16 v[16:31], a[200:203], a[168:171], v[16:31]\n"
"v_exp_f32 v212, v100\n"
"v_exp_f32 v213, v101\n"
"v_cndmask_b32_e64 v7, v227, v128, s[78:79]\n"
"v_cndmask_b32_e64 v8, v227, v128, s[80:81]\n"
"v_add_f32 v214, v214, v210\n"
"v_mfma_f32_32x32x16_bf16 v[16:31], a[204:207], a[172:175], v[16:31]\n"
"ds_read_b128 a[224:227], v224 offset:8192\n"
"v_exp_f32 v208, v102\n"
"v_cndmask_b32_e64 v9, v227, v128, s[82:83]\n"
"v_add_f32 v215, v215, v211\n"
"v_cvt_pk_bf16_f32 v193, v210, v211\n"
"s_waitcnt lgkmcnt(2)\n"
"v_mfma_f32_32x32x16_bf16 v[16:31], a[208:211], a[176:179], v[16:31]\n"
"v_exp_f32 v209, v103\n"
"v_cndmask_b32_e64 v10, v227, v128, s[84:85]\n"
"v_cndmask_b32_e64 v11, v227, v128, s[86:87]\n"
"v_cndmask_b32_e64 v12, v227, v128, s[88:89]\n"
"v_add_f32 v214, v214, v212\n"
"v_mfma_f32_32x32x16_bf16 v[16:31], a[212:215], a[180:183], v[16:31]\n"
"ds_read_b128 a[228:231], v224 offset:9216\n"
"v_exp_f32 v210, v112\n"
"v_cndmask_b32_e64 v13, v227, v128, s[90:91]\n"
"v_add_f32 v215, v215, v213\n"
"v_cvt_pk_bf16_f32 v194, v212, v213\n"
"v_mfma_f32_32x32x16_bf16 v[16:31], a[216:219], a[184:187], v[16:31]\n"
"v_exp_f32 v211, v113\n"
"v_cndmask_b32_e64 v14, v227, v128, s[92:93]\n"
"v_cndmask_b32_e64 v15, v227, v128, s[94:95]\n"
"v_add_f32 v214, v214, v208\n"
"s_waitcnt lgkmcnt(2)\n"
"v_mfma_f32_32x32x16_bf16 v[16:31], a[220:223], a[188:191], v[16:31]\n"
"ds_read_b128 a[232:235], v224 offset:10240\n"
"v_mov_b32 v32, v227\n"
"v_mov_b32 v33, v227\n"
"v_mov_b32 v34, v227\n"
"v_add_f32 v215, v215, v209\n"
"v_mfma_f32_32x32x16_bf16 v[0:15], a[192:195], a[128:131], v[0:15]\n"
"v_mov_b32 v35, v227\n"
"v_mov_b32 v36, v227\n"
"v_mov_b32 v37, v227\n"
"v_mov_b32 v38, v227\n"
"v_mov_b32 v39, v227\n"
"v_cvt_pk_bf16_f32 v195, v208, v209\n"
"v_mfma_f32_32x32x16_bf16 v[0:15], a[196:199], a[132:135], v[0:15]\n"
"ds_read_b128 a[236:239], v224 offset:11264\n"
"v_exp_f32 v212, v114\n"
"v_mov_b32 v40, v227\n"
"v_mov_b32 v41, v227\n"
"v_mov_b32 v42, v227\n"
"v_add_f32 v216, v216, v210\n"
"v_mfma_f32_32x32x16_bf16 v[0:15], a[200:203], a[136:139], v[0:15]\n"
"v_exp_f32 v213, v115\n"
"v_mov_b32 v43, v227\n"
"v_mov_b32 v44, v227\n"
"v_mov_b32 v45, v227\n"
"v_mov_b32 v46, v227\n"
"v_mov_b32 v47, v227\n"
"v_mfma_f32_32x32x16_bf16 v[0:15], a[204:207], a[140:143], v[0:15]\n"
"ds_read_b128 a[240:243], v224 offset:12288\n"
"v_cndmask_b32_e64 v48, v227, v144, s[64:65]\n"
"v_cndmask_b32_e64 v49, v227, v144, s[66:67]\n"
"v_cndmask_b32_e64 v50, v227, v144, s[68:69]\n"
"v_cndmask_b32_e64 v51, v227, v144, s[70:71]\n"
"v_add_f32 v217, v217, v211\n"
"v_mfma_f32_32x32x16_bf16 v[0:15], a[208:211], a[144:147], v[0:15]\n"
"v_exp_f32 v208, v116\n"
"v_cndmask_b32_e64 v52, v227, v144, s[72:73]\n"
"v_cndmask_b32_e64 v53, v227, v144, s[74:75]\n"
"v_cndmask_b32_e64 v54, v227, v144, s[76:77]\n"
"v_cndmask_b32_e64 v55, v227, v144, s[78:79]\n"
"v_cvt_pk_bf16_f32 v196, v210, v211\n"
"v_mfma_f32_32x32x16_bf16 v[0:15], a[212:215], a[148:151], v[0:15]\n"
"ds_read_b128 a[244:247], v224 offset:13312\n"
"v_cndmask_b32_e64 v56, v227, v144, s[80:81]\n"
"v_cndmask_b32_e64 v57, v227, v144, s[82:83]\n"
"v_cndmask_b32_e64 v58, v227, v144, s[84:85]\n"
"v_cndmask_b32_e64 v59, v227, v144, s[86:87]\n"
"v_add_f32 v216, v216, v212\n"
"v_mfma_f32_32x32x16_bf16 v[0:15], a[216:219], a[152:155], v[0:15]\n"
"v_exp_f32 v209, v117\n"
"v_cndmask_b32_e64 v60, v227, v144, s[88:89]\n"
"v_cndmask_b32_e64 v61, v227, v144, s[90:91]\n"
"v_cndmask_b32_e64 v62, v227, v144, s[92:93]\n"
"v_cndmask_b32_e64 v63, v227, v144, s[94:95]\n"
"v_mfma_f32_32x32x16_bf16 v[0:15], a[220:223], a[156:159], v[0:15]\n"
"ds_read_b128 a[248:251], v224 offset:14336\n"
"v_exp_f32 v210, v118\n"
"v_add_f32 v217, v217, v213\n"
"v_cvt_pk_bf16_f32 v197, v212, v213\n"
"s_waitcnt lgkmcnt(3)\n"
"v_mfma_f32_32x32x16_bf16 v[32:47], a[224:227], a[128:131], v[32:47]\n"
"v_exp_f32 v211, v119\n"
"v_exp_f32 v212, v104\n"
"v_add_f32 v216, v216, v208\n"
"v_add_f32 v217, v217, v209\n"
"v_cvt_pk_bf16_f32 v198, v208, v209\n"
"v_mfma_f32_32x32x16_bf16 v[48:63], a[224:227], a[160:163], v[48:63]\n"
"ds_read_b128 a[252:255], v224 offset:15360\n"
"v_exp_f32 v213, v105\n"
"v_add_f32 v216, v216, v210\n"
"v_mfma_f32_32x32x16_bf16 v[32:47], a[228:231], a[132:135], v[32:47]\n"
"v_exp_f32 v208, v106\n"
"PG10_%=:\n"
"s_cbranch_execz RPG10_%=\n"
"v_readlane_b32 s34, v229, s32\n"
"v_readlane_b32 s35, v229, s33\n"
"v_add_f32 v217, v217, v211\n"
"v_cvt_pk_bf16_f32 v199, v210, v211\n"
"v_mfma_f32_32x32x16_bf16 v[48:63], a[228:231], a[164:167], v[48:63]\n"
"v_exp_f32 v209, v107\n"
"v_readlane_b32 s36, v230, s32\n"
"v_readlane_b32 s37, v230, s33\n"
"v_add_f32 v214, v214, v212\n"
"v_add_f32 v215, v215, v213\n"
"v_mfma_f32_32x32x16_bf16 v[32:47], a[232:235], a[136:139], v[32:47]\n"
"v_exp_f32 v210, v108\n"
"v_exp_f32 v211, v109\n"
"v_cvt_pk_bf16_f32 v200, v212, v213\n"
"v_add_f32 v214, v214, v208\n"
"s_add_u32 s50, s50, 2048\n"
"v_mfma_f32_32x32x16_bf16 v[48:63], a[232:235], a[168:171], v[48:63]\n"
"v_exp_f32 v212, v110\n"
"v_add_f32 v215, v215, v209\n"
"v_cvt_pk_bf16_f32 v201, v208, v209\n"
"s_cmp_ge_u32 s50, s51\n"
"s_cselect_b32 s50, 1073741824, s50\n"
"v_mfma_f32_32x32x16_bf16 v[32:47], a[236:239], a[140:143], v[32:47]\n"
"v_exp_f32 v213, v111\n"
"v_add_f32 v214, v214, v210\n"
"s_waitcnt vmcnt(0) lgkmcnt(0)\n"
"s_barrier\n"
"v_mfma_f32_32x32x16_bf16 v[48:63], a[236:239], a[172:175], v[48:63]\n"
"v_exp_f32 v208, v120\n"
"v_add_f32 v215, v215, v211\n"
"v_cvt_pk_bf16_f32 v202, v210, v211\n"
"v_add_f32 v214, v214, v212\n"
"v_mfma_f32_32x32x16_bf16 v[32:47], a[240:243], a[144:147], v[32:47]\n"
"ds_read_b128 a[192:195], v224 offset:49152\n"
"v_exp_f32 v209, v121\n"
"v_add_f32 v215, v215, v213\n"
"v_cvt_pk_bf16_f32 v203, v212, v213\n"
"s_add_u32 m0, s57, 32768\n"
"v_mfma_f32_32x32x16_bf16 v[48:63], a[240:243], a[176:179], v[48:63]\n"
"buffer_load_dwordx4 v251, s[96:99], s34 offen offset:0 sc1 lds\n"
"v_exp_f32 v210, v122\n"
"v_exp_f32 v211, v123\n"
"v_add_f32 v216, v216, v208\n"
"v_mfma_f32_32x32x16_bf16 v[32:47], a[244:247], a[148:151], v[32:47]\n"
"ds_read_b128 a[196:199], v224 offset:50176\n"
"v_exp_f32 v212, v124\n"
"v_add_f32 v217, v217, v209\n"
"v_cvt_pk_bf16_f32 v204, v208, v209\n"
"v_add_f32 v216, v216, v210\n"
"v_mfma_f32_32x32x16_bf16 v[48:63], a[244:247], a[180:183], v[48:63]\n"
"buffer_load_dwordx4 v251, s[96:99], s34 offen offset:1024 sc1 lds\n"
"v_exp_f32 v213, v125\n"
"v_add_f32 v217, v217, v211\n"
"v_cvt_pk_bf16_f32 v205, v210, v211\n"
"v_mfma_f32_32x32x16_bf16 v[32:47], a[248:251], a[152:155], v[32:47]\n"
"ds_read_b128 a[200:203], v224 offset:51200\n"
"v_exp_f32 v208, v126\n"
"v_exp_f32 v209, v127\n"
"v_add_f32 v216, v216, v212\n"
"v_mfma_f32_32x32x16_bf16 v[48:63], a[248:251], a[184:187], v[48:63]\n"
"v_add_f32 v217, v217, v213\n"
"v_cvt_pk_bf16_f32 v206, v212, v213\n"
"v_add_f32 v216, v216, v208\n"
"v_add_f32 v217, v217, v209\n"
"v_cvt_pk_bf16_f32 v207, v208, v209\n"
"v_mfma_f32_32x32x16_bf16 v[32:47], a[252:255], a[156:159], v[32:47]\n"
"ds_read_b128 a[204:207], v224 offset:52224\n"
"v_add_f32 v218, v214, v215\n"
"v_add_f32 v219, v216, v217\n"
"v_mfma_f32_32x32x16_bf16 v[48:63], a[252:255], a[188:191], v[48:63]\n"
"buffer_load_dwordx4 v251, s[96:99], s34 offen offset:2048 sc1 lds\n"
"v_max_f32 v236, v218, v219\n"
"v_cmp_nge_f32 vcc, s42, v236\n"
"s_nop 0\n"
"s_cbranch_vccnz STUB_MIDM_0_%=\n"
"RET_MIDM_0_%=:\n"
"s_waitcnt lgkmcnt(2)\n"
"v_mfma_f32_32x32x16_bf16 a[0:15], a[192:195], v[176:179], a[0:15]\n"
"ds_read_b128 a[208:211], v224 offset:53248\n"
"v_exp_f32 v208, v0\n"
"v_exp_f32 v209, v1\n"
"v_add_f32 v220, v220, v218\n"
"v_mfma_f32_32x32x16_bf16 a[16:31], a[192:195], v[180:183], a[16:31]\n"
"buffer_load_dwordx4 v251, s[96:99], s34 offen offset:3072 sc1 lds\n"
"v_exp_f32 v210, v2\n"
"v_exp_f32 v211, v3\n"
"v_mov_b32 v214, v208\n"
"v_add_f32 v221, v221, v219\n"
"v_mfma_f32_32x32x16_bf16 a[32:47], a[196:199], v[176:179], a[32:47]\n"
"ds_read_b128 a[212:215], v224 offset:54272\n"
"v_mov_b32 v215, v209\n"
"v_cvt_pk_bf16_f32 v160, v208, v209\n"
"s_add_u32 m0, s57, 98304\n"
"v_mfma_f32_32x32x16_bf16 a[48:63], a[196:199], v[180:183], a[48:63]\n"
"buffer_load_dwordx4 v251, s[96:99], s35 offen offset:0 sc1 lds\n"
"v_exp_f32 v212, v4\n"
"v_add_f32 v214, v214, v210\n"
"s_waitcnt lgkmcnt(2)\n"
"v_mfma_f32_32x32x16_bf16 a[64:79], a[200:203], v[176:179], a[64:79]\n"
"ds_read_b128 a[216:219], v224 offset:55296\n"
"v_exp_f32 v213, v5\n"
"v_add_f32 v215, v215, v211\n"
"v_cvt_pk_bf16_f32 v161, v210, v211\n"
"v_mfma_f32_32x32x16_bf16 a[80:95], a[200:203], v[180:183], a[80:95]\n"
"v_exp_f32 v208, v6\n"
"v_exp_f32 v209, v7\n"
"v_add_f32 v214, v214, v212\n"
"v_mfma_f32_32x32x16_bf16 a[96:111], a[204:207], v[176:179], a[96:111]\n"
"ds_read_b128 a[220:223], v224 offset:56320\n"
"v_exp_f32 v210, v16\n"
"v_add_f32 v215, v215, v213\n"
"v_cvt_pk_bf16_f32 v162, v212, v213\n"
"v_mfma_f32_32x32x16_bf16 a[112:127], a[204:207], v[180:183], a[112:127]\n"
"buffer_load_dwordx4 v251, s[96:99], s35 offen offset:1024 sc1 lds\n"
"v_exp_f32 v211, v17\n"
"v_add_f32 v214, v214, v208\n"
"s_waitcnt lgkmcnt(2)\n"
"v_mfma_f32_32x32x16_bf16 a[0:15], a[208:211], v[184:187], a[0:15]\n"
"ds_read_b128 a[224:227], v224 offset:57344\n"
"v_exp_f32 v212, v18\n"
"v_add_f32 v215, v215, v209\n"
"v_cvt_pk_bf16_f32 v163, v208, v209\n"
"v_mfma_f32_32x32x16_bf16 a[16:31], a[208:211], v[188:191], a[16:31]\n"
"buffer_load_dwordx4 v251, s[96:99], s35 offen offset:2048 sc1 lds\n"
"v_exp_f32 v213, v19\n"
"v_mov_b32 v216, v210\n"
"v_mov_b32 v217, v211\n"
"v_mfma_f32_32x32x16_bf16 a[32:47], a[212:215], v[184:187], a[32:47]\n"
"ds_read_b128 a[228:231], v224 offset:58368\n"
"v_exp_f32 v208, v20\n"
"v_cvt_pk_bf16_f32 v164, v210, v211\n"
"v_add_f32 v216, v216, v212\n"
"v_mfma_f32_32x32x16_bf16 a[48:63], a[212:215], v[188:191], a[48:63]\n"
"buffer_load_dwordx4 v251, s[96:99], s35 offen offset:3072 sc1 lds\n"
"v_exp_f32 v209, v21\n"
"v_add_f32 v217, v217, v213\n"
"s_waitcnt lgkmcnt(2)\n"
"v_mfma_f32_32x32x16_bf16 a[64:79], a[216:219], v[184:187], a[64:79]\n"
"ds_read_b128 a[232:235], v224 offset:59392\n"
"v_exp_f32 v210, v22\n"
"v_cvt_pk_bf16_f32 v165, v212, v213\n"
"s_add_u32 m0, s57, 0\n"
"v_mfma_f32_32x32x16_bf16 a[80:95], a[216:219], v[188:191], a[80:95]\n"
"buffer_load_dwordx4 v251, s[96:99], s36 offen offset:0 sc1 lds\n"
"v_exp_f32 v211, v23\n"
"v_add_f32 v216, v216, v208\n"
"v_add_f32 v217, v217, v209\n"
"v_mfma_f32_32x32x16_bf16 a[96:111], a[220:223], v[184:187], a[96:111]\n"
"ds_read_b128 a[236:239], v224 offset:60416\n"
"v_exp_f32 v212, v8\n"
"v_cvt_pk_bf16_f32 v166, v208, v209\n"
"v_add_f32 v216, v216, v210\n"
"v_mfma_f32_32x32x16_bf16 a[112:127], a[220:223], v[188:191], a[112:127]\n"
"v_exp_f32 v213, v9\n"
"v_add_f32 v217, v217, v211\n"
"v_cvt_pk_bf16_f32 v167, v210, v211\n"
"s_waitcnt lgkmcnt(2)\n"
"v_mfma_f32_32x32x16_bf16 a[0:15], a[224:227], v[192:195], a[0:15]\n"
"ds_read_b128 a[240:243], v224 offset:61440\n"
"v_exp_f32 v208, v10\n"
"v_add_f32 v214, v214, v212\n"
"v_mfma_f32_32x32x16_bf16 a[16:31], a[224:227], v[196:199], a[16:31]\n"
"buffer_load_dwordx4 v251, s[96:99], s36 offen offset:1024 sc1 lds\n"
"v_exp_f32 v209, v11\n"
"v_add_f32 v215, v215, v213\n"
"v_cvt_pk_bf16_f32 v168, v212, v213\n"
"v_mfma_f32_32x32x16_bf16 a[32:47], a[228:231], v[192:195], a[32:47]\n"
"ds_read_b128 a[244:247], v224 offset:62464\n"
"v_exp_f32 v210, v12\n"
"v_exp_f32 v211, v13\n"
"v_add_f32 v214, v214, v208\n"
"v_mfma_f32_32x32x16_bf16 a[48:63], a[228:231], v[196:199], a[48:63]\n"
"ds_read_b128 a[192:195], v224 offset:16384\n"
"v_add_f32 v215, v215, v209\n"
"v_cvt_pk_bf16_f32 v169, v208, v209\n"
"s_waitcnt lgkmcnt(3)\n"
"v_mfma_f32_32x32x16_bf16 a[64:79], a[232:235], v[192:195], a[64:79]\n"
"ds_read_b128 a[248:251], v224 offset:63488\n"
"buffer_load_dwordx4 v251, s[96:99], s36 offen offset:2048 sc1 lds\n"
"v_exp_f32 v212, v14\n"
"v_add_f32 v214, v214, v210\n"
"v_mfma_f32_32x32x16_bf16 a[80:95], a[232:235], v[196:199], a[80:95]\n"
"ds_read_b128 a[196:199], v224 offset:17408\n"
"v_exp_f32 v213, v15\n"
"v_add_f32 v215, v215, v211\n"
"v_cvt_pk_bf16_f32 v170, v210, v211\n"
"v_mfma_f32_32x32x16_bf16 a[96:111], a[236:239], v[192:195], a[96:111]\n"
"ds_read_b128 a[252:255], v224 offset:64512\n"
"buffer_load_dwordx4 v251, s[96:99], s36 offen offset:3072 sc1 lds\n"
"v_exp_f32 v208, v24\n"
"v_add_f32 v214, v214, v212\n"
"v_mfma_f32_32x32x16_bf16 a[112:127], a[236:239], v[196:199], a[112:127]\n"
"ds_read_b128 a[200:203], v224 offset:18432\n"
"v_exp_f32 v209, v25\n"
"v_add_f32 v215, v215, v213\n"
"s_add_u32 m0, s57, 65536\n"
"s_waitcnt lgkmcnt(5)\n"
"v_mfma_f32_32x32x16_bf16 a[0:15], a[240:243], v[200:203], a[0:15]\n"
"buffer_load_dwordx4 v251, s[96:99], s37 offen offset:0 sc1 lds\n"
"v_exp_f32 v210, v26\n"
"v_cvt_pk_bf16_f32 v171, v212, v213\n"
"v_add_f32 v216, v216, v208\n"
"v_mfma_f32_32x32x16_bf16 a[16:31], a[240:243], v[204:207], a[16:31]\n"
"ds_read_b128 a[204:207], v224 offset:19456\n"
"v_exp_f32 v211, v27\n"
"v_add_f32 v217, v217, v209\n"
"v_cvt_pk_bf16_f32 v172, v208, v209\n"
"v_mfma_f32_32x32x16_bf16 a[32:47], a[244:247], v[200:203], a[32:47]\n"
"buffer_load_dwordx4 v251, s[96:99], s37 offen offset:1024 sc1 lds\n"
"v_exp_f32 v212, v28\n"
"v_add_f32 v216, v216, v210\n"
"v_mfma_f32_32x32x16_bf16 a[48:63], a[244:247], v[204:207], a[48:63]\n"
"ds_read_b128 a[208:211], v224 offset:20480\n"
"v_exp_f32 v213, v29\n"
"v_add_f32 v217, v217, v211\n"
"s_add_u32 s33, s33, 1\n"
"s_waitcnt lgkmcnt(3)\n"
"v_mfma_f32_32x32x16_bf16 a[64:79], a[248:251], v[200:203], a[64:79]\n"
"buffer_load_dwordx4 v251, s[96:99], s37 offen offset:2048 sc1 lds\n"
"v_exp_f32 v208, v30\n"
"v_cvt_pk_bf16_f32 v173, v210, v211\n"
"v_add_f32 v216, v216, v212\n"
"v_mfma_f32_32x32x16_bf16 a[80:95], a[248:251], v[204:207], a[80:95]\n"
"ds_read_b128 a[212:215], v224 offset:21504\n"
"v_exp_f32 v209, v31\n"
"v_add_f32 v217, v217, v213\n"
"s_add_u32 s32, s32, 1\n"
"v_mfma_f32_32x32x16_bf16 a[96:111], a[252:255], v[200:203], a[96:111]\n"
"buffer_load_dwordx4 v251, s[96:99], s37 offen offset:3072 sc1 lds\n"
"buffer_store_dwordx4 v[232:235], v251, s[96:99], s50 offen sc1\n"
"v_cvt_pk_bf16_f32 v174, v212, v213\n"
"v_add_f32 v216, v216, v208\n"
"v_add_f32 v217, v217, v209\n"
"v_mfma_f32_32x32x16_bf16 a[112:127], a[252:255], v[204:207], a[112:127]\n"
"ds_read_b128 a[216:219], v224 offset:22528\n"
"buffer_store_dwordx4 v[232:235], v251, s[96:99], s50 offen offset:1024 sc1\n"
"v_cvt_pk_bf16_f32 v175, v208, v209\n"
"s_bitcmp1_b32 s26, s32\n"
"s_cbranch_scc1 DISP_1_%=\n"
"s_branch MID_1_%=\n"
"MIDM_1_%=:\n"
"v_exp_f32 v208, v32\n"
"v_exp_f32 v209, v33\n"
"v_cndmask_b32_e64 v64, v227, v128, s[64:65]\n"
"v_cndmask_b32_e64 v65, v227, v128, s[66:67]\n"
"v_cndmask_b32_e64 v66, v227, v128, s[68:69]\n"
"s_waitcnt lgkmcnt(6)\n"
"v_mfma_f32_32x32x16_bf16 v[80:95], a[192:195], a[160:163], v[144:159]\n"
"ds_read_b128 a[220:223], v224 offset:23552\n"
"v_exp_f32 v210, v34\n"
"v_exp_f32 v211, v35\n"
"v_cndmask_b32_e64 v67, v227, v128, s[70:71]\n"
"v_add_f32 v214, v214, v208\n"
"s_waitcnt lgkmcnt(6)\n"
"v_mfma_f32_32x32x16_bf16 v[80:95], a[196:199], a[164:167], v[80:95]\n"
"v_cndmask_b32_e64 v68, v227, v128, s[72:73]\n"
"v_cndmask_b32_e64 v69, v227, v128, s[74:75]\n"
"v_cndmask_b32_e64 v70, v227, v128, s[76:77]\n"
"v_add_f32 v215, v215, v209\n"
"v_cvt_pk_bf16_f32 v192, v208, v209\n"
"s_waitcnt lgkmcnt(4)\n"
"v_mfma_f32_32x32x16_bf16 v[80:95], a[200:203], a[168:171], v[80:95]\n"
"v_exp_f32 v212, v36\n"
"v_exp_f32 v213, v37\n"
"v_cndmask_b32_e64 v71, v227, v128, s[78:79]\n"
"v_cndmask_b32_e64 v72, v227, v128, s[80:81]\n"
"v_add_f32 v214, v214, v210\n"
"v_mfma_f32_32x32x16_bf16 v[80:95], a[204:207], a[172:175], v[80:95]\n"
"ds_read_b128 a[224:227], v224 offset:24576\n"
"v_exp_f32 v208, v38\n"
"v_cndmask_b32_e64 v73, v227, v128, s[82:83]\n"
"v_add_f32 v215, v215, v211\n"
"v_cvt_pk_bf16_f32 v193, v210, v211\n"
"s_waitcnt lgkmcnt(2)\n"
"v_mfma_f32_32x32x16_bf16 v[80:95], a[208:211], a[176:179], v[80:95]\n"
"v_exp_f32 v209, v39\n"
"v_cndmask_b32_e64 v74, v227, v128, s[84:85]\n"
"v_cndmask_b32_e64 v75, v227, v128, s[86:87]\n"
"v_cndmask_b32_e64 v76, v227, v128, s[88:89]\n"
"v_add_f32 v214, v214, v212\n"
"v_mfma_f32_32x32x16_bf16 v[80:95], a[212:215], a[180:183], v[80:95]\n"
"ds_read_b128 a[228:231], v224 offset:25600\n"
"v_exp_f32 v210, v48\n"
"v_cndmask_b32_e64 v77, v227, v128, s[90:91]\n"
"v_add_f32 v215, v215, v213\n"
"v_cvt_pk_bf16_f32 v194, v212, v213\n"
"v_mfma_f32_32x32x16_bf16 v[80:95], a[216:219], a[184:187], v[80:95]\n"
"v_exp_f32 v211, v49\n"
"v_cndmask_b32_e64 v78, v227, v128, s[92:93]\n"
"v_cndmask_b32_e64 v79, v227, v128, s[94:95]\n"
"v_add_f32 v214, v214, v208\n"
"s_waitcnt lgkmcnt(2)\n"
"v_mfma_f32_32x32x16_bf16 v[80:95], a[220:223], a[188:191], v[80:95]\n"
"ds_read_b128 a[232:235], v224 offset:26624\n"
"v_mov_b32 v96, v227\n"
"v_mov_b32 v97, v227\n"
"v_mov_b32 v98, v227\n"
"v_add_f32 v215, v215, v209\n"
"v_mfma_f32_32x32x16_bf16 v[64:79], a[192:195], a[128:131], v[64:79]\n"
"v_mov_b32 v99, v227\n"
"v_mov_b32 v100, v227\n"
"v_mov_b32 v101, v227\n"
"v_mov_b32 v102, v227\n"
"v_mov_b32 v103, v227\n"
"v_cvt_pk_bf16_f32 v195, v208, v209\n"
"v_mfma_f32_32x32x16_bf16 v[64:79], a[196:199], a[132:135], v[64:79]\n"
"ds_read_b128 a[236:239], v224 offset:27648\n"
"v_exp_f32 v212, v50\n"
"v_mov_b32 v104, v227\n"
"v_mov_b32 v105, v227\n"
"v_mov_b32 v106, v227\n"
"v_add_f32 v216, v216, v210\n"
"v_mfma_f32_32x32x16_bf16 v[64:79], a[200:203], a[136:139], v[64:79]\n"
"v_exp_f32 v213, v51\n"
"v_mov_b32 v107, v227\n"
"v_mov_b32 v108, v227\n"
"v_mov_b32 v109, v227\n"
"v_mov_b32 v110, v227\n"
"v_mov_b32 v111, v227\n"
"v_mfma_f32_32x32x16_bf16 v[64:79], a[204:207], a[140:143], v[64:79]\n"
"ds_read_b128 a[240:243], v224 offset:28672\n"
"v_cndmask_b32_e64 v112, v227, v144, s[64:65]\n"
"v_cndmask_b32_e64 v113, v227, v144, s[66:67]\n"
"v_cndmask_b32_e64 v114, v227, v144, s[68:69]\n"
"v_cndmask_b32_e64 v115, v227, v144, s[70:71]\n"
"v_add_f32 v217, v217, v211\n"
"v_mfma_f32_32x32x16_bf16 v[64:79], a[208:211], a[144:147], v[64:79]\n"
"v_exp_f32 v208, v52\n"
"v_cndmask_b32_e64 v116, v227, v144, s[72:73]\n"
"v_cndmask_b32_e64 v117, v227, v144, s[74:75]\n"
"v_cndmask_b32_e64 v118, v227, v144, s[76:77]\n"
"v_cndmask_b32_e64 v119, v227, v144, s[78:79]\n"
"v_cvt_pk_bf16_f32 v196, v210, v211\n"
"v_mfma_f32_32x32x16_bf16 v[64:79], a[212:215], a[148:151], v[64:79]\n"
"ds_read_b128 a[244:247], v224 offset:29696\n"
"v_cndmask_b32_e64 v120, v227, v144, s[80:81]\n"
"v_cndmask_b32_e64 v121, v227, v144, s[82:83]\n"
"v_cndmask_b32_e64 v122, v227, v144, s[84:85]\n"
"v_cndmask_b32_e64 v123, v227, v144, s[86:87]\n"
"v_add_f32 v216, v216, v212\n"
"v_mfma_f32_32x32x16_bf16 v[64:79], a[216:219], a[152:155], v[64:79]\n"
"v_exp_f32 v209, v53\n"
"v_cndmask_b32_e64 v124, v227, v144, s[88:89]\n"
"v_cndmask_b32_e64 v125, v227, v144, s[90:91]\n"
"v_cndmask_b32_e64 v126, v227, v144, s[92:93]\n"
"v_cndmask_b32_e64 v127, v227, v144, s[94:95]\n"
"v_mfma_f32_32x32x16_bf16 v[64:79], a[220:223], a[156:159], v[64:79]\n"
"ds_read_b128 a[248:251], v224 offset:30720\n"
"v_exp_f32 v210, v54\n"
"v_add_f32 v217, v217, v213\n"
"v_cvt_pk_bf16_f32 v197, v212, v213\n"
"s_waitcnt lgkmcnt(3)\n"
"v_mfma_f32_32x32x16_bf16 v[96:111], a[224:227], a[128:131], v[96:111]\n"
"v_exp_f32 v211, v55\n"
"v_exp_f32 v212, v40\n"
"v_add_f32 v216, v216, v208\n"
"v_add_f32 v217, v217, v209\n"
"v_cvt_pk_bf16_f32 v198, v208, v209\n"
"v_mfma_f32_32x32x16_bf16 v[112:127], a[224:227], a[160:163], v[112:127]\n"
"ds_read_b128 a[252:255], v224 offset:31744\n"
"v_exp_f32 v213, v41\n"
"v_add_f32 v216, v216, v210\n"
"v_mfma_f32_32x32x16_bf16 v[96:111], a[228:231], a[132:135], v[96:111]\n"
"v_exp_f32 v208, v42\n"
"v_readlane_b32 s34, v229, s32\n"
"v_readlane_b32 s35, v229, s33\n"
"v_add_f32 v217, v217, v211\n"
"v_cvt_pk_bf16_f32 v199, v210, v211\n"
"v_mfma_f32_32x32x16_bf16 v[112:127], a[228:231], a[164:167], v[112:127]\n"
"v_exp_f32 v209, v43\n"
"v_readlane_b32 s36, v230, s32\n"
"v_readlane_b32 s37, v230, s33\n"
"v_add_f32 v214, v214, v212\n"
"v_add_f32 v215, v215, v213\n"
"v_mfma_f32_32x32x16_bf16 v[96:111], a[232:235], a[136:139], v[96:111]\n"
"v_exp_f32 v210, v44\n"
"v_exp_f32 v211, v45\n"
"v_cvt_pk_bf16_f32 v200, v212, v213\n"
"v_add_f32 v214, v214, v208\n"
"s_add_u32 s50, s50, 2048\n"
"v_mfma_f32_32x32x16_bf16 v[112:127], a[232:235], a[168:171], v[112:127]\n"
"v_exp_f32 v212, v46\n"
"v_add_f32 v215, v215, v209\n"
"v_cvt_pk_bf16_f32 v201, v208, v209\n"
"s_cmp_ge_u32 s50, s51\n"
"s_cselect_b32 s50, 1073741824, s50\n"
"v_mfma_f32_32x32x16_bf16 v[96:111], a[236:239], a[140:143], v[96:111]\n"
"v_exp_f32 v213, v47\n"
"v_add_f32 v214, v214, v210\n"
"s_waitcnt vmcnt(0) lgkmcnt(0)\n"
"s_barrier\n"
"v_mfma_f32_32x32x16_bf16 v[112:127], a[236:239], a[172:175], v[112:127]\n"
"v_exp_f32 v208, v56\n"
"v_add_f32 v215, v215, v211\n"
"v_cvt_pk_bf16_f32 v202, v210, v211\n"
"v_add_f32 v214, v214, v212\n"
"v_mfma_f32_32x32x16_bf16 v[96:111], a[240:243], a[144:147], v[96:111]\n"
"ds_read_b128 a[192:195], v224 offset:32768\n"
"v_exp_f32 v209, v57\n"
"v_add_f32 v215, v215, v213\n"
"v_cvt_pk_bf16_f32 v203, v212, v213\n"
"s_add_u32 m0, s57, 49152\n"
"v_mfma_f32_32x32x16_bf16 v[112:127], a[240:243], a[176:179], v[112:127]\n"
"buffer_load_dwordx4 v251, s[96:99], s34 offen offset:0 sc1 lds\n"
"v_exp_f32 v210, v58\n"
"v_exp_f32 v211, v59\n"
"v_add_f32 v216, v216, v208\n"
"v_mfma_f32_32x32x16_bf16 v[96:111], a[244:247], a[148:151], v[96:111]\n"
"ds_read_b128 a[196:199], v224 offset:33792\n"
"v_exp_f32 v212, v60\n"
"v_add_f32 v217, v217, v209\n"
"v_cvt_pk_bf16_f32 v204, v208, v209\n"
"v_add_f32 v216, v216, v210\n"
"v_mfma_f32_32x32x16_bf16 v[112:127], a[244:247], a[180:183], v[112:127]\n"
"buffer_load_dwordx4 v251, s[96:99], s34 offen offset:1024 sc1 lds\n"
"v_exp_f32 v213, v61\n"
"v_add_f32 v217, v217, v211\n"
"v_cvt_pk_bf16_f32 v205, v210, v211\n"
"v_mfma_f32_32x32x16_bf16 v[96:111], a[248:251], a[152:155], v[96:111]\n"
"ds_read_b128 a[200:203], v224 offset:34816\n"
"v_exp_f32 v208, v62\n"
"v_exp_f32 v209, v63\n"
"v_add_f32 v216, v216, v212\n"
"v_mfma_f32_32x32x16_bf16 v[112:127], a[248:251], a[184:187], v[112:127]\n"
"v_add_f32 v217, v217, v213\n"
"v_cvt_pk_bf16_f32 v206, v212, v213\n"
"v_add_f32 v216, v216, v208\n"
"v_add_f32 v217, v217, v209\n"
"v_cvt_pk_bf16_f32 v207, v208, v209\n"
"v_mfma_f32_32x32x16_bf16 v[96:111], a[252:255], a[156:159], v[96:111]\n"
"ds_read_b128 a[204:207], v224 offset:35840\n"
"v_add_f32 v218, v214, v215\n"
"v_add_f32 v219, v216, v217\n"
"v_mfma_f32_32x32x16_bf16 v[112:127], a[252:255], a[188:191], v[112:127]\n"
"buffer_load_dwordx4 v251, s[96:99], s34 offen offset:2048 sc1 lds\n"
"v_max_f32 v236, v218, v219\n"
"v_cmp_nge_f32 vcc, s42, v236\n"
"s_nop 0\n"
"s_cbranch_vccnz STUB_MIDM_1_%=\n"
"RET_MIDM_1_%=:\n"
"s_waitcnt lgkmcnt(2)\n"
"v_mfma_f32_32x32x16_bf16 a[0:15], a[192:195], v[160:163], a[0:15]\n"
"ds_read_b128 a[208:211], v224 offset:36864\n"
"v_exp_f32 v208, v64\n"
"v_exp_f32 v209, v65\n"
"v_add_f32 v220, v220, v218\n"
"v_mfma_f32_32x32x16_bf16 a[16:31], a[192:195], v[164:167], a[16:31]\n"
"buffer_load_dwordx4 v251, s[96:99], s34 offen offset:3072 sc1 lds\n"
"v_exp_f32 v210, v66\n"
"v_exp_f32 v211, v67\n"
"v_mov_b32 v214, v208\n"
"v_add_f32 v221, v221, v219\n"
"v_mfma_f32_32x32x16_bf16 a[32:47], a[196:199], v[160:163], a[32:47]\n"
"ds_read_b128 a[212:215], v224 offset:37888\n"
"v_mov_b32 v215, v209\n"
"v_cvt_pk_bf16_f32 v176, v208, v209\n"
"s_add_u32 m0, s57, 114688\n"
"v_mfma_f32_32x32x16_bf16 a[48:63], a[196:199], v[164:167], a[48:63]\n"
"buffer_load_dwordx4 v251, s[96:99], s35 offen offset:0 sc1 lds\n"
"v_exp_f32 v212, v68\n"
"v_add_f32 v214, v214, v210\n"
"s_waitcnt lgkmcnt(2)\n"
"v_mfma_f32_32x32x16_bf16 a[64:79], a[200:203], v[160:163], a[64:79]\n"
"ds_read_b128 a[216:219], v224 offset:38912\n"
"v_exp_f32 v213, v69\n"
"v_add_f32 v215, v215, v211\n"
"v_cvt_pk_bf16_f32 v177, v210, v211\n"
"v_mfma_f32_32x32x16_bf16 a[80:95], a[200:203], v[164:167], a[80:95]\n"
"v_exp_f32 v208, v70\n"
"v_exp_f32 v209, v71\n"
"v_add_f32 v214, v214, v212\n"
"v_mfma_f32_32x32x16_bf16 a[96:111], a[204:207], v[160:163], a[96:111]\n"
"ds_read_b128 a[220:223], v224 offset:39936\n"
"v_exp_f32 v210, v80\n"
"v_add_f32 v215, v215, v213\n"
"v_cvt_pk_bf16_f32 v178, v212, v213\n"
"v_mfma_f32_32x32x16_bf16 a[112:127], a[204:207], v[164:167], a[112:127]\n"
"buffer_load_dwordx4 v251, s[96:99], s35 offen offset:1024 sc1 lds\n"
"v_exp_f32 v211, v81\n"
"v_add_f32 v214, v214, v208\n"
"s_waitcnt lgkmcnt(2)\n"
"v_mfma_f32_32x32x16_bf16 a[0:15], a[208:211], v[168:171], a[0:15]\n"
"ds_read_b128 a[224:227], v224 offset:40960\n"
"v_exp_f32 v212, v82\n"
"v_add_f32 v215, v215, v209\n"
"v_cvt_pk_bf16_f32 v179, v208, v209\n"
"v_mfma_f32_32x32x16_bf16 a[16:31], a[208:211], v[172:175], a[16:31]\n"
"buffer_load_dwordx4 v251, s[96:99], s35 offen offset:2048 sc1 lds\n"
"v_exp_f32 v213, v83\n"
"v_mov_b32 v216, v210\n"
"v_mov_b32 v217, v211\n"
"v_mfma_f32_32x32x16_bf16 a[32:47], a[212:215], v[168:171], a[32:47]\n"
"ds_read_b128 a[228:231], v224 offset:41984\n"
"v_exp_f32 v208, v84\n"
"v_cvt_pk_bf16_f32 v180, v210, v211\n"
"v_add_f32 v216, v216, v212\n"
"v_mfma_f32_32x32x16_bf16 a[48:63], a[212:215], v[172:175], a[48:63]\n"
"buffer_load_dwordx4 v251, s[96:99], s35 offen offset:3072 sc1 lds\n"
"v_exp_f32 v209, v85\n"
"v_add_f32 v217, v217, v213\n"
"s_waitcnt lgkmcnt(2)\n"
"v_mfma_f32_32x32x16_bf16 a[64:79], a[216:219], v[168:171], a[64:79]\n"
"ds_read_b128 a[232:235], v224 offset:43008\n"
"v_exp_f32 v210, v86\n"
"v_cvt_pk_bf16_f32 v181, v212, v213\n"
"s_add_u32 m0, s57, 16384\n"
"v_mfma_f32_32x32x16_bf16 a[80:95], a[216:219], v[172:175], a[80:95]\n"
"buffer_load_dwordx4 v251, s[96:99], s36 offen offset:0 sc1 lds\n"
"v_exp_f32 v211, v87\n"
"v_add_f32 v216, v216, v208\n"
"v_add_f32 v217, v217, v209\n"
"v_mfma_f32_32x32x16_bf16 a[96:111], a[220:223], v[168:171], a[96:111]\n"
"ds_read_b128 a[236:239], v224 offset:44032\n"
"v_exp_f32 v212, v72\n"
"v_cvt_pk_bf16_f32 v182, v208, v209\n"
"v_add_f32 v216, v216, v210\n"
"v_mfma_f32_32x32x16_bf16 a[112:127], a[220:223], v[172:175], a[112:127]\n"
"v_exp_f32 v213, v73\n"
"v_add_f32 v217, v217, v211\n"
"v_cvt_pk_bf16_f32 v183, v210, v211\n"
"s_waitcnt lgkmcnt(2)\n"
"v_mfma_f32_32x32x16_bf16 a[0:15], a[224:227], v[192:195], a[0:15]\n"
"ds_read_b128 a[240:243], v224 offset:45056\n"
"v_exp_f32 v208, v74\n"
"v_add_f32 v214, v214, v212\n"
"v_mfma_f32_32x32x16_bf16 a[16:31], a[224:227], v[196:199], a[16:31]\n"
"buffer_load_dwordx4 v251, s[96:99], s36 offen offset:1024 sc1 lds\n"
"v_exp_f32 v209, v75\n"
"v_add_f32 v215, v215, v213\n"
"v_cvt_pk_bf16_f32 v184, v212, v213\n"
"v_mfma_f32_32x32x16_bf16 a[32:47], a[228:231], v[192:195], a[32:47]\n"
"ds_read_b128 a[244:247], v224 offset:46080\n"
"v_exp_f32 v210, v76\n"
"v_exp_f32 v211, v77\n"
"v_add_f32 v214, v214, v208\n"
"v_mfma_f32_32x32x16_bf16 a[48:63], a[228:231], v[196:199], a[48:63]\n"
"ds_read_b128 a[192:195], v224 offset:0\n"
"v_add_f32 v215, v215, v209\n"
"v_cvt_pk_bf16_f32 v185, v208, v209\n"
"s_waitcnt lgkmcnt(3)\n"
"v_mfma_f32_32x32x16_bf16 a[64:79], a[232:235], v[192:195], a[64:79]\n"
"ds_read_b128 a[248:251], v224 offset:47104\n"
"buffer_load_dwordx4 v251, s[96:99], s36 offen offset:2048 sc1 lds\n"
"v_exp_f32 v212, v78\n"
"v_add_f32 v214, v214, v210\n"
"v_mfma_f32_32x32x16_bf16 a[80:95], a[232:235], v[196:199], a[80:95]\n"
"ds_read_b128 a[196:199], v224 offset:1024\n"
"v_exp_f32 v213, v79\n"
"v_add_f32 v215, v215, v211\n"
"v_cvt_pk_bf16_f32 v186, v210, v211\n"
"v_mfma_f32_32x32x16_bf16 a[96:111], a[236:239], v[192:195], a[96:111]\n"
"ds_read_b128 a[252:255], v224 offset:48128\n"
"buffer_load_dwordx4 v251, s[96:99], s36 offen offset:3072 sc1 lds\n"
"v_exp_f32 v208, v88\n"
"v_add_f32 v214, v214, v212\n"
"v_mfma_f32_32x32x16_bf16 a[112:127], a[236:239], v[196:199], a[112:127]\n"
"ds_read_b128 a[200:203], v224 offset:2048\n"
"v_exp_f32 v209, v89\n"
"v_add_f32 v215, v215, v213\n"
"s_add_u32 m0, s57, 81920\n"
"s_waitcnt lgkmcnt(5)\n"
"v_mfma_f32_32x32x16_bf16 a[0:15], a[240:243], v[200:203], a[0:15]\n"
"buffer_load_dwordx4 v251, s[96:99], s37 offen offset:0 sc1 lds\n"
"v_exp_f32 v210, v90\n"
"v_cvt_pk_bf16_f32 v187, v212, v213\n"
"v_add_f32 v216, v216, v208\n"
"v_mfma_f32_32x32x16_bf16 a[16:31], a[240:243], v[204:207], a[16:31]\n"
"ds_read_b128 a[204:207], v224 offset:3072\n"
"v_exp_f32 v211, v91\n"
"v_add_f32 v217, v217, v209\n"
"v_cvt_pk_bf16_f32 v188, v208, v209\n"
"v_mfma_f32_32x32x16_bf16 a[32:47], a[244:247], v[200:203], a[32:47]\n"
"buffer_load_dwordx4 v251, s[96:99], s37 offen offset:1024 sc1 lds\n"
"v_exp_f32 v212, v92\n"
"v_add_f32 v216, v216, v210\n"
"v_mfma_f32_32x32x16_bf16 a[48:63], a[244:247], v[204:207], a[48:63]\n"
"ds_read_b128 a[208:211], v224 offset:4096\n"
"v_exp_f32 v213, v93\n"
"v_add_f32 v217, v217, v211\n"
"s_add_u32 s33, s33, 1\n"
"s_waitcnt lgkmcnt(3)\n"
"v_mfma_f32_32x32x16_bf16 a[64:79], a[248:251], v[200:203], a[64:79]\n"
"buffer_load_dwordx4 v251, s[96:99], s37 offen offset:2048 sc1 lds\n"
"v_exp_f32 v208, v94\n"
"v_cvt_pk_bf16_f32 v189, v210, v211\n"
"v_add_f32 v216, v216, v212\n"
"v_mfma_f32_32x32x16_bf16 a[80:95], a[248:251], v[204:207], a[80:95]\n"
"ds_read_b128 a[212:215], v224 offset:5120\n"
"v_exp_f32 v209, v95\n"
"v_add_f32 v217, v217, v213\n"
"s_add_u32 s32, s32, 1\n"
"v_mfma_f32_32x32x16_bf16 a[96:111], a[252:255], v[200:203], a[96:111]\n"
"buffer_load_dwordx4 v251, s[96:99], s37 offen offset:3072 sc1 lds\n"
"buffer_store_dwordx4 v[232:235], v251, s[96:99], s50 offen sc1\n"
"v_cvt_pk_bf16_f32 v190, v212, v213\n"
"v_add_f32 v216, v216, v208\n"
"v_add_f32 v217, v217, v209\n"
"v_mfma_f32_32x32x16_bf16 a[112:127], a[252:255], v[204:207], a[112:127]\n"
"ds_read_b128 a[216:219], v224 offset:6144\n"
"buffer_store_dwordx4 v[232:235], v251, s[96:99], s50 offen offset:1024 sc1\n"
"v_cvt_pk_bf16_f32 v191, v208, v209\n"
"s_bitcmp1_b32 s26, s32\n"
"s_cbranch_scc1 DISP_0_%=\n"
"s_branch MID_0_%=\n"
"FIRST_0_%=:\n"
"s_waitcnt lgkmcnt(6)\n"
"v_mfma_f32_32x32x16_bf16 v[0:15], a[192:195], a[128:131], 0\n"
"v_readlane_b32 s46, v254, 22\n"
"v_mfma_f32_32x32x16_bf16 v[16:31], a[192:195], a[160:163], 0\n"
"ds_read_b128 a[220:223], v224 offset:7168\n"
"s_waitcnt lgkmcnt(4)\n"
"v_mfma_f32_32x32x16_bf16 v[0:15], a[196:199], a[132:135], v[0:15]\n"
"v_readlane_b32 s47, v254, 26\n"
"v_mfma_f32_32x32x16_bf16 v[16:31], a[196:199], a[164:167], v[16:31]\n"
"ds_read_b128 a[224:227], v224 offset:8192\n"
"v_mfma_f32_32x32x16_bf16 v[0:15], a[200:203], a[136:139], v[0:15]\n"
"s_lshl_b32 s39, %2, 12\n"
"v_mfma_f32_32x32x16_bf16 v[16:31], a[200:203], a[168:171], v[16:31]\n"
"ds_read_b128 a[228:231], v224 offset:9216\n"
"s_add_u32 s39, s39, 98304\n"
"v_mfma_f32_32x32x16_bf16 v[0:15], a[204:207], a[140:143], v[0:15]\n"
"v_subrev_u32 v248, s39, v255\n"
"v_mfma_f32_32x32x16_bf16 v[16:31], a[204:207], a[172:175], v[16:31]\n"
"ds_read_b128 a[232:235], v224 offset:10240\n"
"s_waitcnt lgkmcnt(3)\n"
"v_mfma_f32_32x32x16_bf16 v[0:15], a[208:211], a[144:147], v[0:15]\n"
"v_mfma_f32_32x32x16_bf16 v[16:31], a[208:211], a[176:179], v[16:31]\n"
"ds_read_b128 a[236:239], v224 offset:11264\n"
"v_mfma_f32_32x32x16_bf16 v[0:15], a[212:215], a[148:151], v[0:15]\n"
"v_mfma_f32_32x32x16_bf16 v[16:31], a[212:215], a[180:183], v[16:31]\n"
"ds_read_b128 a[240:243], v224 offset:12288\n"
"v_mfma_f32_32x32x16_bf16 v[0:15], a[216:219], a[152:155], v[0:15]\n"
"v_mfma_f32_32x32x16_bf16 v[16:31], a[216:219], a[184:187], v[16:31]\n"
"ds_read_b128 a[244:247], v224 offset:13312\n"
"ds_read_b64_tr_b16 v[176:177], v248 offset:0\n"
"s_nop 0\n"
"v_mfma_f32_32x32x16_bf16 v[0:15], a[220:223], a[156:159], v[0:15]\n"
"ds_read_b64_tr_b16 v[178:179], v248 offset:512\n"
"v_mfma_f32_32x32x16_bf16 v[16:31], a[220:223], a[188:191], v[16:31]\n"
"ds_read_b128 a[248:251], v224 offset:14336\n"
"ds_read_b64_tr_b16 v[180:181], v248 offset:8\n"
"s_waitcnt lgkmcnt(6)\n"
"v_mfma_f32_32x32x16_bf16 v[32:47], a[224:227], a[128:131], 0\n"
"ds_read_b64_tr_b16 v[182:183], v248 offset:520\n"
"v_mov_b32 v192, 0\n"
"v_mfma_f32_32x32x16_bf16 v[48:63], a[224:227], a[160:163], 0\n"
"ds_read_b128 a[252:255], v224 offset:15360\n"
"ds_read_b64_tr_b16 v[184:185], v248 offset:64\n"
"v_mov_b32 v193, 0\n"
"v_mfma_f32_32x32x16_bf16 v[32:47], a[228:231], a[132:135], v[32:47]\n"
"ds_read_b64_tr_b16 v[186:187], v248 offset:576\n"
"v_readlane_b32 s34, v229, s32\n"
"v_readlane_b32 s35, v229, s33\n"
"v_mov_b32 v194, 0\n"
"v_mfma_f32_32x32x16_bf16 v[48:63], a[228:231], a[164:167], v[48:63]\n"
"ds_read_b64_tr_b16 v[188:189], v248 offset:72\n"
"v_readlane_b32 s36, v230, s32\n"
"v_readlane_b32 s37, v230, s33\n"
"v_mov_b32 v195, 0\n"
"v_mfma_f32_32x32x16_bf16 v[32:47], a[232:235], a[136:139], v[32:47]\n"
"ds_read_b64_tr_b16 v[190:191], v248 offset:584\n"
"v_mfma_f32_32x32x16_bf16 v[48:63], a[232:235], a[168:171], v[48:63]\n"
"v_mfma_f32_32x32x16_bf16 v[32:47], a[236:239], a[140:143], v[32:47]\n"
"s_waitcnt lgkmcnt(0)\n"
"s_barrier\n"
"v_mfma_f32_32x32x16_bf16 v[48:63], a[236:239], a[172:175], v[48:63]\n"
"v_max_f32 v238, v0, v1\n"
"v_max_f32 v239, v2, v3\n"
"v_mfma_f32_32x32x16_bf16 v[32:47], a[240:243], a[144:147], v[32:47]\n"
"v_max3_f32 v238, v238, v4, v5\n"
"v_max3_f32 v239, v239, v6, v7\n"
"PG11_%=:\n"
"s_cbranch_execz RPG11_%=\n"
"s_add_u32 m0, s57, 32768\n"
"v_mfma_f32_32x32x16_bf16 v[48:63], a[240:243], a[176:179], v[48:63]\n"
"buffer_load_dwordx4 v251, s[96:99], s34 offen offset:0 sc1 lds\n"
"v_max3_f32 v238, v238, v8, v9\n"
"v_max3_f32 v239, v239, v10, v11\n"
"v_mfma_f32_32x32x16_bf16 v[32:47], a[244:247], a[148:151], v[32:47]\n"
"v_max3_f32 v238, v238, v12, v13\n"
"v_max3_f32 v239, v239, v14, v15\n"
"v_mfma_f32_32x32x16_bf16 v[48:63], a[244:247], a[180:183], v[48:63]\n"
"v_max_f32 v240, v16, v17\n"
"v_max_f32 v241, v18, v19\n"
"v_mfma_f32_32x32x16_bf16 v[32:47], a[248:251], a[152:155], v[32:47]\n"
"buffer_load_dwordx4 v251, s[96:99], s34 offen offset:1024 sc1 lds\n"
"v_max3_f32 v240, v240, v20, v21\n"
"v_max3_f32 v241, v241, v22, v23\n"
"v_mfma_f32_32x32x16_bf16 v[48:63], a[248:251], a[184:187], v[48:63]\n"
"v_max3_f32 v240, v240, v24, v25\n"
"v_max3_f32 v241, v241, v26, v27\n"
"v_mfma_f32_32x32x16_bf16 v[32:47], a[252:255], a[156:159], v[32:47]\n"
"v_max3_f32 v240, v240, v28, v29\n"
"v_max3_f32 v241, v241, v30, v31\n"
"v_mfma_f32_32x32x16_bf16 v[48:63], a[252:255], a[188:191], v[48:63]\n"
"buffer_load_dwordx4 v251, s[96:99], s34 offen offset:2048 sc1 lds\n"
"v_mfma_f32_32x32x16_bf16 a[0:15], v[192:195], v[192:195], 0\n"
"v_mfma_f32_32x32x16_bf16 a[16:31], v[192:195], v[192:195], 0\n"
"s_nop 5\n"
"v_max3_f32 v238, v238, v32, v33\n"
"v_max3_f32 v239, v239, v34, v35\n"
"v_max3_f32 v238, v238, v36, v37\n"
"v_max3_f32 v239, v239, v38, v39\n"
"buffer_load_dwordx4 v251, s[96:99], s34 offen offset:3072 sc1 lds\n"
"v_mfma_f32_32x32x16_bf16 a[32:47], v[192:195], v[192:195], 0\n"
"v_max3_f32 v238, v238, v40, v41\n"
"v_max3_f32 v239, v239, v42, v43\n"
"v_max3_f32 v238, v238, v44, v45\n"
"v_max3_f32 v239, v239, v46, v47\n"
"v_mfma_f32_32x32x16_bf16 a[48:63], v[192:195], v[192:195], 0\n"
"v_max3_f32 v240, v240, v48, v49\n"
"v_max3_f32 v241, v241, v50, v51\n"
"v_max3_f32 v240, v240, v52, v53\n"
"v_max3_f32 v241, v241, v54, v55\n"
"s_add_u32 m0, s57, 98304\n"
"s_nop 0\n"
"buffer_load_dwordx4 v251, s[96:99], s35 offen offset:0 sc1 lds\n"
"v_mfma_f32_32x32x16_bf16 a[64:79], v[192:195], v[192:195], 0\n"
"v_max3_f32 v240, v240, v56, v57\n"
"v_max3_f32 v241, v241, v58, v59\n"
"v_max3_f32 v240, v240, v60, v61\n"
"v_max3_f32 v241, v241, v62, v63\n"
"v_mfma_f32_32x32x16_bf16 a[80:95], v[192:195], v[192:195], 0\n"
"v_max_f32 v238, v238, v239\n"
"v_mov_b32 v239, v238\n"
"v_max_f32 v240, v240, v241\n"
"v_mov_b32 v241, v240\n"
"buffer_load_dwordx4 v251, s[96:99], s35 offen offset:1024 sc1 lds\n"
"v_mfma_f32_32x32x16_bf16 a[96:111], v[192:195], v[192:195], 0\n"
"v_permlane32_swap_b32 v238, v239\n"
"v_permlane32_swap_b32 v240, v241\n"
"v_max_f32 v222, v238, v239\n"
"v_max_f32 v222, 0xf149f2ca, v222\n"
"v_mfma_f32_32x32x16_bf16 a[112:127], v[192:195], v[192:195], 0\n"
"v_mov_b32 v220, 0\n"
"v_max_f32 v223, v240, v241\n"
"v_max_f32 v223, 0xf149f2ca, v223\n"
"v_mov_b32 v221, 0\n"
"buffer_load_dwordx4 v251, s[96:99], s35 offen offset:2048 sc1 lds\n"
"v_sub_f32 v128, 0, v222\n"
"v_sub_f32 v129, 0, v222\n"
"v_pk_mul_f32 v[130:131], v[128:129], 1.0 op_sel_hi:[1,0]\n"
"v_pk_mul_f32 v[132:133], v[128:129], 1.0 op_sel_hi:[1,0]\n"
"v_pk_mul_f32 v[134:135], v[128:129], 1.0 op_sel_hi:[1,0]\n"
"v_pk_mul_f32 v[136:137], v[128:129], 1.0 op_sel_hi:[1,0]\n"
"v_pk_mul_f32 v[138:139], v[128:129], 1.0 op_sel_hi:[1,0]\n"
"v_pk_mul_f32 v[140:141], v[128:129], 1.0 op_sel_hi:[1,0]\n"
"v_pk_mul_f32 v[142:143], v[128:129], 1.0 op_sel_hi:[1,0]\n"
"v_sub_f32 v144, 0, v223\n"
"buffer_load_dwordx4 v251, s[96:99], s35 offen offset:3072 sc1 lds\n"
"v_sub_f32 v145, 0, v223\n"
"v_pk_mul_f32 v[146:147], v[144:145], 1.0 op_sel_hi:[1,0]\n"
"v_pk_mul_f32 v[148:149], v[144:145], 1.0 op_sel_hi:[1,0]\n"
"v_pk_mul_f32 v[150:151], v[144:145], 1.0 op_sel_hi:[1,0]\n"
"v_pk_mul_f32 v[152:153], v[144:145], 1.0 op_sel_hi:[1,0]\n"
"v_pk_mul_f32 v[154:155], v[144:145], 1.0 op_sel_hi:[1,0]\n"
"v_pk_mul_f32 v[156:157], v[144:145], 1.0 op_sel_hi:[1,0]\n"
"v_pk_mul_f32 v[158:159], v[144:145], 1.0 op_sel_hi:[1,0]\n"
"v_pk_add_f32 v[0:1], v[0:1], v[128:129]\n"
"v_pk_add_f32 v[2:3], v[2:3], v[128:129]\n"
"s_add_u32 m0, s57, 0\n"
"s_nop 0\n"
"buffer_load_dwordx4 v251, s[96:99], s36 offen offset:0 sc1 lds\n"
"v_pk_add_f32 v[4:5], v[4:5], v[128:129]\n"
"v_pk_add_f32 v[6:7], v[6:7], v[128:129]\n"
"v_pk_add_f32 v[8:9], v[8:9], v[128:129]\n"
"v_pk_add_f32 v[10:11], v[10:11], v[128:129]\n"
"v_pk_add_f32 v[12:13], v[12:13], v[128:129]\n"
"v_pk_add_f32 v[14:15], v[14:15], v[128:129]\n"
"v_pk_add_f32 v[16:17], v[16:17], v[144:145]\n"
"v_pk_add_f32 v[18:19], v[18:19], v[144:145]\n"
"v_pk_add_f32 v[20:21], v[20:21], v[144:145]\n"
"v_pk_add_f32 v[22:23], v[22:23], v[144:145]\n"
"buffer_load_dwordx4 v251, s[96:99], s36 offen offset:1024 sc1 lds\n"
"v_pk_add_f32 v[24:25], v[24:25], v[144:145]\n"
"v_pk_add_f32 v[26:27], v[26:27], v[144:145]\n"
"v_pk_add_f32 v[28:29], v[28:29], v[144:145]\n"
"v_pk_add_f32 v[30:31], v[30:31], v[144:145]\n"
"v_pk_add_f32 v[32:33], v[32:33], v[128:129]\n"
"v_pk_add_f32 v[34:35], v[34:35], v[128:129]\n"
"v_pk_add_f32 v[36:37], v[36:37], v[128:129]\n"
"v_pk_add_f32 v[38:39], v[38:39], v[128:129]\n"
"v_pk_add_f32 v[40:41], v[40:41], v[128:129]\n"
"v_pk_add_f32 v[42:43], v[42:43], v[128:129]\n"
"buffer_load_dwordx4 v251, s[96:99], s36 offen offset:2048 sc1 lds\n"
"v_pk_add_f32 v[44:45], v[44:45], v[128:129]\n"
"v_pk_add_f32 v[46:47], v[46:47], v[128:129]\n"
"v_pk_add_f32 v[48:49], v[48:49], v[144:145]\n"
"v_pk_add_f32 v[50:51], v[50:51], v[144:145]\n"
"v_pk_add_f32 v[52:53], v[52:53], v[144:145]\n"
"v_pk_add_f32 v[54:55], v[54:55], v[144:145]\n"
"v_pk_add_f32 v[56:57], v[56:57], v[144:145]\n"
"v_pk_add_f32 v[58:59], v[58:59], v[144:145]\n"
"v_pk_add_f32 v[60:61], v[60:61], v[144:145]\n"
"v_pk_add_f32 v[62:63], v[62:63], v[144:145]\n"
"buffer_load_dwordx4 v251, s[96:99], s36 offen offset:3072 sc1 lds\n"
"v_exp_f32 v208, v0\n"
"v_exp_f32 v209, v1\n"
"v_exp_f32 v210, v2\n"
"v_mov_b32 v214, v208\n"
"v_exp_f32 v211, v3\n"
"v_mov_b32 v215, v209\n"
"v_cvt_pk_bf16_f32 v160, v208, v209\n"
"v_exp_f32 v212, v4\n"
"v_add_f32 v214, v214, v210\n"
"v_exp_f32 v213, v5\n"
"s_add_u32 m0, s57, 65536\n"
"s_nop 0\n"
"buffer_load_dwordx4 v251, s[96:99], s37 offen offset:0 sc1 lds\n"
"v_add_f32 v215, v215, v211\n"
"v_cvt_pk_bf16_f32 v161, v210, v211\n"
"v_exp_f32 v208, v6\n"
"v_add_f32 v214, v214, v212\n"
"v_exp_f32 v209, v7\n"
"v_add_f32 v215, v215, v213\n"
"v_cvt_pk_bf16_f32 v162, v212, v213\n"
"v_exp_f32 v210, v16\n"
"v_add_f32 v214, v214, v208\n"
"v_exp_f32 v211, v17\n"
"buffer_load_dwordx4 v251, s[96:99], s37 offen offset:1024 sc1 lds\n"
"v_add_f32 v215, v215, v209\n"
"v_cvt_pk_bf16_f32 v163, v208, v209\n"
"v_exp_f32 v212, v18\n"
"v_mov_b32 v216, v210\n"
"v_exp_f32 v213, v19\n"
"v_mov_b32 v217, v211\n"
"v_cvt_pk_bf16_f32 v164, v210, v211\n"
"v_exp_f32 v208, v20\n"
"v_add_f32 v216, v216, v212\n"
"v_exp_f32 v209, v21\n"
"buffer_load_dwordx4 v251, s[96:99], s37 offen offset:2048 sc1 lds\n"
"v_add_f32 v217, v217, v213\n"
"v_cvt_pk_bf16_f32 v165, v212, v213\n"
"v_exp_f32 v210, v22\n"
"v_add_f32 v216, v216, v208\n"
"v_exp_f32 v211, v23\n"
"v_add_f32 v217, v217, v209\n"
"v_cvt_pk_bf16_f32 v166, v208, v209\n"
"v_exp_f32 v212, v8\n"
"v_add_f32 v216, v216, v210\n"
"v_exp_f32 v213, v9\n"
"buffer_load_dwordx4 v251, s[96:99], s37 offen offset:3072 sc1 lds\n"
"ds_read_b128 a[192:195], v224 offset:16384\n"
"v_add_f32 v217, v217, v211\n"
"v_cvt_pk_bf16_f32 v167, v210, v211\n"
"v_exp_f32 v208, v10\n"
"v_add_f32 v214, v214, v212\n"
"v_exp_f32 v209, v11\n"
"ds_read_b128 a[196:199], v224 offset:17408\n"
"v_add_f32 v215, v215, v213\n"
"v_cvt_pk_bf16_f32 v168, v212, v213\n"
"v_exp_f32 v210, v12\n"
"v_add_f32 v214, v214, v208\n"
"v_exp_f32 v211, v13\n"
"ds_read_b128 a[200:203], v224 offset:18432\n"
"v_add_f32 v215, v215, v209\n"
"v_cvt_pk_bf16_f32 v169, v208, v209\n"
"v_exp_f32 v212, v14\n"
"v_add_f32 v214, v214, v210\n"
"v_exp_f32 v213, v15\n"
"ds_read_b128 a[204:207], v224 offset:19456\n"
"v_add_f32 v215, v215, v211\n"
"v_cvt_pk_bf16_f32 v170, v210, v211\n"
"v_exp_f32 v208, v24\n"
"v_add_f32 v214, v214, v212\n"
"v_exp_f32 v209, v25\n"
"ds_read_b128 a[208:211], v224 offset:20480\n"
"v_add_f32 v215, v215, v213\n"
"v_cvt_pk_bf16_f32 v171, v212, v213\n"
"v_exp_f32 v210, v26\n"
"v_add_f32 v216, v216, v208\n"
"v_exp_f32 v211, v27\n"
"ds_read_b128 a[212:215], v224 offset:21504\n"
"v_add_f32 v217, v217, v209\n"
"v_cvt_pk_bf16_f32 v172, v208, v209\n"
"v_exp_f32 v212, v28\n"
"v_add_f32 v216, v216, v210\n"
"v_exp_f32 v213, v29\n"
"ds_read_b128 a[216:219], v224 offset:22528\n"
"v_add_f32 v217, v217, v211\n"
"v_cvt_pk_bf16_f32 v173, v210, v211\n"
"v_exp_f32 v208, v30\n"
"v_add_f32 v216, v216, v212\n"
"v_exp_f32 v209, v31\n"
"v_add_f32 v217, v217, v213\n"
"v_cvt_pk_bf16_f32 v174, v212, v213\n"
"v_add_f32 v216, v216, v208\n"
"v_add_f32 v217, v217, v209\n"
"v_cvt_pk_bf16_f32 v175, v208, v209\n"
"buffer_store_dwordx4 v[176:179], v251, s[96:99], s46 offen sc1\n"
"s_add_u32 s46, s46, s47\n"
"buffer_store_dwordx4 v[180:183], v251, s[96:99], s46 offen sc1\n"
"s_add_u32 s46, s46, s47\n"
"buffer_store_dwordx4 v[184:187], v251, s[96:99], s46 offen sc1\n"
"s_add_u32 s46, s46, s47\n"
"buffer_store_dwordx4 v[188:191], v251, s[96:99], s46 offen sc1\n"
"s_add_u32 s32, s32, 1\n"
"s_add_u32 s33, s33, 1\n"
"s_bitcmp1_b32 s26, s32\n"
"s_cbranch_scc1 DISP_1_%=\n"
"s_branch MID_1_%=\n"
"FIRSTM_0_%=:\n"
"v_cndmask_b32_e64 v0, v227, 0, s[64:65]\n"
"v_cndmask_b32_e64 v1, v227, 0, s[66:67]\n"
"v_cndmask_b32_e64 v2, v227, 0, s[68:69]\n"
"s_waitcnt lgkmcnt(6)\n"
"v_mfma_f32_32x32x16_bf16 v[16:31], a[192:195], a[160:163], 0\n"
"ds_read_b128 a[220:223], v224 offset:7168\n"
"v_cndmask_b32_e64 v3, v227, 0, s[70:71]\n"
"v_readlane_b32 s46, v254, 22\n"
"s_waitcnt lgkmcnt(6)\n"
"v_mfma_f32_32x32x16_bf16 v[16:31], a[196:199], a[164:167], v[16:31]\n"
"v_cndmask_b32_e64 v4, v227, 0, s[72:73]\n"
"v_cndmask_b32_e64 v5, v227, 0, s[74:75]\n"
"v_cndmask_b32_e64 v6, v227, 0, s[76:77]\n"
"s_waitcnt lgkmcnt(4)\n"
"v_mfma_f32_32x32x16_bf16 v[16:31], a[200:203], a[168:171], v[16:31]\n"
"v_cndmask_b32_e64 v7, v227, 0, s[78:79]\n"
"v_cndmask_b32_e64 v8, v227, 0, s[80:81]\n"
"v_readlane_b32 s47, v254, 26\n"
"v_mfma_f32_32x32x16_bf16 v[16:31], a[204:207], a[172:175], v[16:31]\n"
"ds_read_b128 a[224:227], v224 offset:8192\n"
"v_cndmask_b32_e64 v9, v227, 0, s[82:83]\n"
"s_waitcnt lgkmcnt(2)\n"
"v_mfma_f32_32x32x16_bf16 v[16:31], a[208:211], a[176:179], v[16:31]\n"
"v_cndmask_b32_e64 v10, v227, 0, s[84:85]\n"
"v_cndmask_b32_e64 v11, v227, 0, s[86:87]\n"
"v_cndmask_b32_e64 v12, v227, 0, s[88:89]\n"
"s_lshl_b32 s39, %2, 12\n"
"v_mfma_f32_32x32x16_bf16 v[16:31], a[212:215], a[180:183], v[16:31]\n"
"ds_read_b128 a[228:231], v224 offset:9216\n"
"v_cndmask_b32_e64 v13, v227, 0, s[90:91]\n"
"s_add_u32 s39, s39, 98304\n"
"v_mfma_f32_32x32x16_bf16 v[16:31], a[216:219], a[184:187], v[16:31]\n"
"v_cndmask_b32_e64 v14, v227, 0, s[92:93]\n"
"v_cndmask_b32_e64 v15, v227, 0, s[94:95]\n"
"v_subrev_u32 v248, s39, v255\n"
"s_waitcnt lgkmcnt(2)\n"
"v_mfma_f32_32x32x16_bf16 v[16:31], a[220:223], a[188:191], v[16:31]\n"
"ds_read_b128 a[232:235], v224 offset:10240\n"
"v_mov_b32 v32, v227\n"
"v_mov_b32 v33, v227\n"
"v_mov_b32 v34, v227\n"
"v_mov_b32 v35, v227\n"
"v_mfma_f32_32x32x16_bf16 v[0:15], a[192:195], a[128:131], v[0:15]\n"
"v_mov_b32 v36, v227\n"
"v_mov_b32 v37, v227\n"
"v_mov_b32 v38, v227\n"
"v_mov_b32 v39, v227\n"
"v_mfma_f32_32x32x16_bf16 v[0:15], a[196:199], a[132:135], v[0:15]\n"
"ds_read_b128 a[236:239], v224 offset:11264\n"
"v_mov_b32 v40, v227\n"
"v_mov_b32 v41, v227\n"
"v_mov_b32 v42, v227\n"
"v_mov_b32 v43, v227\n"
"v_mfma_f32_32x32x16_bf16 v[0:15], a[200:203], a[136:139], v[0:15]\n"
"v_mov_b32 v44, v227\n"
"v_mov_b32 v45, v227\n"
"v_mov_b32 v46, v227\n"
"v_mov_b32 v47, v227\n"
"v_mfma_f32_32x32x16_bf16 v[0:15], a[204:207], a[140:143], v[0:15]\n"
"ds_read_b128 a[240:243], v224 offset:12288\n"
"v_cndmask_b32_e64 v48, v227, 0, s[64:65]\n"
"v_cndmask_b32_e64 v49, v227, 0, s[66:67]\n"
"v_cndmask_b32_e64 v50, v227, 0, s[68:69]\n"
"v_cndmask_b32_e64 v51, v227, 0, s[70:71]\n"
"v_mfma_f32_32x32x16_bf16 v[0:15], a[208:211], a[144:147], v[0:15]\n"
"v_cndmask_b32_e64 v52, v227, 0, s[72:73]\n"
"v_cndmask_b32_e64 v53, v227, 0, s[74:75]\n"
"v_cndmask_b32_e64 v54, v227, 0, s[76:77]\n"
"v_cndmask_b32_e64 v55, v227, 0, s[78:79]\n"
"v_mfma_f32_32x32x16_bf16 v[0:15], a[212:215], a[148:151], v[0:15]\n"
"ds_read_b128 a[244:247], v224 offset:13312\n"
"ds_read_b64_tr_b16 v[176:177], v248 offset:0\n"
"v_cndmask_b32_e64 v56, v227, 0, s[80:81]\n"
"v_cndmask_b32_e64 v57, v227, 0, s[82:83]\n"
"v_cndmask_b32_e64 v58, v227, 0, s[84:85]\n"
"v_cndmask_b32_e64 v59, v227, 0, s[86:87]\n"
"s_nop 0\n"
"v_mfma_f32_32x32x16_bf16 v[0:15], a[216:219], a[152:155], v[0:15]\n"
"ds_read_b64_tr_b16 v[178:179], v248 offset:512\n"
"v_cndmask_b32_e64 v60, v227, 0, s[88:89]\n"
"v_cndmask_b32_e64 v61, v227, 0, s[90:91]\n"
"v_cndmask_b32_e64 v62, v227, 0, s[92:93]\n"
"v_cndmask_b32_e64 v63, v227, 0, s[94:95]\n"
"v_mfma_f32_32x32x16_bf16 v[0:15], a[220:223], a[156:159], v[0:15]\n"
"ds_read_b128 a[248:251], v224 offset:14336\n"
"ds_read_b64_tr_b16 v[180:181], v248 offset:8\n"
"s_waitcnt lgkmcnt(6)\n"
"v_mfma_f32_32x32x16_bf16 v[32:47], a[224:227], a[128:131], v[32:47]\n"
"ds_read_b64_tr_b16 v[182:183], v248 offset:520\n"
"v_mov_b32 v192, 0\n"
"v_mfma_f32_32x32x16_bf16 v[48:63], a[224:227], a[160:163], v[48:63]\n"
"ds_read_b128 a[252:255], v224 offset:15360\n"
"ds_read_b64_tr_b16 v[184:185], v248 offset:64\n"
"v_mov_b32 v193, 0\n"
"v_mfma_f32_32x32x16_bf16 v[32:47], a[228:231], a[132:135], v[32:47]\n"
"ds_read_b64_tr_b16 v[186:187], v248 offset:576\n"
"v_readlane_b32 s34, v229, s32\n"
"v_readlane_b32 s35, v229, s33\n"
"v_mov_b32 v194, 0\n"
"v_mfma_f32_32x32x16_bf16 v[48:63], a[228:231], a[164:167], v[48:63]\n"
"ds_read_b64_tr_b16 v[188:189], v248 offset:72\n"
"v_readlane_b32 s36, v230, s32\n"
"v_readlane_b32 s37, v230, s33\n"
"v_mov_b32 v195, 0\n"
"v_mfma_f32_32x32x16_bf16 v[32:47], a[232:235], a[136:139], v[32:47]\n"
"ds_read_b64_tr_b16 v[190:191], v248 offset:584\n"
"v_mfma_f32_32x32x16_bf16 v[48:63], a[232:235], a[168:171], v[48:63]\n"
"v_mfma_f32_32x32x16_bf16 v[32:47], a[236:239], a[140:143], v[32:47]\n"
"s_waitcnt lgkmcnt(0)\n"
"s_barrier\n"
"v_mfma_f32_32x32x16_bf16 v[48:63], a[236:239], a[172:175], v[48:63]\n"
"v_max_f32 v238, v0, v1\n"
"v_max_f32 v239, v2, v3\n"
"v_mfma_f32_32x32x16_bf16 v[32:47], a[240:243], a[144:147], v[32:47]\n"
"v_max3_f32 v238, v238, v4, v5\n"
"v_max3_f32 v239, v239, v6, v7\n"
"s_add_u32 m0, s57, 32768\n"
"v_mfma_f32_32x32x16_bf16 v[48:63], a[240:243], a[176:179], v[48:63]\n"
"buffer_load_dwordx4 v251, s[96:99], s34 offen offset:0 sc1 lds\n"
"v_max3_f32 v238, v238, v8, v9\n"
"v_max3_f32 v239, v239, v10, v11\n"
"v_mfma_f32_32x32x16_bf16 v[32:47], a[244:247], a[148:151], v[32:47]\n"
"v_max3_f32 v238, v238, v12, v13\n"
"v_max3_f32 v239, v239, v14, v15\n"
"v_mfma_f32_32x32x16_bf16 v[48:63], a[244:247], a[180:183], v[48:63]\n"
"v_max_f32 v240, v16, v17\n"
"v_max_f32 v241, v18, v19\n"
"v_mfma_f32_32x32x16_bf16 v[32:47], a[248:251], a[152:155], v[32:47]\n"
"buffer_load_dwordx4 v251, s[96:99], s34 offen offset:1024 sc1 lds\n"
"v_max3_f32 v240, v240, v20, v21\n"
"v_max3_f32 v241, v241, v22, v23\n"
"v_mfma_f32_32x32x16_bf16 v[48:63], a[248:251], a[184:187], v[48:63]\n"
"v_max3_f32 v240, v240, v24, v25\n"
"v_max3_f32 v241, v241, v26, v27\n"
"v_mfma_f32_32x32x16_bf16 v[32:47], a[252:255], a[156:159], v[32:47]\n"
"v_max3_f32 v240, v240, v28, v29\n"
"v_max3_f32 v241, v241, v30, v31\n"
"v_mfma_f32_32x32x16_bf16 v[48:63], a[252:255], a[188:191], v[48:63]\n"
"buffer_load_dwordx4 v251, s[96:99], s34 offen offset:2048 sc1 lds\n"
"v_mfma_f32_32x32x16_bf16 a[0:15], v[192:195], v[192:195], 0\n"
"v_mfma_f32_32x32x16_bf16 a[16:31], v[192:195], v[192:195], 0\n"
"s_nop 5\n"
"v_max3_f32 v238, v238, v32, v33\n"
"v_max3_f32 v239, v239, v34, v35\n"
"v_max3_f32 v238, v238, v36, v37\n"
"v_max3_f32 v239, v239, v38, v39\n"
"buffer_load_dwordx4 v251, s[96:99], s34 offen offset:3072 sc1 lds\n"
"v_mfma_f32_32x32x16_bf16 a[32:47], v[192:195], v[192:195], 0\n"
"v_max3_f32 v238, v238, v40, v41\n"
"v_max3_f32 v239, v239, v42, v43\n"
"v_max3_f32 v238, v238, v44, v45\n"
"v_max3_f32 v239, v239, v46, v47\n"
"v_mfma_f32_32x32x16_bf16 a[48:63], v[192:195], v[192:195], 0\n"
"v_max3_f32 v240, v240, v48, v49\n"
"v_max3_f32 v241, v241, v50, v51\n"
"v_max3_f32 v240, v240, v52, v53\n"
"v_max3_f32 v241, v241, v54, v55\n"
"s_add_u32 m0, s57, 98304\n"
"s_nop 0\n"
"buffer_load_dwordx4 v251, s[96:99], s35 offen offset:0 sc1 lds\n"
"v_mfma_f32_32x32x16_bf16 a[64:79], v[192:195], v[192:195], 0\n"
"v_max3_f32 v240, v240, v56, v57\n"
"v_max3_f32 v241, v241, v58, v59\n"
"v_max3_f32 v240, v240, v60, v61\n"
"v_max3_f32 v241, v241, v62, v63\n"
"v_mfma_f32_32x32x16_bf16 a[80:95], v[192:195], v[192:195], 0\n"
"v_max_f32 v238, v238, v239\n"
"v_mov_b32 v239, v238\n"
"v_max_f32 v240, v240, v241\n"
"v_mov_b32 v241, v240\n"
"buffer_load_dwordx4 v251, s[96:99], s35 offen offset:1024 sc1 lds\n"
"v_mfma_f32_32x32x16_bf16 a[96:111], v[192:195], v[192:195], 0\n"
"v_permlane32_swap_b32 v238, v239\n"
"v_permlane32_swap_b32 v240, v241\n"
"v_max_f32 v222, v238, v239\n"
"v_max_f32 v222, 0xf149f2ca, v222\n"
"v_mfma_f32_32x32x16_bf16 a[112:127], v[192:195], v[192:195], 0\n"
"v_mov_b32 v220, 0\n"
"v_max_f32 v223, v240, v241\n"
"v_max_f32 v223, 0xf149f2ca, v223\n"
"v_mov_b32 v221, 0\n"
"buffer_load_dwordx4 v251, s[96:99], s35 offen offset:2048 sc1 lds\n"
"v_sub_f32 v128, 0, v222\n"
"v_sub_f32 v129, 0, v222\n"
"v_pk_mul_f32 v[130:131], v[128:129], 1.0 op_sel_hi:[1,0]\n"
"v_pk_mul_f32 v[132:133], v[128:129], 1.0 op_sel_hi:[1,0]\n"
"v_pk_mul_f32 v[134:135], v[128:129], 1.0 op_sel_hi:[1,0]\n"
"v_pk_mul_f32 v[136:137], v[128:129], 1.0 op_sel_hi:[1,0]\n"
"v_pk_mul_f32 v[138:139], v[128:129], 1.0 op_sel_hi:[1,0]\n"
"v_pk_mul_f32 v[140:141], v[128:129], 1.0 op_sel_hi:[1,0]\n"
"v_pk_mul_f32 v[142:143], v[128:129], 1.0 op_sel_hi:[1,0]\n"
"v_sub_f32 v144, 0, v223\n"
"buffer_load_dwordx4 v251, s[96:99], s35 offen offset:3072 sc1 lds\n"
"v_sub_f32 v145, 0, v223\n"
"v_pk_mul_f32 v[146:147], v[144:145], 1.0 op_sel_hi:[1,0]\n"
"v_pk_mul_f32 v[148:149], v[144:145], 1.0 op_sel_hi:[1,0]\n"
"v_pk_mul_f32 v[150:151], v[144:145], 1.0 op_sel_hi:[1,0]\n"
"v_pk_mul_f32 v[152:153], v[144:145], 1.0 op_sel_hi:[1,0]\n"
"v_pk_mul_f32 v[154:155], v[144:145], 1.0 op_sel_hi:[1,0]\n"
"v_pk_mul_f32 v[156:157], v[144:145], 1.0 op_sel_hi:[1,0]\n"
"v_pk_mul_f32 v[158:159], v[144:145], 1.0 op_sel_hi:[1,0]\n"
"v_pk_add_f32 v[0:1], v[0:1], v[128:129]\n"
"v_pk_add_f32 v[2:3], v[2:3], v[128:129]\n"
"s_add_u32 m0, s57, 0\n"
"s_nop 0\n"
"buffer_load_dwordx4 v251, s[96:99], s36 offen offset:0 sc1 lds\n"
"v_pk_add_f32 v[4:5], v[4:5], v[128:129]\n"
"v_pk_add_f32 v[6:7], v[6:7], v[128:129]\n"
"v_pk_add_f32 v[8:9], v[8:9], v[128:129]\n"
"v_pk_add_f32 v[10:11], v[10:11], v[128:129]\n"
"v_pk_add_f32 v[12:13], v[12:13], v[128:129]\n"
"v_pk_add_f32 v[14:15], v[14:15], v[128:129]\n"
"v_pk_add_f32 v[16:17], v[16:17], v[144:145]\n"
"v_pk_add_f32 v[18:19], v[18:19], v[144:145]\n"
"v_pk_add_f32 v[20:21], v[20:21], v[144:145]\n"
"v_pk_add_f32 v[22:23], v[22:23], v[144:145]\n"
"buffer_load_dwordx4 v251, s[96:99], s36 offen offset:1024 sc1 lds\n"
"v_pk_add_f32 v[24:25], v[24:25], v[144:145]\n"
"v_pk_add_f32 v[26:27], v[26:27], v[144:145]\n"
"v_pk_add_f32 v[28:29], v[28:29], v[144:145]\n"
"v_pk_add_f32 v[30:31], v[30:31], v[144:145]\n"
"v_pk_add_f32 v[32:33], v[32:33], v[128:129]\n"
"v_pk_add_f32 v[34:35], v[34:35], v[128:129]\n"
"v_pk_add_f32 v[36:37], v[36:37], v[128:129]\n"
"v_pk_add_f32 v[38:39], v[38:39], v[128:129]\n"
"v_pk_add_f32 v[40:41], v[40:41], v[128:129]\n"
"v_pk_add_f32 v[42:43], v[42:43], v[128:129]\n"
"buffer_load_dwordx4 v251, s[96:99], s36 offen offset:2048 sc1 lds\n"
"v_pk_add_f32 v[44:45], v[44:45], v[128:129]\n"
"v_pk_add_f32 v[46:47], v[46:47], v[128:129]\n"
"v_pk_add_f32 v[48:49], v[48:49], v[144:145]\n"
"v_pk_add_f32 v[50:51], v[50:51], v[144:145]\n"
"v_pk_add_f32 v[52:53], v[52:53], v[144:145]\n"
"v_pk_add_f32 v[54:55], v[54:55], v[144:145]\n"
"v_pk_add_f32 v[56:57], v[56:57], v[144:145]\n"
"v_pk_add_f32 v[58:59], v[58:59], v[144:145]\n"
"v_pk_add_f32 v[60:61], v[60:61], v[144:145]\n"
"v_pk_add_f32 v[62:63], v[62:63], v[144:145]\n"
"buffer_load_dwordx4 v251, s[96:99], s36 offen offset:3072 sc1 lds\n"
"v_exp_f32 v208, v0\n"
"v_exp_f32 v209, v1\n"
"v_exp_f32 v210, v2\n"
"v_mov_b32 v214, v208\n"
"v_exp_f32 v211, v3\n"
"v_mov_b32 v215, v209\n"
"v_cvt_pk_bf16_f32 v160, v208, v209\n"
"v_exp_f32 v212, v4\n"
"v_add_f32 v214, v214, v210\n"
"v_exp_f32 v213, v5\n"
"s_add_u32 m0, s57, 65536\n"
"s_nop 0\n"
"buffer_load_dwordx4 v251, s[96:99], s37 offen offset:0 sc1 lds\n"
"v_add_f32 v215, v215, v211\n"
"v_cvt_pk_bf16_f32 v161, v210, v211\n"
"v_exp_f32 v208, v6\n"
"v_add_f32 v214, v214, v212\n"
"v_exp_f32 v209, v7\n"
"v_add_f32 v215, v215, v213\n"
"v_cvt_pk_bf16_f32 v162, v212, v213\n"
"v_exp_f32 v210, v16\n"
"v_add_f32 v214, v214, v208\n"
"v_exp_f32 v211, v17\n"
"buffer_load_dwordx4 v251, s[96:99], s37 offen offset:1024 sc1 lds\n"
"v_add_f32 v215, v215, v209\n"
"v_cvt_pk_bf16_f32 v163, v208, v209\n"
"v_exp_f32 v212, v18\n"
"v_mov_b32 v216, v210\n"
"v_exp_f32 v213, v19\n"
"v_mov_b32 v217, v211\n"
"v_cvt_pk_bf16_f32 v164, v210, v211\n"
"v_exp_f32 v208, v20\n"
"v_add_f32 v216, v216, v212\n"
"v_exp_f32 v209, v21\n"
"buffer_load_dwordx4 v251, s[96:99], s37 offen offset:2048 sc1 lds\n"
"v_add_f32 v217, v217, v213\n"
"v_cvt_pk_bf16_f32 v165, v212, v213\n"
"v_exp_f32 v210, v22\n"
"v_add_f32 v216, v216, v208\n"
"v_exp_f32 v211, v23\n"
"v_add_f32 v217, v217, v209\n"
"v_cvt_pk_bf16_f32 v166, v208, v209\n"
"v_exp_f32 v212, v8\n"
"v_add_f32 v216, v216, v210\n"
"v_exp_f32 v213, v9\n"
"buffer_load_dwordx4 v251, s[96:99], s37 offen offset:3072 sc1 lds\n"
"ds_read_b128 a[192:195], v224 offset:16384\n"
"v_add_f32 v217, v217, v211\n"
"v_cvt_pk_bf16_f32 v167, v210, v211\n"
"v_exp_f32 v208, v10\n"
"v_add_f32 v214, v214, v212\n"
"v_exp_f32 v209, v11\n"
"ds_read_b128 a[196:199], v224 offset:17408\n"
"v_add_f32 v215, v215, v213\n"
"v_cvt_pk_bf16_f32 v168, v212, v213\n"
"v_exp_f32 v210, v12\n"
"v_add_f32 v214, v214, v208\n"
"v_exp_f32 v211, v13\n"
"ds_read_b128 a[200:203], v224 offset:18432\n"
"v_add_f32 v215, v215, v209\n"
"v_cvt_pk_bf16_f32 v169, v208, v209\n"
"v_exp_f32 v212, v14\n"
"v_add_f32 v214, v214, v210\n"
"v_exp_f32 v213, v15\n"
"ds_read_b128 a[204:207], v224 offset:19456\n"
"v_add_f32 v215, v215, v211\n"
"v_cvt_pk_bf16_f32 v170, v210, v211\n"
"v_exp_f32 v208, v24\n"
"v_add_f32 v214, v214, v212\n"
"v_exp_f32 v209, v25\n"
"ds_read_b128 a[208:211], v224 offset:20480\n"
"v_add_f32 v215, v215, v213\n"
"v_cvt_pk_bf16_f32 v171, v212, v213\n"
"v_exp_f32 v210, v26\n"
"v_add_f32 v216, v216, v208\n"
"v_exp_f32 v211, v27\n"
"ds_read_b128 a[212:215], v224 offset:21504\n"
"v_add_f32 v217, v217, v209\n"
"v_cvt_pk_bf16_f32 v172, v208, v209\n"
"v_exp_f32 v212, v28\n"
"v_add_f32 v216, v216, v210\n"
"v_exp_f32 v213, v29\n"
"ds_read_b128 a[216:219], v224 offset:22528\n"
"v_add_f32 v217, v217, v211\n"
"v_cvt_pk_bf16_f32 v173, v210, v211\n"
"v_exp_f32 v208, v30\n"
"v_add_f32 v216, v216, v212\n"
"v_exp_f32 v209, v31\n"
"v_add_f32 v217, v217, v213\n"
"v_cvt_pk_bf16_f32 v174, v212, v213\n"
"v_add_f32 v216, v216, v208\n"
"v_add_f32 v217, v217, v209\n"
"v_cvt_pk_bf16_f32 v175, v208, v209\n"
"buffer_store_dwordx4 v[176:179], v251, s[96:99], s46 offen sc1\n"
"s_add_u32 s46, s46, s47\n"
"buffer_store_dwordx4 v[180:183], v251, s[96:99], s46 offen sc1\n"
"s_add_u32 s46, s46, s47\n"
"buffer_store_dwordx4 v[184:187], v251, s[96:99], s46 offen sc1\n"
"s_add_u32 s46, s46, s47\n"
"buffer_store_dwordx4 v[188:191], v251, s[96:99], s46 offen sc1\n"
"s_add_u32 s32, s32, 1\n"
"s_add_u32 s33, s33, 1\n"
"s_bitcmp1_b32 s26, s32\n"
"s_cbranch_scc1 DISP_1_%=\n"
"s_branch MID_1_%=\n"
"FIRST2M_0_%=:\n"
"v_cndmask_b32_e64 v0, v227, 0, s[64:65]\n"
"v_cndmask_b32_e64 v1, v227, 0, s[66:67]\n"
"v_cndmask_b32_e64 v2, v227, 0, s[68:69]\n"
"s_waitcnt lgkmcnt(6)\n"
"v_mfma_f32_32x32x16_bf16 v[16:31], a[192:195], a[160:163], 0\n"
"ds_read_b128 a[220:223], v224 offset:7168\n"
"v_cndmask_b32_e64 v3, v227, 0, s[70:71]\n"
"v_accvgpr_read_b32 v88, a56\n"
"v_accvgpr_read_b32 v89, a57\n"
"v_accvgpr_read_b32 v90, a58\n"
"v_accvgpr_read_b32 v91, a59\n"
"s_waitcnt lgkmcnt(6)\n"
"v_mfma_f32_32x32x16_bf16 v[16:31], a[196:199], a[164:167], v[16:31]\n"
"v_cndmask_b32_e64 v4, v227, 0, s[72:73]\n"
"v_cndmask_b32_e64 v5, v227, 0, s[74:75]\n"
"v_cndmask_b32_e64 v6, v227, 0, s[76:77]\n"
"v_accvgpr_read_b32 v92, a60\n"
"v_accvgpr_read_b32 v93, a61\n"
"s_waitcnt lgkmcnt(4)\n"
"v_mfma_f32_32x32x16_bf16 v[16:31], a[200:203], a[168:171], v[16:31]\n"
"v_cndmask_b32_e64 v7, v227, 0, s[78:79]\n"
"v_cndmask_b32_e64 v8, v227, 0, s[80:81]\n"
"v_accvgpr_read_b32 v94, a62\n"
"v_accvgpr_read_b32 v95, a63\n"
"v_mul_f32 v88, v88, v246\n"
"v_mfma_f32_32x32x16_bf16 v[16:31], a[204:207], a[172:175], v[16:31]\n"
"ds_read_b128 a[224:227], v224 offset:8192\n"
"v_cndmask_b32_e64 v9, v227, 0, s[82:83]\n"
"v_mul_f32 v89, v89, v246\n"
"v_mul_f32 v90, v90, v246\n"
"v_mul_f32 v91, v91, v246\n"
"s_waitcnt lgkmcnt(2)\n"
"v_mfma_f32_32x32x16_bf16 v[16:31], a[208:211], a[176:179], v[16:31]\n"
"v_cndmask_b32_e64 v10, v227, 0, s[84:85]\n"
"v_cndmask_b32_e64 v11, v227, 0, s[86:87]\n"
"v_cndmask_b32_e64 v12, v227, 0, s[88:89]\n"
"v_mul_f32 v92, v92, v246\n"
"v_mul_f32 v93, v93, v246\n"
"v_mfma_f32_32x32x16_bf16 v[16:31], a[212:215], a[180:183], v[16:31]\n"
"ds_read_b128 a[228:231], v224 offset:9216\n"
"v_cndmask_b32_e64 v13, v227, 0, s[90:91]\n"
"v_mul_f32 v94, v94, v246\n"
"v_mul_f32 v95, v95, v246\n"
"v_cvt_pk_bf16_f32 v96, v88, v89\n"
"v_cvt_pk_bf16_f32 v97, v90, v91\n"
"v_mfma_f32_32x32x16_bf16 v[16:31], a[216:219], a[184:187], v[16:31]\n"
"v_cndmask_b32_e64 v14, v227, 0, s[92:93]\n"
"v_cndmask_b32_e64 v15, v227, 0, s[94:95]\n"
"v_cvt_pk_bf16_f32 v98, v92, v93\n"
"v_cvt_pk_bf16_f32 v99, v94, v95\n"
"buffer_store_dwordx4 v[96:99], v228, s[96:99], s39 offen offset:1536 sc1\n"
"s_waitcnt lgkmcnt(2)\n"
"v_mfma_f32_32x32x16_bf16 v[16:31], a[220:223], a[188:191], v[16:31]\n"
"ds_read_b128 a[232:235], v224 offset:10240\n"
"v_mov_b32 v32, v227\n"
"v_mov_b32 v33, v227\n"
"PG12_%=:\n"
"s_cbranch_execz RPG12_%=\n"
"v_mov_b32 v34, v227\n"
"v_mov_b32 v35, v227\n"
"v_mfma_f32_32x32x16_bf16 v[0:15], a[192:195], a[128:131], v[0:15]\n"
"v_mov_b32 v36, v227\n"
"v_mov_b32 v37, v227\n"
"v_mov_b32 v38, v227\n"
"v_mov_b32 v39, v227\n"
"v_accvgpr_read_b32 v100, a64\n"
"v_accvgpr_read_b32 v101, a65\n"
"v_mfma_f32_32x32x16_bf16 v[0:15], a[196:199], a[132:135], v[0:15]\n"
"ds_read_b128 a[236:239], v224 offset:11264\n"
"v_mov_b32 v40, v227\n"
"v_mov_b32 v41, v227\n"
"v_mov_b32 v42, v227\n"
"v_mov_b32 v43, v227\n"
"v_accvgpr_read_b32 v102, a66\n"
"v_mfma_f32_32x32x16_bf16 v[0:15], a[200:203], a[136:139], v[0:15]\n"
"v_mov_b32 v44, v227\n"
"v_mov_b32 v45, v227\n"
"v_mov_b32 v46, v227\n"
"v_mov_b32 v47, v227\n"
"v_accvgpr_read_b32 v103, a67\n"
"v_mfma_f32_32x32x16_bf16 v[0:15], a[204:207], a[140:143], v[0:15]\n"
"ds_read_b128 a[240:243], v224 offset:12288\n"
"v_cndmask_b32_e64 v48, v227, 0, s[64:65]\n"
"v_cndmask_b32_e64 v49, v227, 0, s[66:67]\n"
"v_cndmask_b32_e64 v50, v227, 0, s[68:69]\n"
"v_cndmask_b32_e64 v51, v227, 0, s[70:71]\n"
"v_accvgpr_read_b32 v104, a68\n"
"v_mfma_f32_32x32x16_bf16 v[0:15], a[208:211], a[144:147], v[0:15]\n"
"v_cndmask_b32_e64 v52, v227, 0, s[72:73]\n"
"v_cndmask_b32_e64 v53, v227, 0, s[74:75]\n"
"v_cndmask_b32_e64 v54, v227, 0, s[76:77]\n"
"v_cndmask_b32_e64 v55, v227, 0, s[78:79]\n"
"v_accvgpr_read_b32 v105, a69\n"
"v_accvgpr_read_b32 v106, a70\n"
"v_mfma_f32_32x32x16_bf16 v[0:15], a[212:215], a[148:151], v[0:15]\n"
"ds_read_b128 a[244:247], v224 offset:13312\n"
"v_cndmask_b32_e64 v56, v227, 0, s[80:81]\n"
"v_cndmask_b32_e64 v57, v227, 0, s[82:83]\n"
"v_cndmask_b32_e64 v58, v227, 0, s[84:85]\n"
"v_cndmask_b32_e64 v59, v227, 0, s[86:87]\n"
"v_mfma_f32_32x32x16_bf16 v[0:15], a[216:219], a[152:155], v[0:15]\n"
"v_cndmask_b32_e64 v60, v227, 0, s[88:89]\n"
"v_cndmask_b32_e64 v61, v227, 0, s[90:91]\n"
"v_cndmask_b32_e64 v62, v227, 0, s[92:93]\n"
"v_cndmask_b32_e64 v63, v227, 0, s[94:95]\n"
"v_accvgpr_read_b32 v107, a71\n"
"v_mul_f32 v100, v100, v244\n"
"v_mfma_f32_32x32x16_bf16 v[0:15], a[220:223], a[156:159], v[0:15]\n"
"ds_read_b128 a[248:251], v224 offset:14336\n"
"v_mul_f32 v101, v101, v244\n"
"v_mul_f32 v102, v102, v244\n"
"v_mul_f32 v103, v103, v244\n"
"v_mul_f32 v104, v104, v244\n"
"s_waitcnt lgkmcnt(3)\n"
"v_mfma_f32_32x32x16_bf16 v[32:47], a[224:227], a[128:131], v[32:47]\n"
"v_mov_b32 v192, 0\n"
"v_mul_f32 v105, v105, v244\n"
"v_mul_f32 v106, v106, v244\n"
"v_mul_f32 v107, v107, v244\n"
"v_cvt_pk_bf16_f32 v108, v100, v101\n"
"v_mfma_f32_32x32x16_bf16 v[48:63], a[224:227], a[160:163], v[48:63]\n"
"ds_read_b128 a[252:255], v224 offset:15360\n"
"v_mov_b32 v193, 0\n"
"v_cvt_pk_bf16_f32 v109, v102, v103\n"
"v_cvt_pk_bf16_f32 v110, v104, v105\n"
"v_cvt_pk_bf16_f32 v111, v106, v107\n"
"v_mfma_f32_32x32x16_bf16 v[32:47], a[228:231], a[132:135], v[32:47]\n"
"buffer_store_dwordx4 v[108:111], v228, s[96:99], s38 offen offset:2048 sc1\n"
"v_readlane_b32 s34, v229, s32\n"
"v_readlane_b32 s35, v229, s33\n"
"v_mov_b32 v194, 0\n"
"v_accvgpr_read_b32 v112, a72\n"
"v_mfma_f32_32x32x16_bf16 v[48:63], a[228:231], a[164:167], v[48:63]\n"
"v_readlane_b32 s36, v230, s32\n"
"v_readlane_b32 s37, v230, s33\n"
"v_mov_b32 v195, 0\n"
"v_accvgpr_read_b32 v113, a73\n"
"v_accvgpr_read_b32 v114, a74\n"
"v_accvgpr_read_b32 v115, a75\n"
"v_mfma_f32_32x32x16_bf16 v[32:47], a[232:235], a[136:139], v[32:47]\n"
"v_accvgpr_read_b32 v116, a76\n"
"v_accvgpr_read_b32 v117, a77\n"
"v_accvgpr_read_b32 v118, a78\n"
"v_accvgpr_read_b32 v119, a79\n"
"v_mfma_f32_32x32x16_bf16 v[48:63], a[232:235], a[168:171], v[48:63]\n"
"v_mul_f32 v112, v112, v244\n"
"v_mul_f32 v113, v113, v244\n"
"v_mul_f32 v114, v114, v244\n"
"v_mul_f32 v115, v115, v244\n"
"v_mul_f32 v116, v116, v244\n"
"v_mfma_f32_32x32x16_bf16 v[32:47], a[236:239], a[140:143], v[32:47]\n"
"v_mul_f32 v117, v117, v244\n"
"v_mul_f32 v118, v118, v244\n"
"v_mul_f32 v119, v119, v244\n"
"s_waitcnt lgkmcnt(0)\n"
"s_waitcnt vmcnt(4)\n"
"s_barrier\n"
"v_mfma_f32_32x32x16_bf16 v[48:63], a[236:239], a[172:175], v[48:63]\n"
"v_max_f32 v238, v0, v1\n"
"v_max_f32 v239, v2, v3\n"
"v_cvt_pk_bf16_f32 v120, v112, v113\n"
"v_cvt_pk_bf16_f32 v121, v114, v115\n"
"v_cvt_pk_bf16_f32 v122, v116, v117\n"
"v_mfma_f32_32x32x16_bf16 v[32:47], a[240:243], a[144:147], v[32:47]\n"
"v_max3_f32 v238, v238, v4, v5\n"
"v_max3_f32 v239, v239, v6, v7\n"
"v_cvt_pk_bf16_f32 v123, v118, v119\n"
"buffer_store_dwordx4 v[120:123], v228, s[96:99], s38 offen offset:2560 sc1\n"
"s_add_u32 m0, s57, 32768\n"
"v_mfma_f32_32x32x16_bf16 v[48:63], a[240:243], a[176:179], v[48:63]\n"
"buffer_load_dwordx4 v251, s[96:99], s34 offen offset:0 sc1 lds\n"
"v_max3_f32 v238, v238, v8, v9\n"
"v_max3_f32 v239, v239, v10, v11\n"
"v_accvgpr_read_b32 v64, a80\n"
"v_accvgpr_read_b32 v65, a81\n"
"v_accvgpr_read_b32 v66, a82\n"
"v_mfma_f32_32x32x16_bf16 v[32:47], a[244:247], a[148:151], v[32:47]\n"
"v_max3_f32 v238, v238, v12, v13\n"
"v_max3_f32 v239, v239, v14, v15\n"
"v_accvgpr_read_b32 v67, a83\n"
"v_accvgpr_read_b32 v68, a84\n"
"v_accvgpr_read_b32 v69, a85\n"
"v_mfma_f32_32x32x16_bf16 v[48:63], a[244:247], a[180:183], v[48:63]\n"
"v_max_f32 v240, v16, v17\n"
"v_max_f32 v241, v18, v19\n"
"v_accvgpr_read_b32 v70, a86\n"
"v_accvgpr_read_b32 v71, a87\n"
"v_mul_f32 v64, v64, v246\n"
"v_mfma_f32_32x32x16_bf16 v[32:47], a[248:251], a[152:155], v[32:47]\n"
"buffer_load_dwordx4 v251, s[96:99], s34 offen offset:1024 sc1 lds\n"
"v_max3_f32 v240, v240, v20, v21\n"
"v_max3_f32 v241, v241, v22, v23\n"
"v_mul_f32 v65, v65, v246\n"
"v_mul_f32 v66, v66, v246\n"
"v_mfma_f32_32x32x16_bf16 v[48:63], a[248:251], a[184:187], v[48:63]\n"
"v_max3_f32 v240, v240, v24, v25\n"
"v_max3_f32 v241, v241, v26, v27\n"
"v_mul_f32 v67, v67, v246\n"
"v_mul_f32 v68, v68, v246\n"
"v_mul_f32 v69, v69, v246\n"
"v_mfma_f32_32x32x16_bf16 v[32:47], a[252:255], a[156:159], v[32:47]\n"
"v_max3_f32 v240, v240, v28, v29\n"
"v_max3_f32 v241, v241, v30, v31\n"
"v_mul_f32 v70, v70, v246\n"
"v_mul_f32 v71, v71, v246\n"
"v_cvt_pk_bf16_f32 v72, v64, v65\n"
"v_cvt_pk_bf16_f32 v73, v66, v67\n"
"v_mfma_f32_32x32x16_bf16 v[48:63], a[252:255], a[188:191], v[48:63]\n"
"buffer_load_dwordx4 v251, s[96:99], s34 offen offset:2048 sc1 lds\n"
"v_cvt_pk_bf16_f32 v74, v68, v69\n"
"v_cvt_pk_bf16_f32 v75, v70, v71\n"
"buffer_store_dwordx4 v[72:75], v228, s[96:99], s39 offen offset:2048 sc1\n"
"v_accvgpr_read_b32 v76, a88\n"
"v_accvgpr_read_b32 v77, a89\n"
"v_accvgpr_read_b32 v78, a90\n"
"v_accvgpr_read_b32 v79, a91\n"
"v_accvgpr_read_b32 v80, a92\n"
"v_accvgpr_read_b32 v81, a93\n"
"buffer_load_dwordx4 v251, s[96:99], s34 offen offset:3072 sc1 lds\n"
"v_accvgpr_read_b32 v82, a94\n"
"v_accvgpr_read_b32 v83, a95\n"
"v_pk_mul_f32 v[76:77], v[76:77], v[246:247]\n"
"v_pk_mul_f32 v[78:79], v[78:79], v[246:247]\n"
"v_pk_mul_f32 v[80:81], v[80:81], v[246:247]\n"
"v_pk_mul_f32 v[82:83], v[82:83], v[246:247]\n"
"v_cvt_pk_bf16_f32 v84, v76, v77\n"
"v_cvt_pk_bf16_f32 v85, v78, v79\n"
"v_cvt_pk_bf16_f32 v86, v80, v81\n"
"v_cvt_pk_bf16_f32 v87, v82, v83\n"
"buffer_store_dwordx4 v[84:87], v228, s[96:99], s39 offen offset:2560 sc1\n"
"v_accvgpr_read_b32 v88, a96\n"
"v_accvgpr_read_b32 v89, a97\n"
"v_accvgpr_read_b32 v90, a98\n"
"v_accvgpr_read_b32 v91, a99\n"
"s_add_u32 m0, s57, 98304\n"
"s_nop 0\n"
"buffer_load_dwordx4 v251, s[96:99], s35 offen offset:0 sc1 lds\n"
"v_accvgpr_read_b32 v92, a100\n"
"v_accvgpr_read_b32 v93, a101\n"
"v_accvgpr_read_b32 v94, a102\n"
"v_accvgpr_read_b32 v95, a103\n"
"v_pk_mul_f32 v[88:89], v[88:89], v[244:245]\n"
"v_pk_mul_f32 v[90:91], v[90:91], v[244:245]\n"
"v_pk_mul_f32 v[92:93], v[92:93], v[244:245]\n"
"v_pk_mul_f32 v[94:95], v[94:95], v[244:245]\n"
"v_cvt_pk_bf16_f32 v96, v88, v89\n"
"v_cvt_pk_bf16_f32 v97, v90, v91\n"
"v_cvt_pk_bf16_f32 v98, v92, v93\n"
"v_cvt_pk_bf16_f32 v99, v94, v95\n"
"buffer_store_dwordx4 v[96:99], v228, s[96:99], s38 offen offset:3072 sc1\n"
"v_accvgpr_read_b32 v100, a104\n"
"v_accvgpr_read_b32 v101, a105\n"
"buffer_load_dwordx4 v251, s[96:99], s35 offen offset:1024 sc1 lds\n"
"v_accvgpr_read_b32 v102, a106\n"
"v_accvgpr_read_b32 v103, a107\n"
"v_accvgpr_read_b32 v104, a108\n"
"v_accvgpr_read_b32 v105, a109\n"
"v_accvgpr_read_b32 v106, a110\n"
"v_accvgpr_read_b32 v107, a111\n"
"v_pk_mul_f32 v[100:101], v[100:101], v[244:245]\n"
"v_pk_mul_f32 v[102:103], v[102:103], v[244:245]\n"
"v_pk_mul_f32 v[104:105], v[104:105], v[244:245]\n"
"v_pk_mul_f32 v[106:107], v[106:107], v[244:245]\n"
"v_cvt_pk_bf16_f32 v108, v100, v101\n"
"v_cvt_pk_bf16_f32 v109, v102, v103\n"
"v_cvt_pk_bf16_f32 v110, v104, v105\n"
"v_cvt_pk_bf16_f32 v111, v106, v107\n"
"buffer_store_dwordx4 v[108:111], v228, s[96:99], s38 offen offset:3584 sc1\n"
"buffer_load_dwordx4 v251, s[96:99], s35 offen offset:2048 sc1 lds\n"
"v_accvgpr_read_b32 v112, a112\n"
"v_accvgpr_read_b32 v113, a113\n"
"v_accvgpr_read_b32 v114, a114\n"
"v_accvgpr_read_b32 v115, a115\n"
"v_accvgpr_read_b32 v116, a116\n"
"v_accvgpr_read_b32 v117, a117\n"
"v_accvgpr_read_b32 v118, a118\n"
"v_accvgpr_read_b32 v119, a119\n"
"v_pk_mul_f32 v[112:113], v[112:113], v[246:247]\n"
"v_pk_mul_f32 v[114:115], v[114:115], v[246:247]\n"
"v_pk_mul_f32 v[116:117], v[116:117], v[246:247]\n"
"v_pk_mul_f32 v[118:119], v[118:119], v[246:247]\n"
"v_cvt_pk_bf16_f32 v120, v112, v113\n"
"v_cvt_pk_bf16_f32 v121, v114, v115\n"
"v_cvt_pk_bf16_f32 v122, v116, v117\n"
"buffer_load_dwordx4 v251, s[96:99], s35 offen offset:3072 sc1 lds\n"
"v_cvt_pk_bf16_f32 v123, v118, v119\n"
"buffer_store_dwordx4 v[120:123], v228, s[96:99], s39 offen offset:3072 sc1\n"
"v_accvgpr_read_b32 v64, a120\n"
"v_accvgpr_read_b32 v65, a121\n"
"v_accvgpr_read_b32 v66, a122\n"
"v_accvgpr_read_b32 v67, a123\n"
"v_accvgpr_read_b32 v68, a124\n"
"v_accvgpr_read_b32 v69, a125\n"
"v_accvgpr_read_b32 v70, a126\n"
"v_accvgpr_read_b32 v71, a127\n"
"v_pk_mul_f32 v[64:65], v[64:65], v[246:247]\n"
"v_pk_mul_f32 v[66:67], v[66:67], v[246:247]\n"
"v_pk_mul_f32 v[68:69], v[68:69], v[246:247]\n"
"v_pk_mul_f32 v[70:71], v[70:71], v[246:247]\n"
"v_cvt_pk_bf16_f32 v72, v64, v65\n"
"s_add_u32 m0, s57, 0\n"
"s_nop 0\n"
"buffer_load_dwordx4 v251, s[96:99], s36 offen offset:0 sc1 lds\n"
"v_cvt_pk_bf16_f32 v73, v66, v67\n"
"v_cvt_pk_bf16_f32 v74, v68, v69\n"
"v_cvt_pk_bf16_f32 v75, v70, v71\n"
"buffer_store_dwordx4 v[72:75], v228, s[96:99], s39 offen offset:3584 sc1\n"
"s_lshl_b32 s38, s10, 17\n"
"s_lshl_b32 s39, s24, 12\n"
"s_add_u32 s38, s38, s39\n"
"s_add_u32 s38, s38, 50331648\n"
"v_max3_f32 v238, v238, v32, v33\n"
"v_max3_f32 v239, v239, v34, v35\n"
"v_max3_f32 v238, v238, v36, v37\n"
"v_max3_f32 v239, v239, v38, v39\n"
"v_max3_f32 v238, v238, v40, v41\n"
"v_max3_f32 v239, v239, v42, v43\n"
"v_max3_f32 v238, v238, v44, v45\n"
"buffer_load_dwordx4 v251, s[96:99], s36 offen offset:1024 sc1 lds\n"
"v_max3_f32 v239, v239, v46, v47\n"
"v_max3_f32 v240, v240, v48, v49\n"
"v_max3_f32 v241, v241, v50, v51\n"
"v_max3_f32 v240, v240, v52, v53\n"
"v_max3_f32 v241, v241, v54, v55\n"
"v_max3_f32 v240, v240, v56, v57\n"
"v_max3_f32 v241, v241, v58, v59\n"
"v_max3_f32 v240, v240, v60, v61\n"
"v_max3_f32 v241, v241, v62, v63\n"
"v_max_f32 v238, v238, v239\n"
"v_mov_b32 v239, v238\n"
"v_max_f32 v240, v240, v241\n"
"v_mov_b32 v241, v240\n"
"v_permlane32_swap_b32 v238, v239\n"
"s_nop 0\n"
"v_permlane32_swap_b32 v240, v241\n"
"buffer_load_dwordx4 v251, s[96:99], s36 offen offset:2048 sc1 lds\n"
"v_max_f32 v222, v238, v239\n"
"v_max_f32 v222, 0xf149f2ca, v222\n"
"v_mov_b32 v220, 0\n"
"v_max_f32 v223, v240, v241\n"
"v_max_f32 v223, 0xf149f2ca, v223\n"
"v_mov_b32 v221, 0\n"
"v_sub_f32 v128, 0, v222\n"
"v_sub_f32 v129, 0, v222\n"
"v_pk_mul_f32 v[130:131], v[128:129], 1.0 op_sel_hi:[1,0]\n"
"v_pk_mul_f32 v[132:133], v[128:129], 1.0 op_sel_hi:[1,0]\n"
"v_pk_mul_f32 v[134:135], v[128:129], 1.0 op_sel_hi:[1,0]\n"
"v_pk_mul_f32 v[136:137], v[128:129], 1.0 op_sel_hi:[1,0]\n"
"v_pk_mul_f32 v[138:139], v[128:129], 1.0 op_sel_hi:[1,0]\n"
"v_pk_mul_f32 v[140:141], v[128:129], 1.0 op_sel_hi:[1,0]\n"
"v_pk_mul_f32 v[142:143], v[128:129], 1.0 op_sel_hi:[1,0]\n"
"buffer_load_dwordx4 v251, s[96:99], s36 offen offset:3072 sc1 lds\n"
"v_sub_f32 v144, 0, v223\n"
"v_sub_f32 v145, 0, v223\n"
"v_pk_mul_f32 v[146:147], v[144:145], 1.0 op_sel_hi:[1,0]\n"
"v_pk_mul_f32 v[148:149], v[144:145], 1.0 op_sel_hi:[1,0]\n"
"v_pk_mul_f32 v[150:151], v[144:145], 1.0 op_sel_hi:[1,0]\n"
"v_pk_mul_f32 v[152:153], v[144:145], 1.0 op_sel_hi:[1,0]\n"
"v_pk_mul_f32 v[154:155], v[144:145], 1.0 op_sel_hi:[1,0]\n"
"v_pk_mul_f32 v[156:157], v[144:145], 1.0 op_sel_hi:[1,0]\n"
"v_pk_mul_f32 v[158:159], v[144:145], 1.0 op_sel_hi:[1,0]\n"
"v_pk_add_f32 v[0:1], v[0:1], v[128:129]\n"
"v_pk_add_f32 v[2:3], v[2:3], v[128:129]\n"
"v_pk_add_f32 v[4:5], v[4:5], v[128:129]\n"
"v_pk_add_f32 v[6:7], v[6:7], v[128:129]\n"
"v_pk_add_f32 v[8:9], v[8:9], v[128:129]\n"
"v_pk_add_f32 v[10:11], v[10:11], v[128:129]\n"
"s_add_u32 m0, s57, 65536\n"
"s_nop 0\n"
"buffer_load_dwordx4 v251, s[96:99], s37 offen offset:0 sc1 lds\n"
"v_pk_add_f32 v[12:13], v[12:13], v[128:129]\n"
"v_pk_add_f32 v[14:15], v[14:15], v[128:129]\n"
"v_pk_add_f32 v[16:17], v[16:17], v[144:145]\n"
"v_pk_add_f32 v[18:19], v[18:19], v[144:145]\n"
"v_pk_add_f32 v[20:21], v[20:21], v[144:145]\n"
"v_pk_add_f32 v[22:23], v[22:23], v[144:145]\n"
"v_pk_add_f32 v[24:25], v[24:25], v[144:145]\n"
"v_pk_add_f32 v[26:27], v[26:27], v[144:145]\n"
"v_pk_add_f32 v[28:29], v[28:29], v[144:145]\n"
"v_pk_add_f32 v[30:31], v[30:31], v[144:145]\n"
"v_pk_add_f32 v[32:33], v[32:33], v[128:129]\n"
"v_pk_add_f32 v[34:35], v[34:35], v[128:129]\n"
"v_pk_add_f32 v[36:37], v[36:37], v[128:129]\n"
"v_pk_add_f32 v[38:39], v[38:39], v[128:129]\n"
"v_pk_add_f32 v[40:41], v[40:41], v[128:129]\n"
"buffer_load_dwordx4 v251, s[96:99], s37 offen offset:1024 sc1 lds\n"
"v_pk_add_f32 v[42:43], v[42:43], v[128:129]\n"
"v_pk_add_f32 v[44:45], v[44:45], v[128:129]\n"
"v_pk_add_f32 v[46:47], v[46:47], v[128:129]\n"
"v_pk_add_f32 v[48:49], v[48:49], v[144:145]\n"
"v_pk_add_f32 v[50:51], v[50:51], v[144:145]\n"
"v_pk_add_f32 v[52:53], v[52:53], v[144:145]\n"
"v_pk_add_f32 v[54:55], v[54:55], v[144:145]\n"
"v_pk_add_f32 v[56:57], v[56:57], v[144:145]\n"
"v_pk_add_f32 v[58:59], v[58:59], v[144:145]\n"
"v_pk_add_f32 v[60:61], v[60:61], v[144:145]\n"
"v_pk_add_f32 v[62:63], v[62:63], v[144:145]\n"
"v_mfma_f32_32x32x16_bf16 a[0:15], v[192:195], v[192:195], 0\n"
"v_exp_f32 v208, v0\n"
"v_exp_f32 v209, v1\n"
"v_exp_f32 v210, v2\n"
"buffer_load_dwordx4 v251, s[96:99], s37 offen offset:2048 sc1 lds\n"
"v_mov_b32 v214, v208\n"
"v_exp_f32 v211, v3\n"
"v_mov_b32 v215, v209\n"
"v_cvt_pk_bf16_f32 v160, v208, v209\n"
"v_exp_f32 v212, v4\n"
"v_add_f32 v214, v214, v210\n"
"v_mfma_f32_32x32x16_bf16 a[16:31], v[192:195], v[192:195], 0\n"
"v_exp_f32 v213, v5\n"
"v_add_f32 v215, v215, v211\n"
"v_cvt_pk_bf16_f32 v161, v210, v211\n"
"v_exp_f32 v208, v6\n"
"v_add_f32 v214, v214, v212\n"
"v_exp_f32 v209, v7\n"
"v_add_f32 v215, v215, v213\n"
"v_cvt_pk_bf16_f32 v162, v212, v213\n"
"buffer_load_dwordx4 v251, s[96:99], s37 offen offset:3072 sc1 lds\n"
"v_exp_f32 v210, v16\n"
"v_mfma_f32_32x32x16_bf16 a[32:47], v[192:195], v[192:195], 0\n"
"v_add_f32 v214, v214, v208\n"
"v_exp_f32 v211, v17\n"
"v_add_f32 v215, v215, v209\n"
"v_cvt_pk_bf16_f32 v163, v208, v209\n"
"v_exp_f32 v212, v18\n"
"v_mov_b32 v216, v210\n"
"v_exp_f32 v213, v19\n"
"v_mov_b32 v217, v211\n"
"v_cvt_pk_bf16_f32 v164, v210, v211\n"
"v_mfma_f32_32x32x16_bf16 a[48:63], v[192:195], v[192:195], 0\n"
"v_exp_f32 v208, v20\n"
"v_add_f32 v216, v216, v212\n"
"v_exp_f32 v209, v21\n"
"v_add_f32 v217, v217, v213\n"
"v_cvt_pk_bf16_f32 v165, v212, v213\n"
"v_exp_f32 v210, v22\n"
"v_add_f32 v216, v216, v208\n"
"v_exp_f32 v211, v23\n"
"v_add_f32 v217, v217, v209\n"
"v_mfma_f32_32x32x16_bf16 a[64:79], v[192:195], v[192:195], 0\n"
"v_cvt_pk_bf16_f32 v166, v208, v209\n"
"v_exp_f32 v212, v8\n"
"v_add_f32 v216, v216, v210\n"
"v_exp_f32 v213, v9\n"
"v_add_f32 v217, v217, v211\n"
"v_cvt_pk_bf16_f32 v167, v210, v211\n"
"v_exp_f32 v208, v10\n"
"ds_read_b128 a[192:195], v224 offset:16384\n"
"v_add_f32 v214, v214, v212\n"
"v_exp_f32 v209, v11\n"
"v_mfma_f32_32x32x16_bf16 a[80:95], v[192:195], v[192:195], 0\n"
"v_add_f32 v215, v215, v213\n"
"v_cvt_pk_bf16_f32 v168, v212, v213\n"
"ds_read_b128 a[196:199], v224 offset:17408\n"
"v_exp_f32 v210, v12\n"
"v_add_f32 v214, v214, v208\n"
"v_exp_f32 v211, v13\n"
"v_add_f32 v215, v215, v209\n"
"v_cvt_pk_bf16_f32 v169, v208, v209\n"
"ds_read_b128 a[200:203], v224 offset:18432\n"
"v_exp_f32 v212, v14\n"
"v_add_f32 v214, v214, v210\n"
"v_mfma_f32_32x32x16_bf16 a[96:111], v[192:195], v[192:195], 0\n"
"v_exp_f32 v213, v15\n"
"v_add_f32 v215, v215, v211\n"
"ds_read_b128 a[204:207], v224 offset:19456\n"
"v_cvt_pk_bf16_f32 v170, v210, v211\n"
"v_exp_f32 v208, v24\n"
"v_add_f32 v214, v214, v212\n"
"v_exp_f32 v209, v25\n"
"v_add_f32 v215, v215, v213\n"
"ds_read_b128 a[208:211], v224 offset:20480\n"
"v_cvt_pk_bf16_f32 v171, v212, v213\n"
"v_exp_f32 v210, v26\n"
"v_mfma_f32_32x32x16_bf16 a[112:127], v[192:195], v[192:195], 0\n"
"v_add_f32 v216, v216, v208\n"
"v_exp_f32 v211, v27\n"
"ds_read_b128 a[212:215], v224 offset:21504\n"
"v_add_f32 v217, v217, v209\n"
"v_cvt_pk_bf16_f32 v172, v208, v209\n"
"v_exp_f32 v212, v28\n"
"v_add_f32 v216, v216, v210\n"
"v_exp_f32 v213, v29\n"
"ds_read_b128 a[216:219], v224 offset:22528\n"
"v_add_f32 v217, v217, v211\n"
"v_cvt_pk_bf16_f32 v173, v210, v211\n"
"v_exp_f32 v208, v30\n"
"v_add_f32 v216, v216, v212\n"
"v_exp_f32 v209, v31\n"
"v_add_f32 v217, v217, v213\n"
"v_cvt_pk_bf16_f32 v174, v212, v213\n"
"v_add_f32 v216, v216, v208\n"
"v_add_f32 v217, v217, v209\n"
"v_cvt_pk_bf16_f32 v175, v208, v209\n"
"s_add_u32 s32, s32, 1\n"
"s_add_u32 s33, s33, 1\n"
"s_bitcmp1_b32 s26, s32\n"
"s_cbranch_scc1 DISP_1_%=\n"
"s_branch MID_1_%=\n"
"FIRST2_1_%=:\n"
"s_waitcnt lgkmcnt(6)\n"
"v_mfma_f32_32x32x16_bf16 v[64:79], a[192:195], a[128:131], 0\n"
"v_accvgpr_read_b32 v24, a56\n"
"v_accvgpr_read_b32 v25, a57\n"
"v_accvgpr_read_b32 v26, a58\n"
"v_accvgpr_read_b32 v27, a59\n"
"v_accvgpr_read_b32 v28, a60\n"
"v_mfma_f32_32x32x16_bf16 v[80:95], a[192:195], a[160:163], 0\n"
"ds_read_b128 a[220:223], v224 offset:23552\n"
"v_accvgpr_read_b32 v29, a61\n"
"v_accvgpr_read_b32 v30, a62\n"
"v_accvgpr_read_b32 v31, a63\n"
"v_mul_f32 v24, v24, v246\n"
"v_mul_f32 v25, v25, v246\n"
"s_waitcnt lgkmcnt(4)\n"
"v_mfma_f32_32x32x16_bf16 v[64:79], a[196:199], a[132:135], v[64:79]\n"
"v_mul_f32 v26, v26, v246\n"
"v_mul_f32 v27, v27, v246\n"
"v_mul_f32 v28, v28, v246\n"
"v_mul_f32 v29, v29, v246\n"
"v_mfma_f32_32x32x16_bf16 v[80:95], a[196:199], a[164:167], v[80:95]\n"
"ds_read_b128 a[224:227], v224 offset:24576\n"
"v_mul_f32 v30, v30, v246\n"
"v_mul_f32 v31, v31, v246\n"
"v_cvt_pk_bf16_f32 v32, v24, v25\n"
"v_cvt_pk_bf16_f32 v33, v26, v27\n"
"v_cvt_pk_bf16_f32 v34, v28, v29\n"
"v_mfma_f32_32x32x16_bf16 v[64:79], a[200:203], a[136:139], v[64:79]\n"
"v_cvt_pk_bf16_f32 v35, v30, v31\n"
"buffer_store_dwordx4 v[32:35], v228, s[96:99], s39 offen offset:1536 sc1\n"
"v_accvgpr_read_b32 v36, a64\n"
"v_accvgpr_read_b32 v37, a65\n"
"v_accvgpr_read_b32 v38, a66\n"
"v_mfma_f32_32x32x16_bf16 v[80:95], a[200:203], a[168:171], v[80:95]\n"
"ds_read_b128 a[228:231], v224 offset:25600\n"
"v_accvgpr_read_b32 v39, a67\n"
"v_accvgpr_read_b32 v40, a68\n"
"v_accvgpr_read_b32 v41, a69\n"
"v_accvgpr_read_b32 v42, a70\n"
"v_mfma_f32_32x32x16_bf16 v[64:79], a[204:207], a[140:143], v[64:79]\n"
"v_accvgpr_read_b32 v43, a71\n"
"v_mul_f32 v36, v36, v244\n"
"v_mul_f32 v37, v37, v244\n"
"v_mul_f32 v38, v38, v244\n"
"v_mul_f32 v39, v39, v244\n"
"v_mfma_f32_32x32x16_bf16 v[80:95], a[204:207], a[172:175], v[80:95]\n"
"ds_read_b128 a[232:235], v224 offset:26624\n"
"v_mul_f32 v40, v40, v244\n"
"v_mul_f32 v41, v41, v244\n"
"v_mul_f32 v42, v42, v244\n"
"v_mul_f32 v43, v43, v244\n"
"s_waitcnt lgkmcnt(3)\n"
"v_mfma_f32_32x32x16_bf16 v[64:79], a[208:211], a[144:147], v[64:79]\n"
"v_cvt_pk_bf16_f32 v44, v36, v37\n"
"v_cvt_pk_bf16_f32 v45, v38, v39\n"
"v_cvt_pk_bf16_f32 v46, v40, v41\n"
"v_cvt_pk_bf16_f32 v47, v42, v43\n"
"buffer_store_dwordx4 v[44:47], v228, s[96:99], s38 offen offset:2048 sc1\n"
"v_mfma_f32_32x32x16_bf16 v[80:95], a[208:211], a[176:179], v[80:95]\n"
"ds_read_b128 a[236:239], v224 offset:27648\n"
"v_accvgpr_read_b32 v48, a72\n"
"v_accvgpr_read_b32 v49, a73\n"
"v_accvgpr_read_b32 v50, a74\n"
"v_accvgpr_read_b32 v51, a75\n"
"v_mfma_f32_32x32x16_bf16 v[64:79], a[212:215], a[148:151], v[64:79]\n"
"v_accvgpr_read_b32 v52, a76\n"
"v_accvgpr_read_b32 v53, a77\n"
"v_accvgpr_read_b32 v54, a78\n"
"v_accvgpr_read_b32 v55, a79\n"
"v_mul_f32 v48, v48, v244\n"
"v_mfma_f32_32x32x16_bf16 v[80:95], a[212:215], a[180:183], v[80:95]\n"
"ds_read_b128 a[240:243], v224 offset:28672\n"
"v_mul_f32 v49, v49, v244\n"
"v_mul_f32 v50, v50, v244\n"
"v_mul_f32 v51, v51, v244\n"
"v_mul_f32 v52, v52, v244\n"
"v_mfma_f32_32x32x16_bf16 v[64:79], a[216:219], a[152:155], v[64:79]\n"
"v_mul_f32 v53, v53, v244\n"
"v_mul_f32 v54, v54, v244\n"
"v_mul_f32 v55, v55, v244\n"
"v_cvt_pk_bf16_f32 v56, v48, v49\n"
"v_cvt_pk_bf16_f32 v57, v50, v51\n"
"v_mfma_f32_32x32x16_bf16 v[80:95], a[216:219], a[184:187], v[80:95]\n"
"ds_read_b128 a[244:247], v224 offset:29696\n"
"v_cvt_pk_bf16_f32 v58, v52, v53\n"
"v_cvt_pk_bf16_f32 v59, v54, v55\n"
"buffer_store_dwordx4 v[56:59], v228, s[96:99], s38 offen offset:2560 sc1\n"
"v_accvgpr_read_b32 v0, a80\n"
"v_mfma_f32_32x32x16_bf16 v[64:79], a[220:223], a[156:159], v[64:79]\n"
"v_accvgpr_read_b32 v1, a81\n"
"v_accvgpr_read_b32 v2, a82\n"
"v_accvgpr_read_b32 v3, a83\n"
"v_accvgpr_read_b32 v4, a84\n"
"v_accvgpr_read_b32 v5, a85\n"
"v_mfma_f32_32x32x16_bf16 v[80:95], a[220:223], a[188:191], v[80:95]\n"
"ds_read_b128 a[248:251], v224 offset:30720\n"
"v_accvgpr_read_b32 v6, a86\n"
"v_accvgpr_read_b32 v7, a87\n"
"v_mul_f32 v0, v0, v246\n"
"v_mul_f32 v1, v1, v246\n"
"s_waitcnt lgkmcnt(3)\n"
"v_mfma_f32_32x32x16_bf16 v[96:111], a[224:227], a[128:131], 0\n"
"v_mov_b32 v192, 0\n"
"v_mul_f32 v2, v2, v246\n"
"v_mul_f32 v3, v3, v246\n"
"v_mul_f32 v4, v4, v246\n"
"v_mul_f32 v5, v5, v246\n"
"v_mfma_f32_32x32x16_bf16 v[112:127], a[224:227], a[160:163], 0\n"
"ds_read_b128 a[252:255], v224 offset:31744\n"
"v_mov_b32 v193, 0\n"
"v_mul_f32 v6, v6, v246\n"
"v_mul_f32 v7, v7, v246\n"
"v_cvt_pk_bf16_f32 v8, v0, v1\n"
"v_mfma_f32_32x32x16_bf16 v[96:111], a[228:231], a[132:135], v[96:111]\n"
"v_readlane_b32 s34, v229, s32\n"
"v_readlane_b32 s35, v229, s33\n"
"v_mov_b32 v194, 0\n"
"v_cvt_pk_bf16_f32 v9, v2, v3\n"
"v_cvt_pk_bf16_f32 v10, v4, v5\n"
"v_cvt_pk_bf16_f32 v11, v6, v7\n"
"v_mfma_f32_32x32x16_bf16 v[112:127], a[228:231], a[164:167], v[112:127]\n"
"buffer_store_dwordx4 v[8:11], v228, s[96:99], s39 offen offset:2048 sc1\n"
"v_readlane_b32 s36, v230, s32\n"
"v_readlane_b32 s37, v230, s33\n"
"v_mov_b32 v195, 0\n"
"v_accvgpr_read_b32 v12, a88\n"
"v_mfma_f32_32x32x16_bf16 v[96:111], a[232:235], a[136:139], v[96:111]\n"
"v_accvgpr_read_b32 v13, a89\n"
"v_accvgpr_read_b32 v14, a90\n"
"v_accvgpr_read_b32 v15, a91\n"
"v_accvgpr_read_b32 v16, a92\n"
"v_accvgpr_read_b32 v17, a93\n"
"v_mfma_f32_32x32x16_bf16 v[112:127], a[232:235], a[168:171], v[112:127]\n"
"v_accvgpr_read_b32 v18, a94\n"
"v_accvgpr_read_b32 v19, a95\n"
"v_mul_f32 v12, v12, v246\n"
"v_mul_f32 v13, v13, v246\n"
"v_mul_f32 v14, v14, v246\n"
"v_mfma_f32_32x32x16_bf16 v[96:111], a[236:239], a[140:143], v[96:111]\n"
"v_mul_f32 v15, v15, v246\n"
"v_mul_f32 v16, v16, v246\n"
"v_mul_f32 v17, v17, v246\n"
"s_waitcnt lgkmcnt(0)\n"
"s_waitcnt vmcnt(6)\n"
"s_barrier\n"
"v_mfma_f32_32x32x16_bf16 v[112:127], a[236:239], a[172:175], v[112:127]\n"
"v_max_f32 v238, v64, v65\n"
"v_max_f32 v239, v66, v67\n"
"v_mul_f32 v18, v18, v246\n"
"v_mul_f32 v19, v19, v246\n"
"v_cvt_pk_bf16_f32 v20, v12, v13\n"
"v_cvt_pk_bf16_f32 v21, v14, v15\n"
"v_mfma_f32_32x32x16_bf16 v[96:111], a[240:243], a[144:147], v[96:111]\n"
"v_max3_f32 v238, v238, v68, v69\n"
"v_max3_f32 v239, v239, v70, v71\n"
"v_cvt_pk_bf16_f32 v22, v16, v17\n"
"v_cvt_pk_bf16_f32 v23, v18, v19\n"
"s_add_u32 m0, s57, 49152\n"
"v_mfma_f32_32x32x16_bf16 v[112:127], a[240:243], a[176:179], v[112:127]\n"
"buffer_load_dwordx4 v251, s[96:99], s34 offen offset:0 sc1 lds\n"
"buffer_store_dwordx4 v[20:23], v228, s[96:99], s39 offen offset:2560 sc1\n"
"v_max3_f32 v238, v238, v72, v73\n"
"v_max3_f32 v239, v239, v74, v75\n"
"v_accvgpr_read_b32 v24, a96\n"
"v_accvgpr_read_b32 v25, a97\n"
"v_mfma_f32_32x32x16_bf16 v[96:111], a[244:247], a[148:151], v[96:111]\n"
"v_max3_f32 v238, v238, v76, v77\n"
"v_max3_f32 v239, v239, v78, v79\n"
"PG13_%=:\n"
"s_cbranch_execz RPG13_%=\n"
"v_accvgpr_read_b32 v26, a98\n"
"v_accvgpr_read_b32 v27, a99\n"
"v_accvgpr_read_b32 v28, a100\n"
"v_mfma_f32_32x32x16_bf16 v[112:127], a[244:247], a[180:183], v[112:127]\n"
"v_max_f32 v240, v80, v81\n"
"v_max_f32 v241, v82, v83\n"
"v_accvgpr_read_b32 v29, a101\n"
"v_accvgpr_read_b32 v30, a102\n"
"v_accvgpr_read_b32 v31, a103\n"
"v_mfma_f32_32x32x16_bf16 v[96:111], a[248:251], a[152:155], v[96:111]\n"
"buffer_load_dwordx4 v251, s[96:99], s34 offen offset:1024 sc1 lds\n"
"v_max3_f32 v240, v240, v84, v85\n"
"v_max3_f32 v241, v241, v86, v87\n"
"v_mul_f32 v24, v24, v244\n"
"v_mul_f32 v25, v25, v244\n"
"v_mul_f32 v26, v26, v244\n"
"v_mfma_f32_32x32x16_bf16 v[112:127], a[248:251], a[184:187], v[112:127]\n"
"v_max3_f32 v240, v240, v88, v89\n"
"v_max3_f32 v241, v241, v90, v91\n"
"v_mul_f32 v27, v27, v244\n"
"v_mul_f32 v28, v28, v244\n"
"v_mul_f32 v29, v29, v244\n"
"v_mfma_f32_32x32x16_bf16 v[96:111], a[252:255], a[156:159], v[96:111]\n"
"v_max3_f32 v240, v240, v92, v93\n"
"v_max3_f32 v241, v241, v94, v95\n"
"v_mul_f32 v30, v30, v244\n"
"v_mul_f32 v31, v31, v244\n"
"v_cvt_pk_bf16_f32 v32, v24, v25\n"
"v_mfma_f32_32x32x16_bf16 v[112:127], a[252:255], a[188:191], v[112:127]\n"
"buffer_load_dwordx4 v251, s[96:99], s34 offen offset:2048 sc1 lds\n"
"v_cvt_pk_bf16_f32 v33, v26, v27\n"
"v_cvt_pk_bf16_f32 v34, v28, v29\n"
"v_cvt_pk_bf16_f32 v35, v30, v31\n"
"buffer_store_dwordx4 v[32:35], v228, s[96:99], s38 offen offset:3072 sc1\n"
"v_accvgpr_read_b32 v36, a104\n"
"v_accvgpr_read_b32 v37, a105\n"
"v_accvgpr_read_b32 v38, a106\n"
"v_accvgpr_read_b32 v39, a107\n"
"v_accvgpr_read_b32 v40, a108\n"
"v_accvgpr_read_b32 v41, a109\n"
"buffer_load_dwordx4 v251, s[96:99], s34 offen offset:3072 sc1 lds\n"
"v_accvgpr_read_b32 v42, a110\n"
"v_accvgpr_read_b32 v43, a111\n"
"v_pk_mul_f32 v[36:37], v[36:37], v[244:245]\n"
"v_pk_mul_f32 v[38:39], v[38:39], v[244:245]\n"
"v_pk_mul_f32 v[40:41], v[40:41], v[244:245]\n"
"v_pk_mul_f32 v[42:43], v[42:43], v[244:245]\n"
"v_cvt_pk_bf16_f32 v44, v36, v37\n"
"v_cvt_pk_bf16_f32 v45, v38, v39\n"
"v_cvt_pk_bf16_f32 v46, v40, v41\n"
"v_cvt_pk_bf16_f32 v47, v42, v43\n"
"buffer_store_dwordx4 v[44:47], v228, s[96:99], s38 offen offset:3584 sc1\n"
"v_accvgpr_read_b32 v48, a112\n"
"v_accvgpr_read_b32 v49, a113\n"
"s_add_u32 m0, s57, 114688\n"
"s_nop 0\n"
"buffer_load_dwordx4 v251, s[96:99], s35 offen offset:0 sc1 lds\n"
"v_accvgpr_read_b32 v50, a114\n"
"v_accvgpr_read_b32 v51, a115\n"
"v_accvgpr_read_b32 v52, a116\n"
"v_accvgpr_read_b32 v53, a117\n"
"v_accvgpr_read_b32 v54, a118\n"
"v_accvgpr_read_b32 v55, a119\n"
"v_pk_mul_f32 v[48:49], v[48:49], v[246:247]\n"
"v_pk_mul_f32 v[50:51], v[50:51], v[246:247]\n"
"v_pk_mul_f32 v[52:53], v[52:53], v[246:247]\n"
"v_pk_mul_f32 v[54:55], v[54:55], v[246:247]\n"
"v_cvt_pk_bf16_f32 v56, v48, v49\n"
"v_cvt_pk_bf16_f32 v57, v50, v51\n"
"v_cvt_pk_bf16_f32 v58, v52, v53\n"
"buffer_load_dwordx4 v251, s[96:99], s35 offen offset:1024 sc1 lds\n"
"v_cvt_pk_bf16_f32 v59, v54, v55\n"
"buffer_store_dwordx4 v[56:59], v228, s[96:99], s39 offen offset:3072 sc1\n"
"v_accvgpr_read_b32 v0, a120\n"
"v_accvgpr_read_b32 v1, a121\n"
"v_accvgpr_read_b32 v2, a122\n"
"v_accvgpr_read_b32 v3, a123\n"
"v_accvgpr_read_b32 v4, a124\n"
"v_accvgpr_read_b32 v5, a125\n"
"v_accvgpr_read_b32 v6, a126\n"
"v_accvgpr_read_b32 v7, a127\n"
"v_pk_mul_f32 v[0:1], v[0:1], v[246:247]\n"
"v_pk_mul_f32 v[2:3], v[2:3], v[246:247]\n"
"v_pk_mul_f32 v[4:5], v[4:5], v[246:247]\n"
"buffer_load_dwordx4 v251, s[96:99], s35 offen offset:2048 sc1 lds\n"
"v_pk_mul_f32 v[6:7], v[6:7], v[246:247]\n"
"v_cvt_pk_bf16_f32 v8, v0, v1\n"
"v_cvt_pk_bf16_f32 v9, v2, v3\n"
"v_cvt_pk_bf16_f32 v10, v4, v5\n"
"v_cvt_pk_bf16_f32 v11, v6, v7\n"
"buffer_store_dwordx4 v[8:11], v228, s[96:99], s39 offen offset:3584 sc1\n"
"s_lshl_b32 s38, s10, 17\n"
"s_lshl_b32 s39, s24, 12\n"
"s_add_u32 s38, s38, s39\n"
"s_add_u32 s38, s38, 50331648\n"
"v_max3_f32 v238, v238, v96, v97\n"
"v_max3_f32 v239, v239, v98, v99\n"
"v_max3_f32 v238, v238, v100, v101\n"
"buffer_load_dwordx4 v251, s[96:99], s35 offen offset:3072 sc1 lds\n"
"v_max3_f32 v239, v239, v102, v103\n"
"v_max3_f32 v238, v238, v104, v105\n"
"v_max3_f32 v239, v239, v106, v107\n"
"v_max3_f32 v238, v238, v108, v109\n"
"v_max3_f32 v239, v239, v110, v111\n"
"v_max3_f32 v240, v240, v112, v113\n"
"v_max3_f32 v241, v241, v114, v115\n"
"v_max3_f32 v240, v240, v116, v117\n"
"v_max3_f32 v241, v241, v118, v119\n"
"v_max3_f32 v240, v240, v120, v121\n"
"v_max3_f32 v241, v241, v122, v123\n"
"v_max3_f32 v240, v240, v124, v125\n"
"v_max3_f32 v241, v241, v126, v127\n"
"s_add_u32 m0, s57, 16384\n"
"s_nop 0\n"
"buffer_load_dwordx4 v251, s[96:99], s36 offen offset:0 sc1 lds\n"
"v_max_f32 v238, v238, v239\n"
"v_mov_b32 v239, v238\n"
"v_max_f32 v240, v240, v241\n"
"v_mov_b32 v241, v240\n"
"v_permlane32_swap_b32 v238, v239\n"
"s_nop 0\n"
"v_permlane32_swap_b32 v240, v241\n"
"v_max_f32 v222, v238, v239\n"
"v_max_f32 v222, 0xf149f2ca, v222\n"
"v_mov_b32 v220, 0\n"
"v_max_f32 v223, v240, v241\n"
"v_max_f32 v223, 0xf149f2ca, v223\n"
"v_mov_b32 v221, 0\n"
"v_sub_f32 v128, 0, v222\n"
"buffer_load_dwordx4 v251, s[96:99], s36 offen offset:1024 sc1 lds\n"
"v_sub_f32 v129, 0, v222\n"
"v_pk_mul_f32 v[130:131], v[128:129], 1.0 op_sel_hi:[1,0]\n"
"v_pk_mul_f32 v[132:133], v[128:129], 1.0 op_sel_hi:[1,0]\n"
"v_pk_mul_f32 v[134:135], v[128:129], 1.0 op_sel_hi:[1,0]\n"
"v_pk_mul_f32 v[136:137], v[128:129], 1.0 op_sel_hi:[1,0]\n"
"v_pk_mul_f32 v[138:139], v[128:129], 1.0 op_sel_hi:[1,0]\n"
"v_pk_mul_f32 v[140:141], v[128:129], 1.0 op_sel_hi:[1,0]\n"
"v_pk_mul_f32 v[142:143], v[128:129], 1.0 op_sel_hi:[1,0]\n"
"v_sub_f32 v144, 0, v223\n"
"v_sub_f32 v145, 0, v223\n"
"v_pk_mul_f32 v[146:147], v[144:145], 1.0 op_sel_hi:[1,0]\n"
"v_pk_mul_f32 v[148:149], v[144:145], 1.0 op_sel_hi:[1,0]\n"
"v_pk_mul_f32 v[150:151], v[144:145], 1.0 op_sel_hi:[1,0]\n"
"buffer_load_dwordx4 v251, s[96:99], s36 offen offset:2048 sc1 lds\n"
"v_pk_mul_f32 v[152:153], v[144:145], 1.0 op_sel_hi:[1,0]\n"
"v_pk_mul_f32 v[154:155], v[144:145], 1.0 op_sel_hi:[1,0]\n"
"v_pk_mul_f32 v[156:157], v[144:145], 1.0 op_sel_hi:[1,0]\n"
"v_pk_mul_f32 v[158:159], v[144:145], 1.0 op_sel_hi:[1,0]\n"
"v_pk_add_f32 v[64:65], v[64:65], v[128:129]\n"
"v_pk_add_f32 v[66:67], v[66:67], v[128:129]\n"
"v_pk_add_f32 v[68:69], v[68:69], v[128:129]\n"
"v_pk_add_f32 v[70:71], v[70:71], v[128:129]\n"
"v_pk_add_f32 v[72:73], v[72:73], v[128:129]\n"
"v_pk_add_f32 v[74:75], v[74:75], v[128:129]\n"
"v_pk_add_f32 v[76:77], v[76:77], v[128:129]\n"
"v_pk_add_f32 v[78:79], v[78:79], v[128:129]\n"
"v_pk_add_f32 v[80:81], v[80:81], v[144:145]\n"
"buffer_load_dwordx4 v251, s[96:99], s36 offen offset:3072 sc1 lds\n"
"v_pk_add_f32 v[82:83], v[82:83], v[144:145]\n"
"v_pk_add_f32 v[84:85], v[84:85], v[144:145]\n"
"v_pk_add_f32 v[86:87], v[86:87], v[144:145]\n"
"v_pk_add_f32 v[88:89], v[88:89], v[144:145]\n"
"v_pk_add_f32 v[90:91], v[90:91], v[144:145]\n"
"v_pk_add_f32 v[92:93], v[92:93], v[144:145]\n"
"v_pk_add_f32 v[94:95], v[94:95], v[144:145]\n"
"v_pk_add_f32 v[96:97], v[96:97], v[128:129]\n"
"v_pk_add_f32 v[98:99], v[98:99], v[128:129]\n"
"v_pk_add_f32 v[100:101], v[100:101], v[128:129]\n"
"v_pk_add_f32 v[102:103], v[102:103], v[128:129]\n"
"v_pk_add_f32 v[104:105], v[104:105], v[128:129]\n"
"v_pk_add_f32 v[106:107], v[106:107], v[128:129]\n"
"s_add_u32 m0, s57, 81920\n"
"s_nop 0\n"
"buffer_load_dwordx4 v251, s[96:99], s37 offen offset:0 sc1 lds\n"
"v_pk_add_f32 v[108:109], v[108:109], v[128:129]\n"
"v_pk_add_f32 v[110:111], v[110:111], v[128:129]\n"
"v_pk_add_f32 v[112:113], v[112:113], v[144:145]\n"
"v_pk_add_f32 v[114:115], v[114:115], v[144:145]\n"
"v_pk_add_f32 v[116:117], v[116:117], v[144:145]\n"
"v_pk_add_f32 v[118:119], v[118:119], v[144:145]\n"
"v_pk_add_f32 v[120:121], v[120:121], v[144:145]\n"
"v_pk_add_f32 v[122:123], v[122:123], v[144:145]\n"
"v_pk_add_f32 v[124:125], v[124:125], v[144:145]\n"
"v_pk_add_f32 v[126:127], v[126:127], v[144:145]\n"
"v_mfma_f32_32x32x16_bf16 a[0:15], v[192:195], v[192:195], 0\n"
"v_exp_f32 v208, v64\n"
"v_exp_f32 v209, v65\n"
"buffer_load_dwordx4 v251, s[96:99], s37 offen offset:1024 sc1 lds\n"
"v_exp_f32 v210, v66\n"
"v_mov_b32 v214, v208\n"
"v_exp_f32 v211, v67\n"
"v_mov_b32 v215, v209\n"
"v_cvt_pk_bf16_f32 v176, v208, v209\n"
"v_exp_f32 v212, v68\n"
"v_add_f32 v214, v214, v210\n"
"v_mfma_f32_32x32x16_bf16 a[16:31], v[192:195], v[192:195], 0\n"
"v_exp_f32 v213, v69\n"
"v_add_f32 v215, v215, v211\n"
"v_cvt_pk_bf16_f32 v177, v210, v211\n"
"v_exp_f32 v208, v70\n"
"v_add_f32 v214, v214, v212\n"
"buffer_load_dwordx4 v251, s[96:99], s37 offen offset:2048 sc1 lds\n"
"v_exp_f32 v209, v71\n"
"v_add_f32 v215, v215, v213\n"
"v_cvt_pk_bf16_f32 v178, v212, v213\n"
"v_exp_f32 v210, v80\n"
"v_mfma_f32_32x32x16_bf16 a[32:47], v[192:195], v[192:195], 0\n"
"v_add_f32 v214, v214, v208\n"
"v_exp_f32 v211, v81\n"
"v_add_f32 v215, v215, v209\n"
"v_cvt_pk_bf16_f32 v179, v208, v209\n"
"v_exp_f32 v212, v82\n"
"v_mov_b32 v216, v210\n"
"v_exp_f32 v213, v83\n"
"v_mov_b32 v217, v211\n"
"buffer_load_dwordx4 v251, s[96:99], s37 offen offset:3072 sc1 lds\n"
"v_cvt_pk_bf16_f32 v180, v210, v211\n"
"v_mfma_f32_32x32x16_bf16 a[48:63], v[192:195], v[192:195], 0\n"
"v_exp_f32 v208, v84\n"
"v_add_f32 v216, v216, v212\n"
"v_exp_f32 v209, v85\n"
"v_add_f32 v217, v217, v213\n"
"v_cvt_pk_bf16_f32 v181, v212, v213\n"
"v_exp_f32 v210, v86\n"
"v_add_f32 v216, v216, v208\n"
"v_exp_f32 v211, v87\n"
"v_add_f32 v217, v217, v209\n"
"v_mfma_f32_32x32x16_bf16 a[64:79], v[192:195], v[192:195], 0\n"
"v_cvt_pk_bf16_f32 v182, v208, v209\n"
"v_exp_f32 v212, v72\n"
"v_add_f32 v216, v216, v210\n"
"v_exp_f32 v213, v73\n"
"v_add_f32 v217, v217, v211\n"
"v_cvt_pk_bf16_f32 v183, v210, v211\n"
"v_exp_f32 v208, v74\n"
"ds_read_b128 a[192:195], v224 offset:0\n"
"v_add_f32 v214, v214, v212\n"
"v_exp_f32 v209, v75\n"
"v_mfma_f32_32x32x16_bf16 a[80:95], v[192:195], v[192:195], 0\n"
"v_add_f32 v215, v215, v213\n"
"v_cvt_pk_bf16_f32 v184, v212, v213\n"
"ds_read_b128 a[196:199], v224 offset:1024\n"
"v_exp_f32 v210, v76\n"
"v_add_f32 v214, v214, v208\n"
"v_exp_f32 v211, v77\n"
"v_add_f32 v215, v215, v209\n"
"v_cvt_pk_bf16_f32 v185, v208, v209\n"
"ds_read_b128 a[200:203], v224 offset:2048\n"
"v_exp_f32 v212, v78\n"
"v_add_f32 v214, v214, v210\n"
"v_mfma_f32_32x32x16_bf16 a[96:111], v[192:195], v[192:195], 0\n"
"v_exp_f32 v213, v79\n"
"v_add_f32 v215, v215, v211\n"
"ds_read_b128 a[204:207], v224 offset:3072\n"
"v_cvt_pk_bf16_f32 v186, v210, v211\n"
"v_exp_f32 v208, v88\n"
"v_add_f32 v214, v214, v212\n"
"v_exp_f32 v209, v89\n"
"v_add_f32 v215, v215, v213\n"
"ds_read_b128 a[208:211], v224 offset:4096\n"
"v_cvt_pk_bf16_f32 v187, v212, v213\n"
"v_exp_f32 v210, v90\n"
"v_mfma_f32_32x32x16_bf16 a[112:127], v[192:195], v[192:195], 0\n"
"v_add_f32 v216, v216, v208\n"
"v_exp_f32 v211, v91\n"
"ds_read_b128 a[212:215], v224 offset:5120\n"
"v_add_f32 v217, v217, v209\n"
"v_cvt_pk_bf16_f32 v188, v208, v209\n"
"v_exp_f32 v212, v92\n"
"v_add_f32 v216, v216, v210\n"
"v_exp_f32 v213, v93\n"
"ds_read_b128 a[216:219], v224 offset:6144\n"
"v_add_f32 v217, v217, v211\n"
"v_cvt_pk_bf16_f32 v189, v210, v211\n"
"v_exp_f32 v208, v94\n"
"v_add_f32 v216, v216, v212\n"
"v_exp_f32 v209, v95\n"
"v_add_f32 v217, v217, v213\n"
"v_cvt_pk_bf16_f32 v190, v212, v213\n"
"v_add_f32 v216, v216, v208\n"
"v_add_f32 v217, v217, v209\n"
"v_cvt_pk_bf16_f32 v191, v208, v209\n"
"s_add_u32 s32, s32, 1\n"
"s_add_u32 s33, s33, 1\n"
"s_bitcmp1_b32 s26, s32\n"
"s_cbranch_scc1 DISP_0_%=\n"
"s_branch MID_0_%=\n"
"DRAINA_0_%=:\n"
"v_exp_f32 v208, v96\n"
"v_exp_f32 v209, v97\n"
"v_readlane_b32 s34, v229, s32\n"
"v_exp_f32 v210, v98\n"
"v_add_f32 v214, v214, v208\n"
"v_exp_f32 v211, v99\n"
"v_add_f32 v215, v215, v209\n"
"v_readlane_b32 s35, v229, s33\n"
"v_cvt_pk_bf16_f32 v192, v208, v209\n"
"v_exp_f32 v212, v100\n"
"v_add_f32 v214, v214, v210\n"
"v_exp_f32 v213, v101\n"
"v_readlane_b32 s36, v230, s32\n"
"v_add_f32 v215, v215, v211\n"
"v_cvt_pk_bf16_f32 v193, v210, v211\n"
"v_exp_f32 v208, v102\n"
"v_add_f32 v214, v214, v212\n"
"v_readlane_b32 s37, v230, s33\n"
"v_exp_f32 v209, v103\n"
"v_add_f32 v215, v215, v213\n"
"v_cvt_pk_bf16_f32 v194, v212, v213\n"
"v_exp_f32 v210, v112\n"
"v_add_f32 v214, v214, v208\n"
"v_exp_f32 v211, v113\n"
"v_add_f32 v215, v215, v209\n"
"v_cvt_pk_bf16_f32 v195, v208, v209\n"
"v_exp_f32 v212, v114\n"
"v_add_f32 v216, v216, v210\n"
"v_exp_f32 v213, v115\n"
"v_add_f32 v217, v217, v211\n"
"v_cvt_pk_bf16_f32 v196, v210, v211\n"
"v_exp_f32 v208, v116\n"
"v_add_f32 v216, v216, v212\n"
"v_exp_f32 v209, v117\n"
"v_add_f32 v217, v217, v213\n"
"v_cvt_pk_bf16_f32 v197, v212, v213\n"
"v_exp_f32 v210, v118\n"
"v_add_f32 v216, v216, v208\n"
"v_exp_f32 v211, v119\n"
"v_add_f32 v217, v217, v209\n"
"v_cvt_pk_bf16_f32 v198, v208, v209\n"
"v_exp_f32 v212, v104\n"
"v_add_f32 v216, v216, v210\n"
"v_exp_f32 v213, v105\n"
"v_add_f32 v217, v217, v211\n"
"v_cvt_pk_bf16_f32 v199, v210, v211\n"
"v_exp_f32 v208, v106\n"
"v_add_f32 v214, v214, v212\n"
"v_exp_f32 v209, v107\n"
"v_add_f32 v215, v215, v213\n"
"v_cvt_pk_bf16_f32 v200, v212, v213\n"
"v_exp_f32 v210, v108\n"
"v_add_f32 v214, v214, v208\n"
"v_exp_f32 v211, v109\n"
"v_add_f32 v215, v215, v209\n"
"v_cvt_pk_bf16_f32 v201, v208, v209\n"
"v_exp_f32 v212, v110\n"
"v_add_f32 v214, v214, v210\n"
"v_exp_f32 v213, v111\n"
"v_add_f32 v215, v215, v211\n"
"v_cvt_pk_bf16_f32 v202, v210, v211\n"
"v_exp_f32 v208, v120\n"
"v_add_f32 v214, v214, v212\n"
"v_exp_f32 v209, v121\n"
"v_add_f32 v215, v215, v213\n"
"v_cvt_pk_bf16_f32 v203, v212, v213\n"
"v_exp_f32 v210, v122\n"
"v_add_f32 v216, v216, v208\n"
"v_exp_f32 v211, v123\n"
"v_add_f32 v217, v217, v209\n"
"v_cvt_pk_bf16_f32 v204, v208, v209\n"
"v_exp_f32 v212, v124\n"
"v_add_f32 v216, v216, v210\n"
"v_exp_f32 v213, v125\n"
"v_add_f32 v217, v217, v211\n"
"v_cvt_pk_bf16_f32 v205, v210, v211\n"
"v_exp_f32 v208, v126\n"
"v_add_f32 v216, v216, v212\n"
"v_exp_f32 v209, v127\n"
"v_add_f32 v217, v217, v213\n"
"v_cvt_pk_bf16_f32 v206, v212, v213\n"
"v_add_f32 v216, v216, v208\n"
"v_add_f32 v217, v217, v209\n"
"v_cvt_pk_bf16_f32 v207, v208, v209\n"
"v_add_f32 v218, v214, v215\n"
"v_add_f32 v219, v216, v217\n"
"v_max_f32 v236, v218, v219\n"
"v_cmp_nge_f32 vcc, s42, v236\n"
"s_nop 0\n"
"s_cbranch_vccnz STUB_DRAINA_0_%=\n"
"RET_DRAINA_0_%=:\n"
"v_add_f32 v220, v220, v218\n"
"v_add_f32 v221, v221, v219\n"
"v_mov_b32 v236, v220\n"
"v_mov_b32 v237, v221\n"
"s_nop 0\n"
"v_permlane32_swap_b32 v220, v236\n"
"v_permlane32_swap_b32 v221, v237\n"
"v_add_f32 v220, v220, v236\n"
"v_add_f32 v221, v221, v237\n"
"v_rcp_f32 v244, v220\n"
"v_rcp_f32 v246, v221\n"
"v_mov_b32 v245, v244\n"
"v_mov_b32 v247, v246\n"
"s_add_u32 s39, s38, 262144\n"
"s_waitcnt vmcnt(0) lgkmcnt(0)\n"
"s_barrier\n"
"ds_read_b128 a[192:195], v224 offset:49152\n"
"ds_read_b128 a[196:199], v224 offset:53248\n"
"ds_read_b128 a[200:203], v224 offset:57344\n"
"ds_read_b128 a[204:207], v224 offset:61440\n"
"s_waitcnt lgkmcnt(3)\n"
"v_mfma_f32_32x32x16_bf16 a[0:15], a[192:195], v[176:179], a[0:15]\n"
"ds_read_b128 a[208:211], v224 offset:50176\n"
"buffer_load_dwordx4 a[128:131], v251, s[96:99], s43 offen offset:0 sc1\n"
"v_mfma_f32_32x32x16_bf16 a[16:31], a[192:195], v[180:183], a[16:31]\n"
"buffer_load_dwordx4 a[132:135], v251, s[96:99], s43 offen offset:1024 sc1\n"
"s_waitcnt lgkmcnt(3)\n"
"v_mfma_f32_32x32x16_bf16 a[0:15], a[196:199], v[184:187], a[0:15]\n"
"ds_read_b128 a[212:215], v224 offset:54272\n"
"buffer_load_dwordx4 a[136:139], v251, s[96:99], s43 offen offset:2048 sc1\n"
"v_mfma_f32_32x32x16_bf16 a[16:31], a[196:199], v[188:191], a[16:31]\n"
"buffer_load_dwordx4 a[140:143], v251, s[96:99], s43 offen offset:3072 sc1\n"
"s_add_u32 s43, s43, 4096\n"
"s_waitcnt lgkmcnt(3)\n"
"v_mfma_f32_32x32x16_bf16 a[0:15], a[200:203], v[192:195], a[0:15]\n"
"ds_read_b128 a[216:219], v224 offset:58368\n"
"buffer_load_dwordx4 a[144:147], v251, s[96:99], s43 offen offset:0 sc1\n"
"v_mfma_f32_32x32x16_bf16 a[16:31], a[200:203], v[196:199], a[16:31]\n"
"buffer_load_dwordx4 a[148:151], v251, s[96:99], s43 offen offset:1024 sc1\n"
"s_waitcnt lgkmcnt(2)\n"
"v_mfma_f32_32x32x16_bf16 a[0:15], a[204:207], v[200:203], a[0:15]\n"
"ds_read_b128 a[220:223], v224 offset:62464\n"
"buffer_load_dwordx4 a[152:155], v251, s[96:99], s43 offen offset:2048 sc1\n"
"v_mfma_f32_32x32x16_bf16 a[16:31], a[204:207], v[204:207], a[16:31]\n"
"buffer_load_dwordx4 a[156:159], v251, s[96:99], s43 offen offset:3072 sc1\n"
"s_add_u32 s43, s43, 4096\n"
"v_mfma_f32_32x32x16_bf16 a[32:47], a[208:211], v[176:179], a[32:47]\n"
"ds_read_b128 a[224:227], v224 offset:51200\n"
"buffer_load_dwordx4 a[160:163], v251, s[96:99], s43 offen offset:0 sc1\n"
"v_mfma_f32_32x32x16_bf16 a[48:63], a[208:211], v[180:183], a[48:63]\n"
"buffer_load_dwordx4 a[164:167], v251, s[96:99], s43 offen offset:1024 sc1\n"
"s_nop 1\n"
"v_accvgpr_read_b32 v0, a0\n"
"v_accvgpr_read_b32 v1, a1\n"
"v_accvgpr_read_b32 v2, a2\n"
"v_accvgpr_read_b32 v3, a3\n"
"v_accvgpr_read_b32 v4, a4\n"
"v_accvgpr_read_b32 v5, a5\n"
"s_waitcnt lgkmcnt(2)\n"
"v_mfma_f32_32x32x16_bf16 a[32:47], a[212:215], v[184:187], a[32:47]\n"
"ds_read_b128 a[228:231], v224 offset:55296\n"
"buffer_load_dwordx4 a[168:171], v251, s[96:99], s43 offen offset:2048 sc1\n"
"v_accvgpr_read_b32 v6, a6\n"
"v_accvgpr_read_b32 v7, a7\n"
"v_mul_f32 v0, v0, v244\n"
"v_mul_f32 v1, v1, v244\n"
"v_mul_f32 v2, v2, v244\n"
"v_mfma_f32_32x32x16_bf16 a[48:63], a[212:215], v[188:191], a[48:63]\n"
"buffer_load_dwordx4 a[172:175], v251, s[96:99], s43 offen offset:3072 sc1\n"
"v_mul_f32 v3, v3, v244\n"
"v_mul_f32 v4, v4, v244\n"
"v_mul_f32 v5, v5, v244\n"
"v_mul_f32 v6, v6, v244\n"
"s_add_u32 s43, s43, 4096\n"
"v_mfma_f32_32x32x16_bf16 a[32:47], a[216:219], v[192:195], a[32:47]\n"
"ds_read_b128 a[232:235], v224 offset:59392\n"
"buffer_load_dwordx4 a[176:179], v251, s[96:99], s43 offen offset:0 sc1\n"
"v_mul_f32 v7, v7, v244\n"
"v_cvt_pk_bf16_f32 v8, v0, v1\n"
"v_cvt_pk_bf16_f32 v9, v2, v3\n"
"v_cvt_pk_bf16_f32 v10, v4, v5\n"
"v_cvt_pk_bf16_f32 v11, v6, v7\n"
"v_mfma_f32_32x32x16_bf16 a[48:63], a[216:219], v[196:199], a[48:63]\n"
"buffer_load_dwordx4 a[180:183], v251, s[96:99], s43 offen offset:1024 sc1\n"
"buffer_store_dwordx4 v[8:11], v228, s[96:99], s38 offen offset:0 sc1\n"
"v_accvgpr_read_b32 v12, a8\n"
"v_accvgpr_read_b32 v13, a9\n"
"v_accvgpr_read_b32 v14, a10\n"
"v_accvgpr_read_b32 v15, a11\n"
"s_waitcnt lgkmcnt(2)\n"
"v_mfma_f32_32x32x16_bf16 a[32:47], a[220:223], v[200:203], a[32:47]\n"
"ds_read_b128 a[236:239], v224 offset:63488\n"
"buffer_load_dwordx4 a[184:187], v251, s[96:99], s43 offen offset:2048 sc1\n"
"v_accvgpr_read_b32 v16, a12\n"
"v_accvgpr_read_b32 v17, a13\n"
"v_accvgpr_read_b32 v18, a14\n"
"v_accvgpr_read_b32 v19, a15\n"
"v_mul_f32 v12, v12, v244\n"
"v_mfma_f32_32x32x16_bf16 a[48:63], a[220:223], v[204:207], a[48:63]\n"
"buffer_load_dwordx4 a[188:191], v251, s[96:99], s43 offen offset:3072 sc1\n"
"v_mul_f32 v13, v13, v244\n"
"v_mul_f32 v14, v14, v244\n"
"v_mul_f32 v15, v15, v244\n"
"v_mul_f32 v16, v16, v244\n"
"s_add_u32 s43, s43, 4096\n"
"s_add_u32 m0, s57, 32768\n"
"v_mfma_f32_32x32x16_bf16 a[64:79], a[224:227], v[176:179], a[64:79]\n"
"ds_read_b128 a[240:243], v224 offset:52224\n"
"buffer_load_dwordx4 v251, s[96:99], s34 offen offset:0 sc1 lds\n"
"v_mul_f32 v17, v17, v244\n"
"v_mul_f32 v18, v18, v244\n"
"v_mul_f32 v19, v19, v244\n"
"v_cvt_pk_bf16_f32 v20, v12, v13\n"
"v_mfma_f32_32x32x16_bf16 a[80:95], a[224:227], v[180:183], a[80:95]\n"
"buffer_load_dwordx4 v251, s[96:99], s34 offen offset:1024 sc1 lds\n"
"v_cvt_pk_bf16_f32 v21, v14, v15\n"
"v_cvt_pk_bf16_f32 v22, v16, v17\n"
"v_cvt_pk_bf16_f32 v23, v18, v19\n"
"buffer_store_dwordx4 v[20:23], v228, s[96:99], s38 offen offset:512 sc1\n"
"v_accvgpr_read_b32 v24, a16\n"
"v_accvgpr_read_b32 v25, a17\n"
"s_waitcnt lgkmcnt(2)\n"
"v_mfma_f32_32x32x16_bf16 a[64:79], a[228:231], v[184:187], a[64:79]\n"
"ds_read_b128 a[244:247], v224 offset:56320\n"
"ds_read_b128 a[192:195], v225 offset:16384\n"
"buffer_load_dwordx4 v251, s[96:99], s34 offen offset:2048 sc1 lds\n"
"v_accvgpr_read_b32 v26, a18\n"
"v_accvgpr_read_b32 v27, a19\n"
"v_accvgpr_read_b32 v28, a20\n"
"v_accvgpr_read_b32 v29, a21\n"
"v_mfma_f32_32x32x16_bf16 a[80:95], a[228:231], v[188:191], a[80:95]\n"
"buffer_load_dwordx4 v251, s[96:99], s34 offen offset:3072 sc1 lds\n"
"v_accvgpr_read_b32 v30, a22\n"
"v_accvgpr_read_b32 v31, a23\n"
"v_mul_f32 v24, v24, v246\n"
"v_mul_f32 v25, v25, v246\n"
"s_add_u32 m0, s57, 98304\n"
"v_mfma_f32_32x32x16_bf16 a[64:79], a[232:235], v[192:195], a[64:79]\n"
"ds_read_b128 a[248:251], v224 offset:60416\n"
"ds_read_b128 a[196:199], v225 offset:17408\n"
"buffer_load_dwordx4 v251, s[96:99], s35 offen offset:0 sc1 lds\n"
"v_mul_f32 v26, v26, v246\n"
"v_mul_f32 v27, v27, v246\n"
"v_mul_f32 v28, v28, v246\n"
"v_mul_f32 v29, v29, v246\n"
"v_mfma_f32_32x32x16_bf16 a[80:95], a[232:235], v[196:199], a[80:95]\n"
"buffer_load_dwordx4 v251, s[96:99], s35 offen offset:1024 sc1 lds\n"
"v_mul_f32 v30, v30, v246\n"
"v_mul_f32 v31, v31, v246\n"
"v_cvt_pk_bf16_f32 v32, v24, v25\n"
"v_cvt_pk_bf16_f32 v33, v26, v27\n"
"v_cvt_pk_bf16_f32 v34, v28, v29\n"
"s_waitcnt lgkmcnt(4)\n"
"v_mfma_f32_32x32x16_bf16 a[64:79], a[236:239], v[200:203], a[64:79]\n"
"ds_read_b128 a[252:255], v224 offset:64512\n"
"ds_read_b128 a[200:203], v225 offset:18432\n"
"buffer_load_dwordx4 v251, s[96:99], s35 offen offset:2048 sc1 lds\n"
"v_cvt_pk_bf16_f32 v35, v30, v31\n"
"buffer_store_dwordx4 v[32:35], v228, s[96:99], s39 offen offset:0 sc1\n"
"v_accvgpr_read_b32 v36, a24\n"
"v_accvgpr_read_b32 v37, a25\n"
"v_mfma_f32_32x32x16_bf16 a[80:95], a[236:239], v[204:207], a[80:95]\n"
"buffer_load_dwordx4 v251, s[96:99], s35 offen offset:3072 sc1 lds\n"
"v_accvgpr_read_b32 v38, a26\n"
"v_accvgpr_read_b32 v39, a27\n"
"v_accvgpr_read_b32 v40, a28\n"
"v_accvgpr_read_b32 v41, a29\n"
"v_accvgpr_read_b32 v42, a30\n"
"s_add_u32 m0, s57, 0\n"
"v_mfma_f32_32x32x16_bf16 a[96:111], a[240:243], v[176:179], a[96:111]\n"
"ds_read_b128 a[204:207], v225 offset:19456\n"
"buffer_load_dwordx4 v251, s[96:99], s36 offen offset:0 sc1 lds\n"
"v_accvgpr_read_b32 v43, a31\n"
"v_mul_f32 v36, v36, v246\n"
"v_mul_f32 v37, v37, v246\n"
"v_mul_f32 v38, v38, v246\n"
"v_mfma_f32_32x32x16_bf16 a[112:127], a[240:243], v[180:183], a[112:127]\n"
"buffer_load_dwordx4 v251, s[96:99], s36 offen offset:1024 sc1 lds\n"
"v_mul_f32 v39, v39, v246\n"
"v_mul_f32 v40, v40, v246\n"
"v_mul_f32 v41, v41, v246\n"
"v_mul_f32 v42, v42, v246\n"
"v_mul_f32 v43, v43, v246\n"
"v_cvt_pk_bf16_f32 v44, v36, v37\n"
"s_waitcnt lgkmcnt(3)\n"
"v_mfma_f32_32x32x16_bf16 a[96:111], a[244:247], v[184:187], a[96:111]\n"
"ds_read_b128 a[208:211], v225 offset:20480\n"
"buffer_load_dwordx4 v251, s[96:99], s36 offen offset:2048 sc1 lds\n"
"v_cvt_pk_bf16_f32 v45, v38, v39\n"
"v_cvt_pk_bf16_f32 v46, v40, v41\n"
"v_cvt_pk_bf16_f32 v47, v42, v43\n"
"buffer_store_dwordx4 v[44:47], v228, s[96:99], s39 offen offset:512 sc1\n"
"v_mfma_f32_32x32x16_bf16 a[112:127], a[244:247], v[188:191], a[112:127]\n"
"buffer_load_dwordx4 v251, s[96:99], s36 offen offset:3072 sc1 lds\n"
"v_accvgpr_read_b32 v48, a32\n"
"v_accvgpr_read_b32 v49, a33\n"
"v_accvgpr_read_b32 v50, a34\n"
"v_accvgpr_read_b32 v51, a35\n"
"v_accvgpr_read_b32 v52, a36\n"
"s_add_u32 m0, s57, 65536\n"
"v_mfma_f32_32x32x16_bf16 a[96:111], a[248:251], v[192:195], a[96:111]\n"
"ds_read_b128 a[212:215], v225 offset:21504\n"
"buffer_load_dwordx4 v251, s[96:99], s37 offen offset:0 sc1 lds\n"
"v_accvgpr_read_b32 v53, a37\n"
"v_accvgpr_read_b32 v54, a38\n"
"v_accvgpr_read_b32 v55, a39\n"
"v_mul_f32 v48, v48, v244\n"
"v_mul_f32 v49, v49, v244\n"
"v_mfma_f32_32x32x16_bf16 a[112:127], a[248:251], v[196:199], a[112:127]\n"
"buffer_load_dwordx4 v251, s[96:99], s37 offen offset:1024 sc1 lds\n"
"v_mul_f32 v50, v50, v244\n"
"v_mul_f32 v51, v51, v244\n"
"v_mul_f32 v52, v52, v244\n"
"v_mul_f32 v53, v53, v244\n"
"v_mul_f32 v54, v54, v244\n"
"v_mul_f32 v55, v55, v244\n"
"s_waitcnt lgkmcnt(2)\n"
"v_mfma_f32_32x32x16_bf16 a[96:111], a[252:255], v[200:203], a[96:111]\n"
"buffer_load_dwordx4 v251, s[96:99], s37 offen offset:2048 sc1 lds\n"
"v_cvt_pk_bf16_f32 v56, v48, v49\n"
"v_cvt_pk_bf16_f32 v57, v50, v51\n"
"v_cvt_pk_bf16_f32 v58, v52, v53\n"
"v_cvt_pk_bf16_f32 v59, v54, v55\n"
"buffer_store_dwordx4 v[56:59], v228, s[96:99], s38 offen offset:1024 sc1\n"
"v_mfma_f32_32x32x16_bf16 a[112:127], a[252:255], v[204:207], a[112:127]\n"
"buffer_load_dwordx4 v251, s[96:99], s37 offen offset:3072 sc1 lds\n"
"ds_read_b128 a[216:219], v225 offset:22528\n"
"v_mov_b32 v224, v225\n"
"s_add_u32 s32, s32, 1\n"
"s_add_u32 s33, s33, 1\n"
"v_accvgpr_read_b32 v0, a40\n"
"v_accvgpr_read_b32 v1, a41\n"
"v_accvgpr_read_b32 v2, a42\n"
"v_accvgpr_read_b32 v3, a43\n"
"v_accvgpr_read_b32 v4, a44\n"
"v_accvgpr_read_b32 v5, a45\n"
"v_accvgpr_read_b32 v6, a46\n"
"v_accvgpr_read_b32 v7, a47\n"
"v_pk_mul_f32 v[0:1], v[0:1], v[244:245]\n"
"v_pk_mul_f32 v[2:3], v[2:3], v[244:245]\n"
"v_pk_mul_f32 v[4:5], v[4:5], v[244:245]\n"
"v_pk_mul_f32 v[6:7], v[6:7], v[244:245]\n"
"v_cvt_pk_bf16_f32 v8, v0, v1\n"
"v_cvt_pk_bf16_f32 v9, v2, v3\n"
"v_cvt_pk_bf16_f32 v10, v4, v5\n"
"v_cvt_pk_bf16_f32 v11, v6, v7\n"
"buffer_store_dwordx4 v[8:11], v228, s[96:99], s38 offen offset:1536 sc1\n"
"v_accvgpr_read_b32 v12, a48\n"
"v_accvgpr_read_b32 v13, a49\n"
"v_accvgpr_read_b32 v14, a50\n"
"v_accvgpr_read_b32 v15, a51\n"
"v_accvgpr_read_b32 v16, a52\n"
"v_accvgpr_read_b32 v17, a53\n"
"v_accvgpr_read_b32 v18, a54\n"
"PG14_%=:\n"
"s_cbranch_execz RPG14_%=\n"
"v_accvgpr_read_b32 v19, a55\n"
"v_pk_mul_f32 v[12:13], v[12:13], v[246:247]\n"
"v_pk_mul_f32 v[14:15], v[14:15], v[246:247]\n"
"v_pk_mul_f32 v[16:17], v[16:17], v[246:247]\n"
"v_pk_mul_f32 v[18:19], v[18:19], v[246:247]\n"
"v_cvt_pk_bf16_f32 v20, v12, v13\n"
"v_cvt_pk_bf16_f32 v21, v14, v15\n"
"v_cvt_pk_bf16_f32 v22, v16, v17\n"
"v_cvt_pk_bf16_f32 v23, v18, v19\n"
"buffer_store_dwordx4 v[20:23], v228, s[96:99], s39 offen offset:1024 sc1\n"
"s_waitcnt vmcnt(22)\n"
"s_nop 0\n"
"s_bitcmp1_b32 s26, s32\n"
"s_cbranch_scc1 DISP_1_%=\n"
"s_branch MID_1_%=\n"
"DRAINA_1_%=:\n"
"v_exp_f32 v208, v32\n"
"v_exp_f32 v209, v33\n"
"v_readlane_b32 s34, v229, s32\n"
"v_exp_f32 v210, v34\n"
"v_add_f32 v214, v214, v208\n"
"v_exp_f32 v211, v35\n"
"v_add_f32 v215, v215, v209\n"
"v_readlane_b32 s35, v229, s33\n"
"v_cvt_pk_bf16_f32 v192, v208, v209\n"
"v_exp_f32 v212, v36\n"
"v_add_f32 v214, v214, v210\n"
"v_exp_f32 v213, v37\n"
"v_readlane_b32 s36, v230, s32\n"
"v_add_f32 v215, v215, v211\n"
"v_cvt_pk_bf16_f32 v193, v210, v211\n"
"v_exp_f32 v208, v38\n"
"v_add_f32 v214, v214, v212\n"
"v_readlane_b32 s37, v230, s33\n"
"v_exp_f32 v209, v39\n"
"v_add_f32 v215, v215, v213\n"
"v_cvt_pk_bf16_f32 v194, v212, v213\n"
"v_exp_f32 v210, v48\n"
"v_add_f32 v214, v214, v208\n"
"v_exp_f32 v211, v49\n"
"v_add_f32 v215, v215, v209\n"
"v_cvt_pk_bf16_f32 v195, v208, v209\n"
"v_exp_f32 v212, v50\n"
"v_add_f32 v216, v216, v210\n"
"v_exp_f32 v213, v51\n"
"v_add_f32 v217, v217, v211\n"
"v_cvt_pk_bf16_f32 v196, v210, v211\n"
"v_exp_f32 v208, v52\n"
"v_add_f32 v216, v216, v212\n"
"v_exp_f32 v209, v53\n"
"v_add_f32 v217, v217, v213\n"
"v_cvt_pk_bf16_f32 v197, v212, v213\n"
"v_exp_f32 v210, v54\n"
"v_add_f32 v216, v216, v208\n"
"v_exp_f32 v211, v55\n"
"v_add_f32 v217, v217, v209\n"
"v_cvt_pk_bf16_f32 v198, v208, v209\n"
"v_exp_f32 v212, v40\n"
"v_add_f32 v216, v216, v210\n"
"v_exp_f32 v213, v41\n"
"v_add_f32 v217, v217, v211\n"
"v_cvt_pk_bf16_f32 v199, v210, v211\n"
"v_exp_f32 v208, v42\n"
"v_add_f32 v214, v214, v212\n"
"v_exp_f32 v209, v43\n"
"v_add_f32 v215, v215, v213\n"
"v_cvt_pk_bf16_f32 v200, v212, v213\n"
"v_exp_f32 v210, v44\n"
"v_add_f32 v214, v214, v208\n"
"v_exp_f32 v211, v45\n"
"v_add_f32 v215, v215, v209\n"
"v_cvt_pk_bf16_f32 v201, v208, v209\n"
"v_exp_f32 v212, v46\n"
"v_add_f32 v214, v214, v210\n"
"v_exp_f32 v213, v47\n"
"v_add_f32 v215, v215, v211\n"
"v_cvt_pk_bf16_f32 v202, v210, v211\n"
"v_exp_f32 v208, v56\n"
"v_add_f32 v214, v214, v212\n"
"v_exp_f32 v209, v57\n"
"v_add_f32 v215, v215, v213\n"
"v_cvt_pk_bf16_f32 v203, v212, v213\n"
"v_exp_f32 v210, v58\n"
"v_add_f32 v216, v216, v208\n"
"v_exp_f32 v211, v59\n"
"v_add_f32 v217, v217, v209\n"
"v_cvt_pk_bf16_f32 v204, v208, v209\n"
"v_exp_f32 v212, v60\n"
"v_add_f32 v216, v216, v210\n"
"v_exp_f32 v213, v61\n"
"v_add_f32 v217, v217, v211\n"
"v_cvt_pk_bf16_f32 v205, v210, v211\n"
"v_exp_f32 v208, v62\n"
"v_add_f32 v216, v216, v212\n"
"v_exp_f32 v209, v63\n"
"v_add_f32 v217, v217, v213\n"
"v_cvt_pk_bf16_f32 v206, v212, v213\n"
"v_add_f32 v216, v216, v208\n"
"v_add_f32 v217, v217, v209\n"
"v_cvt_pk_bf16_f32 v207, v208, v209\n"
"v_add_f32 v218, v214, v215\n"
"v_add_f32 v219, v216, v217\n"
"v_max_f32 v236, v218, v219\n"
"v_cmp_nge_f32 vcc, s42, v236\n"
"s_nop 0\n"
"s_cbranch_vccnz STUB_DRAINA_1_%=\n"
"RET_DRAINA_1_%=:\n"
"v_add_f32 v220, v220, v218\n"
"v_add_f32 v221, v221, v219\n"
"v_mov_b32 v236, v220\n"
"v_mov_b32 v237, v221\n"
"s_nop 0\n"
"v_permlane32_swap_b32 v220, v236\n"
"v_permlane32_swap_b32 v221, v237\n"
"v_add_f32 v220, v220, v236\n"
"v_add_f32 v221, v221, v237\n"
"v_rcp_f32 v244, v220\n"
"v_rcp_f32 v246, v221\n"
"v_mov_b32 v245, v244\n"
"v_mov_b32 v247, v246\n"
"s_add_u32 s39, s38, 262144\n"
"s_waitcnt vmcnt(0) lgkmcnt(0)\n"
"s_barrier\n"
"ds_read_b128 a[192:195], v224 offset:32768\n"
"ds_read_b128 a[196:199], v224 offset:36864\n"
"ds_read_b128 a[200:203], v224 offset:40960\n"
"ds_read_b128 a[204:207], v224 offset:45056\n"
"s_waitcnt lgkmcnt(3)\n"
"v_mfma_f32_32x32x16_bf16 a[0:15], a[192:195], v[160:163], a[0:15]\n"
"ds_read_b128 a[208:211], v224 offset:33792\n"
"buffer_load_dwordx4 a[128:131], v251, s[96:99], s43 offen offset:0 sc1\n"
"v_mfma_f32_32x32x16_bf16 a[16:31], a[192:195], v[164:167], a[16:31]\n"
"buffer_load_dwordx4 a[132:135], v251, s[96:99], s43 offen offset:1024 sc1\n"
"s_waitcnt lgkmcnt(3)\n"
"v_mfma_f32_32x32x16_bf16 a[0:15], a[196:199], v[168:171], a[0:15]\n"
"ds_read_b128 a[212:215], v224 offset:37888\n"
"buffer_load_dwordx4 a[136:139], v251, s[96:99], s43 offen offset:2048 sc1\n"
"v_mfma_f32_32x32x16_bf16 a[16:31], a[196:199], v[172:175], a[16:31]\n"
"buffer_load_dwordx4 a[140:143], v251, s[96:99], s43 offen offset:3072 sc1\n"
"s_add_u32 s43, s43, 4096\n"
"s_waitcnt lgkmcnt(3)\n"
"v_mfma_f32_32x32x16_bf16 a[0:15], a[200:203], v[192:195], a[0:15]\n"
"ds_read_b128 a[216:219], v224 offset:41984\n"
"buffer_load_dwordx4 a[144:147], v251, s[96:99], s43 offen offset:0 sc1\n"
"v_mfma_f32_32x32x16_bf16 a[16:31], a[200:203], v[196:199], a[16:31]\n"
"buffer_load_dwordx4 a[148:151], v251, s[96:99], s43 offen offset:1024 sc1\n"
"s_waitcnt lgkmcnt(2)\n"
"v_mfma_f32_32x32x16_bf16 a[0:15], a[204:207], v[200:203], a[0:15]\n"
"ds_read_b128 a[220:223], v224 offset:46080\n"
"buffer_load_dwordx4 a[152:155], v251, s[96:99], s43 offen offset:2048 sc1\n"
"v_mfma_f32_32x32x16_bf16 a[16:31], a[204:207], v[204:207], a[16:31]\n"
"buffer_load_dwordx4 a[156:159], v251, s[96:99], s43 offen offset:3072 sc1\n"
"s_add_u32 s43, s43, 4096\n"
"v_mfma_f32_32x32x16_bf16 a[32:47], a[208:211], v[160:163], a[32:47]\n"
"ds_read_b128 a[224:227], v224 offset:34816\n"
"buffer_load_dwordx4 a[160:163], v251, s[96:99], s43 offen offset:0 sc1\n"
"v_mfma_f32_32x32x16_bf16 a[48:63], a[208:211], v[164:167], a[48:63]\n"
"buffer_load_dwordx4 a[164:167], v251, s[96:99], s43 offen offset:1024 sc1\n"
"s_nop 1\n"
"v_accvgpr_read_b32 v64, a0\n"
"v_accvgpr_read_b32 v65, a1\n"
"v_accvgpr_read_b32 v66, a2\n"
"v_accvgpr_read_b32 v67, a3\n"
"v_accvgpr_read_b32 v68, a4\n"
"v_accvgpr_read_b32 v69, a5\n"
"s_waitcnt lgkmcnt(2)\n"
"v_mfma_f32_32x32x16_bf16 a[32:47], a[212:215], v[168:171], a[32:47]\n"
"ds_read_b128 a[228:231], v224 offset:38912\n"
"buffer_load_dwordx4 a[168:171], v251, s[96:99], s43 offen offset:2048 sc1\n"
"v_accvgpr_read_b32 v70, a6\n"
"v_accvgpr_read_b32 v71, a7\n"
"v_mul_f32 v64, v64, v244\n"
"v_mul_f32 v65, v65, v244\n"
"v_mul_f32 v66, v66, v244\n"
"v_mfma_f32_32x32x16_bf16 a[48:63], a[212:215], v[172:175], a[48:63]\n"
"buffer_load_dwordx4 a[172:175], v251, s[96:99], s43 offen offset:3072 sc1\n"
"v_mul_f32 v67, v67, v244\n"
"v_mul_f32 v68, v68, v244\n"
"v_mul_f32 v69, v69, v244\n"
"v_mul_f32 v70, v70, v244\n"
"s_add_u32 s43, s43, 4096\n"
"v_mfma_f32_32x32x16_bf16 a[32:47], a[216:219], v[192:195], a[32:47]\n"
"ds_read_b128 a[232:235], v224 offset:43008\n"
"buffer_load_dwordx4 a[176:179], v251, s[96:99], s43 offen offset:0 sc1\n"
"v_mul_f32 v71, v71, v244\n"
"v_cvt_pk_bf16_f32 v72, v64, v65\n"
"v_cvt_pk_bf16_f32 v73, v66, v67\n"
"v_cvt_pk_bf16_f32 v74, v68, v69\n"
"v_cvt_pk_bf16_f32 v75, v70, v71\n"
"v_mfma_f32_32x32x16_bf16 a[48:63], a[216:219], v[196:199], a[48:63]\n"
"buffer_load_dwordx4 a[180:183], v251, s[96:99], s43 offen offset:1024 sc1\n"
"buffer_store_dwordx4 v[72:75], v228, s[96:99], s38 offen offset:0 sc1\n"
"v_accvgpr_read_b32 v76, a8\n"
"v_accvgpr_read_b32 v77, a9\n"
"v_accvgpr_read_b32 v78, a10\n"
"v_accvgpr_read_b32 v79, a11\n"
"s_waitcnt lgkmcnt(2)\n"
"v_mfma_f32_32x32x16_bf16 a[32:47], a[220:223], v[200:203], a[32:47]\n"
"ds_read_b128 a[236:239], v224 offset:47104\n"
"buffer_load_dwordx4 a[184:187], v251, s[96:99], s43 offen offset:2048 sc1\n"
"v_accvgpr_read_b32 v80, a12\n"
"v_accvgpr_read_b32 v81, a13\n"
"v_accvgpr_read_b32 v82, a14\n"
"v_accvgpr_read_b32 v83, a15\n"
"v_mul_f32 v76, v76, v244\n"
"v_mfma_f32_32x32x16_bf16 a[48:63], a[220:223], v[204:207], a[48:63]\n"
"buffer_load_dwordx4 a[188:191], v251, s[96:99], s43 offen offset:3072 sc1\n"
"v_mul_f32 v77, v77, v244\n"
"v_mul_f32 v78, v78, v244\n"
"v_mul_f32 v79, v79, v244\n"
"v_mul_f32 v80, v80, v244\n"
"s_add_u32 s43, s43, 4096\n"
"s_add_u32 m0, s57, 49152\n"
"v_mfma_f32_32x32x16_bf16 a[64:79], a[224:227], v[160:163], a[64:79]\n"
"ds_read_b128 a[240:243], v224 offset:35840\n"
"buffer_load_dwordx4 v251, s[96:99], s34 offen offset:0 sc1 lds\n"
"v_mul_f32 v81, v81, v244\n"
"v_mul_f32 v82, v82, v244\n"
"v_mul_f32 v83, v83, v244\n"
"v_cvt_pk_bf16_f32 v84, v76, v77\n"
"v_mfma_f32_32x32x16_bf16 a[80:95], a[224:227], v[164:167], a[80:95]\n"
"buffer_load_dwordx4 v251, s[96:99], s34 offen offset:1024 sc1 lds\n"
"v_cvt_pk_bf16_f32 v85, v78, v79\n"
"v_cvt_pk_bf16_f32 v86, v80, v81\n"
"v_cvt_pk_bf16_f32 v87, v82, v83\n"
"buffer_store_dwordx4 v[84:87], v228, s[96:99], s38 offen offset:512 sc1\n"
"v_accvgpr_read_b32 v88, a16\n"
"v_accvgpr_read_b32 v89, a17\n"
"s_waitcnt lgkmcnt(2)\n"
"v_mfma_f32_32x32x16_bf16 a[64:79], a[228:231], v[168:171], a[64:79]\n"
"ds_read_b128 a[244:247], v224 offset:39936\n"
"ds_read_b128 a[192:195], v225 offset:0\n"
"buffer_load_dwordx4 v251, s[96:99], s34 offen offset:2048 sc1 lds\n"
"v_accvgpr_read_b32 v90, a18\n"
"v_accvgpr_read_b32 v91, a19\n"
"v_accvgpr_read_b32 v92, a20\n"
"v_accvgpr_read_b32 v93, a21\n"
"v_mfma_f32_32x32x16_bf16 a[80:95], a[228:231], v[172:175], a[80:95]\n"
"buffer_load_dwordx4 v251, s[96:99], s34 offen offset:3072 sc1 lds\n"
"v_accvgpr_read_b32 v94, a22\n"
"v_accvgpr_read_b32 v95, a23\n"
"v_mul_f32 v88, v88, v246\n"
"v_mul_f32 v89, v89, v246\n"
"s_add_u32 m0, s57, 114688\n"
"v_mfma_f32_32x32x16_bf16 a[64:79], a[232:235], v[192:195], a[64:79]\n"
"ds_read_b128 a[248:251], v224 offset:44032\n"
"ds_read_b128 a[196:199], v225 offset:1024\n"
"buffer_load_dwordx4 v251, s[96:99], s35 offen offset:0 sc1 lds\n"
"v_mul_f32 v90, v90, v246\n"
"v_mul_f32 v91, v91, v246\n"
"v_mul_f32 v92, v92, v246\n"
"v_mul_f32 v93, v93, v246\n"
"v_mfma_f32_32x32x16_bf16 a[80:95], a[232:235], v[196:199], a[80:95]\n"
"buffer_load_dwordx4 v251, s[96:99], s35 offen offset:1024 sc1 lds\n"
"v_mul_f32 v94, v94, v246\n"
"v_mul_f32 v95, v95, v246\n"
"v_cvt_pk_bf16_f32 v96, v88, v89\n"
"v_cvt_pk_bf16_f32 v97, v90, v91\n"
"v_cvt_pk_bf16_f32 v98, v92, v93\n"
"s_waitcnt lgkmcnt(4)\n"
"v_mfma_f32_32x32x16_bf16 a[64:79], a[236:239], v[200:203], a[64:79]\n"
"ds_read_b128 a[252:255], v224 offset:48128\n"
"ds_read_b128 a[200:203], v225 offset:2048\n"
"buffer_load_dwordx4 v251, s[96:99], s35 offen offset:2048 sc1 lds\n"
"v_cvt_pk_bf16_f32 v99, v94, v95\n"
"buffer_store_dwordx4 v[96:99], v228, s[96:99], s39 offen offset:0 sc1\n"
"v_accvgpr_read_b32 v100, a24\n"
"v_accvgpr_read_b32 v101, a25\n"
"v_mfma_f32_32x32x16_bf16 a[80:95], a[236:239], v[204:207], a[80:95]\n"
"buffer_load_dwordx4 v251, s[96:99], s35 offen offset:3072 sc1 lds\n"
"v_accvgpr_read_b32 v102, a26\n"
"v_accvgpr_read_b32 v103, a27\n"
"v_accvgpr_read_b32 v104, a28\n"
"v_accvgpr_read_b32 v105, a29\n"
"v_accvgpr_read_b32 v106, a30\n"
"s_add_u32 m0, s57, 16384\n"
"v_mfma_f32_32x32x16_bf16 a[96:111], a[240:243], v[160:163], a[96:111]\n"
"ds_read_b128 a[204:207], v225 offset:3072\n"
"buffer_load_dwordx4 v251, s[96:99], s36 offen offset:0 sc1 lds\n"
"v_accvgpr_read_b32 v107, a31\n"
"v_mul_f32 v100, v100, v246\n"
"v_mul_f32 v101, v101, v246\n"
"v_mul_f32 v102, v102, v246\n"
"v_mfma_f32_32x32x16_bf16 a[112:127], a[240:243], v[164:167], a[112:127]\n"
"buffer_load_dwordx4 v251, s[96:99], s36 offen offset:1024 sc1 lds\n"
"v_mul_f32 v103, v103, v246\n"
"v_mul_f32 v104, v104, v246\n"
"v_mul_f32 v105, v105, v246\n"
"v_mul_f32 v106, v106, v246\n"
"v_mul_f32 v107, v107, v246\n"
"v_cvt_pk_bf16_f32 v108, v100, v101\n"
"s_waitcnt lgkmcnt(3)\n"
"v_mfma_f32_32x32x16_bf16 a[96:111], a[244:247], v[168:171], a[96:111]\n"
"ds_read_b128 a[208:211], v225 offset:4096\n"
"buffer_load_dwordx4 v251, s[96:99], s36 offen offset:2048 sc1 lds\n"
"v_cvt_pk_bf16_f32 v109, v102, v103\n"
"v_cvt_pk_bf16_f32 v110, v104, v105\n"
"v_cvt_pk_bf16_f32 v111, v106, v107\n"
"buffer_store_dwordx4 v[108:111], v228, s[96:99], s39 offen offset:512 sc1\n"
"v_mfma_f32_32x32x16_bf16 a[112:127], a[244:247], v[172:175], a[112:127]\n"
"buffer_load_dwordx4 v251, s[96:99], s36 offen offset:3072 sc1 lds\n"
"v_accvgpr_read_b32 v112, a32\n"
"v_accvgpr_read_b32 v113, a33\n"
"v_accvgpr_read_b32 v114, a34\n"
"v_accvgpr_read_b32 v115, a35\n"
"v_accvgpr_read_b32 v116, a36\n"
"s_add_u32 m0, s57, 81920\n"
"v_mfma_f32_32x32x16_bf16 a[96:111], a[248:251], v[192:195], a[96:111]\n"
"ds_read_b128 a[212:215], v225 offset:5120\n"
"buffer_load_dwordx4 v251, s[96:99], s37 offen offset:0 sc1 lds\n"
"v_accvgpr_read_b32 v117, a37\n"
"v_accvgpr_read_b32 v118, a38\n"
"v_accvgpr_read_b32 v119, a39\n"
"v_mul_f32 v112, v112, v244\n"
"v_mul_f32 v113, v113, v244\n"
"v_mfma_f32_32x32x16_bf16 a[112:127], a[248:251], v[196:199], a[112:127]\n"
"buffer_load_dwordx4 v251, s[96:99], s37 offen offset:1024 sc1 lds\n"
"v_mul_f32 v114, v114, v244\n"
"v_mul_f32 v115, v115, v244\n"
"v_mul_f32 v116, v116, v244\n"
"v_mul_f32 v117, v117, v244\n"
"v_mul_f32 v118, v118, v244\n"
"v_mul_f32 v119, v119, v244\n"
"s_waitcnt lgkmcnt(2)\n"
"v_mfma_f32_32x32x16_bf16 a[96:111], a[252:255], v[200:203], a[96:111]\n"
"buffer_load_dwordx4 v251, s[96:99], s37 offen offset:2048 sc1 lds\n"
"v_cvt_pk_bf16_f32 v120, v112, v113\n"
"v_cvt_pk_bf16_f32 v121, v114, v115\n"
"v_cvt_pk_bf16_f32 v122, v116, v117\n"
"v_cvt_pk_bf16_f32 v123, v118, v119\n"
"buffer_store_dwordx4 v[120:123], v228, s[96:99], s38 offen offset:1024 sc1\n"
"v_mfma_f32_32x32x16_bf16 a[112:127], a[252:255], v[204:207], a[112:127]\n"
"buffer_load_dwordx4 v251, s[96:99], s37 offen offset:3072 sc1 lds\n"
"ds_read_b128 a[216:219], v225 offset:6144\n"
"v_mov_b32 v224, v225\n"
"s_add_u32 s32, s32, 1\n"
"s_add_u32 s33, s33, 1\n"
"v_accvgpr_read_b32 v64, a40\n"
"v_accvgpr_read_b32 v65, a41\n"
"v_accvgpr_read_b32 v66, a42\n"
"v_accvgpr_read_b32 v67, a43\n"
"v_accvgpr_read_b32 v68, a44\n"
"v_accvgpr_read_b32 v69, a45\n"
"v_accvgpr_read_b32 v70, a46\n"
"v_accvgpr_read_b32 v71, a47\n"
"v_pk_mul_f32 v[64:65], v[64:65], v[244:245]\n"
"v_pk_mul_f32 v[66:67], v[66:67], v[244:245]\n"
"v_pk_mul_f32 v[68:69], v[68:69], v[244:245]\n"
"v_pk_mul_f32 v[70:71], v[70:71], v[244:245]\n"
"v_cvt_pk_bf16_f32 v72, v64, v65\n"
"v_cvt_pk_bf16_f32 v73, v66, v67\n"
"v_cvt_pk_bf16_f32 v74, v68, v69\n"
"v_cvt_pk_bf16_f32 v75, v70, v71\n"
"buffer_store_dwordx4 v[72:75], v228, s[96:99], s38 offen offset:1536 sc1\n"
"v_accvgpr_read_b32 v76, a48\n"
"v_accvgpr_read_b32 v77, a49\n"
"v_accvgpr_read_b32 v78, a50\n"
"v_accvgpr_read_b32 v79, a51\n"
"v_accvgpr_read_b32 v80, a52\n"
"v_accvgpr_read_b32 v81, a53\n"
"v_accvgpr_read_b32 v82, a54\n"
"v_accvgpr_read_b32 v83, a55\n"
"v_pk_mul_f32 v[76:77], v[76:77], v[246:247]\n"
"v_pk_mul_f32 v[78:79], v[78:79], v[246:247]\n"
"v_pk_mul_f32 v[80:81], v[80:81], v[246:247]\n"
"v_pk_mul_f32 v[82:83], v[82:83], v[246:247]\n"
"v_cvt_pk_bf16_f32 v84, v76, v77\n"
"v_cvt_pk_bf16_f32 v85, v78, v79\n"
"v_cvt_pk_bf16_f32 v86, v80, v81\n"
"v_cvt_pk_bf16_f32 v87, v82, v83\n"
"buffer_store_dwordx4 v[84:87], v228, s[96:99], s39 offen offset:1024 sc1\n"
"s_waitcnt vmcnt(22)\n"
"s_nop 0\n"
"s_bitcmp1_b32 s26, s32\n"
"s_cbranch_scc1 DISP_0_%=\n"
"s_branch MID_0_%=\n"
"FINALZ_%=:\n"
"v_accvgpr_read_b32 v24, a56\n"
"v_accvgpr_read_b32 v25, a57\n"
"v_accvgpr_read_b32 v26, a58\n"
"v_accvgpr_read_b32 v27, a59\n"
"v_accvgpr_read_b32 v28, a60\n"
"v_accvgpr_read_b32 v29, a61\n"
"v_accvgpr_read_b32 v30, a62\n"
"v_accvgpr_read_b32 v31, a63\n"
"v_pk_mul_f32 v[24:25], v[24:25], v[246:247]\n"
"v_pk_mul_f32 v[26:27], v[26:27], v[246:247]\n"
"v_pk_mul_f32 v[28:29], v[28:29], v[246:247]\n"
"v_pk_mul_f32 v[30:31], v[30:31], v[246:247]\n"
"v_cvt_pk_bf16_f32 v32, v24, v25\n"
"v_cvt_pk_bf16_f32 v33, v26, v27\n"
"v_cvt_pk_bf16_f32 v34, v28, v29\n"
"v_cvt_pk_bf16_f32 v35, v30, v31\n"
"buffer_store_dwordx4 v[32:35], v228, s[96:99], s39 offen offset:1536 sc1\n"
"v_accvgpr_read_b32 v36, a64\n"
"v_accvgpr_read_b32 v37, a65\n"
"v_accvgpr_read_b32 v38, a66\n"
"v_accvgpr_read_b32 v39, a67\n"
"v_accvgpr_read_b32 v40, a68\n"
"v_accvgpr_read_b32 v41, a69\n"
"v_accvgpr_read_b32 v42, a70\n"
"v_accvgpr_read_b32 v43, a71\n"
"v_pk_mul_f32 v[36:37], v[36:37], v[244:245]\n"
"v_pk_mul_f32 v[38:39], v[38:39], v[244:245]\n"
"v_pk_mul_f32 v[40:41], v[40:41], v[244:245]\n"
"v_pk_mul_f32 v[42:43], v[42:43], v[244:245]\n"
"v_cvt_pk_bf16_f32 v44, v36, v37\n"
"v_cvt_pk_bf16_f32 v45, v38, v39\n"
"v_cvt_pk_bf16_f32 v46, v40, v41\n"
"v_cvt_pk_bf16_f32 v47, v42, v43\n"
"buffer_store_dwordx4 v[44:47], v228, s[96:99], s38 offen offset:2048 sc1\n"
"v_accvgpr_read_b32 v48, a72\n"
"v_accvgpr_read_b32 v49, a73\n"
"v_accvgpr_read_b32 v50, a74\n"
"v_accvgpr_read_b32 v51, a75\n"
"v_accvgpr_read_b32 v52, a76\n"
"v_accvgpr_read_b32 v53, a77\n"
"v_accvgpr_read_b32 v54, a78\n"
"v_accvgpr_read_b32 v55, a79\n"
"v_pk_mul_f32 v[48:49], v[48:49], v[244:245]\n"
"v_pk_mul_f32 v[50:51], v[50:51], v[244:245]\n"
"v_pk_mul_f32 v[52:53], v[52:53], v[244:245]\n"
"v_pk_mul_f32 v[54:55], v[54:55], v[244:245]\n"
"v_cvt_pk_bf16_f32 v56, v48, v49\n"
"v_cvt_pk_bf16_f32 v57, v50, v51\n"
"v_cvt_pk_bf16_f32 v58, v52, v53\n"
"v_cvt_pk_bf16_f32 v59, v54, v55\n"
"buffer_store_dwordx4 v[56:59], v228, s[96:99], s38 offen offset:2560 sc1\n"
"v_accvgpr_read_b32 v0, a80\n"
"v_accvgpr_read_b32 v1, a81\n"
"v_accvgpr_read_b32 v2, a82\n"
"v_accvgpr_read_b32 v3, a83\n"
"v_accvgpr_read_b32 v4, a84\n"
"v_accvgpr_read_b32 v5, a85\n"
"v_accvgpr_read_b32 v6, a86\n"
"v_accvgpr_read_b32 v7, a87\n"
"v_pk_mul_f32 v[0:1], v[0:1], v[246:247]\n"
"v_pk_mul_f32 v[2:3], v[2:3], v[246:247]\n"
"v_pk_mul_f32 v[4:5], v[4:5], v[246:247]\n"
"v_pk_mul_f32 v[6:7], v[6:7], v[246:247]\n"
"v_cvt_pk_bf16_f32 v8, v0, v1\n"
"v_cvt_pk_bf16_f32 v9, v2, v3\n"
"v_cvt_pk_bf16_f32 v10, v4, v5\n"
"v_cvt_pk_bf16_f32 v11, v6, v7\n"
"buffer_store_dwordx4 v[8:11], v228, s[96:99], s39 offen offset:2048 sc1\n"
"v_accvgpr_read_b32 v12, a88\n"
"v_accvgpr_read_b32 v13, a89\n"
"v_accvgpr_read_b32 v14, a90\n"
"v_accvgpr_read_b32 v15, a91\n"
"v_accvgpr_read_b32 v16, a92\n"
"v_accvgpr_read_b32 v17, a93\n"
"v_accvgpr_read_b32 v18, a94\n"
"v_accvgpr_read_b32 v19, a95\n"
"v_pk_mul_f32 v[12:13], v[12:13], v[246:247]\n"
"v_pk_mul_f32 v[14:15], v[14:15], v[246:247]\n"
"v_pk_mul_f32 v[16:17], v[16:17], v[246:247]\n"
"v_pk_mul_f32 v[18:19], v[18:19], v[246:247]\n"
"v_cvt_pk_bf16_f32 v20, v12, v13\n"
"v_cvt_pk_bf16_f32 v21, v14, v15\n"
"v_cvt_pk_bf16_f32 v22, v16, v17\n"
"v_cvt_pk_bf16_f32 v23, v18, v19\n"
"buffer_store_dwordx4 v[20:23], v228, s[96:99], s39 offen offset:2560 sc1\n"
"v_accvgpr_read_b32 v24, a96\n"
"v_accvgpr_read_b32 v25, a97\n"
"v_accvgpr_read_b32 v26, a98\n"
"v_accvgpr_read_b32 v27, a99\n"
"v_accvgpr_read_b32 v28, a100\n"
"v_accvgpr_read_b32 v29, a101\n"
"v_accvgpr_read_b32 v30, a102\n"
"v_accvgpr_read_b32 v31, a103\n"
"v_pk_mul_f32 v[24:25], v[24:25], v[244:245]\n"
"v_pk_mul_f32 v[26:27], v[26:27], v[244:245]\n"
"v_pk_mul_f32 v[28:29], v[28:29], v[244:245]\n"
"v_pk_mul_f32 v[30:31], v[30:31], v[244:245]\n"
"v_cvt_pk_bf16_f32 v32, v24, v25\n"
"v_cvt_pk_bf16_f32 v33, v26, v27\n"
"v_cvt_pk_bf16_f32 v34, v28, v29\n"
"v_cvt_pk_bf16_f32 v35, v30, v31\n"
"buffer_store_dwordx4 v[32:35], v228, s[96:99], s38 offen offset:3072 sc1\n"
"v_accvgpr_read_b32 v36, a104\n"
"v_accvgpr_read_b32 v37, a105\n"
"v_accvgpr_read_b32 v38, a106\n"
"v_accvgpr_read_b32 v39, a107\n"
"v_accvgpr_read_b32 v40, a108\n"
"v_accvgpr_read_b32 v41, a109\n"
"v_accvgpr_read_b32 v42, a110\n"
"v_accvgpr_read_b32 v43, a111\n"
"v_pk_mul_f32 v[36:37], v[36:37], v[244:245]\n"
"v_pk_mul_f32 v[38:39], v[38:39], v[244:245]\n"
"v_pk_mul_f32 v[40:41], v[40:41], v[244:245]\n"
"v_pk_mul_f32 v[42:43], v[42:43], v[244:245]\n"
"v_cvt_pk_bf16_f32 v44, v36, v37\n"
"v_cvt_pk_bf16_f32 v45, v38, v39\n"
"v_cvt_pk_bf16_f32 v46, v40, v41\n"
"v_cvt_pk_bf16_f32 v47, v42, v43\n"
"buffer_store_dwordx4 v[44:47], v228, s[96:99], s38 offen offset:3584 sc1\n"
"v_accvgpr_read_b32 v48, a112\n"
"v_accvgpr_read_b32 v49, a113\n"
"v_accvgpr_read_b32 v50, a114\n"
"v_accvgpr_read_b32 v51, a115\n"
"v_accvgpr_read_b32 v52, a116\n"
"v_accvgpr_read_b32 v53, a117\n"
"v_accvgpr_read_b32 v54, a118\n"
"v_accvgpr_read_b32 v55, a119\n"
"v_pk_mul_f32 v[48:49], v[48:49], v[246:247]\n"
"v_pk_mul_f32 v[50:51], v[50:51], v[246:247]\n"
"v_pk_mul_f32 v[52:53], v[52:53], v[246:247]\n"
"v_pk_mul_f32 v[54:55], v[54:55], v[246:247]\n"
"v_cvt_pk_bf16_f32 v56, v48, v49\n"
"v_cvt_pk_bf16_f32 v57, v50, v51\n"
"v_cvt_pk_bf16_f32 v58, v52, v53\n"
"v_cvt_pk_bf16_f32 v59, v54, v55\n"
"buffer_store_dwordx4 v[56:59], v228, s[96:99], s39 offen offset:3072 sc1\n"
"v_accvgpr_read_b32 v0, a120\n"
"v_accvgpr_read_b32 v1, a121\n"
"v_accvgpr_read_b32 v2, a122\n"
"v_accvgpr_read_b32 v3, a123\n"
"v_accvgpr_read_b32 v4, a124\n"
"v_accvgpr_read_b32 v5, a125\n"
"v_accvgpr_read_b32 v6, a126\n"
"v_accvgpr_read_b32 v7, a127\n"
"v_pk_mul_f32 v[0:1], v[0:1], v[246:247]\n"
"v_pk_mul_f32 v[2:3], v[2:3], v[246:247]\n"
"v_pk_mul_f32 v[4:5], v[4:5], v[246:247]\n"
"v_pk_mul_f32 v[6:7], v[6:7], v[246:247]\n"
"v_cvt_pk_bf16_f32 v8, v0, v1\n"
"v_cvt_pk_bf16_f32 v9, v2, v3\n"
"v_cvt_pk_bf16_f32 v10, v4, v5\n"
"v_cvt_pk_bf16_f32 v11, v6, v7\n"
"buffer_store_dwordx4 v[8:11], v228, s[96:99], s39 offen offset:3584 sc1\n"
"v_mov_b32 v192, 0\n"
"v_mov_b32 v193, 0\n"
"v_mov_b32 v194, 0\n"
"v_mov_b32 v195, 0\n"
"s_nop 1\n"
"v_mfma_f32_32x32x16_bf16 a[0:15], v[192:195], v[192:195], 0\n"
"v_mfma_f32_32x32x16_bf16 a[16:31], v[192:195], v[192:195], 0\n"
"v_mfma_f32_32x32x16_bf16 a[32:47], v[192:195], v[192:195], 0\n"
"v_mfma_f32_32x32x16_bf16 a[48:63], v[192:195], v[192:195], 0\n"
"v_mfma_f32_32x32x16_bf16 a[64:79], v[192:195], v[192:195], 0\n"
"v_mfma_f32_32x32x16_bf16 a[80:95], v[192:195], v[192:195], 0\n"
"v_mfma_f32_32x32x16_bf16 a[96:111], v[192:195], v[192:195], 0\n"
"v_mfma_f32_32x32x16_bf16 a[112:127], v[192:195], v[192:195], 0\n"
"v_mov_b32 v220, 0\n"
"v_mov_b32 v222, 0xf149f2ca\n"
"v_mov_b32 v221, 0\n"
"v_mov_b32 v223, 0xf149f2ca\n"
"s_and_b32 s39, %2, 1\n"
"s_lshl_b32 s39, s39, 14\n"
"s_lshr_b32 s40, %2, 1\n"
"s_lshl_b32 s40, s40, 16\n"
"s_add_u32 s39, s39, s40\n"
"s_add_u32 s40, s39, %3\n"
"v_add_u32 v240, s40, v251\n"
"s_sub_u32 s39, 81920, s39\n"
"s_add_u32 s39, s39, %3\n"
"v_add_u32 v241, s39, v251\n"
"s_lshl_b32 s39, %2, 10\n"
"s_add_u32 s40, s39, %3\n"
"s_add_u32 s40, s40, 131072\n"
"v_add_u32 v242, s40, v251\n"
"s_sub_u32 s39, 134144, s39\n"
"s_add_u32 s39, s39, %3\n"
"v_add_u32 v243, s39, v251\n"
"ds_write_b128 v242, v[220:223]\n"
"s_lshl_b32 s38, s10, 17\n"
"s_lshl_b32 s39, s24, 12\n"
"s_add_u32 s38, s38, s39\n"
"s_add_u32 s38, s38, 50331648\n"
"s_add_u32 s39, s38, 262144\n"
"s_waitcnt vmcnt(0) lgkmcnt(0)\n"
"s_barrier\n"
"ds_read_b128 v[244:247], v243\n"
"s_waitcnt lgkmcnt(0)\n"
"v_max_f32 v236, v222, v246\n"
"v_sub_f32 v237, v222, v236\n"
"v_sub_f32 v238, v246, v236\n"
"v_exp_f32 v237, v237\n"
"v_exp_f32 v238, v238\n"
"v_mul_f32 v239, v220, v237\n"
"v_fmac_f32 v239, v244, v238\n"
"v_rcp_f32 v239, v239\n"
"s_nop 0\n"
"v_mul_f32 v128, v237, v239\n"
"v_mul_f32 v130, v238, v239\n"
"v_mov_b32 v129, v128\n"
"v_mov_b32 v131, v130\n"
"v_max_f32 v236, v223, v247\n"
"v_sub_f32 v237, v223, v236\n"
"v_sub_f32 v238, v247, v236\n"
"v_exp_f32 v237, v237\n"
"v_exp_f32 v238, v238\n"
"v_mul_f32 v239, v221, v237\n"
"v_fmac_f32 v239, v245, v238\n"
"v_rcp_f32 v239, v239\n"
"s_nop 0\n"
"v_mul_f32 v132, v237, v239\n"
"v_mul_f32 v134, v238, v239\n"
"v_mov_b32 v133, v132\n"
"v_mov_b32 v135, v134\n"
"ds_write_b128 v240, a[64:67] offset:0\n"
"ds_write_b128 v240, a[68:71] offset:1024\n"
"ds_write_b128 v240, a[72:75] offset:2048\n"
"ds_write_b128 v240, a[76:79] offset:3072\n"
"ds_write_b128 v240, a[80:83] offset:4096\n"
"ds_write_b128 v240, a[84:87] offset:5120\n"
"ds_write_b128 v240, a[88:91] offset:6144\n"
"ds_write_b128 v240, a[92:95] offset:7168\n"
"ds_write_b128 v240, a[96:99] offset:8192\n"
"ds_write_b128 v240, a[100:103] offset:9216\n"
"ds_write_b128 v240, a[104:107] offset:10240\n"
"ds_write_b128 v240, a[108:111] offset:11264\n"
"ds_write_b128 v240, a[112:115] offset:12288\n"
"ds_write_b128 v240, a[116:119] offset:13312\n"
"ds_write_b128 v240, a[120:123] offset:14336\n"
"ds_write_b128 v240, a[124:127] offset:15360\n"
"PG15_%=:\n"
"s_cbranch_execz RPG15_%=\n"
"s_branch FIN_LOC_%=\n"
"FINAL_1_%=:\n"
"v_exp_f32 v208, v32\n"
"v_exp_f32 v209, v33\n"
"v_exp_f32 v210, v34\n"
"v_add_f32 v214, v214, v208\n"
"v_exp_f32 v211, v35\n"
"v_add_f32 v215, v215, v209\n"
"v_cvt_pk_bf16_f32 v192, v208, v209\n"
"v_exp_f32 v212, v36\n"
"v_add_f32 v214, v214, v210\n"
"v_exp_f32 v213, v37\n"
"v_add_f32 v215, v215, v211\n"
"v_cvt_pk_bf16_f32 v193, v210, v211\n"
"v_exp_f32 v208, v38\n"
"v_add_f32 v214, v214, v212\n"
"v_exp_f32 v209, v39\n"
"v_add_f32 v215, v215, v213\n"
"v_cvt_pk_bf16_f32 v194, v212, v213\n"
"v_exp_f32 v210, v48\n"
"v_add_f32 v214, v214, v208\n"
"v_exp_f32 v211, v49\n"
"v_add_f32 v215, v215, v209\n"
"v_cvt_pk_bf16_f32 v195, v208, v209\n"
"v_exp_f32 v212, v50\n"
"v_add_f32 v216, v216, v210\n"
"v_exp_f32 v213, v51\n"
"v_add_f32 v217, v217, v211\n"
"v_cvt_pk_bf16_f32 v196, v210, v211\n"
"v_exp_f32 v208, v52\n"
"v_add_f32 v216, v216, v212\n"
"v_exp_f32 v209, v53\n"
"v_add_f32 v217, v217, v213\n"
"v_cvt_pk_bf16_f32 v197, v212, v213\n"
"v_exp_f32 v210, v54\n"
"v_add_f32 v216, v216, v208\n"
"v_exp_f32 v211, v55\n"
"v_add_f32 v217, v217, v209\n"
"v_cvt_pk_bf16_f32 v198, v208, v209\n"
"v_exp_f32 v212, v40\n"
"v_add_f32 v216, v216, v210\n"
"v_exp_f32 v213, v41\n"
"v_add_f32 v217, v217, v211\n"
"v_cvt_pk_bf16_f32 v199, v210, v211\n"
"v_exp_f32 v208, v42\n"
"v_add_f32 v214, v214, v212\n"
"v_exp_f32 v209, v43\n"
"v_add_f32 v215, v215, v213\n"
"v_cvt_pk_bf16_f32 v200, v212, v213\n"
"v_exp_f32 v210, v44\n"
"v_add_f32 v214, v214, v208\n"
"v_exp_f32 v211, v45\n"
"v_add_f32 v215, v215, v209\n"
"v_cvt_pk_bf16_f32 v201, v208, v209\n"
"v_exp_f32 v212, v46\n"
"v_add_f32 v214, v214, v210\n"
"v_exp_f32 v213, v47\n"
"v_add_f32 v215, v215, v211\n"
"v_cvt_pk_bf16_f32 v202, v210, v211\n"
"v_exp_f32 v208, v56\n"
"v_add_f32 v214, v214, v212\n"
"v_exp_f32 v209, v57\n"
"v_add_f32 v215, v215, v213\n"
"v_cvt_pk_bf16_f32 v203, v212, v213\n"
"v_exp_f32 v210, v58\n"
"v_add_f32 v216, v216, v208\n"
"v_exp_f32 v211, v59\n"
"v_add_f32 v217, v217, v209\n"
"v_cvt_pk_bf16_f32 v204, v208, v209\n"
"v_exp_f32 v212, v60\n"
"v_add_f32 v216, v216, v210\n"
"v_exp_f32 v213, v61\n"
"v_add_f32 v217, v217, v211\n"
"v_cvt_pk_bf16_f32 v205, v210, v211\n"
"v_exp_f32 v208, v62\n"
"v_add_f32 v216, v216, v212\n"
"v_exp_f32 v209, v63\n"
"v_add_f32 v217, v217, v213\n"
"v_cvt_pk_bf16_f32 v206, v212, v213\n"
"v_add_f32 v216, v216, v208\n"
"v_add_f32 v217, v217, v209\n"
"v_cvt_pk_bf16_f32 v207, v208, v209\n"
"v_add_f32 v218, v214, v215\n"
"v_add_f32 v219, v216, v217\n"
"v_max_f32 v236, v218, v219\n"
"v_cmp_nge_f32 vcc, s42, v236\n"
"s_nop 0\n"
"s_cbranch_vccnz STUB_FINAL_1_%=\n"
"RET_FINAL_1_%=:\n"
"v_add_f32 v220, v220, v218\n"
"v_add_f32 v221, v221, v219\n"
"v_mov_b32 v236, v220\n"
"v_mov_b32 v237, v221\n"
"s_nop 0\n"
"v_permlane32_swap_b32 v220, v236\n"
"v_permlane32_swap_b32 v221, v237\n"
"v_add_f32 v220, v220, v236\n"
"v_add_f32 v221, v221, v237\n"
"s_and_b32 s39, %2, 1\n"
"s_lshl_b32 s39, s39, 14\n"
"s_lshr_b32 s40, %2, 1\n"
"s_lshl_b32 s40, s40, 16\n"
"s_add_u32 s39, s39, s40\n"
"s_add_u32 s40, s39, %3\n"
"v_add_u32 v240, s40, v251\n"
"s_sub_u32 s39, 81920, s39\n"
"s_add_u32 s39, s39, %3\n"
"v_add_u32 v241, s39, v251\n"
"s_lshl_b32 s39, %2, 10\n"
"s_add_u32 s40, s39, %3\n"
"s_add_u32 s40, s40, 131072\n"
"v_add_u32 v242, s40, v251\n"
"s_sub_u32 s39, 134144, s39\n"
"s_add_u32 s39, s39, %3\n"
"v_add_u32 v243, s39, v251\n"
"ds_write_b128 v242, v[220:223]\n"
"s_lshl_b32 s38, s10, 17\n"
"s_lshl_b32 s39, s24, 12\n"
"s_add_u32 s38, s38, s39\n"
"s_add_u32 s38, s38, 50331648\n"
"s_add_u32 s39, s38, 262144\n"
"s_waitcnt vmcnt(0) lgkmcnt(0)\n"
"s_barrier\n"
"s_cmp_lt_u32 %2, 2\n"
"s_cbranch_scc1 FIN_LO_%=\n"
"ds_read_b128 a[192:195], v224 offset:32768\n"
"ds_read_b128 a[196:199], v224 offset:33792\n"
"ds_read_b128 a[200:203], v224 offset:36864\n"
"ds_read_b128 a[204:207], v224 offset:37888\n"
"s_waitcnt lgkmcnt(3)\n"
"v_mfma_f32_32x32x16_bf16 a[0:15], a[192:195], v[160:163], a[0:15]\n"
"ds_read_b128 a[208:211], v224 offset:40960\n"
"v_mfma_f32_32x32x16_bf16 a[16:31], a[192:195], v[164:167], a[16:31]\n"
"ds_read_b128 v[244:247], v243\n"
"s_waitcnt lgkmcnt(0)\n"
"v_max_f32 v236, v222, v246\n"
"v_mfma_f32_32x32x16_bf16 a[32:47], a[196:199], v[160:163], a[32:47]\n"
"ds_read_b128 a[212:215], v224 offset:41984\n"
"v_sub_f32 v237, v222, v236\n"
"v_sub_f32 v238, v246, v236\n"
"v_mfma_f32_32x32x16_bf16 a[48:63], a[196:199], v[164:167], a[48:63]\n"
"v_exp_f32 v237, v237\n"
"v_exp_f32 v238, v238\n"
"v_mfma_f32_32x32x16_bf16 a[0:15], a[200:203], v[168:171], a[0:15]\n"
"ds_read_b128 a[216:219], v224 offset:45056\n"
"v_mul_f32 v239, v220, v237\n"
"v_fmac_f32 v239, v244, v238\n"
"v_mfma_f32_32x32x16_bf16 a[16:31], a[200:203], v[172:175], a[16:31]\n"
"v_rcp_f32 v239, v239\n"
"s_nop 0\n"
"v_mfma_f32_32x32x16_bf16 a[32:47], a[204:207], v[168:171], a[32:47]\n"
"ds_read_b128 a[220:223], v224 offset:46080\n"
"v_mul_f32 v128, v237, v239\n"
"v_mul_f32 v130, v238, v239\n"
"v_mfma_f32_32x32x16_bf16 a[48:63], a[204:207], v[172:175], a[48:63]\n"
"v_mov_b32 v129, v128\n"
"v_mov_b32 v131, v130\n"
"v_mfma_f32_32x32x16_bf16 a[0:15], a[208:211], v[192:195], a[0:15]\n"
"ds_read_b128 a[224:227], v224 offset:34816\n"
"v_max_f32 v236, v223, v247\n"
"v_sub_f32 v237, v223, v236\n"
"v_mfma_f32_32x32x16_bf16 a[16:31], a[208:211], v[196:199], a[16:31]\n"
"v_exp_f32 v237, v237\n"
"v_sub_f32 v238, v247, v236\n"
"s_waitcnt lgkmcnt(2)\n"
"v_mfma_f32_32x32x16_bf16 a[32:47], a[212:215], v[192:195], a[32:47]\n"
"ds_read_b128 a[228:231], v224 offset:35840\n"
"v_exp_f32 v238, v238\n"
"v_mul_f32 v239, v221, v237\n"
"v_mfma_f32_32x32x16_bf16 a[48:63], a[212:215], v[196:199], a[48:63]\n"
"v_fmac_f32 v239, v245, v238\n"
"v_rcp_f32 v239, v239\n"
"v_mfma_f32_32x32x16_bf16 a[0:15], a[216:219], v[200:203], a[0:15]\n"
"ds_read_b128 a[232:235], v224 offset:38912\n"
"v_mul_f32 v132, v237, v239\n"
"s_nop 0\n"
"v_mfma_f32_32x32x16_bf16 a[16:31], a[216:219], v[204:207], a[16:31]\n"
"v_mul_f32 v134, v238, v239\n"
"v_mov_b32 v133, v132\n"
"s_waitcnt lgkmcnt(2)\n"
"v_mfma_f32_32x32x16_bf16 a[32:47], a[220:223], v[200:203], a[32:47]\n"
"ds_read_b128 a[236:239], v224 offset:39936\n"
"v_mov_b32 v135, v134\n"
"v_mfma_f32_32x32x16_bf16 a[48:63], a[220:223], v[204:207], a[48:63]\n"
"v_mfma_f32_32x32x16_bf16 a[64:79], a[224:227], v[160:163], a[64:79]\n"
"ds_read_b128 a[240:243], v224 offset:43008\n"
"v_mfma_f32_32x32x16_bf16 a[80:95], a[224:227], v[164:167], a[80:95]\n"
"ds_write_b128 v240, a[0:3] offset:0\n"
"ds_write_b128 v240, a[4:7] offset:1024\n"
"s_waitcnt lgkmcnt(4)\n"
"v_mfma_f32_32x32x16_bf16 a[96:111], a[228:231], v[160:163], a[96:111]\n"
"ds_read_b128 a[244:247], v224 offset:44032\n"
"ds_write_b128 v240, a[8:11] offset:2048\n"
"v_mfma_f32_32x32x16_bf16 a[112:127], a[228:231], v[164:167], a[112:127]\n"
"ds_write_b128 v240, a[12:15] offset:3072\n"
"v_mfma_f32_32x32x16_bf16 a[64:79], a[232:235], v[168:171], a[64:79]\n"
"ds_read_b128 a[248:251], v224 offset:47104\n"
"ds_write_b128 v240, a[16:19] offset:4096\n"
"v_mfma_f32_32x32x16_bf16 a[80:95], a[232:235], v[172:175], a[80:95]\n"
"ds_write_b128 v240, a[20:23] offset:5120\n"
"s_waitcnt lgkmcnt(8)\n"
"v_mfma_f32_32x32x16_bf16 a[96:111], a[236:239], v[168:171], a[96:111]\n"
"ds_read_b128 a[252:255], v224 offset:48128\n"
"ds_write_b128 v240, a[24:27] offset:6144\n"
"v_mfma_f32_32x32x16_bf16 a[112:127], a[236:239], v[172:175], a[112:127]\n"
"ds_write_b128 v240, a[28:31] offset:7168\n"
"v_mfma_f32_32x32x16_bf16 a[64:79], a[240:243], v[192:195], a[64:79]\n"
"ds_write_b128 v240, a[32:35] offset:8192\n"
"v_mfma_f32_32x32x16_bf16 a[80:95], a[240:243], v[196:199], a[80:95]\n"
"ds_write_b128 v240, a[36:39] offset:9216\n"
"s_waitcnt lgkmcnt(6)\n"
"v_mfma_f32_32x32x16_bf16 a[96:111], a[244:247], v[192:195], a[96:111]\n"
"ds_write_b128 v240, a[40:43] offset:10240\n"
"v_mfma_f32_32x32x16_bf16 a[112:127], a[244:247], v[196:199], a[112:127]\n"
"ds_write_b128 v240, a[44:47] offset:11264\n"
"v_mfma_f32_32x32x16_bf16 a[64:79], a[248:251], v[200:203], a[64:79]\n"
"ds_write_b128 v240, a[48:51] offset:12288\n"
"v_mfma_f32_32x32x16_bf16 a[80:95], a[248:251], v[204:207], a[80:95]\n"
"ds_write_b128 v240, a[52:55] offset:13312\n"
"s_waitcnt lgkmcnt(5)\n"
"v_mfma_f32_32x32x16_bf16 a[96:111], a[252:255], v[200:203], a[96:111]\n"
"ds_write_b128 v240, a[56:59] offset:14336\n"
"v_mfma_f32_32x32x16_bf16 a[112:127], a[252:255], v[204:207], a[112:127]\n"
"ds_write_b128 v240, a[60:63] offset:15360\n"
"s_waitcnt lgkmcnt(0)\n"
"s_barrier\n"
"ds_read_b128 v[0:3], v241 offset:0\n"
"ds_read_b128 v[4:7], v241 offset:1024\n"
"ds_read_b128 v[8:11], v241 offset:2048\n"
"ds_read_b128 v[12:15], v241 offset:3072\n"
"ds_read_b128 v[16:19], v241 offset:4096\n"
"ds_read_b128 v[20:23], v241 offset:5120\n"
"ds_read_b128 v[24:27], v241 offset:6144\n"
"ds_read_b128 v[28:31], v241 offset:7168\n"
"ds_read_b128 v[32:35], v241 offset:8192\n"
"ds_read_b128 v[36:39], v241 offset:9216\n"
"ds_read_b128 v[40:43], v241 offset:10240\n"
"ds_read_b128 v[44:47], v241 offset:11264\n"
"ds_read_b128 v[48:51], v241 offset:12288\n"
"ds_read_b128 v[52:55], v241 offset:13312\n"
"ds_read_b128 v[56:59], v241 offset:14336\n"
"ds_read_b128 v[60:63], v241 offset:15360\n"
"v_accvgpr_read_b32 v64, a64\n"
"v_accvgpr_read_b32 v65, a65\n"
"v_accvgpr_read_b32 v66, a66\n"
"v_accvgpr_read_b32 v67, a67\n"
"v_accvgpr_read_b32 v68, a68\n"
"v_accvgpr_read_b32 v69, a69\n"
"v_accvgpr_read_b32 v70, a70\n"
"v_accvgpr_read_b32 v71, a71\n"
"v_accvgpr_read_b32 v72, a72\n"
"v_accvgpr_read_b32 v73, a73\n"
"v_accvgpr_read_b32 v74, a74\n"
"v_accvgpr_read_b32 v75, a75\n"
"v_accvgpr_read_b32 v76, a76\n"
"v_accvgpr_read_b32 v77, a77\n"
"v_accvgpr_read_b32 v78, a78\n"
"v_accvgpr_read_b32 v79, a79\n"
"v_pk_mul_f32 v[64:65], v[64:65], v[128:129]\n"
"v_pk_mul_f32 v[66:67], v[66:67], v[128:129]\n"
"v_pk_mul_f32 v[68:69], v[68:69], v[128:129]\n"
"v_pk_mul_f32 v[70:71], v[70:71], v[128:129]\n"
"v_pk_mul_f32 v[72:73], v[72:73], v[128:129]\n"
"v_pk_mul_f32 v[74:75], v[74:75], v[128:129]\n"
"v_pk_mul_f32 v[76:77], v[76:77], v[128:129]\n"
"v_pk_mul_f32 v[78:79], v[78:79], v[128:129]\n"
"s_waitcnt lgkmcnt(15)\n"
"v_pk_fma_f32 v[64:65], v[0:1], v[130:131], v[64:65]\n"
"v_pk_fma_f32 v[66:67], v[2:3], v[130:131], v[66:67]\n"
"s_waitcnt lgkmcnt(14)\n"
"v_pk_fma_f32 v[68:69], v[4:5], v[130:131], v[68:69]\n"
"v_pk_fma_f32 v[70:71], v[6:7], v[130:131], v[70:71]\n"
"v_cvt_pk_bf16_f32 v160, v64, v65\n"
"v_cvt_pk_bf16_f32 v161, v66, v67\n"
"v_cvt_pk_bf16_f32 v162, v68, v69\n"
"v_cvt_pk_bf16_f32 v163, v70, v71\n"
"buffer_store_dwordx4 v[160:163], v228, s[96:99], s38 offen offset:2048 sc1\n"
"s_waitcnt lgkmcnt(9)\n"
"v_pk_fma_f32 v[72:73], v[8:9], v[130:131], v[72:73]\n"
"v_pk_fma_f32 v[74:75], v[10:11], v[130:131], v[74:75]\n"
"v_pk_fma_f32 v[76:77], v[12:13], v[130:131], v[76:77]\n"
"v_pk_fma_f32 v[78:79], v[14:15], v[130:131], v[78:79]\n"
"v_cvt_pk_bf16_f32 v164, v72, v73\n"
"v_cvt_pk_bf16_f32 v165, v74, v75\n"
"v_cvt_pk_bf16_f32 v166, v76, v77\n"
"v_cvt_pk_bf16_f32 v167, v78, v79\n"
"buffer_store_dwordx4 v[164:167], v228, s[96:99], s38 offen offset:2560 sc1\n"
"v_accvgpr_read_b32 v80, a80\n"
"v_accvgpr_read_b32 v81, a81\n"
"v_accvgpr_read_b32 v82, a82\n"
"v_accvgpr_read_b32 v83, a83\n"
"v_accvgpr_read_b32 v84, a84\n"
"v_accvgpr_read_b32 v85, a85\n"
"v_accvgpr_read_b32 v86, a86\n"
"v_accvgpr_read_b32 v87, a87\n"
"v_accvgpr_read_b32 v88, a88\n"
"v_accvgpr_read_b32 v89, a89\n"
"v_accvgpr_read_b32 v90, a90\n"
"v_accvgpr_read_b32 v91, a91\n"
"v_accvgpr_read_b32 v92, a92\n"
"v_accvgpr_read_b32 v93, a93\n"
"v_accvgpr_read_b32 v94, a94\n"
"v_accvgpr_read_b32 v95, a95\n"
"v_pk_mul_f32 v[80:81], v[80:81], v[132:133]\n"
"v_pk_mul_f32 v[82:83], v[82:83], v[132:133]\n"
"v_pk_mul_f32 v[84:85], v[84:85], v[132:133]\n"
"v_pk_mul_f32 v[86:87], v[86:87], v[132:133]\n"
"v_pk_mul_f32 v[88:89], v[88:89], v[132:133]\n"
"v_pk_mul_f32 v[90:91], v[90:91], v[132:133]\n"
"v_pk_mul_f32 v[92:93], v[92:93], v[132:133]\n"
"v_pk_mul_f32 v[94:95], v[94:95], v[132:133]\n"
"v_pk_fma_f32 v[80:81], v[16:17], v[134:135], v[80:81]\n"
"v_pk_fma_f32 v[82:83], v[18:19], v[134:135], v[82:83]\n"
"v_pk_fma_f32 v[84:85], v[20:21], v[134:135], v[84:85]\n"
"v_pk_fma_f32 v[86:87], v[22:23], v[134:135], v[86:87]\n"
"v_cvt_pk_bf16_f32 v168, v80, v81\n"
"v_cvt_pk_bf16_f32 v169, v82, v83\n"
"v_cvt_pk_bf16_f32 v170, v84, v85\n"
"v_cvt_pk_bf16_f32 v171, v86, v87\n"
"buffer_store_dwordx4 v[168:171], v228, s[96:99], s39 offen offset:2048 sc1\n"
"v_pk_fma_f32 v[88:89], v[24:25], v[134:135], v[88:89]\n"
"v_pk_fma_f32 v[90:91], v[26:27], v[134:135], v[90:91]\n"
"s_waitcnt lgkmcnt(0)\n"
"v_pk_fma_f32 v[92:93], v[28:29], v[134:135], v[92:93]\n"
"v_pk_fma_f32 v[94:95], v[30:31], v[134:135], v[94:95]\n"
"v_cvt_pk_bf16_f32 v172, v88, v89\n"
"v_cvt_pk_bf16_f32 v173, v90, v91\n"
"v_cvt_pk_bf16_f32 v174, v92, v93\n"
"v_cvt_pk_bf16_f32 v175, v94, v95\n"
"buffer_store_dwordx4 v[172:175], v228, s[96:99], s39 offen offset:2560 sc1\n"
"v_accvgpr_read_b32 v96, a96\n"
"v_accvgpr_read_b32 v97, a97\n"
"v_accvgpr_read_b32 v98, a98\n"
"v_accvgpr_read_b32 v99, a99\n"
"v_accvgpr_read_b32 v100, a100\n"
"v_accvgpr_read_b32 v101, a101\n"
"v_accvgpr_read_b32 v102, a102\n"
"v_accvgpr_read_b32 v103, a103\n"
"v_accvgpr_read_b32 v104, a104\n"
"v_accvgpr_read_b32 v105, a105\n"
"v_accvgpr_read_b32 v106, a106\n"
"v_accvgpr_read_b32 v107, a107\n"
"v_accvgpr_read_b32 v108, a108\n"
"v_accvgpr_read_b32 v109, a109\n"
"v_accvgpr_read_b32 v110, a110\n"
"v_accvgpr_read_b32 v111, a111\n"
"v_pk_mul_f32 v[96:97], v[96:97], v[128:129]\n"
"v_pk_mul_f32 v[98:99], v[98:99], v[128:129]\n"
"v_pk_mul_f32 v[100:101], v[100:101], v[128:129]\n"
"v_pk_mul_f32 v[102:103], v[102:103], v[128:129]\n"
"v_pk_mul_f32 v[104:105], v[104:105], v[128:129]\n"
"v_pk_mul_f32 v[106:107], v[106:107], v[128:129]\n"
"v_pk_mul_f32 v[108:109], v[108:109], v[128:129]\n"
"v_pk_mul_f32 v[110:111], v[110:111], v[128:129]\n"
"v_pk_fma_f32 v[96:97], v[32:33], v[130:131], v[96:97]\n"
"v_pk_fma_f32 v[98:99], v[34:35], v[130:131], v[98:99]\n"
"v_pk_fma_f32 v[100:101], v[36:37], v[130:131], v[100:101]\n"
"v_pk_fma_f32 v[102:103], v[38:39], v[130:131], v[102:103]\n"
"v_cvt_pk_bf16_f32 v176, v96, v97\n"
"v_cvt_pk_bf16_f32 v177, v98, v99\n"
"v_cvt_pk_bf16_f32 v178, v100, v101\n"
"v_cvt_pk_bf16_f32 v179, v102, v103\n"
"buffer_store_dwordx4 v[176:179], v228, s[96:99], s38 offen offset:3072 sc1\n"
"v_pk_fma_f32 v[104:105], v[40:41], v[130:131], v[104:105]\n"
"v_pk_fma_f32 v[106:107], v[42:43], v[130:131], v[106:107]\n"
"v_pk_fma_f32 v[108:109], v[44:45], v[130:131], v[108:109]\n"
"v_pk_fma_f32 v[110:111], v[46:47], v[130:131], v[110:111]\n"
"v_cvt_pk_bf16_f32 v180, v104, v105\n"
"v_cvt_pk_bf16_f32 v181, v106, v107\n"
"v_cvt_pk_bf16_f32 v182, v108, v109\n"
"v_cvt_pk_bf16_f32 v183, v110, v111\n"
"buffer_store_dwordx4 v[180:183], v228, s[96:99], s38 offen offset:3584 sc1\n"
"v_accvgpr_read_b32 v112, a112\n"
"v_accvgpr_read_b32 v113, a113\n"
"v_accvgpr_read_b32 v114, a114\n"
"v_accvgpr_read_b32 v115, a115\n"
"v_accvgpr_read_b32 v116, a116\n"
"v_accvgpr_read_b32 v117, a117\n"
"v_accvgpr_read_b32 v118, a118\n"
"v_accvgpr_read_b32 v119, a119\n"
"v_accvgpr_read_b32 v120, a120\n"
"v_accvgpr_read_b32 v121, a121\n"
"v_accvgpr_read_b32 v122, a122\n"
"v_accvgpr_read_b32 v123, a123\n"
"v_accvgpr_read_b32 v124, a124\n"
"v_accvgpr_read_b32 v125, a125\n"
"v_accvgpr_read_b32 v126, a126\n"
"v_accvgpr_read_b32 v127, a127\n"
"v_pk_mul_f32 v[112:113], v[112:113], v[132:133]\n"
"v_pk_mul_f32 v[114:115], v[114:115], v[132:133]\n"
"v_pk_mul_f32 v[116:117], v[116:117], v[132:133]\n"
"v_pk_mul_f32 v[118:119], v[118:119], v[132:133]\n"
"v_pk_mul_f32 v[120:121], v[120:121], v[132:133]\n"
"v_pk_mul_f32 v[122:123], v[122:123], v[132:133]\n"
"v_pk_mul_f32 v[124:125], v[124:125], v[132:133]\n"
"v_pk_mul_f32 v[126:127], v[126:127], v[132:133]\n"
"v_pk_fma_f32 v[112:113], v[48:49], v[134:135], v[112:113]\n"
"v_pk_fma_f32 v[114:115], v[50:51], v[134:135], v[114:115]\n"
"v_pk_fma_f32 v[116:117], v[52:53], v[134:135], v[116:117]\n"
"v_pk_fma_f32 v[118:119], v[54:55], v[134:135], v[118:119]\n"
"v_cvt_pk_bf16_f32 v184, v112, v113\n"
"v_cvt_pk_bf16_f32 v185, v114, v115\n"
"v_cvt_pk_bf16_f32 v186, v116, v117\n"
"v_cvt_pk_bf16_f32 v187, v118, v119\n"
"buffer_store_dwordx4 v[184:187], v228, s[96:99], s39 offen offset:3072 sc1\n"
"v_pk_fma_f32 v[120:121], v[56:57], v[134:135], v[120:121]\n"
"v_pk_fma_f32 v[122:123], v[58:59], v[134:135], v[122:123]\n"
"v_pk_fma_f32 v[124:125], v[60:61], v[134:135], v[124:125]\n"
"v_pk_fma_f32 v[126:127], v[62:63], v[134:135], v[126:127]\n"
"v_cvt_pk_bf16_f32 v188, v120, v121\n"
"v_cvt_pk_bf16_f32 v189, v122, v123\n"
"v_cvt_pk_bf16_f32 v190, v124, v125\n"
"v_cvt_pk_bf16_f32 v191, v126, v127\n"
"buffer_store_dwordx4 v[188:191], v228, s[96:99], s39 offen offset:3584 sc1\n"
"s_branch END_%=\n"
"FIN_LO_%=:\n"
"ds_read_b128 a[192:195], v224 offset:34816\n"
"ds_read_b128 a[196:199], v224 offset:35840\n"
"ds_read_b128 a[200:203], v224 offset:38912\n"
"ds_read_b128 a[204:207], v224 offset:39936\n"
"s_waitcnt lgkmcnt(3)\n"
"v_mfma_f32_32x32x16_bf16 a[64:79], a[192:195], v[160:163], a[64:79]\n"
"ds_read_b128 a[208:211], v224 offset:43008\n"
"v_mfma_f32_32x32x16_bf16 a[80:95], a[192:195], v[164:167], a[80:95]\n"
"ds_read_b128 v[244:247], v243\n"
"s_waitcnt lgkmcnt(0)\n"
"v_max_f32 v236, v222, v246\n"
"v_mfma_f32_32x32x16_bf16 a[96:111], a[196:199], v[160:163], a[96:111]\n"
"ds_read_b128 a[212:215], v224 offset:44032\n"
"v_sub_f32 v237, v222, v236\n"
"v_sub_f32 v238, v246, v236\n"
"v_mfma_f32_32x32x16_bf16 a[112:127], a[196:199], v[164:167], a[112:127]\n"
"v_exp_f32 v237, v237\n"
"v_exp_f32 v238, v238\n"
"v_mfma_f32_32x32x16_bf16 a[64:79], a[200:203], v[168:171], a[64:79]\n"
"ds_read_b128 a[216:219], v224 offset:47104\n"
"v_mul_f32 v239, v220, v237\n"
"v_fmac_f32 v239, v244, v238\n"
"v_mfma_f32_32x32x16_bf16 a[80:95], a[200:203], v[172:175], a[80:95]\n"
"v_rcp_f32 v239, v239\n"
"s_nop 0\n"
"v_mfma_f32_32x32x16_bf16 a[96:111], a[204:207], v[168:171], a[96:111]\n"
"ds_read_b128 a[220:223], v224 offset:48128\n"
"v_mul_f32 v128, v237, v239\n"
"v_mul_f32 v130, v238, v239\n"
"v_mfma_f32_32x32x16_bf16 a[112:127], a[204:207], v[172:175], a[112:127]\n"
"v_mov_b32 v129, v128\n"
"v_mov_b32 v131, v130\n"
"v_mfma_f32_32x32x16_bf16 a[64:79], a[208:211], v[192:195], a[64:79]\n"
"ds_read_b128 a[224:227], v224 offset:32768\n"
"v_max_f32 v236, v223, v247\n"
"v_sub_f32 v237, v223, v236\n"
"v_mfma_f32_32x32x16_bf16 a[80:95], a[208:211], v[196:199], a[80:95]\n"
"v_exp_f32 v237, v237\n"
"v_sub_f32 v238, v247, v236\n"
"s_waitcnt lgkmcnt(2)\n"
"v_mfma_f32_32x32x16_bf16 a[96:111], a[212:215], v[192:195], a[96:111]\n"
"ds_read_b128 a[228:231], v224 offset:33792\n"
"v_exp_f32 v238, v238\n"
"v_mul_f32 v239, v221, v237\n"
"v_mfma_f32_32x32x16_bf16 a[112:127], a[212:215], v[196:199], a[112:127]\n"
"v_fmac_f32 v239, v245, v238\n"
"v_rcp_f32 v239, v239\n"
"v_mfma_f32_32x32x16_bf16 a[64:79], a[216:219], v[200:203], a[64:79]\n"
"ds_read_b128 a[232:235], v224 offset:36864\n"
"v_mul_f32 v132, v237, v239\n"
"s_nop 0\n"
"v_mfma_f32_32x32x16_bf16 a[80:95], a[216:219], v[204:207], a[80:95]\n"
"v_mul_f32 v134, v238, v239\n"
"v_mov_b32 v133, v132\n"
"s_waitcnt lgkmcnt(2)\n"
"v_mfma_f32_32x32x16_bf16 a[96:111], a[220:223], v[200:203], a[96:111]\n"
"ds_read_b128 a[236:239], v224 offset:37888\n"
"v_mov_b32 v135, v134\n"
"v_mfma_f32_32x32x16_bf16 a[112:127], a[220:223], v[204:207], a[112:127]\n"
"v_mfma_f32_32x32x16_bf16 a[0:15], a[224:227], v[160:163], a[0:15]\n"
"ds_read_b128 a[240:243], v224 offset:40960\n"
"v_mfma_f32_32x32x16_bf16 a[16:31], a[224:227], v[164:167], a[16:31]\n"
"ds_write_b128 v240, a[64:67] offset:0\n"
"ds_write_b128 v240, a[68:71] offset:1024\n"
"s_waitcnt lgkmcnt(4)\n"
"v_mfma_f32_32x32x16_bf16 a[32:47], a[228:231], v[160:163], a[32:47]\n"
"ds_read_b128 a[244:247], v224 offset:41984\n"
"ds_write_b128 v240, a[72:75] offset:2048\n"
"v_mfma_f32_32x32x16_bf16 a[48:63], a[228:231], v[164:167], a[48:63]\n"
"ds_write_b128 v240, a[76:79] offset:3072\n"
"v_mfma_f32_32x32x16_bf16 a[0:15], a[232:235], v[168:171], a[0:15]\n"
"ds_read_b128 a[248:251], v224 offset:45056\n"
"ds_write_b128 v240, a[80:83] offset:4096\n"
"v_mfma_f32_32x32x16_bf16 a[16:31], a[232:235], v[172:175], a[16:31]\n"
"ds_write_b128 v240, a[84:87] offset:5120\n"
"s_waitcnt lgkmcnt(8)\n"
"v_mfma_f32_32x32x16_bf16 a[32:47], a[236:239], v[168:171], a[32:47]\n"
"ds_read_b128 a[252:255], v224 offset:46080\n"
"ds_write_b128 v240, a[88:91] offset:6144\n"
"v_mfma_f32_32x32x16_bf16 a[48:63], a[236:239], v[172:175], a[48:63]\n"
"ds_write_b128 v240, a[92:95] offset:7168\n"
"v_mfma_f32_32x32x16_bf16 a[0:15], a[240:243], v[192:195], a[0:15]\n"
"ds_write_b128 v240, a[96:99] offset:8192\n"
"v_mfma_f32_32x32x16_bf16 a[16:31], a[240:243], v[196:199], a[16:31]\n"
"ds_write_b128 v240, a[100:103] offset:9216\n"
"s_waitcnt lgkmcnt(6)\n"
"v_mfma_f32_32x32x16_bf16 a[32:47], a[244:247], v[192:195], a[32:47]\n"
"ds_write_b128 v240, a[104:107] offset:10240\n"
"v_mfma_f32_32x32x16_bf16 a[48:63], a[244:247], v[196:199], a[48:63]\n"
"ds_write_b128 v240, a[108:111] offset:11264\n"
"v_mfma_f32_32x32x16_bf16 a[0:15], a[248:251], v[200:203], a[0:15]\n"
"ds_write_b128 v240, a[112:115] offset:12288\n"
"v_mfma_f32_32x32x16_bf16 a[16:31], a[248:251], v[204:207], a[16:31]\n"
"ds_write_b128 v240, a[116:119] offset:13312\n"
"s_waitcnt lgkmcnt(5)\n"
"v_mfma_f32_32x32x16_bf16 a[32:47], a[252:255], v[200:203], a[32:47]\n"
"ds_write_b128 v240, a[120:123] offset:14336\n"
"v_mfma_f32_32x32x16_bf16 a[48:63], a[252:255], v[204:207], a[48:63]\n"
"ds_write_b128 v240, a[124:127] offset:15360\n"
"FIN_LOC_%=:\n"
"s_waitcnt lgkmcnt(0)\n"
"s_barrier\n"
"ds_read_b128 v[0:3], v241 offset:0\n"
"ds_read_b128 v[4:7], v241 offset:1024\n"
"ds_read_b128 v[8:11], v241 offset:2048\n"
"ds_read_b128 v[12:15], v241 offset:3072\n"
"ds_read_b128 v[16:19], v241 offset:4096\n"
"ds_read_b128 v[20:23], v241 offset:5120\n"
"ds_read_b128 v[24:27], v241 offset:6144\n"
"ds_read_b128 v[28:31], v241 offset:7168\n"
"ds_read_b128 v[32:35], v241 offset:8192\n"
"ds_read_b128 v[36:39], v241 offset:9216\n"
"ds_read_b128 v[40:43], v241 offset:10240\n"
"ds_read_b128 v[44:47], v241 offset:11264\n"
"ds_read_b128 v[48:51], v241 offset:12288\n"
"ds_read_b128 v[52:55], v241 offset:13312\n"
"ds_read_b128 v[56:59], v241 offset:14336\n"
"ds_read_b128 v[60:63], v241 offset:15360\n"
"v_accvgpr_read_b32 v64, a0\n"
"v_accvgpr_read_b32 v65, a1\n"
"v_accvgpr_read_b32 v66, a2\n"
"v_accvgpr_read_b32 v67, a3\n"
"v_accvgpr_read_b32 v68, a4\n"
"v_accvgpr_read_b32 v69, a5\n"
"v_accvgpr_read_b32 v70, a6\n"
"v_accvgpr_read_b32 v71, a7\n"
"v_accvgpr_read_b32 v72, a8\n"
"v_accvgpr_read_b32 v73, a9\n"
"v_accvgpr_read_b32 v74, a10\n"
"v_accvgpr_read_b32 v75, a11\n"
"v_accvgpr_read_b32 v76, a12\n"
"v_accvgpr_read_b32 v77, a13\n"
"v_accvgpr_read_b32 v78, a14\n"
"v_accvgpr_read_b32 v79, a15\n"
"v_pk_mul_f32 v[64:65], v[64:65], v[128:129]\n"
"v_pk_mul_f32 v[66:67], v[66:67], v[128:129]\n"
"v_pk_mul_f32 v[68:69], v[68:69], v[128:129]\n"
"v_pk_mul_f32 v[70:71], v[70:71], v[128:129]\n"
"v_pk_mul_f32 v[72:73], v[72:73], v[128:129]\n"
"v_pk_mul_f32 v[74:75], v[74:75], v[128:129]\n"
"v_pk_mul_f32 v[76:77], v[76:77], v[128:129]\n"
"v_pk_mul_f32 v[78:79], v[78:79], v[128:129]\n"
"s_waitcnt lgkmcnt(15)\n"
"v_pk_fma_f32 v[64:65], v[0:1], v[130:131], v[64:65]\n"
"v_pk_fma_f32 v[66:67], v[2:3], v[130:131], v[66:67]\n"
"s_waitcnt lgkmcnt(14)\n"
"v_pk_fma_f32 v[68:69], v[4:5], v[130:131], v[68:69]\n"
"v_pk_fma_f32 v[70:71], v[6:7], v[130:131], v[70:71]\n"
"v_cvt_pk_bf16_f32 v160, v64, v65\n"
"v_cvt_pk_bf16_f32 v161, v66, v67\n"
"v_cvt_pk_bf16_f32 v162, v68, v69\n"
"v_cvt_pk_bf16_f32 v163, v70, v71\n"
"buffer_store_dwordx4 v[160:163], v228, s[96:99], s38 offen offset:0 sc1\n"
"s_waitcnt lgkmcnt(9)\n"
"v_pk_fma_f32 v[72:73], v[8:9], v[130:131], v[72:73]\n"
"v_pk_fma_f32 v[74:75], v[10:11], v[130:131], v[74:75]\n"
"v_pk_fma_f32 v[76:77], v[12:13], v[130:131], v[76:77]\n"
"v_pk_fma_f32 v[78:79], v[14:15], v[130:131], v[78:79]\n"
"v_cvt_pk_bf16_f32 v164, v72, v73\n"
"v_cvt_pk_bf16_f32 v165, v74, v75\n"
"v_cvt_pk_bf16_f32 v166, v76, v77\n"
"v_cvt_pk_bf16_f32 v167, v78, v79\n"
"buffer_store_dwordx4 v[164:167], v228, s[96:99], s38 offen offset:512 sc1\n"
"v_accvgpr_read_b32 v80, a16\n"
"v_accvgpr_read_b32 v81, a17\n"
"v_accvgpr_read_b32 v82, a18\n"
"v_accvgpr_read_b32 v83, a19\n"
"v_accvgpr_read_b32 v84, a20\n"
"v_accvgpr_read_b32 v85, a21\n"
"v_accvgpr_read_b32 v86, a22\n"
"v_accvgpr_read_b32 v87, a23\n"
"v_accvgpr_read_b32 v88, a24\n"
"v_accvgpr_read_b32 v89, a25\n"
"v_accvgpr_read_b32 v90, a26\n"
"v_accvgpr_read_b32 v91, a27\n"
"v_accvgpr_read_b32 v92, a28\n"
"v_accvgpr_read_b32 v93, a29\n"
"v_accvgpr_read_b32 v94, a30\n"
"v_accvgpr_read_b32 v95, a31\n"
"v_pk_mul_f32 v[80:81], v[80:81], v[132:133]\n"
"v_pk_mul_f32 v[82:83], v[82:83], v[132:133]\n"
"v_pk_mul_f32 v[84:85], v[84:85], v[132:133]\n"
"v_pk_mul_f32 v[86:87], v[86:87], v[132:133]\n"
"v_pk_mul_f32 v[88:89], v[88:89], v[132:133]\n"
"v_pk_mul_f32 v[90:91], v[90:91], v[132:133]\n"
"v_pk_mul_f32 v[92:93], v[92:93], v[132:133]\n"
"v_pk_mul_f32 v[94:95], v[94:95], v[132:133]\n"
"v_pk_fma_f32 v[80:81], v[16:17], v[134:135], v[80:81]\n"
"v_pk_fma_f32 v[82:83], v[18:19], v[134:135], v[82:83]\n"
"v_pk_fma_f32 v[84:85], v[20:21], v[134:135], v[84:85]\n"
"PG16_%=:\n"
"s_cbranch_execz RPG16_%=\n"
"v_pk_fma_f32 v[86:87], v[22:23], v[134:135], v[86:87]\n"
"v_cvt_pk_bf16_f32 v168, v80, v81\n"
"v_cvt_pk_bf16_f32 v169, v82, v83\n"
"v_cvt_pk_bf16_f32 v170, v84, v85\n"
"v_cvt_pk_bf16_f32 v171, v86, v87\n"
"buffer_store_dwordx4 v[168:171], v228, s[96:99], s39 offen offset:0 sc1\n"
"v_pk_fma_f32 v[88:89], v[24:25], v[134:135], v[88:89]\n"
"v_pk_fma_f32 v[90:91], v[26:27], v[134:135], v[90:91]\n"
"s_waitcnt lgkmcnt(0)\n"
"v_pk_fma_f32 v[92:93], v[28:29], v[134:135], v[92:93]\n"
"v_pk_fma_f32 v[94:95], v[30:31], v[134:135], v[94:95]\n"
"v_cvt_pk_bf16_f32 v172, v88, v89\n"
"v_cvt_pk_bf16_f32 v173, v90, v91\n"
"v_cvt_pk_bf16_f32 v174, v92, v93\n"
"v_cvt_pk_bf16_f32 v175, v94, v95\n"
"buffer_store_dwordx4 v[172:175], v228, s[96:99], s39 offen offset:512 sc1\n"
"v_accvgpr_read_b32 v96, a32\n"
"v_accvgpr_read_b32 v97, a33\n"
"v_accvgpr_read_b32 v98, a34\n"
"v_accvgpr_read_b32 v99, a35\n"
"v_accvgpr_read_b32 v100, a36\n"
"v_accvgpr_read_b32 v101, a37\n"
"v_accvgpr_read_b32 v102, a38\n"
"v_accvgpr_read_b32 v103, a39\n"
"v_accvgpr_read_b32 v104, a40\n"
"v_accvgpr_read_b32 v105, a41\n"
"v_accvgpr_read_b32 v106, a42\n"
"v_accvgpr_read_b32 v107, a43\n"
"v_accvgpr_read_b32 v108, a44\n"
"v_accvgpr_read_b32 v109, a45\n"
"v_accvgpr_read_b32 v110, a46\n"
"v_accvgpr_read_b32 v111, a47\n"
"v_pk_mul_f32 v[96:97], v[96:97], v[128:129]\n"
"v_pk_mul_f32 v[98:99], v[98:99], v[128:129]\n"
"v_pk_mul_f32 v[100:101], v[100:101], v[128:129]\n"
"v_pk_mul_f32 v[102:103], v[102:103], v[128:129]\n"
"v_pk_mul_f32 v[104:105], v[104:105], v[128:129]\n"
"v_pk_mul_f32 v[106:107], v[106:107], v[128:129]\n"
"v_pk_mul_f32 v[108:109], v[108:109], v[128:129]\n"
"v_pk_mul_f32 v[110:111], v[110:111], v[128:129]\n"
"v_pk_fma_f32 v[96:97], v[32:33], v[130:131], v[96:97]\n"
"v_pk_fma_f32 v[98:99], v[34:35], v[130:131], v[98:99]\n"
"v_pk_fma_f32 v[100:101], v[36:37], v[130:131], v[100:101]\n"
"v_pk_fma_f32 v[102:103], v[38:39], v[130:131], v[102:103]\n"
"v_cvt_pk_bf16_f32 v176, v96, v97\n"
"v_cvt_pk_bf16_f32 v177, v98, v99\n"
"v_cvt_pk_bf16_f32 v178, v100, v101\n"
"v_cvt_pk_bf16_f32 v179, v102, v103\n"
"buffer_store_dwordx4 v[176:179], v228, s[96:99], s38 offen offset:1024 sc1\n"
"v_pk_fma_f32 v[104:105], v[40:41], v[130:131], v[104:105]\n"
"v_pk_fma_f32 v[106:107], v[42:43], v[130:131], v[106:107]\n"
"v_pk_fma_f32 v[108:109], v[44:45], v[130:131], v[108:109]\n"
"v_pk_fma_f32 v[110:111], v[46:47], v[130:131], v[110:111]\n"
"v_cvt_pk_bf16_f32 v180, v104, v105\n"
"v_cvt_pk_bf16_f32 v181, v106, v107\n"
"v_cvt_pk_bf16_f32 v182, v108, v109\n"
"v_cvt_pk_bf16_f32 v183, v110, v111\n"
"buffer_store_dwordx4 v[180:183], v228, s[96:99], s38 offen offset:1536 sc1\n"
"v_accvgpr_read_b32 v112, a48\n"
"v_accvgpr_read_b32 v113, a49\n"
"v_accvgpr_read_b32 v114, a50\n"
"v_accvgpr_read_b32 v115, a51\n"
"v_accvgpr_read_b32 v116, a52\n"
"v_accvgpr_read_b32 v117, a53\n"
"v_accvgpr_read_b32 v118, a54\n"
"v_accvgpr_read_b32 v119, a55\n"
"v_accvgpr_read_b32 v120, a56\n"
"v_accvgpr_read_b32 v121, a57\n"
"v_accvgpr_read_b32 v122, a58\n"
"v_accvgpr_read_b32 v123, a59\n"
"v_accvgpr_read_b32 v124, a60\n"
"v_accvgpr_read_b32 v125, a61\n"
"v_accvgpr_read_b32 v126, a62\n"
"v_accvgpr_read_b32 v127, a63\n"
"v_pk_mul_f32 v[112:113], v[112:113], v[132:133]\n"
"v_pk_mul_f32 v[114:115], v[114:115], v[132:133]\n"
"v_pk_mul_f32 v[116:117], v[116:117], v[132:133]\n"
"v_pk_mul_f32 v[118:119], v[118:119], v[132:133]\n"
"v_pk_mul_f32 v[120:121], v[120:121], v[132:133]\n"
"v_pk_mul_f32 v[122:123], v[122:123], v[132:133]\n"
"v_pk_mul_f32 v[124:125], v[124:125], v[132:133]\n"
"v_pk_mul_f32 v[126:127], v[126:127], v[132:133]\n"
"v_pk_fma_f32 v[112:113], v[48:49], v[134:135], v[112:113]\n"
"v_pk_fma_f32 v[114:115], v[50:51], v[134:135], v[114:115]\n"
"v_pk_fma_f32 v[116:117], v[52:53], v[134:135], v[116:117]\n"
"v_pk_fma_f32 v[118:119], v[54:55], v[134:135], v[118:119]\n"
"v_cvt_pk_bf16_f32 v184, v112, v113\n"
"v_cvt_pk_bf16_f32 v185, v114, v115\n"
"v_cvt_pk_bf16_f32 v186, v116, v117\n"
"v_cvt_pk_bf16_f32 v187, v118, v119\n"
"buffer_store_dwordx4 v[184:187], v228, s[96:99], s39 offen offset:1024 sc1\n"
"v_pk_fma_f32 v[120:121], v[56:57], v[134:135], v[120:121]\n"
"v_pk_fma_f32 v[122:123], v[58:59], v[134:135], v[122:123]\n"
"v_pk_fma_f32 v[124:125], v[60:61], v[134:135], v[124:125]\n"
"v_pk_fma_f32 v[126:127], v[62:63], v[134:135], v[126:127]\n"
"v_cvt_pk_bf16_f32 v188, v120, v121\n"
"v_cvt_pk_bf16_f32 v189, v122, v123\n"
"v_cvt_pk_bf16_f32 v190, v124, v125\n"
"v_cvt_pk_bf16_f32 v191, v126, v127\n"
"buffer_store_dwordx4 v[188:191], v228, s[96:99], s39 offen offset:1536 sc1\n"
"END_%=:\n"
"s_and_b32 s68, %1, 7\n"
"s_lshr_b32 s69, %1, 6\n"
"s_lshr_b32 s64, s68, 1\n"
"s_lshl_b32 s64, s64, 2\n"
"s_add_u32 s64, s64, s69\n"
"s_lshl_b32 s64, s64, 1\n"
"s_and_b32 s69, s68, 1\n"
"s_add_u32 s64, s64, s69\n"
"s_bfe_u32 s65, %1, 0x30003\n"
"s_lshr_b32 s69, %1, 7\n"
"s_lshl_b32 s66, s68, 1\n"
"s_add_u32 s66, s66, s69\n"
"s_bfe_u32 s67, %1, 0x40003\n"
"s_and_b32 s43, s65, 1\n"
"s_lshl_b32 s42, s64, 2\n"
"s_add_u32 s42, s42, %2\n"
"v_lshrrev_b32 v248, 4, v252\n"
"v_and_b32 v249, 15, v252\n"
"s_mov_b32 s60, 105920512\n"
"buffer_load_dwordx4 v[240:243], v251, s[96:99], s60 offen sc1\n"
"buffer_load_dwordx4 v[244:247], v251, s[96:99], s60 offen offset:1024 sc1\n"
"s_mov_b64 s[28:29], s[18:19]\n"
"s_mov_b32 s30, 2621440\n"
"s_mov_b32 s31, 0x20000\n"
"s_mov_b64 s[32:33], s[20:21]\n"
"s_mov_b32 s34, 2621440\n"
"s_mov_b32 s35, 0x20000\n"
"s_and_b32 s45, s64, 1\n"
"s_lshl_b32 s45, s45, 16\n"
"s_mul_i32 s38, s65, 5\n"
"s_add_u32 s70, s38, s43\n"
"s_add_u32 s70, s70, %2\n"
"s_lshl_b32 s70, s70, 17\n"
"s_add_u32 s70, s70, s45\n"
"s_add_u32 s70, s70, 67108864\n"
"s_cmp_eq_u32 s43, 0\n"
"s_cselect_b32 s39, 4, 0\n"
"s_add_u32 s71, s38, s39\n"
"s_lshl_b32 s71, s71, 17\n"
"s_add_u32 s71, s71, s45\n"
"s_lshl_b32 s39, %2, 10\n"
"s_add_u32 s71, s71, s39\n"
"s_add_u32 s71, s71, 67108864\n"
"s_add_u32 s75, s39, %3\n"
"s_add_u32 s75, s75, 16384\n"
"s_lshl_b32 s74, %2, 12\n"
"s_add_u32 s74, s74, %3\n"
"s_lshl_b32 s72, s42, 17\n"
"s_add_u32 s72, s72, s45\n"
"s_add_u32 s72, s72, 50331648\n"
"s_xor_b32 s73, s72, 524288\n"
"s_lshl_b32 s76, s64, 3\n"
"s_add_u32 s76, s76, s65\n"
"s_lshl_b32 s76, s76, 2\n"
"s_add_u32 s76, s76, %2\n"
"s_mul_i32 s38, s65, 5\n"
"s_add_u32 s40, s38, s43\n"
"s_cmp_eq_u32 s43, 0\n"
"s_cselect_b32 s39, 4, 0\n"
"s_add_u32 s41, s38, s39\n"
"s_lshr_b32 s41, s41, 1\n"
"s_lshl_b32 s49, s40, 4\n"
"s_lshl_b32 s50, s41, 5\n"
"s_lshl_b32 s39, s43, 2\n"
"s_add_u32 s50, s50, s39\n"
"s_lshl_b32 s40, s40, 9\n"
"s_lshl_b32 s41, s41, 10\n"
"s_lshl_b32 s38, s49, 2\n"
"v_lshl_add_u32 v254, v248, 5, s38\n"
"s_lshl_b32 s38, s50, 2\n"
"v_lshl_add_u32 v255, v248, 5, s38\n"
"s_add_u32 s38, %3, 0\n"
"v_add_u32 v93, s38, v251\n"
"s_add_u32 s38, %3, 61440\n"
"v_add_u32 v94, s38, v251\n"
"s_add_u32 s38, %3, 122880\n"
"v_add_u32 v95, s38, v251\n"
"s_waitcnt vmcnt(1)\n"
"v_xor_b32 v240, s14, v240\n"
"v_xor_b32 v241, s15, v241\n"
"v_xor_b32 v242, s14, v242\n"
"v_xor_b32 v243, s15, v243\n"
"s_waitcnt vmcnt(0)\n"
"v_xor_b32 v244, s14, v244\n"
"v_xor_b32 v245, s15, v245\n"
"v_xor_b32 v246, s14, v246\n"
"v_xor_b32 v247, s15, v247\n"
"v_or3_b32 v240, v240, v241, v242\n"
"v_or3_b32 v243, v243, v244, v245\n"
"v_or3_b32 v240, v240, v246, v247\n"
"v_or_b32 v240, v240, v243\n"
"v_cmp_ne_u32 vcc, 0, v240\n"
"s_nop 0\n"
"s_cbranch_vccnz SLOW_RT_%=\n"
"OK_RT_%=:\n"
"s_mov_b64 exec, 0\n"
"s_cmp_lg_u32 %2, 0\n"
"s_cbranch_scc1 TWC0_%=\n"
"s_branch PG17_%=\n"
"RPG17_%=:\n"
"s_nop 0\n"
"s_nop 0\n"
"s_nop 0\n"
"s_nop 0\n"
"s_nop 0\n"
"TWC0_%=:\n"
"s_cmp_lg_u32 %2, 1\n"
"s_cbranch_scc1 TWC1_%=\n"
"s_branch PG18_%=\n"
"RPG18_%=:\n"
"s_nop 0\n"
"s_nop 0\n"
"s_nop 0\n"
"s_nop 0\n"
"s_nop 0\n"
"TWC1_%=:\n"
"s_cmp_lg_u32 %2, 2\n"
"s_cbranch_scc1 TWC2_%=\n"
"s_branch PG19_%=\n"
"RPG19_%=:\n"
"s_nop 0\n"
"s_nop 0\n"
"s_nop 0\n"
"s_nop 0\n"
"s_nop 0\n"
"TWC2_%=:\n"
"s_cmp_lg_u32 %2, 3\n"
"s_cbranch_scc1 TWC3_%=\n"
"s_branch PG20_%=\n"
"RPG20_%=:\n"
"s_nop 0\n"
"s_nop 0\n"
"s_nop 0\n"
"s_nop 0\n"
"s_nop 0\n"
"TWC3_%=:\n"
"s_mov_b64 exec, -1\n"
"s_barrier\n"
"s_cmp_lg_u32 %2, 0\n"
"s_cbranch_scc1 PUB_CTX_%=\n"
"s_mul_i32 s38, s25, 256\n"
"s_lshl_b32 s39, s24, 3\n"
"s_add_u32 s38, s38, s39\n"
"s_add_u32 s38, s38, 105914368\n"
"v_min_u32 v243, 1, v252\n"
"v_lshlrev_b32 v243, 30, v243\n"
"v_mov_b32 v244, s14\n"
"v_mov_b32 v245, s15\n"
"buffer_store_dwordx2 v[244:245], v243, s[96:99], s38 offen sc1\n"
"PUB_CTX_%=:\n"
"s_add_u32 m0, s74, 0\n"
"s_nop 0\n"
"buffer_load_dwordx4 v251, s[96:99], s70 offen offset:0 sc1 lds\n"
"buffer_load_dwordx4 v251, s[96:99], s70 offen offset:1024 sc1 lds\n"
"buffer_load_dwordx4 v251, s[96:99], s70 offen offset:2048 sc1 lds\n"
"buffer_load_dwordx4 v251, s[96:99], s70 offen offset:3072 sc1 lds\n"
"s_add_u32 m0, s75, 0\n"
"s_add_u32 s70, s70, 4096\n"
"buffer_load_dwordx4 v251, s[96:99], s71 offen sc1 lds\n"
"s_add_u32 s71, s71, 4096\n"
"s_add_u32 m0, s74, 20480\n"
"s_nop 0\n"
"buffer_load_dwordx4 v251, s[96:99], s70 offen offset:0 sc1 lds\n"
"buffer_load_dwordx4 v251, s[96:99], s70 offen offset:1024 sc1 lds\n"
"buffer_load_dwordx4 v251, s[96:99], s70 offen offset:2048 sc1 lds\n"
"buffer_load_dwordx4 v251, s[96:99], s70 offen offset:3072 sc1 lds\n"
"s_add_u32 m0, s75, 20480\n"
"s_add_u32 s70, s70, 4096\n"
"buffer_load_dwordx4 v251, s[96:99], s71 offen sc1 lds\n"
"s_add_u32 s71, s71, 4096\n"
"s_add_u32 m0, s74, 40960\n"
"s_nop 0\n"
"buffer_load_dwordx4 v251, s[96:99], s70 offen offset:0 sc1 lds\n"
"buffer_load_dwordx4 v251, s[96:99], s70 offen offset:1024 sc1 lds\n"
"buffer_load_dwordx4 v251, s[96:99], s70 offen offset:2048 sc1 lds\n"
"buffer_load_dwordx4 v251, s[96:99], s70 offen offset:3072 sc1 lds\n"
"s_add_u32 m0, s75, 40960\n"
"s_add_u32 s70, s70, 4096\n"
"buffer_load_dwordx4 v251, s[96:99], s71 offen sc1 lds\n"
"s_add_u32 s71, s71, 4096\n"
"s_add_u32 m0, s74, 61440\n"
"s_nop 0\n"
"buffer_load_dwordx4 v251, s[96:99], s70 offen offset:0 sc1 lds\n"
"buffer_load_dwordx4 v251, s[96:99], s70 offen offset:1024 sc1 lds\n"
"buffer_load_dwordx4 v251, s[96:99], s70 offen offset:2048 sc1 lds\n"
"buffer_load_dwordx4 v251, s[96:99], s70 offen offset:3072 sc1 lds\n"
"s_add_u32 m0, s75, 61440\n"
"s_add_u32 s70, s70, 4096\n"
"buffer_load_dwordx4 v251, s[96:99], s71 offen sc1 lds\n"
"s_add_u32 s71, s71, 4096\n"
"s_add_u32 m0, s74, 81920\n"
"s_nop 0\n"
"buffer_load_dwordx4 v251, s[96:99], s70 offen offset:0 sc1 lds\n"
"buffer_load_dwordx4 v251, s[96:99], s70 offen offset:1024 sc1 lds\n"
"buffer_load_dwordx4 v251, s[96:99], s70 offen offset:2048 sc1 lds\n"
"buffer_load_dwordx4 v251, s[96:99], s70 offen offset:3072 sc1 lds\n"
"s_add_u32 m0, s75, 81920\n"
"s_add_u32 s70, s70, 4096\n"
"buffer_load_dwordx4 v251, s[96:99], s71 offen sc1 lds\n"
"s_add_u32 s71, s71, 4096\n"
"s_add_u32 m0, s74, 102400\n"
"s_nop 0\n"
"buffer_load_dwordx4 v251, s[96:99], s70 offen offset:0 sc1 lds\n"
"buffer_load_dwordx4 v251, s[96:99], s70 offen offset:1024 sc1 lds\n"
"buffer_load_dwordx4 v251, s[96:99], s70 offen offset:2048 sc1 lds\n"
"buffer_load_dwordx4 v251, s[96:99], s70 offen offset:3072 sc1 lds\n"
"s_add_u32 m0, s75, 102400\n"
"s_add_u32 s70, s70, 4096\n"
"buffer_load_dwordx4 v251, s[96:99], s71 offen sc1 lds\n"
"s_add_u32 s71, s71, 4096\n"
"s_add_u32 m0, s74, 122880\n"
"s_nop 0\n"
"buffer_load_dwordx4 v251, s[96:99], s70 offen offset:0 sc1 lds\n"
"buffer_load_dwordx4 v251, s[96:99], s70 offen offset:1024 sc1 lds\n"
"buffer_load_dwordx4 v251, s[96:99], s70 offen offset:2048 sc1 lds\n"
"buffer_load_dwordx4 v251, s[96:99], s70 offen offset:3072 sc1 lds\n"
"s_add_u32 m0, s75, 122880\n"
"s_add_u32 s70, s70, 4096\n"
"buffer_load_dwordx4 v251, s[96:99], s71 offen sc1 lds\n"
"s_add_u32 s71, s71, 4096\n"
"v_lshl_add_u32 v250, s43, 3, v251\n"
"s_mov_b32 s56, 0xbdd2d3e8\n"
"s_mov_b32 s57, 0xbdd2d3e8\n"
"v_mov_b32 v206, 0xc0135761\n"
"v_mov_b32 v207, 0xc0135761\n"
"v_mov_b32 v208, 1.0\n"
"v_mov_b32 v209, 1.0\n"
"v_lshl_add_u32 v246, s42, 4, v249\n"
"v_mul_u32_u24 v246, 1280, v246\n"
"v_lshl_add_u32 v246, v248, 4, v246\n"
"s_lshl_b32 s38, s49, 1\n"
"v_add_u32 v240, s38, v246\n"
"s_lshl_b32 s38, s50, 1\n"
"v_add_u32 v241, s38, v246\n"
"buffer_load_dwordx4 v[220:223], v240, s[28:31], 0 offen offset:0\n"
"buffer_load_dwordx4 v[224:227], v240, s[28:31], 0 offen offset:64\n"
"buffer_load_dwordx2 v[228:229], v241, s[28:31], 0 offen\n"
"v_mul_u32_u24 v232, 1280, v249\n"
"v_lshl_add_u32 v232, v248, 4, v232\n"
"v_cmp_gt_u32 vcc, 3, v249\n"
"v_mov_b32 v233, 1073741824\n"
"s_nop 0\n"
"v_cndmask_b32 v232, v233, v232, vcc\n"
"v_lshl_add_u32 v205, s42, 4, v249\n"
"v_lshlrev_b32 v205, 8, v205\n"
"v_lshl_add_u32 v205, v248, 4, v205\n"
"v_lshrrev_b32 v233, 1, v248\n"
"v_lshl_add_u32 v205, v233, 30, v205\n"
"s_mov_b32 s38, 104923136\n"
"buffer_load_dwordx4 a[176:179], v232, s[96:99], s38 offen offset:0 sc1\n"
"buffer_load_dwordx4 a[180:183], v232, s[96:99], s38 offen offset:64 sc1\n"
"buffer_load_dwordx4 a[184:187], v232, s[96:99], s38 offen offset:128 sc1\n"
"buffer_load_dwordx4 a[188:191], v232, s[96:99], s38 offen offset:192 sc1\n"
"buffer_load_dwordx4 a[192:195], v232, s[96:99], s38 offen offset:256 sc1\n"
"buffer_load_dwordx4 a[196:199], v232, s[96:99], s38 offen offset:320 sc1\n"
"buffer_load_dwordx4 a[200:203], v232, s[96:99], s38 offen offset:384 sc1\n"
"buffer_load_dwordx4 a[204:207], v232, s[96:99], s38 offen offset:448 sc1\n"
"buffer_load_dwordx4 a[208:211], v232, s[96:99], s38 offen offset:512 sc1\n"
"buffer_load_dwordx4 a[212:215], v232, s[96:99], s38 offen offset:576 sc1\n"
"buffer_load_dwordx4 a[216:219], v232, s[96:99], s38 offen offset:640 sc1\n"
"buffer_load_dwordx4 a[220:223], v232, s[96:99], s38 offen offset:704 sc1\n"
"buffer_load_dwordx4 a[224:227], v232, s[96:99], s38 offen offset:768 sc1\n"
"buffer_load_dwordx4 a[228:231], v232, s[96:99], s38 offen offset:832 sc1\n"
"buffer_load_dwordx4 a[232:235], v232, s[96:99], s38 offen offset:896 sc1\n"
"buffer_load_dwordx4 a[236:239], v232, s[96:99], s38 offen offset:960 sc1\n"
"buffer_load_dwordx4 a[240:243], v232, s[96:99], s38 offen offset:1024 sc1\n"
"buffer_load_dwordx4 a[244:247], v232, s[96:99], s38 offen offset:1088 sc1\n"
"buffer_load_dwordx4 a[248:251], v232, s[96:99], s38 offen offset:1152 sc1\n"
"buffer_load_dwordx4 a[252:255], v232, s[96:99], s38 offen offset:1216 sc1\n"
"s_mul_i32 s47, s65, 102400\n"
"s_add_u32 s47, s47, 73400320\n"
"s_mul_i32 s38, s43, 20480\n"
"s_add_u32 s46, s47, s38\n"
"s_cmp_eq_u32 s43, 0\n"
"s_cselect_b32 s38, 81920, 0\n"
"s_add_u32 s47, s47, s38\n"
"s_lshr_b32 s38, s64, 1\n"
"s_sub_u32 s39, 15, s38\n"
"s_min_u32 s39, s38, s39\n"
"s_lshl_b32 s39, s39, 1\n"
"s_and_b32 s38, s64, 1\n"
"s_add_u32 s39, s39, s38\n"
"s_lshl_b32 s39, s39, 4\n"
"v_min_u32 v243, 15, v252\n"
"v_add_u32 v243, s39, v243\n"
"v_and_b32 v243, 255, v243\n"
"v_lshlrev_b32 v243, 3, v243\n"
"s_mov_b32 s60, 105914368\n"
"buffer_load_dwordx2 v[244:245], v243, s[96:99], s60 offen sc1\n"
"s_add_u32 m0, s74, 143360\n"
"s_nop 0\n"
"buffer_load_dwordx4 v251, s[96:99], s70 offen offset:0 sc1 lds\n"
"buffer_load_dwordx4 v251, s[96:99], s70 offen offset:1024 sc1 lds\n"
"buffer_load_dwordx4 v251, s[96:99], s70 offen offset:2048 sc1 lds\n"
"buffer_load_dwordx4 v251, s[96:99], s70 offen offset:3072 sc1 lds\n"
"s_add_u32 m0, s75, 143360\n"
"s_add_u32 s70, s70, 4096\n"
"buffer_load_dwordx4 v251, s[96:99], s71 offen sc1 lds\n"
"s_add_u32 s71, s71, 4096\n"
"s_waitcnt vmcnt(5)\n"
"v_cmp_ne_u64 vcc, s[14:15], v[244:245]\n"
"s_nop 0\n"
"s_cbranch_vccnz SLOW_CTX_%=\n"
"OK_CTX_%=:\n"
"buffer_load_dwordx4 a[0:3], v251, s[96:99], s72 offen offset:0 sc1\n"
"buffer_load_dwordx4 a[4:7], v251, s[96:99], s73 offen offset:0 sc1\n"
"buffer_load_dwordx4 a[8:11], v251, s[96:99], s72 offen offset:1024 sc1\n"
"buffer_load_dwordx4 a[12:15], v251, s[96:99], s73 offen offset:1024 sc1\n"
"buffer_load_dwordx4 a[16:19], v251, s[96:99], s72 offen offset:2048 sc1\n"
"buffer_load_dwordx4 a[20:23], v251, s[96:99], s73 offen offset:2048 sc1\n"
"buffer_load_dwordx4 a[24:27], v251, s[96:99], s72 offen offset:3072 sc1\n"
"buffer_load_dwordx4 a[28:31], v251, s[96:99], s73 offen offset:3072 sc1\n"
"s_add_u32 s72, s72, 4096\n"
"s_add_u32 s73, s73, 4096\n"
"buffer_load_dwordx4 a[32:35], v251, s[96:99], s72 offen offset:0 sc1\n"
"buffer_load_dwordx4 a[36:39], v251, s[96:99], s73 offen offset:0 sc1\n"
"buffer_load_dwordx4 a[40:43], v251, s[96:99], s72 offen offset:1024 sc1\n"
"buffer_load_dwordx4 a[44:47], v251, s[96:99], s73 offen offset:1024 sc1\n"
"buffer_load_dwordx4 a[48:51], v251, s[96:99], s72 offen offset:2048 sc1\n"
"buffer_load_dwordx4 a[52:55], v251, s[96:99], s73 offen offset:2048 sc1\n"
"buffer_load_dwordx4 a[56:59], v251, s[96:99], s72 offen offset:3072 sc1\n"
"buffer_load_dwordx4 a[60:63], v251, s[96:99], s73 offen offset:3072 sc1\n"
"s_add_u32 s72, s72, 4096\n"
"s_add_u32 s73, s73, 4096\n"
"s_barrier\n"
"ds_read_b128 v[40:43], v93 offset:0\n"
"ds_read_b128 v[44:47], v93 offset:4096\n"
"ds_read_b128 v[48:51], v93 offset:8192\n"
"ds_read_b128 v[52:55], v93 offset:12288\n"
"ds_read_b128 v[56:59], v93 offset:16384\n"
"ds_read_b128 v[60:63], v93 offset:1024\n"
"s_waitcnt vmcnt(15) lgkmcnt(5)\n"
"v_mfma_f32_16x16x32_bf16 v[0:3], v[40:43], a[0:3], 0\n"
"ds_read_b128 v[64:67], v93 offset:5120\n"
"s_waitcnt vmcnt(14)\n"
"v_mfma_f32_16x16x32_bf16 v[20:23], v[40:43], a[4:7], 0\n"
"s_waitcnt lgkmcnt(5)\n"
"v_mfma_f32_16x16x32_bf16 v[4:7], v[44:47], a[0:3], 0\n"
"ds_read_b128 v[68:71], v93 offset:9216\n"
"v_mfma_f32_16x16x32_bf16 v[24:27], v[44:47], a[4:7], 0\n"
"s_waitcnt lgkmcnt(5)\n"
"v_mfma_f32_16x16x32_bf16 v[8:11], v[48:51], a[0:3], 0\n"
"ds_read_b128 v[40:43], v93 offset:13312\n"
"v_mfma_f32_16x16x32_bf16 v[28:31], v[48:51], a[4:7], 0\n"
"s_waitcnt lgkmcnt(5)\n"
"v_mfma_f32_16x16x32_bf16 v[12:15], v[52:55], a[0:3], 0\n"
"ds_read_b128 v[44:47], v93 offset:17408\n"
"v_mfma_f32_16x16x32_bf16 v[32:35], v[52:55], a[4:7], 0\n"
"s_waitcnt lgkmcnt(5)\n"
"v_mfma_f32_16x16x32_bf16 v[16:19], v[56:59], a[0:3], 0\n"
"ds_read_b128 v[48:51], v93 offset:2048\n"
"v_mfma_f32_16x16x32_bf16 v[36:39], v[56:59], a[4:7], 0\n"
"buffer_load_dwordx4 a[64:67], v251, s[96:99], s72 offen offset:0 sc1\n"
"buffer_load_dwordx4 a[68:71], v251, s[96:99], s73 offen offset:0 sc1\n"
"buffer_load_dwordx4 a[72:75], v251, s[96:99], s72 offen offset:1024 sc1\n"
"buffer_load_dwordx4 a[76:79], v251, s[96:99], s73 offen offset:1024 sc1\n"
"s_waitcnt vmcnt(17) lgkmcnt(5)\n"
"v_mfma_f32_16x16x32_bf16 v[0:3], v[60:63], a[8:11], v[0:3]\n"
"ds_read_b128 v[52:55], v93 offset:6144\n"
"s_waitcnt vmcnt(16)\n"
"v_mfma_f32_16x16x32_bf16 v[20:23], v[60:63], a[12:15], v[20:23]\n"
"s_waitcnt lgkmcnt(5)\n"
"v_mfma_f32_16x16x32_bf16 v[4:7], v[64:67], a[8:11], v[4:7]\n"
"ds_read_b128 v[56:59], v93 offset:10240\n"
"v_mfma_f32_16x16x32_bf16 v[24:27], v[64:67], a[12:15], v[24:27]\n"
"s_waitcnt lgkmcnt(5)\n"
"v_mfma_f32_16x16x32_bf16 v[8:11], v[68:71], a[8:11], v[8:11]\n"
"ds_read_b128 v[60:63], v93 offset:14336\n"
"v_mfma_f32_16x16x32_bf16 v[28:31], v[68:71], a[12:15], v[28:31]\n"
"s_waitcnt lgkmcnt(5)\n"
"v_mfma_f32_16x16x32_bf16 v[12:15], v[40:43], a[8:11], v[12:15]\n"
"ds_read_b128 v[64:67], v93 offset:18432\n"
"v_mfma_f32_16x16x32_bf16 v[32:35], v[40:43], a[12:15], v[32:35]\n"
"s_waitcnt lgkmcnt(5)\n"
"v_mfma_f32_16x16x32_bf16 v[16:19], v[44:47], a[8:11], v[16:19]\n"
"ds_read_b128 v[68:71], v93 offset:3072\n"
"v_mfma_f32_16x16x32_bf16 v[36:39], v[44:47], a[12:15], v[36:39]\n"
"buffer_load_dwordx4 a[80:83], v251, s[96:99], s72 offen offset:2048 sc1\n"
"buffer_load_dwordx4 a[84:87], v251, s[96:99], s73 offen offset:2048 sc1\n"
"buffer_load_dwordx4 a[88:91], v251, s[96:99], s72 offen offset:3072 sc1\n"
"buffer_load_dwordx4 a[92:95], v251, s[96:99], s73 offen offset:3072 sc1\n"
"s_add_u32 s72, s72, 4096\n"
"s_add_u32 s73, s73, 4096\n"
"s_waitcnt vmcnt(19) lgkmcnt(4)\n"
"v_mfma_f32_16x16x32_bf16 v[0:3], v[48:51], a[16:19], v[0:3]\n"
"ds_read_b128 v[40:43], v93 offset:7168\n"
"s_waitcnt vmcnt(18)\n"
"v_mfma_f32_16x16x32_bf16 v[20:23], v[48:51], a[20:23], v[20:23]\n"
"v_mfma_f32_16x16x32_bf16 v[4:7], v[52:55], a[16:19], v[4:7]\n"
"ds_read_b128 v[44:47], v93 offset:11264\n"
"v_mfma_f32_16x16x32_bf16 v[24:27], v[52:55], a[20:23], v[24:27]\n"
"s_waitcnt lgkmcnt(4)\n"
"v_mfma_f32_16x16x32_bf16 v[8:11], v[56:59], a[16:19], v[8:11]\n"
"ds_read_b128 v[48:51], v93 offset:15360\n"
"v_mfma_f32_16x16x32_bf16 v[28:31], v[56:59], a[20:23], v[28:31]\n"
"v_mfma_f32_16x16x32_bf16 v[12:15], v[60:63], a[16:19], v[12:15]\n"
"ds_read_b128 v[52:55], v93 offset:19456\n"
"v_mfma_f32_16x16x32_bf16 v[32:35], v[60:63], a[20:23], v[32:35]\n"
"s_waitcnt lgkmcnt(0)\n"
"s_barrier\n"
"v_mfma_f32_16x16x32_bf16 v[16:19], v[64:67], a[16:19], v[16:19]\n"
"ds_read_b128 v[56:59], v93 offset:20480\n"
"v_mfma_f32_16x16x32_bf16 v[36:39], v[64:67], a[20:23], v[36:39]\n"
"buffer_load_dwordx4 a[96:99], v251, s[96:99], s72 offen offset:0 sc1\n"
"buffer_load_dwordx4 a[100:103], v251, s[96:99], s73 offen offset:0 sc1\n"
"buffer_load_dwordx4 a[104:107], v251, s[96:99], s72 offen offset:1024 sc1\n"
"buffer_load_dwordx4 a[108:111], v251, s[96:99], s73 offen offset:1024 sc1\n"
"s_add_u32 m0, s74, 0\n"
"s_nop 0\n"
"buffer_load_dwordx4 v251, s[96:99], s70 offen offset:0 sc1 lds\n"
"buffer_load_dwordx4 v251, s[96:99], s70 offen offset:1024 sc1 lds\n"
"buffer_load_dwordx4 v251, s[96:99], s70 offen offset:2048 sc1 lds\n"
"buffer_load_dwordx4 v251, s[96:99], s70 offen offset:3072 sc1 lds\n"
"s_add_u32 m0, s75, 0\n"
"s_nop 0\n"
"buffer_load_dwordx4 v251, s[96:99], s71 offen sc1 lds\n"
"s_add_u32 s70, s70, 4096\n"
"s_add_u32 s71, s71, 4096\n"
"s_nop 0\n"
"s_waitcnt vmcnt(26)\n"
"v_mfma_f32_16x16x32_bf16 v[0:3], v[68:71], a[24:27], v[0:3]\n"
"ds_read_b128 v[60:63], v93 offset:24576\n"
"s_waitcnt vmcnt(25)\n"
"v_mfma_f32_16x16x32_bf16 v[20:23], v[68:71], a[28:31], v[20:23]\n"
"v_mfma_f32_16x16x32_bf16 v[4:7], v[40:43], a[24:27], v[4:7]\n"
"ds_read_b128 v[64:67], v93 offset:28672\n"
"v_mfma_f32_16x16x32_bf16 v[24:27], v[40:43], a[28:31], v[24:27]\n"
"v_mfma_f32_16x16x32_bf16 v[8:11], v[44:47], a[24:27], v[8:11]\n"
"ds_read_b128 v[68:71], v93 offset:32768\n"
"v_mfma_f32_16x16x32_bf16 v[28:31], v[44:47], a[28:31], v[28:31]\n"
"v_mfma_f32_16x16x32_bf16 v[12:15], v[48:51], a[24:27], v[12:15]\n"
"ds_read_b128 v[40:43], v93 offset:36864\n"
"v_mfma_f32_16x16x32_bf16 v[32:35], v[48:51], a[28:31], v[32:35]\n"
"v_mfma_f32_16x16x32_bf16 v[16:19], v[52:55], a[24:27], v[16:19]\n"
"ds_read_b128 v[44:47], v93 offset:21504\n"
"v_mfma_f32_16x16x32_bf16 v[36:39], v[52:55], a[28:31], v[36:39]\n"
"buffer_load_dwordx4 a[112:115], v251, s[96:99], s72 offen offset:2048 sc1\n"
"buffer_load_dwordx4 a[116:119], v251, s[96:99], s73 offen offset:2048 sc1\n"
"buffer_load_dwordx4 a[120:123], v251, s[96:99], s72 offen offset:3072 sc1\n"
"buffer_load_dwordx4 a[124:127], v251, s[96:99], s73 offen offset:3072 sc1\n"
"s_add_u32 s72, s72, 4096\n"
"s_add_u32 s73, s73, 4096\n"
"s_waitcnt vmcnt(28) lgkmcnt(4)\n"
"v_mfma_f32_16x16x32_bf16 v[0:3], v[56:59], a[32:35], v[0:3]\n"
"ds_read_b128 v[48:51], v93 offset:25600\n"
"s_waitcnt vmcnt(27)\n"
"v_mfma_f32_16x16x32_bf16 v[20:23], v[56:59], a[36:39], v[20:23]\n"
"v_mfma_f32_16x16x32_bf16 v[4:7], v[60:63], a[32:35], v[4:7]\n"
"ds_read_b128 v[52:55], v93 offset:29696\n"
"v_mfma_f32_16x16x32_bf16 v[24:27], v[60:63], a[36:39], v[24:27]\n"
"s_waitcnt lgkmcnt(4)\n"
"v_mfma_f32_16x16x32_bf16 v[8:11], v[64:67], a[32:35], v[8:11]\n"
"ds_read_b128 v[56:59], v93 offset:33792\n"
"v_mfma_f32_16x16x32_bf16 v[28:31], v[64:67], a[36:39], v[28:31]\n"
"v_mfma_f32_16x16x32_bf16 v[12:15], v[68:71], a[32:35], v[12:15]\n"
"ds_read_b128 v[60:63], v93 offset:37888\n"
"v_mfma_f32_16x16x32_bf16 v[32:35], v[68:71], a[36:39], v[32:35]\n"
"s_waitcnt lgkmcnt(4)\n"
"v_mfma_f32_16x16x32_bf16 v[16:19], v[40:43], a[32:35], v[16:19]\n"
"ds_read_b128 v[64:67], v93 offset:22528\n"
"v_mfma_f32_16x16x32_bf16 v[36:39], v[40:43], a[36:39], v[36:39]\n"
"buffer_load_dwordx4 a[0:3], v251, s[96:99], s72 offen offset:0 sc1\n"
"buffer_load_dwordx4 a[4:7], v251, s[96:99], s73 offen offset:0 sc1\n"
"buffer_load_dwordx4 a[8:11], v251, s[96:99], s72 offen offset:1024 sc1\n"
"buffer_load_dwordx4 a[12:15], v251, s[96:99], s73 offen offset:1024 sc1\n"
"s_waitcnt vmcnt(30)\n"
"v_mfma_f32_16x16x32_bf16 v[0:3], v[44:47], a[40:43], v[0:3]\n"
"ds_read_b128 v[68:71], v93 offset:26624\n"
"s_waitcnt vmcnt(29)\n"
"v_mfma_f32_16x16x32_bf16 v[20:23], v[44:47], a[44:47], v[20:23]\n"
"s_waitcnt lgkmcnt(5)\n"
"v_mfma_f32_16x16x32_bf16 v[4:7], v[48:51], a[40:43], v[4:7]\n"
"ds_read_b128 v[40:43], v93 offset:30720\n"
"v_mfma_f32_16x16x32_bf16 v[24:27], v[48:51], a[44:47], v[24:27]\n"
"s_waitcnt lgkmcnt(5)\n"
"v_mfma_f32_16x16x32_bf16 v[8:11], v[52:55], a[40:43], v[8:11]\n"
"ds_read_b128 v[44:47], v93 offset:34816\n"
"v_mfma_f32_16x16x32_bf16 v[28:31], v[52:55], a[44:47], v[28:31]\n"
"s_waitcnt lgkmcnt(5)\n"
"v_mfma_f32_16x16x32_bf16 v[12:15], v[56:59], a[40:43], v[12:15]\n"
"ds_read_b128 v[48:51], v93 offset:38912\n"
"v_mfma_f32_16x16x32_bf16 v[32:35], v[56:59], a[44:47], v[32:35]\n"
"s_waitcnt lgkmcnt(5)\n"
"v_mfma_f32_16x16x32_bf16 v[16:19], v[60:63], a[40:43], v[16:19]\n"
"ds_read_b128 v[52:55], v93 offset:23552\n"
"v_mfma_f32_16x16x32_bf16 v[36:39], v[60:63], a[44:47], v[36:39]\n"
"buffer_load_dwordx4 a[16:19], v251, s[96:99], s72 offen offset:2048 sc1\n"
"buffer_load_dwordx4 a[20:23], v251, s[96:99], s73 offen offset:2048 sc1\n"
"buffer_load_dwordx4 a[24:27], v251, s[96:99], s72 offen offset:3072 sc1\n"
"buffer_load_dwordx4 a[28:31], v251, s[96:99], s73 offen offset:3072 sc1\n"
"s_add_u32 s72, s72, 4096\n"
"s_add_u32 s73, s73, 4096\n"
"s_waitcnt vmcnt(32) lgkmcnt(4)\n"
"v_mfma_f32_16x16x32_bf16 v[0:3], v[64:67], a[48:51], v[0:3]\n"
"ds_read_b128 v[56:59], v93 offset:27648\n"
"s_waitcnt vmcnt(31)\n"
"v_mfma_f32_16x16x32_bf16 v[20:23], v[64:67], a[52:55], v[20:23]\n"
"v_mfma_f32_16x16x32_bf16 v[4:7], v[68:71], a[48:51], v[4:7]\n"
"ds_read_b128 v[60:63], v93 offset:31744\n"
"v_mfma_f32_16x16x32_bf16 v[24:27], v[68:71], a[52:55], v[24:27]\n"
"s_waitcnt lgkmcnt(4)\n"
"v_mfma_f32_16x16x32_bf16 v[8:11], v[40:43], a[48:51], v[8:11]\n"
"ds_read_b128 v[64:67], v93 offset:35840\n"
"v_mfma_f32_16x16x32_bf16 v[28:31], v[40:43], a[52:55], v[28:31]\n"
"v_mfma_f32_16x16x32_bf16 v[12:15], v[44:47], a[48:51], v[12:15]\n"
"ds_read_b128 v[68:71], v93 offset:39936\n"
"v_mfma_f32_16x16x32_bf16 v[32:35], v[44:47], a[52:55], v[32:35]\n"
"s_waitcnt lgkmcnt(0)\n"
"s_barrier\n"
"v_mfma_f32_16x16x32_bf16 v[16:19], v[48:51], a[48:51], v[16:19]\n"
"ds_read_b128 v[40:43], v93 offset:40960\n"
"v_mfma_f32_16x16x32_bf16 v[36:39], v[48:51], a[52:55], v[36:39]\n"
"buffer_load_dwordx4 a[32:35], v251, s[96:99], s72 offen offset:0 sc1\n"
"buffer_load_dwordx4 a[36:39], v251, s[96:99], s73 offen offset:0 sc1\n"
"buffer_load_dwordx4 a[40:43], v251, s[96:99], s72 offen offset:1024 sc1\n"
"buffer_load_dwordx4 a[44:47], v251, s[96:99], s73 offen offset:1024 sc1\n"
"s_add_u32 m0, s74, 20480\n"
"s_nop 0\n"
"buffer_load_dwordx4 v251, s[96:99], s70 offen offset:0 sc1 lds\n"
"buffer_load_dwordx4 v251, s[96:99], s70 offen offset:1024 sc1 lds\n"
"buffer_load_dwordx4 v251, s[96:99], s70 offen offset:2048 sc1 lds\n"
"PG17_%=:\n"
"s_cbranch_execz RPG17_%=\n"
"buffer_load_dwordx4 v251, s[96:99], s70 offen offset:3072 sc1 lds\n"
"s_add_u32 m0, s75, 20480\n"
"s_nop 0\n"
"buffer_load_dwordx4 v251, s[96:99], s71 offen sc1 lds\n"
"s_add_u32 s70, s70, 4096\n"
"s_add_u32 s71, s71, 4096\n"
"s_nop 0\n"
"s_waitcnt vmcnt(39)\n"
"v_mfma_f32_16x16x32_bf16 v[0:3], v[52:55], a[56:59], v[0:3]\n"
"ds_read_b128 v[44:47], v93 offset:45056\n"
"s_waitcnt vmcnt(38)\n"
"v_mfma_f32_16x16x32_bf16 v[20:23], v[52:55], a[60:63], v[20:23]\n"
"v_mfma_f32_16x16x32_bf16 v[4:7], v[56:59], a[56:59], v[4:7]\n"
"ds_read_b128 v[48:51], v93 offset:49152\n"
"v_mfma_f32_16x16x32_bf16 v[24:27], v[56:59], a[60:63], v[24:27]\n"
"v_mfma_f32_16x16x32_bf16 v[8:11], v[60:63], a[56:59], v[8:11]\n"
"ds_read_b128 v[52:55], v93 offset:53248\n"
"v_mfma_f32_16x16x32_bf16 v[28:31], v[60:63], a[60:63], v[28:31]\n"
"v_mfma_f32_16x16x32_bf16 v[12:15], v[64:67], a[56:59], v[12:15]\n"
"ds_read_b128 v[56:59], v93 offset:57344\n"
"v_mfma_f32_16x16x32_bf16 v[32:35], v[64:67], a[60:63], v[32:35]\n"
"v_mfma_f32_16x16x32_bf16 v[16:19], v[68:71], a[56:59], v[16:19]\n"
"ds_read_b128 v[60:63], v93 offset:41984\n"
"v_mfma_f32_16x16x32_bf16 v[36:39], v[68:71], a[60:63], v[36:39]\n"
"buffer_load_dwordx4 a[48:51], v251, s[96:99], s72 offen offset:2048 sc1\n"
"buffer_load_dwordx4 a[52:55], v251, s[96:99], s73 offen offset:2048 sc1\n"
"s_waitcnt vmcnt(39) lgkmcnt(5)\n"
"v_mfma_f32_16x16x32_bf16 v[0:3], v[40:43], a[64:67], v[0:3]\n"
"ds_read_b128 v[64:67], v93 offset:46080\n"
"s_waitcnt vmcnt(38)\n"
"v_mfma_f32_16x16x32_bf16 v[20:23], v[40:43], a[68:71], v[20:23]\n"
"s_waitcnt lgkmcnt(5)\n"
"v_mfma_f32_16x16x32_bf16 v[4:7], v[44:47], a[64:67], v[4:7]\n"
"ds_read_b128 v[68:71], v93 offset:50176\n"
"v_mfma_f32_16x16x32_bf16 v[24:27], v[44:47], a[68:71], v[24:27]\n"
"s_waitcnt lgkmcnt(5)\n"
"v_mfma_f32_16x16x32_bf16 v[8:11], v[48:51], a[64:67], v[8:11]\n"
"ds_read_b128 v[40:43], v93 offset:54272\n"
"v_mfma_f32_16x16x32_bf16 v[28:31], v[48:51], a[68:71], v[28:31]\n"
"s_waitcnt lgkmcnt(5)\n"
"v_mfma_f32_16x16x32_bf16 v[12:15], v[52:55], a[64:67], v[12:15]\n"
"ds_read_b128 v[44:47], v93 offset:58368\n"
"v_mfma_f32_16x16x32_bf16 v[32:35], v[52:55], a[68:71], v[32:35]\n"
"s_waitcnt lgkmcnt(5)\n"
"v_mfma_f32_16x16x32_bf16 v[16:19], v[56:59], a[64:67], v[16:19]\n"
"ds_read_b128 v[48:51], v93 offset:43008\n"
"v_mfma_f32_16x16x32_bf16 v[36:39], v[56:59], a[68:71], v[36:39]\n"
"buffer_load_dwordx4 a[56:59], v251, s[96:99], s72 offen offset:3072 sc1\n"
"buffer_load_dwordx4 a[60:63], v251, s[96:99], s73 offen offset:3072 sc1\n"
"s_add_u32 s72, s72, 4096\n"
"s_add_u32 s73, s73, 4096\n"
"s_waitcnt vmcnt(39) lgkmcnt(5)\n"
"v_mfma_f32_16x16x32_bf16 v[0:3], v[60:63], a[72:75], v[0:3]\n"
"ds_read_b128 v[52:55], v93 offset:47104\n"
"s_waitcnt vmcnt(38)\n"
"v_mfma_f32_16x16x32_bf16 v[20:23], v[60:63], a[76:79], v[20:23]\n"
"s_waitcnt lgkmcnt(5)\n"
"v_mfma_f32_16x16x32_bf16 v[4:7], v[64:67], a[72:75], v[4:7]\n"
"ds_read_b128 v[56:59], v93 offset:51200\n"
"v_mfma_f32_16x16x32_bf16 v[24:27], v[64:67], a[76:79], v[24:27]\n"
"s_waitcnt lgkmcnt(5)\n"
"v_mfma_f32_16x16x32_bf16 v[8:11], v[68:71], a[72:75], v[8:11]\n"
"ds_read_b128 v[60:63], v93 offset:55296\n"
"v_mfma_f32_16x16x32_bf16 v[28:31], v[68:71], a[76:79], v[28:31]\n"
"s_waitcnt lgkmcnt(5)\n"
"v_mfma_f32_16x16x32_bf16 v[12:15], v[40:43], a[72:75], v[12:15]\n"
"ds_read_b128 v[64:67], v93 offset:59392\n"
"v_mfma_f32_16x16x32_bf16 v[32:35], v[40:43], a[76:79], v[32:35]\n"
"s_waitcnt lgkmcnt(5)\n"
"v_mfma_f32_16x16x32_bf16 v[16:19], v[44:47], a[72:75], v[16:19]\n"
"ds_read_b128 v[68:71], v93 offset:44032\n"
"v_mfma_f32_16x16x32_bf16 v[36:39], v[44:47], a[76:79], v[36:39]\n"
"buffer_load_dwordx4 a[64:67], v251, s[96:99], s72 offen offset:0 sc1\n"
"buffer_load_dwordx4 a[68:71], v251, s[96:99], s73 offen offset:0 sc1\n"
"s_waitcnt vmcnt(39) lgkmcnt(5)\n"
"v_mfma_f32_16x16x32_bf16 v[0:3], v[48:51], a[80:83], v[0:3]\n"
"ds_read_b128 v[40:43], v93 offset:48128\n"
"s_waitcnt vmcnt(38)\n"
"v_mfma_f32_16x16x32_bf16 v[20:23], v[48:51], a[84:87], v[20:23]\n"
"s_waitcnt lgkmcnt(5)\n"
"v_mfma_f32_16x16x32_bf16 v[4:7], v[52:55], a[80:83], v[4:7]\n"
"ds_read_b128 v[44:47], v93 offset:52224\n"
"v_mfma_f32_16x16x32_bf16 v[24:27], v[52:55], a[84:87], v[24:27]\n"
"s_waitcnt lgkmcnt(5)\n"
"v_mfma_f32_16x16x32_bf16 v[8:11], v[56:59], a[80:83], v[8:11]\n"
"ds_read_b128 v[48:51], v93 offset:56320\n"
"v_mfma_f32_16x16x32_bf16 v[28:31], v[56:59], a[84:87], v[28:31]\n"
"s_waitcnt lgkmcnt(5)\n"
"v_mfma_f32_16x16x32_bf16 v[12:15], v[60:63], a[80:83], v[12:15]\n"
"ds_read_b128 v[52:55], v93 offset:60416\n"
"v_mfma_f32_16x16x32_bf16 v[32:35], v[60:63], a[84:87], v[32:35]\n"
"s_waitcnt lgkmcnt(0)\n"
"s_barrier\n"
"v_mfma_f32_16x16x32_bf16 v[16:19], v[64:67], a[80:83], v[16:19]\n"
"ds_read_b128 v[56:59], v94 offset:0\n"
"v_mfma_f32_16x16x32_bf16 v[36:39], v[64:67], a[84:87], v[36:39]\n"
"buffer_load_dwordx4 a[72:75], v251, s[96:99], s72 offen offset:1024 sc1\n"
"buffer_load_dwordx4 a[76:79], v251, s[96:99], s73 offen offset:1024 sc1\n"
"s_add_u32 m0, s74, 40960\n"
"s_nop 0\n"
"buffer_load_dwordx4 v251, s[96:99], s70 offen offset:0 sc1 lds\n"
"buffer_load_dwordx4 v251, s[96:99], s70 offen offset:1024 sc1 lds\n"
"buffer_load_dwordx4 v251, s[96:99], s70 offen offset:2048 sc1 lds\n"
"buffer_load_dwordx4 v251, s[96:99], s70 offen offset:3072 sc1 lds\n"
"s_add_u32 m0, s75, 40960\n"
"s_nop 0\n"
"buffer_load_dwordx4 v251, s[96:99], s71 offen sc1 lds\n"
"s_add_u32 s70, s70, 4096\n"
"s_add_u32 s71, s71, 4096\n"
"s_nop 0\n"
"s_waitcnt vmcnt(44)\n"
"v_mfma_f32_16x16x32_bf16 v[0:3], v[68:71], a[88:91], v[0:3]\n"
"ds_read_b128 v[60:63], v94 offset:4096\n"
"s_waitcnt vmcnt(43)\n"
"v_mfma_f32_16x16x32_bf16 v[20:23], v[68:71], a[92:95], v[20:23]\n"
"v_mfma_f32_16x16x32_bf16 v[4:7], v[40:43], a[88:91], v[4:7]\n"
"ds_read_b128 v[64:67], v94 offset:8192\n"
"v_mfma_f32_16x16x32_bf16 v[24:27], v[40:43], a[92:95], v[24:27]\n"
"v_mfma_f32_16x16x32_bf16 v[8:11], v[44:47], a[88:91], v[8:11]\n"
"ds_read_b128 v[68:71], v94 offset:12288\n"
"v_mfma_f32_16x16x32_bf16 v[28:31], v[44:47], a[92:95], v[28:31]\n"
"v_mfma_f32_16x16x32_bf16 v[12:15], v[48:51], a[88:91], v[12:15]\n"
"ds_read_b128 v[40:43], v94 offset:16384\n"
"v_mfma_f32_16x16x32_bf16 v[32:35], v[48:51], a[92:95], v[32:35]\n"
"v_mfma_f32_16x16x32_bf16 v[16:19], v[52:55], a[88:91], v[16:19]\n"
"ds_read_b128 v[44:47], v94 offset:1024\n"
"v_mfma_f32_16x16x32_bf16 v[36:39], v[52:55], a[92:95], v[36:39]\n"
"buffer_load_dwordx4 a[80:83], v251, s[96:99], s72 offen offset:2048 sc1\n"
"buffer_load_dwordx4 a[84:87], v251, s[96:99], s73 offen offset:2048 sc1\n"
"s_waitcnt vmcnt(44) lgkmcnt(5)\n"
"v_mfma_f32_16x16x32_bf16 v[0:3], v[56:59], a[96:99], v[0:3]\n"
"ds_read_b128 v[48:51], v94 offset:5120\n"
"s_waitcnt vmcnt(43)\n"
"v_mfma_f32_16x16x32_bf16 v[20:23], v[56:59], a[100:103], v[20:23]\n"
"s_waitcnt lgkmcnt(5)\n"
"v_mfma_f32_16x16x32_bf16 v[4:7], v[60:63], a[96:99], v[4:7]\n"
"ds_read_b128 v[52:55], v94 offset:9216\n"
"v_mfma_f32_16x16x32_bf16 v[24:27], v[60:63], a[100:103], v[24:27]\n"
"s_waitcnt lgkmcnt(5)\n"
"v_mfma_f32_16x16x32_bf16 v[8:11], v[64:67], a[96:99], v[8:11]\n"
"ds_read_b128 v[56:59], v94 offset:13312\n"
"v_mfma_f32_16x16x32_bf16 v[28:31], v[64:67], a[100:103], v[28:31]\n"
"s_waitcnt lgkmcnt(5)\n"
"v_mfma_f32_16x16x32_bf16 v[12:15], v[68:71], a[96:99], v[12:15]\n"
"ds_read_b128 v[60:63], v94 offset:17408\n"
"v_mfma_f32_16x16x32_bf16 v[32:35], v[68:71], a[100:103], v[32:35]\n"
"s_waitcnt lgkmcnt(5)\n"
"v_mfma_f32_16x16x32_bf16 v[16:19], v[40:43], a[96:99], v[16:19]\n"
"ds_read_b128 v[64:67], v94 offset:2048\n"
"v_mfma_f32_16x16x32_bf16 v[36:39], v[40:43], a[100:103], v[36:39]\n"
"buffer_load_dwordx4 a[88:91], v251, s[96:99], s72 offen offset:3072 sc1\n"
"buffer_load_dwordx4 a[92:95], v251, s[96:99], s73 offen offset:3072 sc1\n"
"s_add_u32 s72, s72, 4096\n"
"s_add_u32 s73, s73, 4096\n"
"s_waitcnt vmcnt(44) lgkmcnt(5)\n"
"v_mfma_f32_16x16x32_bf16 v[0:3], v[44:47], a[104:107], v[0:3]\n"
"ds_read_b128 v[68:71], v94 offset:6144\n"
"s_waitcnt vmcnt(43)\n"
"v_mfma_f32_16x16x32_bf16 v[20:23], v[44:47], a[108:111], v[20:23]\n"
"s_waitcnt lgkmcnt(5)\n"
"v_mfma_f32_16x16x32_bf16 v[4:7], v[48:51], a[104:107], v[4:7]\n"
"ds_read_b128 v[40:43], v94 offset:10240\n"
"v_mfma_f32_16x16x32_bf16 v[24:27], v[48:51], a[108:111], v[24:27]\n"
"s_waitcnt lgkmcnt(5)\n"
"v_mfma_f32_16x16x32_bf16 v[8:11], v[52:55], a[104:107], v[8:11]\n"
"ds_read_b128 v[44:47], v94 offset:14336\n"
"v_mfma_f32_16x16x32_bf16 v[28:31], v[52:55], a[108:111], v[28:31]\n"
"s_waitcnt lgkmcnt(5)\n"
"v_mfma_f32_16x16x32_bf16 v[12:15], v[56:59], a[104:107], v[12:15]\n"
"ds_read_b128 v[48:51], v94 offset:18432\n"
"v_mfma_f32_16x16x32_bf16 v[32:35], v[56:59], a[108:111], v[32:35]\n"
"s_waitcnt lgkmcnt(5)\n"
"v_mfma_f32_16x16x32_bf16 v[16:19], v[60:63], a[104:107], v[16:19]\n"
"ds_read_b128 v[52:55], v94 offset:3072\n"
"v_mfma_f32_16x16x32_bf16 v[36:39], v[60:63], a[108:111], v[36:39]\n"
"buffer_load_dwordx4 a[96:99], v251, s[96:99], s72 offen offset:0 sc1\n"
"buffer_load_dwordx4 a[100:103], v251, s[96:99], s73 offen offset:0 sc1\n"
"s_waitcnt vmcnt(39) lgkmcnt(5)\n"
"v_mfma_f32_16x16x32_bf16 v[0:3], v[64:67], a[112:115], v[0:3]\n"
"ds_read_b128 v[56:59], v94 offset:7168\n"
"s_waitcnt vmcnt(38)\n"
"v_mfma_f32_16x16x32_bf16 v[20:23], v[64:67], a[116:119], v[20:23]\n"
"s_waitcnt lgkmcnt(5)\n"
"v_mfma_f32_16x16x32_bf16 v[4:7], v[68:71], a[112:115], v[4:7]\n"
"ds_read_b128 v[60:63], v94 offset:11264\n"
"v_mfma_f32_16x16x32_bf16 v[24:27], v[68:71], a[116:119], v[24:27]\n"
"s_waitcnt lgkmcnt(5)\n"
"v_mfma_f32_16x16x32_bf16 v[8:11], v[40:43], a[112:115], v[8:11]\n"
"ds_read_b128 v[64:67], v94 offset:15360\n"
"v_mfma_f32_16x16x32_bf16 v[28:31], v[40:43], a[116:119], v[28:31]\n"
"s_waitcnt lgkmcnt(5)\n"
"v_mfma_f32_16x16x32_bf16 v[12:15], v[44:47], a[112:115], v[12:15]\n"
"ds_read_b128 v[68:71], v94 offset:19456\n"
"v_mfma_f32_16x16x32_bf16 v[32:35], v[44:47], a[116:119], v[32:35]\n"
"s_waitcnt lgkmcnt(0)\n"
"s_barrier\n"
"v_mfma_f32_16x16x32_bf16 v[16:19], v[48:51], a[112:115], v[16:19]\n"
"ds_read_b128 v[40:43], v94 offset:20480\n"
"v_mfma_f32_16x16x32_bf16 v[36:39], v[48:51], a[116:119], v[36:39]\n"
"buffer_load_dwordx4 a[104:107], v251, s[96:99], s72 offen offset:1024 sc1\n"
"buffer_load_dwordx4 a[108:111], v251, s[96:99], s73 offen offset:1024 sc1\n"
"s_add_u32 m0, s74, 61440\n"
"s_nop 0\n"
"buffer_load_dwordx4 v251, s[96:99], s70 offen offset:0 sc1 lds\n"
"buffer_load_dwordx4 v251, s[96:99], s70 offen offset:1024 sc1 lds\n"
"buffer_load_dwordx4 v251, s[96:99], s70 offen offset:2048 sc1 lds\n"
"buffer_load_dwordx4 v251, s[96:99], s70 offen offset:3072 sc1 lds\n"
"s_add_u32 m0, s75, 61440\n"
"s_nop 0\n"
"buffer_load_dwordx4 v251, s[96:99], s71 offen sc1 lds\n"
"s_add_u32 s70, s70, 4096\n"
"s_add_u32 s71, s71, 4096\n"
"s_nop 0\n"
"s_waitcnt vmcnt(44)\n"
"v_mfma_f32_16x16x32_bf16 v[0:3], v[52:55], a[120:123], v[0:3]\n"
"ds_read_b128 v[44:47], v94 offset:24576\n"
"s_waitcnt vmcnt(43)\n"
"v_mfma_f32_16x16x32_bf16 v[20:23], v[52:55], a[124:127], v[20:23]\n"
"v_mfma_f32_16x16x32_bf16 v[4:7], v[56:59], a[120:123], v[4:7]\n"
"ds_read_b128 v[48:51], v94 offset:28672\n"
"v_mfma_f32_16x16x32_bf16 v[24:27], v[56:59], a[124:127], v[24:27]\n"
"v_mfma_f32_16x16x32_bf16 v[8:11], v[60:63], a[120:123], v[8:11]\n"
"ds_read_b128 v[52:55], v94 offset:32768\n"
"v_mfma_f32_16x16x32_bf16 v[28:31], v[60:63], a[124:127], v[28:31]\n"
"v_mfma_f32_16x16x32_bf16 v[12:15], v[64:67], a[120:123], v[12:15]\n"
"ds_read_b128 v[56:59], v94 offset:36864\n"
"v_mfma_f32_16x16x32_bf16 v[32:35], v[64:67], a[124:127], v[32:35]\n"
"v_mfma_f32_16x16x32_bf16 v[16:19], v[68:71], a[120:123], v[16:19]\n"
"ds_read_b128 v[60:63], v94 offset:21504\n"
"v_mfma_f32_16x16x32_bf16 v[36:39], v[68:71], a[124:127], v[36:39]\n"
"buffer_load_dwordx4 a[112:115], v251, s[96:99], s72 offen offset:2048 sc1\n"
"buffer_load_dwordx4 a[116:119], v251, s[96:99], s73 offen offset:2048 sc1\n"
"s_waitcnt vmcnt(44) lgkmcnt(5)\n"
"v_mfma_f32_16x16x32_bf16 v[0:3], v[40:43], a[0:3], v[0:3]\n"
"ds_read_b128 v[64:67], v94 offset:25600\n"
"s_waitcnt vmcnt(43)\n"
"v_mfma_f32_16x16x32_bf16 v[20:23], v[40:43], a[4:7], v[20:23]\n"
"v_mfma_f32_16x16x32_bf16 v[96:99], a[176:179], a[176:179], 0\n"
"s_waitcnt lgkmcnt(4)\n"
"v_mfma_f32_16x16x32_bf16 v[4:7], v[44:47], a[0:3], v[4:7]\n"
"ds_read_b128 v[68:71], v94 offset:29696\n"
"v_mfma_f32_16x16x32_bf16 v[24:27], v[44:47], a[4:7], v[24:27]\n"
"v_mfma_f32_16x16x32_bf16 v[96:99], a[180:183], a[180:183], v[96:99]\n"
"v_mfma_f32_16x16x32_bf16 v[8:11], v[48:51], a[0:3], v[8:11]\n"
"ds_read_b128 v[40:43], v94 offset:33792\n"
"v_mfma_f32_16x16x32_bf16 v[28:31], v[48:51], a[4:7], v[28:31]\n"
"s_waitcnt lgkmcnt(4)\n"
"v_mfma_f32_16x16x32_bf16 v[12:15], v[52:55], a[0:3], v[12:15]\n"
"ds_read_b128 v[44:47], v94 offset:37888\n"
"v_mfma_f32_16x16x32_bf16 v[32:35], v[52:55], a[4:7], v[32:35]\n"
"v_mfma_f32_16x16x32_bf16 v[96:99], a[184:187], a[184:187], v[96:99]\n"
"v_mfma_f32_16x16x32_bf16 v[16:19], v[56:59], a[0:3], v[16:19]\n"
"ds_read_b128 v[48:51], v94 offset:22528\n"
"v_mfma_f32_16x16x32_bf16 v[36:39], v[56:59], a[4:7], v[36:39]\n"
"buffer_load_dwordx4 a[120:123], v251, s[96:99], s72 offen offset:3072 sc1\n"
"buffer_load_dwordx4 a[124:127], v251, s[96:99], s73 offen offset:3072 sc1\n"
"s_add_u32 s72, s72, 4096\n"
"s_add_u32 s73, s73, 4096\n"
"s_waitcnt vmcnt(44) lgkmcnt(4)\n"
"v_mfma_f32_16x16x32_bf16 v[0:3], v[60:63], a[8:11], v[0:3]\n"
"ds_read_b128 v[52:55], v94 offset:26624\n"
"s_waitcnt vmcnt(43)\n"
"v_mfma_f32_16x16x32_bf16 v[20:23], v[60:63], a[12:15], v[20:23]\n"
"v_mfma_f32_16x16x32_bf16 v[96:99], a[188:191], a[188:191], v[96:99]\n"
"v_mfma_f32_16x16x32_bf16 v[4:7], v[64:67], a[8:11], v[4:7]\n"
"ds_read_b128 v[56:59], v94 offset:30720\n"
"v_mfma_f32_16x16x32_bf16 v[24:27], v[64:67], a[12:15], v[24:27]\n"
"s_waitcnt lgkmcnt(4)\n"
"v_mfma_f32_16x16x32_bf16 v[8:11], v[68:71], a[8:11], v[8:11]\n"
"ds_read_b128 v[60:63], v94 offset:34816\n"
"v_mfma_f32_16x16x32_bf16 v[28:31], v[68:71], a[12:15], v[28:31]\n"
"v_mfma_f32_16x16x32_bf16 v[96:99], a[192:195], a[192:195], v[96:99]\n"
"v_mfma_f32_16x16x32_bf16 v[12:15], v[40:43], a[8:11], v[12:15]\n"
"ds_read_b128 v[64:67], v94 offset:38912\n"
"v_mfma_f32_16x16x32_bf16 v[32:35], v[40:43], a[12:15], v[32:35]\n"
"s_waitcnt lgkmcnt(4)\n"
"v_mfma_f32_16x16x32_bf16 v[16:19], v[44:47], a[8:11], v[16:19]\n"
"ds_read_b128 v[68:71], v94 offset:23552\n"
"v_mfma_f32_16x16x32_bf16 v[36:39], v[44:47], a[12:15], v[36:39]\n"
"buffer_load_dwordx4 a[0:3], v251, s[96:99], s72 offen offset:0 sc1\n"
"buffer_load_dwordx4 a[4:7], v251, s[96:99], s73 offen offset:0 sc1\n"
"s_waitcnt vmcnt(44)\n"
"v_mfma_f32_16x16x32_bf16 v[0:3], v[48:51], a[16:19], v[0:3]\n"
"ds_read_b128 v[40:43], v94 offset:27648\n"
"s_waitcnt vmcnt(43)\n"
"v_mfma_f32_16x16x32_bf16 v[20:23], v[48:51], a[20:23], v[20:23]\n"
"v_mfma_f32_16x16x32_bf16 v[96:99], a[196:199], a[196:199], v[96:99]\n"
"s_waitcnt lgkmcnt(4)\n"
"v_mfma_f32_16x16x32_bf16 v[4:7], v[52:55], a[16:19], v[4:7]\n"
"ds_read_b128 v[44:47], v94 offset:31744\n"
"v_mfma_f32_16x16x32_bf16 v[24:27], v[52:55], a[20:23], v[24:27]\n"
"v_mfma_f32_16x16x32_bf16 v[96:99], a[200:203], a[200:203], v[96:99]\n"
"v_mfma_f32_16x16x32_bf16 v[8:11], v[56:59], a[16:19], v[8:11]\n"
"ds_read_b128 v[48:51], v94 offset:35840\n"
"v_mfma_f32_16x16x32_bf16 v[28:31], v[56:59], a[20:23], v[28:31]\n"
"s_waitcnt lgkmcnt(4)\n"
"v_mfma_f32_16x16x32_bf16 v[12:15], v[60:63], a[16:19], v[12:15]\n"
"ds_read_b128 v[52:55], v94 offset:39936\n"
"v_mfma_f32_16x16x32_bf16 v[32:35], v[60:63], a[20:23], v[32:35]\n"
"s_waitcnt lgkmcnt(0)\n"
"s_barrier\n"
"v_mfma_f32_16x16x32_bf16 v[16:19], v[64:67], a[16:19], v[16:19]\n"
"ds_read_b128 v[56:59], v94 offset:40960\n"
"v_mfma_f32_16x16x32_bf16 v[36:39], v[64:67], a[20:23], v[36:39]\n"
"buffer_load_dwordx4 a[8:11], v251, s[96:99], s72 offen offset:1024 sc1\n"
"buffer_load_dwordx4 a[12:15], v251, s[96:99], s73 offen offset:1024 sc1\n"
"s_add_u32 m0, s74, 81920\n"
"s_nop 0\n"
"buffer_load_dwordx4 v251, s[96:99], s70 offen offset:0 sc1 lds\n"
"buffer_load_dwordx4 v251, s[96:99], s70 offen offset:1024 sc1 lds\n"
"buffer_load_dwordx4 v251, s[96:99], s70 offen offset:2048 sc1 lds\n"
"buffer_load_dwordx4 v251, s[96:99], s70 offen offset:3072 sc1 lds\n"
"s_add_u32 m0, s75, 81920\n"
"s_nop 0\n"
"buffer_load_dwordx4 v251, s[96:99], s71 offen sc1 lds\n"
"s_add_u32 s70, s70, 4096\n"
"s_add_u32 s71, s71, 4096\n"
"s_nop 0\n"
"s_waitcnt vmcnt(49)\n"
"v_mfma_f32_16x16x32_bf16 v[0:3], v[68:71], a[24:27], v[0:3]\n"
"ds_read_b128 v[60:63], v94 offset:45056\n"
"s_waitcnt vmcnt(48)\n"
"v_mfma_f32_16x16x32_bf16 v[20:23], v[68:71], a[28:31], v[20:23]\n"
"v_mfma_f32_16x16x32_bf16 v[96:99], a[204:207], a[204:207], v[96:99]\n"
"v_mfma_f32_16x16x32_bf16 v[4:7], v[40:43], a[24:27], v[4:7]\n"
"ds_read_b128 v[64:67], v94 offset:49152\n"
"v_mfma_f32_16x16x32_bf16 v[24:27], v[40:43], a[28:31], v[24:27]\n"
"v_mfma_f32_16x16x32_bf16 v[96:99], a[208:211], a[208:211], v[96:99]\n"
"v_mfma_f32_16x16x32_bf16 v[8:11], v[44:47], a[24:27], v[8:11]\n"
"ds_read_b128 v[68:71], v94 offset:53248\n"
"v_mfma_f32_16x16x32_bf16 v[28:31], v[44:47], a[28:31], v[28:31]\n"
"v_mfma_f32_16x16x32_bf16 v[96:99], a[212:215], a[212:215], v[96:99]\n"
"v_mfma_f32_16x16x32_bf16 v[12:15], v[48:51], a[24:27], v[12:15]\n"
"ds_read_b128 v[40:43], v94 offset:57344\n"
"v_mfma_f32_16x16x32_bf16 v[32:35], v[48:51], a[28:31], v[32:35]\n"
"v_mfma_f32_16x16x32_bf16 v[16:19], v[52:55], a[24:27], v[16:19]\n"
"ds_read_b128 v[44:47], v94 offset:41984\n"
"v_mfma_f32_16x16x32_bf16 v[36:39], v[52:55], a[28:31], v[36:39]\n"
"buffer_load_dwordx4 a[16:19], v251, s[96:99], s72 offen offset:2048 sc1\n"
"buffer_load_dwordx4 a[20:23], v251, s[96:99], s73 offen offset:2048 sc1\n"
"s_waitcnt vmcnt(49) lgkmcnt(4)\n"
"v_mfma_f32_16x16x32_bf16 v[0:3], v[56:59], a[32:35], v[0:3]\n"
"ds_read_b128 v[48:51], v94 offset:46080\n"
"s_waitcnt vmcnt(48)\n"
"v_mfma_f32_16x16x32_bf16 v[20:23], v[56:59], a[36:39], v[20:23]\n"
"v_mfma_f32_16x16x32_bf16 v[96:99], a[216:219], a[216:219], v[96:99]\n"
"v_mfma_f32_16x16x32_bf16 v[4:7], v[60:63], a[32:35], v[4:7]\n"
"ds_read_b128 v[52:55], v94 offset:50176\n"
"v_mfma_f32_16x16x32_bf16 v[24:27], v[60:63], a[36:39], v[24:27]\n"
"v_mfma_f32_16x16x32_bf16 v[96:99], a[220:223], a[220:223], v[96:99]\n"
"s_waitcnt lgkmcnt(4)\n"
"v_mfma_f32_16x16x32_bf16 v[8:11], v[64:67], a[32:35], v[8:11]\n"
"ds_read_b128 v[56:59], v94 offset:54272\n"
"v_mfma_f32_16x16x32_bf16 v[28:31], v[64:67], a[36:39], v[28:31]\n"
"v_mfma_f32_16x16x32_bf16 v[12:15], v[68:71], a[32:35], v[12:15]\n"
"ds_read_b128 v[60:63], v94 offset:58368\n"
"v_mfma_f32_16x16x32_bf16 v[32:35], v[68:71], a[36:39], v[32:35]\n"
"v_mfma_f32_16x16x32_bf16 v[96:99], a[224:227], a[224:227], v[96:99]\n"
"s_waitcnt lgkmcnt(3)\n"
"v_mfma_f32_16x16x32_bf16 v[16:19], v[40:43], a[32:35], v[16:19]\n"
"ds_read_b128 v[64:67], v94 offset:43008\n"
"v_mfma_f32_16x16x32_bf16 v[36:39], v[40:43], a[36:39], v[36:39]\n"
"buffer_load_dwordx4 a[24:27], v251, s[96:99], s72 offen offset:3072 sc1\n"
"buffer_load_dwordx4 a[28:31], v251, s[96:99], s73 offen offset:3072 sc1\n"
"s_add_u32 s72, s72, 4096\n"
"s_add_u32 s73, s73, 4096\n"
"s_waitcnt vmcnt(49)\n"
"v_mfma_f32_16x16x32_bf16 v[0:3], v[44:47], a[40:43], v[0:3]\n"
"ds_read_b128 v[68:71], v94 offset:47104\n"
"s_waitcnt vmcnt(48)\n"
"v_mfma_f32_16x16x32_bf16 v[20:23], v[44:47], a[44:47], v[20:23]\n"
"v_mfma_f32_16x16x32_bf16 v[96:99], a[228:231], a[228:231], v[96:99]\n"
"v_mfma_f32_16x16x32_bf16 v[4:7], v[48:51], a[40:43], v[4:7]\n"
"ds_read_b128 v[40:43], v94 offset:51200\n"
"v_mfma_f32_16x16x32_bf16 v[24:27], v[48:51], a[44:47], v[24:27]\n"
"s_waitcnt lgkmcnt(4)\n"
"v_mfma_f32_16x16x32_bf16 v[8:11], v[52:55], a[40:43], v[8:11]\n"
"ds_read_b128 v[44:47], v94 offset:55296\n"
"v_mfma_f32_16x16x32_bf16 v[28:31], v[52:55], a[44:47], v[28:31]\n"
"v_mfma_f32_16x16x32_bf16 v[96:99], a[232:235], a[232:235], v[96:99]\n"
"v_mfma_f32_16x16x32_bf16 v[12:15], v[56:59], a[40:43], v[12:15]\n"
"ds_read_b128 v[48:51], v94 offset:59392\n"
"v_mfma_f32_16x16x32_bf16 v[32:35], v[56:59], a[44:47], v[32:35]\n"
"s_waitcnt lgkmcnt(4)\n"
"v_mfma_f32_16x16x32_bf16 v[16:19], v[60:63], a[40:43], v[16:19]\n"
"ds_read_b128 v[52:55], v94 offset:44032\n"
"v_mfma_f32_16x16x32_bf16 v[36:39], v[60:63], a[44:47], v[36:39]\n"
"buffer_load_dwordx4 a[32:35], v251, s[96:99], s72 offen offset:0 sc1\n"
"buffer_load_dwordx4 a[36:39], v251, s[96:99], s73 offen offset:0 sc1\n"
"s_waitcnt vmcnt(44)\n"
"v_mfma_f32_16x16x32_bf16 v[0:3], v[64:67], a[48:51], v[0:3]\n"
"ds_read_b128 v[56:59], v94 offset:48128\n"
"s_waitcnt vmcnt(43)\n"
"v_mfma_f32_16x16x32_bf16 v[20:23], v[64:67], a[52:55], v[20:23]\n"
"v_mfma_f32_16x16x32_bf16 v[96:99], a[236:239], a[236:239], v[96:99]\n"
"s_waitcnt lgkmcnt(4)\n"
"v_mfma_f32_16x16x32_bf16 v[4:7], v[68:71], a[48:51], v[4:7]\n"
"ds_read_b128 v[60:63], v94 offset:52224\n"
"v_mfma_f32_16x16x32_bf16 v[24:27], v[68:71], a[52:55], v[24:27]\n"
"v_mfma_f32_16x16x32_bf16 v[96:99], a[240:243], a[240:243], v[96:99]\n"
"v_mfma_f32_16x16x32_bf16 v[8:11], v[40:43], a[48:51], v[8:11]\n"
"ds_read_b128 v[64:67], v94 offset:56320\n"
"v_mfma_f32_16x16x32_bf16 v[28:31], v[40:43], a[52:55], v[28:31]\n"
"s_waitcnt lgkmcnt(4)\n"
"v_mfma_f32_16x16x32_bf16 v[12:15], v[44:47], a[48:51], v[12:15]\n"
"ds_read_b128 v[68:71], v94 offset:60416\n"
"v_mfma_f32_16x16x32_bf16 v[32:35], v[44:47], a[52:55], v[32:35]\n"
"s_waitcnt lgkmcnt(0)\n"
"s_barrier\n"
"v_mfma_f32_16x16x32_bf16 v[16:19], v[48:51], a[48:51], v[16:19]\n"
"ds_read_b128 v[40:43], v95 offset:0\n"
"v_mfma_f32_16x16x32_bf16 v[36:39], v[48:51], a[52:55], v[36:39]\n"
"buffer_load_dwordx4 a[40:43], v251, s[96:99], s72 offen offset:1024 sc1\n"
"buffer_load_dwordx4 a[44:47], v251, s[96:99], s73 offen offset:1024 sc1\n"
"s_add_u32 m0, s74, 102400\n"
"s_nop 0\n"
"buffer_load_dwordx4 v251, s[96:99], s70 offen offset:0 sc1 lds\n"
"buffer_load_dwordx4 v251, s[96:99], s70 offen offset:1024 sc1 lds\n"
"buffer_load_dwordx4 v251, s[96:99], s70 offen offset:2048 sc1 lds\n"
"buffer_load_dwordx4 v251, s[96:99], s70 offen offset:3072 sc1 lds\n"
"s_add_u32 m0, s75, 102400\n"
"s_nop 0\n"
"buffer_load_dwordx4 v251, s[96:99], s71 offen sc1 lds\n"
"s_add_u32 s70, s70, 4096\n"
"s_add_u32 s71, s71, 4096\n"
"s_nop 0\n"
"s_waitcnt vmcnt(49)\n"
"v_mfma_f32_16x16x32_bf16 v[0:3], v[52:55], a[56:59], v[0:3]\n"
"ds_read_b128 v[44:47], v95 offset:4096\n"
"s_waitcnt vmcnt(48)\n"
"v_mfma_f32_16x16x32_bf16 v[20:23], v[52:55], a[60:63], v[20:23]\n"
"v_mfma_f32_16x16x32_bf16 v[96:99], a[244:247], a[244:247], v[96:99]\n"
"v_mfma_f32_16x16x32_bf16 v[4:7], v[56:59], a[56:59], v[4:7]\n"
"ds_read_b128 v[48:51], v95 offset:8192\n"
"v_mfma_f32_16x16x32_bf16 v[24:27], v[56:59], a[60:63], v[24:27]\n"
"v_mfma_f32_16x16x32_bf16 v[96:99], a[248:251], a[248:251], v[96:99]\n"
"v_mfma_f32_16x16x32_bf16 v[8:11], v[60:63], a[56:59], v[8:11]\n"
"ds_read_b128 v[52:55], v95 offset:12288\n"
"v_mfma_f32_16x16x32_bf16 v[28:31], v[60:63], a[60:63], v[28:31]\n"
"v_mfma_f32_16x16x32_bf16 v[96:99], a[252:255], a[252:255], v[96:99]\n"
"v_mfma_f32_16x16x32_bf16 v[12:15], v[64:67], a[56:59], v[12:15]\n"
"ds_read_b128 v[56:59], v95 offset:16384\n"
"v_mfma_f32_16x16x32_bf16 v[32:35], v[64:67], a[60:63], v[32:35]\n"
"v_mfma_f32_16x16x32_bf16 v[16:19], v[68:71], a[56:59], v[16:19]\n"
"ds_read_b128 v[60:63], v95 offset:1024\n"
"v_mfma_f32_16x16x32_bf16 v[36:39], v[68:71], a[60:63], v[36:39]\n"
"buffer_load_dwordx4 a[48:51], v251, s[96:99], s72 offen offset:2048 sc1\n"
"buffer_load_dwordx4 a[52:55], v251, s[96:99], s73 offen offset:2048 sc1\n"
"s_waitcnt vmcnt(49) lgkmcnt(4)\n"
"v_mfma_f32_16x16x32_bf16 v[0:3], v[40:43], a[64:67], v[0:3]\n"
"ds_read_b128 v[64:67], v95 offset:5120\n"
"s_waitcnt vmcnt(48)\n"
"v_mfma_f32_16x16x32_bf16 v[20:23], v[40:43], a[68:71], v[20:23]\n"
"v_mfma_f32_16x16x32_bf16 v[4:7], v[44:47], a[64:67], v[4:7]\n"
"ds_read_b128 v[68:71], v95 offset:9216\n"
"v_mfma_f32_16x16x32_bf16 v[24:27], v[44:47], a[68:71], v[24:27]\n"
"s_waitcnt lgkmcnt(4)\n"
"v_mfma_f32_16x16x32_bf16 v[8:11], v[48:51], a[64:67], v[8:11]\n"
"ds_read_b128 v[40:43], v95 offset:13312\n"
"v_mfma_f32_16x16x32_bf16 v[28:31], v[48:51], a[68:71], v[28:31]\n"
"v_mfma_f32_16x16x32_bf16 v[12:15], v[52:55], a[64:67], v[12:15]\n"
"ds_read_b128 v[44:47], v95 offset:17408\n"
"v_mfma_f32_16x16x32_bf16 v[32:35], v[52:55], a[68:71], v[32:35]\n"
"s_waitcnt lgkmcnt(5)\n"
"v_mfma_f32_16x16x32_bf16 v[16:19], v[56:59], a[64:67], v[16:19]\n"
"ds_read_b128 v[48:51], v95 offset:2048\n"
"v_mfma_f32_16x16x32_bf16 v[36:39], v[56:59], a[68:71], v[36:39]\n"
"buffer_load_dwordx4 a[56:59], v251, s[96:99], s72 offen offset:3072 sc1\n"
"buffer_load_dwordx4 a[60:63], v251, s[96:99], s73 offen offset:3072 sc1\n"
"s_add_u32 s72, s72, 4096\n"
"s_add_u32 s73, s73, 4096\n"
"s_waitcnt vmcnt(49) lgkmcnt(5)\n"
"v_mfma_f32_16x16x32_bf16 v[0:3], v[60:63], a[72:75], v[0:3]\n"
"ds_read_b128 v[52:55], v95 offset:6144\n"
"s_waitcnt vmcnt(48)\n"
"v_mfma_f32_16x16x32_bf16 v[20:23], v[60:63], a[76:79], v[20:23]\n"
"s_waitcnt lgkmcnt(5)\n"
"v_mfma_f32_16x16x32_bf16 v[4:7], v[64:67], a[72:75], v[4:7]\n"
"ds_read_b128 v[56:59], v95 offset:10240\n"
"v_mfma_f32_16x16x32_bf16 v[24:27], v[64:67], a[76:79], v[24:27]\n"
"s_waitcnt lgkmcnt(5)\n"
"v_mfma_f32_16x16x32_bf16 v[8:11], v[68:71], a[72:75], v[8:11]\n"
"ds_read_b128 v[60:63], v95 offset:14336\n"
"v_mfma_f32_16x16x32_bf16 v[28:31], v[68:71], a[76:79], v[28:31]\n"
"s_waitcnt lgkmcnt(5)\n"
"v_mfma_f32_16x16x32_bf16 v[12:15], v[40:43], a[72:75], v[12:15]\n"
"ds_read_b128 v[64:67], v95 offset:18432\n"
"v_mfma_f32_16x16x32_bf16 v[32:35], v[40:43], a[76:79], v[32:35]\n"
"s_waitcnt lgkmcnt(5)\n"
"v_mfma_f32_16x16x32_bf16 v[16:19], v[44:47], a[72:75], v[16:19]\n"
"ds_read_b128 v[68:71], v95 offset:3072\n"
"v_mfma_f32_16x16x32_bf16 v[36:39], v[44:47], a[76:79], v[36:39]\n"
"buffer_load_dwordx4 a[64:67], v251, s[96:99], s72 offen offset:0 sc1\n"
"buffer_load_dwordx4 a[68:71], v251, s[96:99], s73 offen offset:0 sc1\n"
"s_waitcnt vmcnt(44) lgkmcnt(5)\n"
"v_mfma_f32_16x16x32_bf16 v[0:3], v[48:51], a[80:83], v[0:3]\n"
"ds_read_b128 v[40:43], v95 offset:7168\n"
"s_waitcnt vmcnt(43)\n"
"v_mfma_f32_16x16x32_bf16 v[20:23], v[48:51], a[84:87], v[20:23]\n"
"s_waitcnt lgkmcnt(5)\n"
"v_mfma_f32_16x16x32_bf16 v[4:7], v[52:55], a[80:83], v[4:7]\n"
"ds_read_b128 v[44:47], v95 offset:11264\n"
"v_mfma_f32_16x16x32_bf16 v[24:27], v[52:55], a[84:87], v[24:27]\n"
"s_waitcnt lgkmcnt(5)\n"
"v_mfma_f32_16x16x32_bf16 v[8:11], v[56:59], a[80:83], v[8:11]\n"
"ds_read_b128 v[48:51], v95 offset:15360\n"
"v_mfma_f32_16x16x32_bf16 v[28:31], v[56:59], a[84:87], v[28:31]\n"
"s_waitcnt lgkmcnt(5)\n"
"v_mfma_f32_16x16x32_bf16 v[12:15], v[60:63], a[80:83], v[12:15]\n"
"ds_read_b128 v[52:55], v95 offset:19456\n"
"v_mfma_f32_16x16x32_bf16 v[32:35], v[60:63], a[84:87], v[32:35]\n"
"s_waitcnt lgkmcnt(0)\n"
"s_barrier\n"
"v_mfma_f32_16x16x32_bf16 v[16:19], v[64:67], a[80:83], v[16:19]\n"
"ds_read_b128 v[56:59], v95 offset:20480\n"
"v_mfma_f32_16x16x32_bf16 v[36:39], v[64:67], a[84:87], v[36:39]\n"
"buffer_load_dwordx4 a[72:75], v251, s[96:99], s72 offen offset:1024 sc1\n"
"buffer_load_dwordx4 a[76:79], v251, s[96:99], s73 offen offset:1024 sc1\n"
"s_add_u32 m0, s74, 122880\n"
"s_nop 0\n"
"buffer_load_dwordx4 v251, s[96:99], s70 offen offset:0 sc1 lds\n"
"buffer_load_dwordx4 v251, s[96:99], s70 offen offset:1024 sc1 lds\n"
"buffer_load_dwordx4 v251, s[96:99], s70 offen offset:2048 sc1 lds\n"
"buffer_load_dwordx4 v251, s[96:99], s70 offen offset:3072 sc1 lds\n"
"s_add_u32 m0, s75, 122880\n"
"s_nop 0\n"
"buffer_load_dwordx4 v251, s[96:99], s71 offen sc1 lds\n"
"s_add_u32 s70, s70, 4096\n"
"s_add_u32 s71, s71, 4096\n"
"s_nop 0\n"
"s_waitcnt vmcnt(49)\n"
"v_mfma_f32_16x16x32_bf16 v[0:3], v[68:71], a[88:91], v[0:3]\n"
"ds_read_b128 v[60:63], v95 offset:24576\n"
"s_waitcnt vmcnt(48)\n"
"v_mfma_f32_16x16x32_bf16 v[20:23], v[68:71], a[92:95], v[20:23]\n"
"v_mfma_f32_16x16x32_bf16 v[4:7], v[40:43], a[88:91], v[4:7]\n"
"ds_read_b128 v[64:67], v95 offset:28672\n"
"v_mfma_f32_16x16x32_bf16 v[24:27], v[40:43], a[92:95], v[24:27]\n"
"v_mfma_f32_16x16x32_bf16 v[8:11], v[44:47], a[88:91], v[8:11]\n"
"ds_read_b128 v[68:71], v95 offset:32768\n"
"v_mfma_f32_16x16x32_bf16 v[28:31], v[44:47], a[92:95], v[28:31]\n"
"v_mfma_f32_16x16x32_bf16 v[12:15], v[48:51], a[88:91], v[12:15]\n"
"ds_read_b128 v[40:43], v95 offset:36864\n"
"v_mfma_f32_16x16x32_bf16 v[32:35], v[48:51], a[92:95], v[32:35]\n"
"v_mfma_f32_16x16x32_bf16 v[16:19], v[52:55], a[88:91], v[16:19]\n"
"ds_read_b128 v[44:47], v95 offset:21504\n"
"v_mfma_f32_16x16x32_bf16 v[36:39], v[52:55], a[92:95], v[36:39]\n"
"buffer_load_dwordx4 a[80:83], v251, s[96:99], s72 offen offset:2048 sc1\n"
"buffer_load_dwordx4 a[84:87], v251, s[96:99], s73 offen offset:2048 sc1\n"
"s_waitcnt vmcnt(49) lgkmcnt(5)\n"
"v_mfma_f32_16x16x32_bf16 v[0:3], v[56:59], a[96:99], v[0:3]\n"
"ds_read_b128 v[48:51], v95 offset:25600\n"
"s_waitcnt vmcnt(48)\n"
"v_mfma_f32_16x16x32_bf16 v[20:23], v[56:59], a[100:103], v[20:23]\n"
"s_waitcnt lgkmcnt(5)\n"
"v_mfma_f32_16x16x32_bf16 v[4:7], v[60:63], a[96:99], v[4:7]\n"
"ds_read_b128 v[52:55], v95 offset:29696\n"
"v_mfma_f32_16x16x32_bf16 v[24:27], v[60:63], a[100:103], v[24:27]\n"
"s_waitcnt lgkmcnt(5)\n"
"v_mfma_f32_16x16x32_bf16 v[8:11], v[64:67], a[96:99], v[8:11]\n"
"ds_read_b128 v[56:59], v95 offset:33792\n"
"v_mfma_f32_16x16x32_bf16 v[28:31], v[64:67], a[100:103], v[28:31]\n"
"s_waitcnt lgkmcnt(5)\n"
"v_mfma_f32_16x16x32_bf16 v[12:15], v[68:71], a[96:99], v[12:15]\n"
"ds_read_b128 v[60:63], v95 offset:37888\n"
"v_mfma_f32_16x16x32_bf16 v[32:35], v[68:71], a[100:103], v[32:35]\n"
"s_waitcnt lgkmcnt(5)\n"
"v_mfma_f32_16x16x32_bf16 v[16:19], v[40:43], a[96:99], v[16:19]\n"
"ds_read_b128 v[64:67], v95 offset:22528\n"
"v_mfma_f32_16x16x32_bf16 v[36:39], v[40:43], a[100:103], v[36:39]\n"
"PG18_%=:\n"
"s_cbranch_execz RPG18_%=\n"
"buffer_load_dwordx4 a[88:91], v251, s[96:99], s72 offen offset:3072 sc1\n"
"buffer_load_dwordx4 a[92:95], v251, s[96:99], s73 offen offset:3072 sc1\n"
"s_add_u32 s72, s72, 4096\n"
"s_add_u32 s73, s73, 4096\n"
"s_waitcnt vmcnt(49) lgkmcnt(5)\n"
"v_mfma_f32_16x16x32_bf16 v[0:3], v[44:47], a[104:107], v[0:3]\n"
"ds_read_b128 v[68:71], v95 offset:26624\n"
"s_waitcnt vmcnt(48)\n"
"v_mfma_f32_16x16x32_bf16 v[20:23], v[44:47], a[108:111], v[20:23]\n"
"s_waitcnt lgkmcnt(5)\n"
"v_mfma_f32_16x16x32_bf16 v[4:7], v[48:51], a[104:107], v[4:7]\n"
"ds_read_b128 v[40:43], v95 offset:30720\n"
"v_mfma_f32_16x16x32_bf16 v[24:27], v[48:51], a[108:111], v[24:27]\n"
"s_waitcnt lgkmcnt(5)\n"
"v_mfma_f32_16x16x32_bf16 v[8:11], v[52:55], a[104:107], v[8:11]\n"
"ds_read_b128 v[44:47], v95 offset:34816\n"
"v_mfma_f32_16x16x32_bf16 v[28:31], v[52:55], a[108:111], v[28:31]\n"
"s_waitcnt lgkmcnt(5)\n"
"v_mfma_f32_16x16x32_bf16 v[12:15], v[56:59], a[104:107], v[12:15]\n"
"ds_read_b128 v[48:51], v95 offset:38912\n"
"v_mfma_f32_16x16x32_bf16 v[32:35], v[56:59], a[108:111], v[32:35]\n"
"s_waitcnt lgkmcnt(5)\n"
"v_mfma_f32_16x16x32_bf16 v[16:19], v[60:63], a[104:107], v[16:19]\n"
"ds_read_b128 v[52:55], v95 offset:23552\n"
"v_mfma_f32_16x16x32_bf16 v[36:39], v[60:63], a[108:111], v[36:39]\n"
"buffer_load_dwordx4 a[96:99], v251, s[96:99], s72 offen offset:0 sc1\n"
"buffer_load_dwordx4 a[100:103], v251, s[96:99], s73 offen offset:0 sc1\n"
"s_waitcnt vmcnt(44) lgkmcnt(5)\n"
"v_mfma_f32_16x16x32_bf16 v[0:3], v[64:67], a[112:115], v[0:3]\n"
"ds_read_b128 v[56:59], v95 offset:27648\n"
"s_waitcnt vmcnt(43)\n"
"v_mfma_f32_16x16x32_bf16 v[20:23], v[64:67], a[116:119], v[20:23]\n"
"s_waitcnt lgkmcnt(5)\n"
"v_mfma_f32_16x16x32_bf16 v[4:7], v[68:71], a[112:115], v[4:7]\n"
"ds_read_b128 v[60:63], v95 offset:31744\n"
"v_mfma_f32_16x16x32_bf16 v[24:27], v[68:71], a[116:119], v[24:27]\n"
"s_waitcnt lgkmcnt(5)\n"
"v_mfma_f32_16x16x32_bf16 v[8:11], v[40:43], a[112:115], v[8:11]\n"
"ds_read_b128 v[64:67], v95 offset:35840\n"
"v_mfma_f32_16x16x32_bf16 v[28:31], v[40:43], a[116:119], v[28:31]\n"
"s_waitcnt lgkmcnt(5)\n"
"v_mfma_f32_16x16x32_bf16 v[12:15], v[44:47], a[112:115], v[12:15]\n"
"ds_read_b128 v[68:71], v95 offset:39936\n"
"v_mfma_f32_16x16x32_bf16 v[32:35], v[44:47], a[116:119], v[32:35]\n"
"s_waitcnt lgkmcnt(0)\n"
"s_barrier\n"
"v_mfma_f32_16x16x32_bf16 v[16:19], v[48:51], a[112:115], v[16:19]\n"
"ds_read_b128 v[40:43], v93 offset:0\n"
"v_mfma_f32_16x16x32_bf16 v[36:39], v[48:51], a[116:119], v[36:39]\n"
"buffer_load_dwordx4 a[104:107], v251, s[96:99], s72 offen offset:1024 sc1\n"
"buffer_load_dwordx4 a[108:111], v251, s[96:99], s73 offen offset:1024 sc1\n"
"s_add_u32 m0, s74, 143360\n"
"s_nop 0\n"
"buffer_load_dwordx4 v251, s[96:99], s70 offen offset:0 sc1 lds\n"
"buffer_load_dwordx4 v251, s[96:99], s70 offen offset:1024 sc1 lds\n"
"buffer_load_dwordx4 v251, s[96:99], s70 offen offset:2048 sc1 lds\n"
"buffer_load_dwordx4 v251, s[96:99], s70 offen offset:3072 sc1 lds\n"
"s_add_u32 m0, s75, 143360\n"
"s_nop 0\n"
"buffer_load_dwordx4 v251, s[96:99], s71 offen sc1 lds\n"
"s_add_u32 s70, s70, 4096\n"
"s_add_u32 s71, s71, 4096\n"
"s_nop 0\n"
"s_waitcnt vmcnt(49)\n"
"v_mfma_f32_16x16x32_bf16 v[0:3], v[52:55], a[120:123], v[0:3]\n"
"ds_read_b128 v[44:47], v93 offset:4096\n"
"s_waitcnt vmcnt(48)\n"
"v_mfma_f32_16x16x32_bf16 v[20:23], v[52:55], a[124:127], v[20:23]\n"
"v_mfma_f32_16x16x32_bf16 v[4:7], v[56:59], a[120:123], v[4:7]\n"
"ds_read_b128 v[48:51], v93 offset:8192\n"
"v_mfma_f32_16x16x32_bf16 v[24:27], v[56:59], a[124:127], v[24:27]\n"
"v_mfma_f32_16x16x32_bf16 v[8:11], v[60:63], a[120:123], v[8:11]\n"
"ds_read_b128 v[52:55], v93 offset:12288\n"
"v_mfma_f32_16x16x32_bf16 v[28:31], v[60:63], a[124:127], v[28:31]\n"
"v_mfma_f32_16x16x32_bf16 v[12:15], v[64:67], a[120:123], v[12:15]\n"
"ds_read_b128 v[56:59], v93 offset:16384\n"
"v_mfma_f32_16x16x32_bf16 v[32:35], v[64:67], a[124:127], v[32:35]\n"
"v_mfma_f32_16x16x32_bf16 v[16:19], v[68:71], a[120:123], v[16:19]\n"
"ds_read_b128 v[60:63], v93 offset:1024\n"
"v_mfma_f32_16x16x32_bf16 v[36:39], v[68:71], a[124:127], v[36:39]\n"
"buffer_load_dwordx4 a[112:115], v251, s[96:99], s72 offen offset:2048 sc1\n"
"buffer_load_dwordx4 a[116:119], v251, s[96:99], s73 offen offset:2048 sc1\n"
"s_waitcnt vmcnt(49) lgkmcnt(5)\n"
"v_mfma_f32_16x16x32_bf16 v[0:3], v[40:43], a[0:3], v[0:3]\n"
"ds_read_b128 v[64:67], v93 offset:5120\n"
"s_waitcnt vmcnt(48)\n"
"v_mfma_f32_16x16x32_bf16 v[20:23], v[40:43], a[4:7], v[20:23]\n"
"s_waitcnt lgkmcnt(5)\n"
"v_mfma_f32_16x16x32_bf16 v[4:7], v[44:47], a[0:3], v[4:7]\n"
"ds_read_b128 v[68:71], v93 offset:9216\n"
"v_mfma_f32_16x16x32_bf16 v[24:27], v[44:47], a[4:7], v[24:27]\n"
"s_waitcnt lgkmcnt(5)\n"
"v_mfma_f32_16x16x32_bf16 v[8:11], v[48:51], a[0:3], v[8:11]\n"
"ds_read_b128 v[40:43], v93 offset:13312\n"
"v_mfma_f32_16x16x32_bf16 v[28:31], v[48:51], a[4:7], v[28:31]\n"
"s_waitcnt lgkmcnt(5)\n"
"v_mfma_f32_16x16x32_bf16 v[12:15], v[52:55], a[0:3], v[12:15]\n"
"ds_read_b128 v[44:47], v93 offset:17408\n"
"v_mfma_f32_16x16x32_bf16 v[32:35], v[52:55], a[4:7], v[32:35]\n"
"s_waitcnt lgkmcnt(5)\n"
"v_mfma_f32_16x16x32_bf16 v[16:19], v[56:59], a[0:3], v[16:19]\n"
"ds_read_b128 v[48:51], v93 offset:2048\n"
"v_mfma_f32_16x16x32_bf16 v[36:39], v[56:59], a[4:7], v[36:39]\n"
"buffer_load_dwordx4 a[120:123], v251, s[96:99], s72 offen offset:3072 sc1\n"
"buffer_load_dwordx4 a[124:127], v251, s[96:99], s73 offen offset:3072 sc1\n"
"s_add_u32 s72, s72, 4096\n"
"s_add_u32 s73, s73, 4096\n"
"s_waitcnt vmcnt(49) lgkmcnt(5)\n"
"v_mfma_f32_16x16x32_bf16 v[0:3], v[60:63], a[8:11], v[0:3]\n"
"ds_read_b128 v[52:55], v93 offset:6144\n"
"s_waitcnt vmcnt(48)\n"
"v_mfma_f32_16x16x32_bf16 v[20:23], v[60:63], a[12:15], v[20:23]\n"
"s_waitcnt lgkmcnt(5)\n"
"v_mfma_f32_16x16x32_bf16 v[4:7], v[64:67], a[8:11], v[4:7]\n"
"ds_read_b128 v[56:59], v93 offset:10240\n"
"v_mfma_f32_16x16x32_bf16 v[24:27], v[64:67], a[12:15], v[24:27]\n"
"s_waitcnt lgkmcnt(5)\n"
"v_mfma_f32_16x16x32_bf16 v[8:11], v[68:71], a[8:11], v[8:11]\n"
"ds_read_b128 v[60:63], v93 offset:14336\n"
"v_mfma_f32_16x16x32_bf16 v[28:31], v[68:71], a[12:15], v[28:31]\n"
"s_waitcnt lgkmcnt(5)\n"
"v_mfma_f32_16x16x32_bf16 v[12:15], v[40:43], a[8:11], v[12:15]\n"
"ds_read_b128 v[64:67], v93 offset:18432\n"
"v_mfma_f32_16x16x32_bf16 v[32:35], v[40:43], a[12:15], v[32:35]\n"
"s_waitcnt lgkmcnt(5)\n"
"v_mfma_f32_16x16x32_bf16 v[16:19], v[44:47], a[8:11], v[16:19]\n"
"ds_read_b128 v[68:71], v93 offset:3072\n"
"v_mfma_f32_16x16x32_bf16 v[36:39], v[44:47], a[12:15], v[36:39]\n"
"buffer_load_dwordx4 a[0:3], v251, s[96:99], s72 offen offset:0 sc1\n"
"buffer_load_dwordx4 a[4:7], v251, s[96:99], s73 offen offset:0 sc1\n"
"s_waitcnt vmcnt(44) lgkmcnt(5)\n"
"v_mfma_f32_16x16x32_bf16 v[0:3], v[48:51], a[16:19], v[0:3]\n"
"ds_read_b128 v[40:43], v93 offset:7168\n"
"s_waitcnt vmcnt(43)\n"
"v_mfma_f32_16x16x32_bf16 v[20:23], v[48:51], a[20:23], v[20:23]\n"
"s_waitcnt lgkmcnt(5)\n"
"v_mfma_f32_16x16x32_bf16 v[4:7], v[52:55], a[16:19], v[4:7]\n"
"ds_read_b128 v[44:47], v93 offset:11264\n"
"v_mfma_f32_16x16x32_bf16 v[24:27], v[52:55], a[20:23], v[24:27]\n"
"s_waitcnt lgkmcnt(5)\n"
"v_mfma_f32_16x16x32_bf16 v[8:11], v[56:59], a[16:19], v[8:11]\n"
"ds_read_b128 v[48:51], v93 offset:15360\n"
"v_mfma_f32_16x16x32_bf16 v[28:31], v[56:59], a[20:23], v[28:31]\n"
"s_waitcnt lgkmcnt(5)\n"
"v_mfma_f32_16x16x32_bf16 v[12:15], v[60:63], a[16:19], v[12:15]\n"
"ds_read_b128 v[52:55], v93 offset:19456\n"
"v_mfma_f32_16x16x32_bf16 v[32:35], v[60:63], a[20:23], v[32:35]\n"
"s_waitcnt lgkmcnt(0)\n"
"s_barrier\n"
"v_mfma_f32_16x16x32_bf16 v[16:19], v[64:67], a[16:19], v[16:19]\n"
"ds_read_b128 v[56:59], v93 offset:20480\n"
"v_mfma_f32_16x16x32_bf16 v[36:39], v[64:67], a[20:23], v[36:39]\n"
"buffer_load_dwordx4 a[8:11], v251, s[96:99], s72 offen offset:1024 sc1\n"
"buffer_load_dwordx4 a[12:15], v251, s[96:99], s73 offen offset:1024 sc1\n"
"s_waitcnt vmcnt(44)\n"
"v_mfma_f32_16x16x32_bf16 v[0:3], v[68:71], a[24:27], v[0:3]\n"
"ds_read_b128 v[60:63], v93 offset:24576\n"
"s_waitcnt vmcnt(43)\n"
"v_mfma_f32_16x16x32_bf16 v[20:23], v[68:71], a[28:31], v[20:23]\n"
"v_mfma_f32_16x16x32_bf16 v[4:7], v[40:43], a[24:27], v[4:7]\n"
"ds_read_b128 v[64:67], v93 offset:28672\n"
"v_mfma_f32_16x16x32_bf16 v[24:27], v[40:43], a[28:31], v[24:27]\n"
"v_mfma_f32_16x16x32_bf16 v[8:11], v[44:47], a[24:27], v[8:11]\n"
"ds_read_b128 v[68:71], v93 offset:32768\n"
"v_mfma_f32_16x16x32_bf16 v[28:31], v[44:47], a[28:31], v[28:31]\n"
"v_mfma_f32_16x16x32_bf16 v[12:15], v[48:51], a[24:27], v[12:15]\n"
"ds_read_b128 v[40:43], v93 offset:36864\n"
"v_mfma_f32_16x16x32_bf16 v[32:35], v[48:51], a[28:31], v[32:35]\n"
"v_mfma_f32_16x16x32_bf16 v[16:19], v[52:55], a[24:27], v[16:19]\n"
"ds_read_b128 v[44:47], v93 offset:21504\n"
"v_mfma_f32_16x16x32_bf16 v[36:39], v[52:55], a[28:31], v[36:39]\n"
"buffer_load_dwordx4 a[16:19], v251, s[96:99], s72 offen offset:2048 sc1\n"
"buffer_load_dwordx4 a[20:23], v251, s[96:99], s73 offen offset:2048 sc1\n"
"s_waitcnt vmcnt(44) lgkmcnt(5)\n"
"v_mfma_f32_16x16x32_bf16 v[0:3], v[56:59], a[32:35], v[0:3]\n"
"ds_read_b128 v[48:51], v93 offset:25600\n"
"s_waitcnt vmcnt(43)\n"
"v_mfma_f32_16x16x32_bf16 v[20:23], v[56:59], a[36:39], v[20:23]\n"
"s_waitcnt lgkmcnt(5)\n"
"v_mfma_f32_16x16x32_bf16 v[4:7], v[60:63], a[32:35], v[4:7]\n"
"ds_read_b128 v[52:55], v93 offset:29696\n"
"v_mfma_f32_16x16x32_bf16 v[24:27], v[60:63], a[36:39], v[24:27]\n"
"s_waitcnt lgkmcnt(5)\n"
"v_mfma_f32_16x16x32_bf16 v[8:11], v[64:67], a[32:35], v[8:11]\n"
"ds_read_b128 v[56:59], v93 offset:33792\n"
"v_mfma_f32_16x16x32_bf16 v[28:31], v[64:67], a[36:39], v[28:31]\n"
"s_waitcnt lgkmcnt(5)\n"
"v_mfma_f32_16x16x32_bf16 v[12:15], v[68:71], a[32:35], v[12:15]\n"
"ds_read_b128 v[60:63], v93 offset:37888\n"
"v_mfma_f32_16x16x32_bf16 v[32:35], v[68:71], a[36:39], v[32:35]\n"
"s_waitcnt lgkmcnt(5)\n"
"v_mfma_f32_16x16x32_bf16 v[16:19], v[40:43], a[32:35], v[16:19]\n"
"ds_read_b128 v[64:67], v93 offset:22528\n"
"v_mfma_f32_16x16x32_bf16 v[36:39], v[40:43], a[36:39], v[36:39]\n"
"buffer_load_dwordx4 a[24:27], v251, s[96:99], s72 offen offset:3072 sc1\n"
"buffer_load_dwordx4 a[28:31], v251, s[96:99], s73 offen offset:3072 sc1\n"
"s_add_u32 s72, s72, 4096\n"
"s_add_u32 s73, s73, 4096\n"
"s_waitcnt vmcnt(44) lgkmcnt(5)\n"
"v_mfma_f32_16x16x32_bf16 v[0:3], v[44:47], a[40:43], v[0:3]\n"
"ds_read_b128 v[68:71], v93 offset:26624\n"
"s_waitcnt vmcnt(43)\n"
"v_mfma_f32_16x16x32_bf16 v[20:23], v[44:47], a[44:47], v[20:23]\n"
"s_waitcnt lgkmcnt(5)\n"
"v_mfma_f32_16x16x32_bf16 v[4:7], v[48:51], a[40:43], v[4:7]\n"
"ds_read_b128 v[40:43], v93 offset:30720\n"
"v_mfma_f32_16x16x32_bf16 v[24:27], v[48:51], a[44:47], v[24:27]\n"
"s_waitcnt lgkmcnt(5)\n"
"v_mfma_f32_16x16x32_bf16 v[8:11], v[52:55], a[40:43], v[8:11]\n"
"ds_read_b128 v[44:47], v93 offset:34816\n"
"v_mfma_f32_16x16x32_bf16 v[28:31], v[52:55], a[44:47], v[28:31]\n"
"s_waitcnt lgkmcnt(5)\n"
"v_mfma_f32_16x16x32_bf16 v[12:15], v[56:59], a[40:43], v[12:15]\n"
"ds_read_b128 v[48:51], v93 offset:38912\n"
"v_mfma_f32_16x16x32_bf16 v[32:35], v[56:59], a[44:47], v[32:35]\n"
"s_waitcnt lgkmcnt(5)\n"
"v_mfma_f32_16x16x32_bf16 v[16:19], v[60:63], a[40:43], v[16:19]\n"
"ds_read_b128 v[52:55], v93 offset:23552\n"
"v_mfma_f32_16x16x32_bf16 v[36:39], v[60:63], a[44:47], v[36:39]\n"
"buffer_load_dwordx4 a[32:35], v251, s[96:99], s72 offen offset:0 sc1\n"
"buffer_load_dwordx4 a[36:39], v251, s[96:99], s73 offen offset:0 sc1\n"
"s_waitcnt vmcnt(39) lgkmcnt(5)\n"
"v_mfma_f32_16x16x32_bf16 v[0:3], v[64:67], a[48:51], v[0:3]\n"
"ds_read_b128 v[56:59], v93 offset:27648\n"
"s_waitcnt vmcnt(38)\n"
"v_mfma_f32_16x16x32_bf16 v[20:23], v[64:67], a[52:55], v[20:23]\n"
"s_waitcnt lgkmcnt(5)\n"
"v_mfma_f32_16x16x32_bf16 v[4:7], v[68:71], a[48:51], v[4:7]\n"
"ds_read_b128 v[60:63], v93 offset:31744\n"
"v_mfma_f32_16x16x32_bf16 v[24:27], v[68:71], a[52:55], v[24:27]\n"
"s_waitcnt lgkmcnt(5)\n"
"v_mfma_f32_16x16x32_bf16 v[8:11], v[40:43], a[48:51], v[8:11]\n"
"ds_read_b128 v[64:67], v93 offset:35840\n"
"v_mfma_f32_16x16x32_bf16 v[28:31], v[40:43], a[52:55], v[28:31]\n"
"s_waitcnt lgkmcnt(5)\n"
"v_mfma_f32_16x16x32_bf16 v[12:15], v[44:47], a[48:51], v[12:15]\n"
"ds_read_b128 v[68:71], v93 offset:39936\n"
"v_mfma_f32_16x16x32_bf16 v[32:35], v[44:47], a[52:55], v[32:35]\n"
"s_waitcnt lgkmcnt(0)\n"
"s_barrier\n"
"v_mfma_f32_16x16x32_bf16 v[16:19], v[48:51], a[48:51], v[16:19]\n"
"ds_read_b128 v[40:43], v93 offset:40960\n"
"v_mfma_f32_16x16x32_bf16 v[36:39], v[48:51], a[52:55], v[36:39]\n"
"buffer_load_dwordx4 a[40:43], v251, s[96:99], s72 offen offset:1024 sc1\n"
"buffer_load_dwordx4 a[44:47], v251, s[96:99], s73 offen offset:1024 sc1\n"
"s_waitcnt vmcnt(39)\n"
"v_mfma_f32_16x16x32_bf16 v[0:3], v[52:55], a[56:59], v[0:3]\n"
"ds_read_b128 v[44:47], v93 offset:45056\n"
"s_waitcnt vmcnt(38)\n"
"v_mfma_f32_16x16x32_bf16 v[20:23], v[52:55], a[60:63], v[20:23]\n"
"v_mfma_f32_16x16x32_bf16 v[4:7], v[56:59], a[56:59], v[4:7]\n"
"ds_read_b128 v[48:51], v93 offset:49152\n"
"v_mfma_f32_16x16x32_bf16 v[24:27], v[56:59], a[60:63], v[24:27]\n"
"v_mfma_f32_16x16x32_bf16 v[8:11], v[60:63], a[56:59], v[8:11]\n"
"ds_read_b128 v[52:55], v93 offset:53248\n"
"v_mfma_f32_16x16x32_bf16 v[28:31], v[60:63], a[60:63], v[28:31]\n"
"v_mfma_f32_16x16x32_bf16 v[12:15], v[64:67], a[56:59], v[12:15]\n"
"ds_read_b128 v[56:59], v93 offset:57344\n"
"v_mfma_f32_16x16x32_bf16 v[32:35], v[64:67], a[60:63], v[32:35]\n"
"v_mfma_f32_16x16x32_bf16 v[16:19], v[68:71], a[56:59], v[16:19]\n"
"ds_read_b128 v[60:63], v93 offset:41984\n"
"v_mfma_f32_16x16x32_bf16 v[36:39], v[68:71], a[60:63], v[36:39]\n"
"buffer_load_dwordx4 a[48:51], v251, s[96:99], s72 offen offset:2048 sc1\n"
"buffer_load_dwordx4 a[52:55], v251, s[96:99], s73 offen offset:2048 sc1\n"
"s_waitcnt vmcnt(39) lgkmcnt(5)\n"
"v_mfma_f32_16x16x32_bf16 v[0:3], v[40:43], a[64:67], v[0:3]\n"
"ds_read_b128 v[64:67], v93 offset:46080\n"
"s_waitcnt vmcnt(38)\n"
"v_mfma_f32_16x16x32_bf16 v[20:23], v[40:43], a[68:71], v[20:23]\n"
"s_waitcnt lgkmcnt(5)\n"
"v_mfma_f32_16x16x32_bf16 v[4:7], v[44:47], a[64:67], v[4:7]\n"
"ds_read_b128 v[68:71], v93 offset:50176\n"
"v_mfma_f32_16x16x32_bf16 v[24:27], v[44:47], a[68:71], v[24:27]\n"
"s_waitcnt lgkmcnt(5)\n"
"v_mfma_f32_16x16x32_bf16 v[8:11], v[48:51], a[64:67], v[8:11]\n"
"ds_read_b128 v[40:43], v93 offset:54272\n"
"v_mfma_f32_16x16x32_bf16 v[28:31], v[48:51], a[68:71], v[28:31]\n"
"s_waitcnt lgkmcnt(5)\n"
"v_mfma_f32_16x16x32_bf16 v[12:15], v[52:55], a[64:67], v[12:15]\n"
"ds_read_b128 v[44:47], v93 offset:58368\n"
"v_mfma_f32_16x16x32_bf16 v[32:35], v[52:55], a[68:71], v[32:35]\n"
"s_waitcnt lgkmcnt(5)\n"
"v_mfma_f32_16x16x32_bf16 v[16:19], v[56:59], a[64:67], v[16:19]\n"
"ds_read_b128 v[48:51], v93 offset:43008\n"
"v_mfma_f32_16x16x32_bf16 v[36:39], v[56:59], a[68:71], v[36:39]\n"
"buffer_load_dwordx4 a[56:59], v251, s[96:99], s72 offen offset:3072 sc1\n"
"buffer_load_dwordx4 a[60:63], v251, s[96:99], s73 offen offset:3072 sc1\n"
"s_add_u32 s72, s72, 4096\n"
"s_add_u32 s73, s73, 4096\n"
"s_waitcnt vmcnt(39) lgkmcnt(5)\n"
"v_mfma_f32_16x16x32_bf16 v[0:3], v[60:63], a[72:75], v[0:3]\n"
"ds_read_b128 v[52:55], v93 offset:47104\n"
"s_waitcnt vmcnt(38)\n"
"v_mfma_f32_16x16x32_bf16 v[20:23], v[60:63], a[76:79], v[20:23]\n"
"s_waitcnt lgkmcnt(5)\n"
"v_mfma_f32_16x16x32_bf16 v[4:7], v[64:67], a[72:75], v[4:7]\n"
"ds_read_b128 v[56:59], v93 offset:51200\n"
"v_mfma_f32_16x16x32_bf16 v[24:27], v[64:67], a[76:79], v[24:27]\n"
"s_waitcnt lgkmcnt(5)\n"
"v_mfma_f32_16x16x32_bf16 v[8:11], v[68:71], a[72:75], v[8:11]\n"
"ds_read_b128 v[60:63], v93 offset:55296\n"
"v_mfma_f32_16x16x32_bf16 v[28:31], v[68:71], a[76:79], v[28:31]\n"
"s_waitcnt lgkmcnt(5)\n"
"v_mfma_f32_16x16x32_bf16 v[12:15], v[40:43], a[72:75], v[12:15]\n"
"ds_read_b128 v[64:67], v93 offset:59392\n"
"v_mfma_f32_16x16x32_bf16 v[32:35], v[40:43], a[76:79], v[32:35]\n"
"s_waitcnt lgkmcnt(5)\n"
"v_mfma_f32_16x16x32_bf16 v[16:19], v[44:47], a[72:75], v[16:19]\n"
"ds_read_b128 v[68:71], v93 offset:44032\n"
"v_mfma_f32_16x16x32_bf16 v[36:39], v[44:47], a[76:79], v[36:39]\n"
"buffer_load_dwordx4 a[64:67], v251, s[96:99], s72 offen offset:0 sc1\n"
"buffer_load_dwordx4 a[68:71], v251, s[96:99], s73 offen offset:0 sc1\n"
"s_waitcnt vmcnt(34) lgkmcnt(5)\n"
"v_mfma_f32_16x16x32_bf16 v[0:3], v[48:51], a[80:83], v[0:3]\n"
"ds_read_b128 v[40:43], v93 offset:48128\n"
"s_waitcnt vmcnt(33)\n"
"v_mfma_f32_16x16x32_bf16 v[20:23], v[48:51], a[84:87], v[20:23]\n"
"s_waitcnt lgkmcnt(5)\n"
"v_mfma_f32_16x16x32_bf16 v[4:7], v[52:55], a[80:83], v[4:7]\n"
"ds_read_b128 v[44:47], v93 offset:52224\n"
"v_mfma_f32_16x16x32_bf16 v[24:27], v[52:55], a[84:87], v[24:27]\n"
"s_waitcnt lgkmcnt(5)\n"
"v_mfma_f32_16x16x32_bf16 v[8:11], v[56:59], a[80:83], v[8:11]\n"
"ds_read_b128 v[48:51], v93 offset:56320\n"
"v_mfma_f32_16x16x32_bf16 v[28:31], v[56:59], a[84:87], v[28:31]\n"
"s_waitcnt lgkmcnt(5)\n"
"v_mfma_f32_16x16x32_bf16 v[12:15], v[60:63], a[80:83], v[12:15]\n"
"ds_read_b128 v[52:55], v93 offset:60416\n"
"v_mfma_f32_16x16x32_bf16 v[32:35], v[60:63], a[84:87], v[32:35]\n"
"s_waitcnt lgkmcnt(0)\n"
"s_barrier\n"
"v_mfma_f32_16x16x32_bf16 v[16:19], v[64:67], a[80:83], v[16:19]\n"
"ds_read_b128 v[56:59], v94 offset:0\n"
"v_mfma_f32_16x16x32_bf16 v[36:39], v[64:67], a[84:87], v[36:39]\n"
"buffer_load_dwordx4 a[72:75], v251, s[96:99], s72 offen offset:1024 sc1\n"
"buffer_load_dwordx4 a[76:79], v251, s[96:99], s73 offen offset:1024 sc1\n"
"s_waitcnt vmcnt(34)\n"
"v_mfma_f32_16x16x32_bf16 v[0:3], v[68:71], a[88:91], v[0:3]\n"
"ds_read_b128 v[60:63], v94 offset:4096\n"
"s_waitcnt vmcnt(33)\n"
"v_mfma_f32_16x16x32_bf16 v[20:23], v[68:71], a[92:95], v[20:23]\n"
"v_mfma_f32_16x16x32_bf16 v[4:7], v[40:43], a[88:91], v[4:7]\n"
"ds_read_b128 v[64:67], v94 offset:8192\n"
"v_mfma_f32_16x16x32_bf16 v[24:27], v[40:43], a[92:95], v[24:27]\n"
"v_mfma_f32_16x16x32_bf16 v[8:11], v[44:47], a[88:91], v[8:11]\n"
"ds_read_b128 v[68:71], v94 offset:12288\n"
"v_mfma_f32_16x16x32_bf16 v[28:31], v[44:47], a[92:95], v[28:31]\n"
"v_mfma_f32_16x16x32_bf16 v[12:15], v[48:51], a[88:91], v[12:15]\n"
"ds_read_b128 v[40:43], v94 offset:16384\n"
"v_mfma_f32_16x16x32_bf16 v[32:35], v[48:51], a[92:95], v[32:35]\n"
"v_mfma_f32_16x16x32_bf16 v[16:19], v[52:55], a[88:91], v[16:19]\n"
"ds_read_b128 v[44:47], v94 offset:1024\n"
"v_mfma_f32_16x16x32_bf16 v[36:39], v[52:55], a[92:95], v[36:39]\n"
"buffer_load_dwordx4 a[80:83], v251, s[96:99], s72 offen offset:2048 sc1\n"
"buffer_load_dwordx4 a[84:87], v251, s[96:99], s73 offen offset:2048 sc1\n"
"s_waitcnt vmcnt(34) lgkmcnt(5)\n"
"v_mfma_f32_16x16x32_bf16 v[0:3], v[56:59], a[96:99], v[0:3]\n"
"ds_read_b128 v[48:51], v94 offset:5120\n"
"s_waitcnt vmcnt(33)\n"
"v_mfma_f32_16x16x32_bf16 v[20:23], v[56:59], a[100:103], v[20:23]\n"
"s_waitcnt lgkmcnt(5)\n"
"v_mfma_f32_16x16x32_bf16 v[4:7], v[60:63], a[96:99], v[4:7]\n"
"ds_read_b128 v[52:55], v94 offset:9216\n"
"v_mfma_f32_16x16x32_bf16 v[24:27], v[60:63], a[100:103], v[24:27]\n"
"s_waitcnt lgkmcnt(5)\n"
"v_mfma_f32_16x16x32_bf16 v[8:11], v[64:67], a[96:99], v[8:11]\n"
"ds_read_b128 v[56:59], v94 offset:13312\n"
"v_mfma_f32_16x16x32_bf16 v[28:31], v[64:67], a[100:103], v[28:31]\n"
"s_waitcnt lgkmcnt(5)\n"
"v_mfma_f32_16x16x32_bf16 v[12:15], v[68:71], a[96:99], v[12:15]\n"
"ds_read_b128 v[60:63], v94 offset:17408\n"
"v_mfma_f32_16x16x32_bf16 v[32:35], v[68:71], a[100:103], v[32:35]\n"
"s_waitcnt lgkmcnt(5)\n"
"v_mfma_f32_16x16x32_bf16 v[16:19], v[40:43], a[96:99], v[16:19]\n"
"ds_read_b128 v[64:67], v94 offset:2048\n"
"v_mfma_f32_16x16x32_bf16 v[36:39], v[40:43], a[100:103], v[36:39]\n"
"buffer_load_dwordx4 a[88:91], v251, s[96:99], s72 offen offset:3072 sc1\n"
"buffer_load_dwordx4 a[92:95], v251, s[96:99], s73 offen offset:3072 sc1\n"
"s_add_u32 s72, s72, 4096\n"
"s_add_u32 s73, s73, 4096\n"
"s_waitcnt vmcnt(34) lgkmcnt(5)\n"
"v_mfma_f32_16x16x32_bf16 v[0:3], v[44:47], a[104:107], v[0:3]\n"
"ds_read_b128 v[68:71], v94 offset:6144\n"
"s_waitcnt vmcnt(33)\n"
"v_mfma_f32_16x16x32_bf16 v[20:23], v[44:47], a[108:111], v[20:23]\n"
"s_waitcnt lgkmcnt(5)\n"
"v_mfma_f32_16x16x32_bf16 v[4:7], v[48:51], a[104:107], v[4:7]\n"
"ds_read_b128 v[40:43], v94 offset:10240\n"
"v_mfma_f32_16x16x32_bf16 v[24:27], v[48:51], a[108:111], v[24:27]\n"
"s_waitcnt lgkmcnt(5)\n"
"v_mfma_f32_16x16x32_bf16 v[8:11], v[52:55], a[104:107], v[8:11]\n"
"ds_read_b128 v[44:47], v94 offset:14336\n"
"v_mfma_f32_16x16x32_bf16 v[28:31], v[52:55], a[108:111], v[28:31]\n"
"s_waitcnt lgkmcnt(5)\n"
"v_mfma_f32_16x16x32_bf16 v[12:15], v[56:59], a[104:107], v[12:15]\n"
"ds_read_b128 v[48:51], v94 offset:18432\n"
"v_mfma_f32_16x16x32_bf16 v[32:35], v[56:59], a[108:111], v[32:35]\n"
"s_waitcnt lgkmcnt(5)\n"
"v_mfma_f32_16x16x32_bf16 v[16:19], v[60:63], a[104:107], v[16:19]\n"
"ds_read_b128 v[52:55], v94 offset:3072\n"
"v_mfma_f32_16x16x32_bf16 v[36:39], v[60:63], a[108:111], v[36:39]\n"
"buffer_load_dwordx4 a[96:99], v251, s[96:99], s72 offen offset:0 sc1\n"
"buffer_load_dwordx4 a[100:103], v251, s[96:99], s73 offen offset:0 sc1\n"
"s_waitcnt vmcnt(29) lgkmcnt(5)\n"
"v_mfma_f32_16x16x32_bf16 v[0:3], v[64:67], a[112:115], v[0:3]\n"
"ds_read_b128 v[56:59], v94 offset:7168\n"
"s_waitcnt vmcnt(28)\n"
"v_mfma_f32_16x16x32_bf16 v[20:23], v[64:67], a[116:119], v[20:23]\n"
"s_waitcnt lgkmcnt(5)\n"
"v_mfma_f32_16x16x32_bf16 v[4:7], v[68:71], a[112:115], v[4:7]\n"
"ds_read_b128 v[60:63], v94 offset:11264\n"
"v_mfma_f32_16x16x32_bf16 v[24:27], v[68:71], a[116:119], v[24:27]\n"
"s_waitcnt lgkmcnt(5)\n"
"v_mfma_f32_16x16x32_bf16 v[8:11], v[40:43], a[112:115], v[8:11]\n"
"ds_read_b128 v[64:67], v94 offset:15360\n"
"v_mfma_f32_16x16x32_bf16 v[28:31], v[40:43], a[116:119], v[28:31]\n"
"s_waitcnt lgkmcnt(5)\n"
"v_mfma_f32_16x16x32_bf16 v[12:15], v[44:47], a[112:115], v[12:15]\n"
"ds_read_b128 v[68:71], v94 offset:19456\n"
"v_mfma_f32_16x16x32_bf16 v[32:35], v[44:47], a[116:119], v[32:35]\n"
"s_waitcnt lgkmcnt(0)\n"
"s_barrier\n"
"v_mfma_f32_16x16x32_bf16 v[16:19], v[48:51], a[112:115], v[16:19]\n"
"ds_read_b128 v[40:43], v94 offset:20480\n"
"v_mfma_f32_16x16x32_bf16 v[36:39], v[48:51], a[116:119], v[36:39]\n"
"buffer_load_dwordx4 a[104:107], v251, s[96:99], s72 offen offset:1024 sc1\n"
"buffer_load_dwordx4 a[108:111], v251, s[96:99], s73 offen offset:1024 sc1\n"
"s_waitcnt vmcnt(29)\n"
"v_mfma_f32_16x16x32_bf16 v[0:3], v[52:55], a[120:123], v[0:3]\n"
"ds_read_b128 v[44:47], v94 offset:24576\n"
"s_waitcnt vmcnt(28)\n"
"v_mfma_f32_16x16x32_bf16 v[20:23], v[52:55], a[124:127], v[20:23]\n"
"v_mfma_f32_16x16x32_bf16 v[4:7], v[56:59], a[120:123], v[4:7]\n"
"ds_read_b128 v[48:51], v94 offset:28672\n"
"v_mfma_f32_16x16x32_bf16 v[24:27], v[56:59], a[124:127], v[24:27]\n"
"v_mfma_f32_16x16x32_bf16 v[8:11], v[60:63], a[120:123], v[8:11]\n"
"ds_read_b128 v[52:55], v94 offset:32768\n"
"v_mfma_f32_16x16x32_bf16 v[28:31], v[60:63], a[124:127], v[28:31]\n"
"v_mfma_f32_16x16x32_bf16 v[12:15], v[64:67], a[120:123], v[12:15]\n"
"ds_read_b128 v[56:59], v94 offset:36864\n"
"v_mfma_f32_16x16x32_bf16 v[32:35], v[64:67], a[124:127], v[32:35]\n"
"v_mfma_f32_16x16x32_bf16 v[16:19], v[68:71], a[120:123], v[16:19]\n"
"ds_read_b128 v[60:63], v94 offset:21504\n"
"v_mfma_f32_16x16x32_bf16 v[36:39], v[68:71], a[124:127], v[36:39]\n"
"buffer_load_dwordx4 a[112:115], v251, s[96:99], s72 offen offset:2048 sc1\n"
"buffer_load_dwordx4 a[116:119], v251, s[96:99], s73 offen offset:2048 sc1\n"
"s_waitcnt vmcnt(29) lgkmcnt(5)\n"
"v_mfma_f32_16x16x32_bf16 v[0:3], v[40:43], a[0:3], v[0:3]\n"
"ds_read_b128 v[64:67], v94 offset:25600\n"
"s_waitcnt vmcnt(28)\n"
"v_mfma_f32_16x16x32_bf16 v[20:23], v[40:43], a[4:7], v[20:23]\n"
"s_waitcnt lgkmcnt(5)\n"
"v_mfma_f32_16x16x32_bf16 v[4:7], v[44:47], a[0:3], v[4:7]\n"
"ds_read_b128 v[68:71], v94 offset:29696\n"
"v_mfma_f32_16x16x32_bf16 v[24:27], v[44:47], a[4:7], v[24:27]\n"
"s_waitcnt lgkmcnt(5)\n"
"v_mfma_f32_16x16x32_bf16 v[8:11], v[48:51], a[0:3], v[8:11]\n"
"ds_read_b128 v[40:43], v94 offset:33792\n"
"v_mfma_f32_16x16x32_bf16 v[28:31], v[48:51], a[4:7], v[28:31]\n"
"s_waitcnt lgkmcnt(5)\n"
"v_mfma_f32_16x16x32_bf16 v[12:15], v[52:55], a[0:3], v[12:15]\n"
"ds_read_b128 v[44:47], v94 offset:37888\n"
"v_mfma_f32_16x16x32_bf16 v[32:35], v[52:55], a[4:7], v[32:35]\n"
"s_waitcnt lgkmcnt(5)\n"
"v_mfma_f32_16x16x32_bf16 v[16:19], v[56:59], a[0:3], v[16:19]\n"
"ds_read_b128 v[48:51], v94 offset:22528\n"
"v_mfma_f32_16x16x32_bf16 v[36:39], v[56:59], a[4:7], v[36:39]\n"
"buffer_load_dwordx4 a[120:123], v251, s[96:99], s72 offen offset:3072 sc1\n"
"buffer_load_dwordx4 a[124:127], v251, s[96:99], s73 offen offset:3072 sc1\n"
"s_add_u32 s72, s72, 4096\n"
"s_add_u32 s73, s73, 4096\n"
"s_waitcnt vmcnt(29) lgkmcnt(5)\n"
"v_mfma_f32_16x16x32_bf16 v[0:3], v[60:63], a[8:11], v[0:3]\n"
"ds_read_b128 v[52:55], v94 offset:26624\n"
"s_waitcnt vmcnt(28)\n"
"v_mfma_f32_16x16x32_bf16 v[20:23], v[60:63], a[12:15], v[20:23]\n"
"s_waitcnt lgkmcnt(5)\n"
"v_mfma_f32_16x16x32_bf16 v[4:7], v[64:67], a[8:11], v[4:7]\n"
"ds_read_b128 v[56:59], v94 offset:30720\n"
"v_mfma_f32_16x16x32_bf16 v[24:27], v[64:67], a[12:15], v[24:27]\n"
"s_waitcnt lgkmcnt(5)\n"
"v_mfma_f32_16x16x32_bf16 v[8:11], v[68:71], a[8:11], v[8:11]\n"
"ds_read_b128 v[60:63], v94 offset:34816\n"
"v_mfma_f32_16x16x32_bf16 v[28:31], v[68:71], a[12:15], v[28:31]\n"
"s_waitcnt lgkmcnt(5)\n"
"v_mfma_f32_16x16x32_bf16 v[12:15], v[40:43], a[8:11], v[12:15]\n"
"ds_read_b128 v[64:67], v94 offset:38912\n"
"v_mfma_f32_16x16x32_bf16 v[32:35], v[40:43], a[12:15], v[32:35]\n"
"s_waitcnt lgkmcnt(5)\n"
"v_mfma_f32_16x16x32_bf16 v[16:19], v[44:47], a[8:11], v[16:19]\n"
"ds_read_b128 v[68:71], v94 offset:23552\n"
"v_mfma_f32_16x16x32_bf16 v[36:39], v[44:47], a[12:15], v[36:39]\n"
"s_waitcnt vmcnt(27) lgkmcnt(5)\n"
"v_mfma_f32_16x16x32_bf16 v[0:3], v[48:51], a[16:19], v[0:3]\n"
"ds_read_b128 v[40:43], v94 offset:27648\n"
"s_waitcnt vmcnt(26)\n"
"v_mfma_f32_16x16x32_bf16 v[20:23], v[48:51], a[20:23], v[20:23]\n"
"s_waitcnt lgkmcnt(5)\n"
"v_mfma_f32_16x16x32_bf16 v[4:7], v[52:55], a[16:19], v[4:7]\n"
"ds_read_b128 v[44:47], v94 offset:31744\n"
"v_mfma_f32_16x16x32_bf16 v[24:27], v[52:55], a[20:23], v[24:27]\n"
"s_waitcnt lgkmcnt(5)\n"
"v_mfma_f32_16x16x32_bf16 v[8:11], v[56:59], a[16:19], v[8:11]\n"
"ds_read_b128 v[48:51], v94 offset:35840\n"
"v_mfma_f32_16x16x32_bf16 v[28:31], v[56:59], a[20:23], v[28:31]\n"
"s_waitcnt lgkmcnt(5)\n"
"v_mfma_f32_16x16x32_bf16 v[12:15], v[60:63], a[16:19], v[12:15]\n"
"ds_read_b128 v[52:55], v94 offset:39936\n"
"v_mfma_f32_16x16x32_bf16 v[32:35], v[60:63], a[20:23], v[32:35]\n"
"s_waitcnt lgkmcnt(0)\n"
"s_barrier\n"
"v_mfma_f32_16x16x32_bf16 v[16:19], v[64:67], a[16:19], v[16:19]\n"
"ds_read_b128 v[56:59], v94 offset:40960\n"
"v_mfma_f32_16x16x32_bf16 v[36:39], v[64:67], a[20:23], v[36:39]\n"
"s_waitcnt vmcnt(25)\n"
"v_mfma_f32_16x16x32_bf16 v[0:3], v[68:71], a[24:27], v[0:3]\n"
"ds_read_b128 v[60:63], v94 offset:45056\n"
"s_waitcnt vmcnt(24)\n"
"v_mfma_f32_16x16x32_bf16 v[20:23], v[68:71], a[28:31], v[20:23]\n"
"v_mfma_f32_16x16x32_bf16 v[4:7], v[40:43], a[24:27], v[4:7]\n"
"ds_read_b128 v[64:67], v94 offset:49152\n"
"v_mfma_f32_16x16x32_bf16 v[24:27], v[40:43], a[28:31], v[24:27]\n"
"v_mfma_f32_16x16x32_bf16 v[8:11], v[44:47], a[24:27], v[8:11]\n"
"ds_read_b128 v[68:71], v94 offset:53248\n"
"v_mfma_f32_16x16x32_bf16 v[28:31], v[44:47], a[28:31], v[28:31]\n"
"v_mfma_f32_16x16x32_bf16 v[12:15], v[48:51], a[24:27], v[12:15]\n"
"ds_read_b128 v[40:43], v94 offset:57344\n"
"v_mfma_f32_16x16x32_bf16 v[32:35], v[48:51], a[28:31], v[32:35]\n"
"v_mfma_f32_16x16x32_bf16 v[16:19], v[52:55], a[24:27], v[16:19]\n"
"ds_read_b128 v[44:47], v94 offset:41984\n"
"v_mfma_f32_16x16x32_bf16 v[36:39], v[52:55], a[28:31], v[36:39]\n"
"s_waitcnt vmcnt(23) lgkmcnt(5)\n"
"v_mfma_f32_16x16x32_bf16 v[0:3], v[56:59], a[32:35], v[0:3]\n"
"ds_read_b128 v[48:51], v94 offset:46080\n"
"s_mov_b32 s60, 105914368\n"
"s_waitcnt vmcnt(22)\n"
"v_mfma_f32_16x16x32_bf16 v[20:23], v[56:59], a[36:39], v[20:23]\n"
"buffer_load_dwordx4 v[100:103], v251, s[96:99], s60 offen sc1\n"
"s_waitcnt lgkmcnt(5)\n"
"v_mfma_f32_16x16x32_bf16 v[4:7], v[60:63], a[32:35], v[4:7]\n"
"ds_read_b128 v[52:55], v94 offset:50176\n"
"v_mfma_f32_16x16x32_bf16 v[24:27], v[60:63], a[36:39], v[24:27]\n"
"buffer_load_dwordx4 v[104:107], v251, s[96:99], s60 offen offset:1024 sc1\n"
"s_waitcnt lgkmcnt(5)\n"
"v_mfma_f32_16x16x32_bf16 v[8:11], v[64:67], a[32:35], v[8:11]\n"
"ds_read_b128 v[56:59], v94 offset:54272\n"
"v_mfma_f32_16x16x32_bf16 v[28:31], v[64:67], a[36:39], v[28:31]\n"
"s_waitcnt lgkmcnt(5)\n"
"v_mfma_f32_16x16x32_bf16 v[12:15], v[68:71], a[32:35], v[12:15]\n"
"ds_read_b128 v[60:63], v94 offset:58368\n"
"v_mfma_f32_16x16x32_bf16 v[32:35], v[68:71], a[36:39], v[32:35]\n"
"s_waitcnt lgkmcnt(5)\n"
"v_mfma_f32_16x16x32_bf16 v[16:19], v[40:43], a[32:35], v[16:19]\n"
"ds_read_b128 v[64:67], v94 offset:43008\n"
"v_mfma_f32_16x16x32_bf16 v[36:39], v[40:43], a[36:39], v[36:39]\n"
"s_waitcnt vmcnt(23) lgkmcnt(5)\n"
"v_mfma_f32_16x16x32_bf16 v[0:3], v[44:47], a[40:43], v[0:3]\n"
"ds_read_b128 v[68:71], v94 offset:47104\n"
"s_waitcnt vmcnt(22)\n"
"v_mfma_f32_16x16x32_bf16 v[20:23], v[44:47], a[44:47], v[20:23]\n"
"s_waitcnt lgkmcnt(5)\n"
"v_mfma_f32_16x16x32_bf16 v[4:7], v[48:51], a[40:43], v[4:7]\n"
"ds_read_b128 v[40:43], v94 offset:51200\n"
"v_mfma_f32_16x16x32_bf16 v[24:27], v[48:51], a[44:47], v[24:27]\n"
"s_waitcnt lgkmcnt(5)\n"
"v_mfma_f32_16x16x32_bf16 v[8:11], v[52:55], a[40:43], v[8:11]\n"
"ds_read_b128 v[44:47], v94 offset:55296\n"
"v_mfma_f32_16x16x32_bf16 v[28:31], v[52:55], a[44:47], v[28:31]\n"
"s_waitcnt lgkmcnt(5)\n"
"v_mfma_f32_16x16x32_bf16 v[12:15], v[56:59], a[40:43], v[12:15]\n"
"ds_read_b128 v[48:51], v94 offset:59392\n"
"v_mfma_f32_16x16x32_bf16 v[32:35], v[56:59], a[44:47], v[32:35]\n"
"s_waitcnt lgkmcnt(5)\n"
"v_mfma_f32_16x16x32_bf16 v[16:19], v[60:63], a[40:43], v[16:19]\n"
"ds_read_b128 v[52:55], v94 offset:44032\n"
"v_mfma_f32_16x16x32_bf16 v[36:39], v[60:63], a[44:47], v[36:39]\n"
"s_waitcnt vmcnt(21) lgkmcnt(5)\n"
"v_mfma_f32_16x16x32_bf16 v[0:3], v[64:67], a[48:51], v[0:3]\n"
"ds_read_b128 v[56:59], v94 offset:48128\n"
"s_waitcnt vmcnt(20)\n"
"PG19_%=:\n"
"s_cbranch_execz RPG19_%=\n"
"v_mfma_f32_16x16x32_bf16 v[20:23], v[64:67], a[52:55], v[20:23]\n"
"s_waitcnt lgkmcnt(5)\n"
"v_mfma_f32_16x16x32_bf16 v[4:7], v[68:71], a[48:51], v[4:7]\n"
"ds_read_b128 v[60:63], v94 offset:52224\n"
"v_mfma_f32_16x16x32_bf16 v[24:27], v[68:71], a[52:55], v[24:27]\n"
"s_waitcnt lgkmcnt(5)\n"
"v_mfma_f32_16x16x32_bf16 v[8:11], v[40:43], a[48:51], v[8:11]\n"
"ds_read_b128 v[64:67], v94 offset:56320\n"
"v_mfma_f32_16x16x32_bf16 v[28:31], v[40:43], a[52:55], v[28:31]\n"
"s_waitcnt lgkmcnt(5)\n"
"v_mfma_f32_16x16x32_bf16 v[12:15], v[44:47], a[48:51], v[12:15]\n"
"ds_read_b128 v[68:71], v94 offset:60416\n"
"v_mfma_f32_16x16x32_bf16 v[32:35], v[44:47], a[52:55], v[32:35]\n"
"s_waitcnt lgkmcnt(0)\n"
"s_barrier\n"
"v_mfma_f32_16x16x32_bf16 v[16:19], v[48:51], a[48:51], v[16:19]\n"
"ds_read_b128 v[40:43], v95 offset:0\n"
"v_mfma_f32_16x16x32_bf16 v[36:39], v[48:51], a[52:55], v[36:39]\n"
"s_waitcnt vmcnt(19)\n"
"v_mfma_f32_16x16x32_bf16 v[0:3], v[52:55], a[56:59], v[0:3]\n"
"ds_read_b128 v[44:47], v95 offset:4096\n"
"s_waitcnt vmcnt(18)\n"
"v_mfma_f32_16x16x32_bf16 v[20:23], v[52:55], a[60:63], v[20:23]\n"
"v_mfma_f32_16x16x32_bf16 v[4:7], v[56:59], a[56:59], v[4:7]\n"
"ds_read_b128 v[48:51], v95 offset:8192\n"
"v_mfma_f32_16x16x32_bf16 v[24:27], v[56:59], a[60:63], v[24:27]\n"
"v_mfma_f32_16x16x32_bf16 v[8:11], v[60:63], a[56:59], v[8:11]\n"
"ds_read_b128 v[52:55], v95 offset:12288\n"
"v_mfma_f32_16x16x32_bf16 v[28:31], v[60:63], a[60:63], v[28:31]\n"
"v_mfma_f32_16x16x32_bf16 v[12:15], v[64:67], a[56:59], v[12:15]\n"
"ds_read_b128 v[56:59], v95 offset:16384\n"
"v_mfma_f32_16x16x32_bf16 v[32:35], v[64:67], a[60:63], v[32:35]\n"
"v_mfma_f32_16x16x32_bf16 v[16:19], v[68:71], a[56:59], v[16:19]\n"
"ds_read_b128 v[60:63], v95 offset:1024\n"
"v_mfma_f32_16x16x32_bf16 v[36:39], v[68:71], a[60:63], v[36:39]\n"
"s_waitcnt vmcnt(17) lgkmcnt(5)\n"
"v_mfma_f32_16x16x32_bf16 v[0:3], v[40:43], a[64:67], v[0:3]\n"
"ds_read_b128 v[64:67], v95 offset:5120\n"
"s_waitcnt vmcnt(16)\n"
"v_mfma_f32_16x16x32_bf16 v[20:23], v[40:43], a[68:71], v[20:23]\n"
"s_waitcnt lgkmcnt(5)\n"
"v_mfma_f32_16x16x32_bf16 v[4:7], v[44:47], a[64:67], v[4:7]\n"
"ds_read_b128 v[68:71], v95 offset:9216\n"
"v_mfma_f32_16x16x32_bf16 v[24:27], v[44:47], a[68:71], v[24:27]\n"
"s_waitcnt lgkmcnt(5)\n"
"v_mfma_f32_16x16x32_bf16 v[8:11], v[48:51], a[64:67], v[8:11]\n"
"ds_read_b128 v[40:43], v95 offset:13312\n"
"v_mfma_f32_16x16x32_bf16 v[28:31], v[48:51], a[68:71], v[28:31]\n"
"s_waitcnt lgkmcnt(5)\n"
"v_mfma_f32_16x16x32_bf16 v[12:15], v[52:55], a[64:67], v[12:15]\n"
"ds_read_b128 v[44:47], v95 offset:17408\n"
"v_mfma_f32_16x16x32_bf16 v[32:35], v[52:55], a[68:71], v[32:35]\n"
"s_waitcnt lgkmcnt(5)\n"
"v_mfma_f32_16x16x32_bf16 v[16:19], v[56:59], a[64:67], v[16:19]\n"
"ds_read_b128 v[48:51], v95 offset:2048\n"
"v_mfma_f32_16x16x32_bf16 v[36:39], v[56:59], a[68:71], v[36:39]\n"
"s_waitcnt vmcnt(15) lgkmcnt(5)\n"
"v_mfma_f32_16x16x32_bf16 v[0:3], v[60:63], a[72:75], v[0:3]\n"
"ds_read_b128 v[52:55], v95 offset:6144\n"
"s_waitcnt vmcnt(14)\n"
"v_mfma_f32_16x16x32_bf16 v[20:23], v[60:63], a[76:79], v[20:23]\n"
"s_waitcnt lgkmcnt(5)\n"
"v_mfma_f32_16x16x32_bf16 v[4:7], v[64:67], a[72:75], v[4:7]\n"
"ds_read_b128 v[56:59], v95 offset:10240\n"
"v_mfma_f32_16x16x32_bf16 v[24:27], v[64:67], a[76:79], v[24:27]\n"
"s_waitcnt lgkmcnt(5)\n"
"v_mfma_f32_16x16x32_bf16 v[8:11], v[68:71], a[72:75], v[8:11]\n"
"ds_read_b128 v[60:63], v95 offset:14336\n"
"v_mfma_f32_16x16x32_bf16 v[28:31], v[68:71], a[76:79], v[28:31]\n"
"s_waitcnt lgkmcnt(5)\n"
"v_mfma_f32_16x16x32_bf16 v[12:15], v[40:43], a[72:75], v[12:15]\n"
"ds_read_b128 v[64:67], v95 offset:18432\n"
"v_mfma_f32_16x16x32_bf16 v[32:35], v[40:43], a[76:79], v[32:35]\n"
"s_waitcnt lgkmcnt(5)\n"
"v_mfma_f32_16x16x32_bf16 v[16:19], v[44:47], a[72:75], v[16:19]\n"
"ds_read_b128 v[68:71], v95 offset:3072\n"
"v_mfma_f32_16x16x32_bf16 v[36:39], v[44:47], a[76:79], v[36:39]\n"
"s_waitcnt vmcnt(13) lgkmcnt(5)\n"
"v_mfma_f32_16x16x32_bf16 v[0:3], v[48:51], a[80:83], v[0:3]\n"
"ds_read_b128 v[40:43], v95 offset:7168\n"
"s_waitcnt vmcnt(12)\n"
"v_mfma_f32_16x16x32_bf16 v[20:23], v[48:51], a[84:87], v[20:23]\n"
"s_waitcnt lgkmcnt(5)\n"
"v_mfma_f32_16x16x32_bf16 v[4:7], v[52:55], a[80:83], v[4:7]\n"
"ds_read_b128 v[44:47], v95 offset:11264\n"
"v_mfma_f32_16x16x32_bf16 v[24:27], v[52:55], a[84:87], v[24:27]\n"
"s_waitcnt lgkmcnt(5)\n"
"v_mfma_f32_16x16x32_bf16 v[8:11], v[56:59], a[80:83], v[8:11]\n"
"ds_read_b128 v[48:51], v95 offset:15360\n"
"v_mfma_f32_16x16x32_bf16 v[28:31], v[56:59], a[84:87], v[28:31]\n"
"s_waitcnt lgkmcnt(5)\n"
"v_mfma_f32_16x16x32_bf16 v[12:15], v[60:63], a[80:83], v[12:15]\n"
"ds_read_b128 v[52:55], v95 offset:19456\n"
"v_mfma_f32_16x16x32_bf16 v[32:35], v[60:63], a[84:87], v[32:35]\n"
"s_waitcnt lgkmcnt(0)\n"
"s_barrier\n"
"v_mfma_f32_16x16x32_bf16 v[16:19], v[64:67], a[80:83], v[16:19]\n"
"ds_read_b128 v[56:59], v95 offset:20480\n"
"v_mfma_f32_16x16x32_bf16 v[36:39], v[64:67], a[84:87], v[36:39]\n"
"s_waitcnt vmcnt(11)\n"
"v_mfma_f32_16x16x32_bf16 v[0:3], v[68:71], a[88:91], v[0:3]\n"
"ds_read_b128 v[60:63], v95 offset:24576\n"
"s_waitcnt vmcnt(10)\n"
"v_mfma_f32_16x16x32_bf16 v[20:23], v[68:71], a[92:95], v[20:23]\n"
"v_mfma_f32_16x16x32_bf16 v[4:7], v[40:43], a[88:91], v[4:7]\n"
"ds_read_b128 v[64:67], v95 offset:28672\n"
"v_mfma_f32_16x16x32_bf16 v[24:27], v[40:43], a[92:95], v[24:27]\n"
"v_mfma_f32_16x16x32_bf16 v[8:11], v[44:47], a[88:91], v[8:11]\n"
"ds_read_b128 v[68:71], v95 offset:32768\n"
"v_mfma_f32_16x16x32_bf16 v[28:31], v[44:47], a[92:95], v[28:31]\n"
"v_mfma_f32_16x16x32_bf16 v[12:15], v[48:51], a[88:91], v[12:15]\n"
"ds_read_b128 v[40:43], v95 offset:36864\n"
"v_mfma_f32_16x16x32_bf16 v[32:35], v[48:51], a[92:95], v[32:35]\n"
"v_mfma_f32_16x16x32_bf16 v[16:19], v[52:55], a[88:91], v[16:19]\n"
"ds_read_b128 v[44:47], v95 offset:21504\n"
"v_mfma_f32_16x16x32_bf16 v[36:39], v[52:55], a[92:95], v[36:39]\n"
"s_waitcnt vmcnt(9) lgkmcnt(5)\n"
"v_mfma_f32_16x16x32_bf16 v[0:3], v[56:59], a[96:99], v[0:3]\n"
"ds_read_b128 v[48:51], v95 offset:25600\n"
"s_waitcnt vmcnt(8)\n"
"v_mfma_f32_16x16x32_bf16 v[20:23], v[56:59], a[100:103], v[20:23]\n"
"s_waitcnt lgkmcnt(5)\n"
"v_mfma_f32_16x16x32_bf16 v[4:7], v[60:63], a[96:99], v[4:7]\n"
"ds_read_b128 v[52:55], v95 offset:29696\n"
"v_mfma_f32_16x16x32_bf16 v[24:27], v[60:63], a[100:103], v[24:27]\n"
"s_waitcnt lgkmcnt(5)\n"
"v_mfma_f32_16x16x32_bf16 v[8:11], v[64:67], a[96:99], v[8:11]\n"
"ds_read_b128 v[56:59], v95 offset:33792\n"
"v_mfma_f32_16x16x32_bf16 v[28:31], v[64:67], a[100:103], v[28:31]\n"
"s_waitcnt lgkmcnt(5)\n"
"v_mfma_f32_16x16x32_bf16 v[12:15], v[68:71], a[96:99], v[12:15]\n"
"ds_read_b128 v[60:63], v95 offset:37888\n"
"v_mfma_f32_16x16x32_bf16 v[32:35], v[68:71], a[100:103], v[32:35]\n"
"s_waitcnt lgkmcnt(5)\n"
"v_mfma_f32_16x16x32_bf16 v[16:19], v[40:43], a[96:99], v[16:19]\n"
"ds_read_b128 v[64:67], v95 offset:22528\n"
"v_mfma_f32_16x16x32_bf16 v[36:39], v[40:43], a[100:103], v[36:39]\n"
"s_waitcnt vmcnt(7) lgkmcnt(5)\n"
"v_mfma_f32_16x16x32_bf16 v[0:3], v[44:47], a[104:107], v[0:3]\n"
"ds_read_b128 v[68:71], v95 offset:26624\n"
"s_waitcnt vmcnt(6)\n"
"v_mfma_f32_16x16x32_bf16 v[20:23], v[44:47], a[108:111], v[20:23]\n"
"s_waitcnt lgkmcnt(5)\n"
"v_mfma_f32_16x16x32_bf16 v[4:7], v[48:51], a[104:107], v[4:7]\n"
"ds_read_b128 v[40:43], v95 offset:30720\n"
"v_mfma_f32_16x16x32_bf16 v[24:27], v[48:51], a[108:111], v[24:27]\n"
"s_waitcnt lgkmcnt(5)\n"
"v_mfma_f32_16x16x32_bf16 v[8:11], v[52:55], a[104:107], v[8:11]\n"
"ds_read_b128 v[44:47], v95 offset:34816\n"
"v_mfma_f32_16x16x32_bf16 v[28:31], v[52:55], a[108:111], v[28:31]\n"
"s_waitcnt lgkmcnt(5)\n"
"v_mfma_f32_16x16x32_bf16 v[12:15], v[56:59], a[104:107], v[12:15]\n"
"ds_read_b128 v[48:51], v95 offset:38912\n"
"v_mfma_f32_16x16x32_bf16 v[32:35], v[56:59], a[108:111], v[32:35]\n"
"s_waitcnt lgkmcnt(5)\n"
"v_mfma_f32_16x16x32_bf16 v[16:19], v[60:63], a[104:107], v[16:19]\n"
"ds_read_b128 v[52:55], v95 offset:23552\n"
"v_mfma_f32_16x16x32_bf16 v[36:39], v[60:63], a[108:111], v[36:39]\n"
"s_waitcnt vmcnt(5) lgkmcnt(5)\n"
"v_mfma_f32_16x16x32_bf16 v[0:3], v[64:67], a[112:115], v[0:3]\n"
"ds_read_b128 v[56:59], v95 offset:27648\n"
"s_waitcnt vmcnt(4)\n"
"v_mfma_f32_16x16x32_bf16 v[20:23], v[64:67], a[116:119], v[20:23]\n"
"s_waitcnt lgkmcnt(5)\n"
"v_mfma_f32_16x16x32_bf16 v[4:7], v[68:71], a[112:115], v[4:7]\n"
"ds_read_b128 v[60:63], v95 offset:31744\n"
"v_mfma_f32_16x16x32_bf16 v[24:27], v[68:71], a[116:119], v[24:27]\n"
"s_waitcnt lgkmcnt(5)\n"
"v_mfma_f32_16x16x32_bf16 v[8:11], v[40:43], a[112:115], v[8:11]\n"
"ds_read_b128 v[64:67], v95 offset:35840\n"
"s_waitcnt vmcnt(1)\n"
"v_xor_b32 v100, s14, v100\n"
"v_mfma_f32_16x16x32_bf16 v[28:31], v[40:43], a[116:119], v[28:31]\n"
"v_xor_b32 v101, s15, v101\n"
"s_waitcnt lgkmcnt(5)\n"
"v_mfma_f32_16x16x32_bf16 v[12:15], v[44:47], a[112:115], v[12:15]\n"
"ds_read_b128 v[68:71], v95 offset:39936\n"
"v_mfma_f32_16x16x32_bf16 v[32:35], v[44:47], a[116:119], v[32:35]\n"
"v_xor_b32 v102, s14, v102\n"
"s_waitcnt lgkmcnt(5)\n"
"v_mfma_f32_16x16x32_bf16 v[16:19], v[48:51], a[112:115], v[16:19]\n"
"v_xor_b32 v103, s15, v103\n"
"v_mfma_f32_16x16x32_bf16 v[36:39], v[48:51], a[116:119], v[36:39]\n"
"s_waitcnt vmcnt(0)\n"
"v_xor_b32 v104, s14, v104\n"
"s_waitcnt lgkmcnt(4)\n"
"v_mfma_f32_16x16x32_bf16 v[0:3], v[52:55], a[120:123], v[0:3]\n"
"v_xor_b32 v105, s15, v105\n"
"v_mfma_f32_16x16x32_bf16 v[20:23], v[52:55], a[124:127], v[20:23]\n"
"v_xor_b32 v106, s14, v106\n"
"s_waitcnt lgkmcnt(3)\n"
"v_mfma_f32_16x16x32_bf16 v[4:7], v[56:59], a[120:123], v[4:7]\n"
"v_xor_b32 v107, s15, v107\n"
"v_mfma_f32_16x16x32_bf16 v[24:27], v[56:59], a[124:127], v[24:27]\n"
"v_or3_b32 v100, v100, v101, v102\n"
"s_waitcnt lgkmcnt(2)\n"
"v_mfma_f32_16x16x32_bf16 v[8:11], v[60:63], a[120:123], v[8:11]\n"
"v_or3_b32 v103, v103, v104, v105\n"
"v_mfma_f32_16x16x32_bf16 v[28:31], v[60:63], a[124:127], v[28:31]\n"
"v_or3_b32 v100, v100, v106, v107\n"
"s_waitcnt lgkmcnt(1)\n"
"v_mfma_f32_16x16x32_bf16 v[12:15], v[64:67], a[120:123], v[12:15]\n"
"v_or_b32 v100, v100, v103\n"
"v_mfma_f32_16x16x32_bf16 v[32:35], v[64:67], a[124:127], v[32:35]\n"
"v_cmp_ne_u32 vcc, 0, v100\n"
"s_waitcnt lgkmcnt(0)\n"
"v_mfma_f32_16x16x32_bf16 v[16:19], v[68:71], a[120:123], v[16:19]\n"
"v_mfma_f32_16x16x32_bf16 v[36:39], v[68:71], a[124:127], v[36:39]\n"
"s_nop 0\n"
"s_cbranch_vccnz SLOW_FL_%=\n"
"OK_FL_%=:\n"
"s_xor_b32 s38, s76, 32\n"
"s_mul_i32 s38, s38, 5120\n"
"s_add_u32 s38, s38, 122814464\n"
"s_add_u32 s39, s38, 4096\n"
"buffer_store_dwordx4 v[20:23], v251, s[96:99], s38 offen offset:0 sc1\n"
"buffer_store_dwordx4 v[24:27], v251, s[96:99], s38 offen offset:1024 sc1\n"
"buffer_store_dwordx4 v[28:31], v251, s[96:99], s38 offen offset:2048 sc1\n"
"buffer_store_dwordx4 v[32:35], v251, s[96:99], s38 offen offset:3072 sc1\n"
"buffer_store_dwordx4 v[36:39], v251, s[96:99], s39 offen offset:0 sc1\n"
"s_mov_b32 s38, 104862720\n"
"buffer_load_dwordx4 v[124:127], v254, s[96:99], s38 offen offset:0 sc1\n"
"buffer_load_dwordx4 v[128:131], v254, s[96:99], s38 offen offset:16 sc1\n"
"buffer_load_dwordx4 v[132:135], v254, s[96:99], s38 offen offset:128 sc1\n"
"buffer_load_dwordx4 v[136:139], v254, s[96:99], s38 offen offset:144 sc1\n"
"buffer_load_dwordx4 v[140:143], v255, s[96:99], s38 offen sc1\n"
"s_mov_b32 s38, 104872960\n"
"buffer_load_dwordx4 v[144:147], v254, s[96:99], s38 offen offset:0 sc1\n"
"buffer_load_dwordx4 v[148:151], v254, s[96:99], s38 offen offset:16 sc1\n"
"buffer_load_dwordx4 v[152:155], v254, s[96:99], s38 offen offset:128 sc1\n"
"buffer_load_dwordx4 v[156:159], v254, s[96:99], s38 offen offset:144 sc1\n"
"buffer_load_dwordx4 v[160:163], v255, s[96:99], s38 offen sc1\n"
"s_mov_b32 s38, 104865280\n"
"buffer_load_dwordx4 v[164:167], v254, s[96:99], s38 offen offset:0 sc1\n"
"buffer_load_dwordx4 v[168:171], v254, s[96:99], s38 offen offset:16 sc1\n"
"buffer_load_dwordx4 v[172:175], v254, s[96:99], s38 offen offset:128 sc1\n"
"buffer_load_dwordx4 v[176:179], v254, s[96:99], s38 offen offset:144 sc1\n"
"buffer_load_dwordx4 v[180:183], v255, s[96:99], s38 offen sc1\n"
"s_barrier\n"
"s_mul_i32 s72, s76, 5120\n"
"s_add_u32 s72, s72, 122814464\n"
"s_add_u32 s73, s72, 4096\n"
"v_readlane_b32 s51, v96, 2\n"
"v_readlane_b32 s52, v97, 2\n"
"v_readlane_b32 s53, v96, 0\n"
"v_readlane_b32 s54, v96, 1\n"
"v_readlane_b32 s55, v97, 1\n"
"v_lshlrev_b32 v184, 16, v220\n"
"v_and_b32 v185, 0xffff0000, v220\n"
"v_lshlrev_b32 v186, 16, v221\n"
"v_and_b32 v187, 0xffff0000, v221\n"
"v_lshlrev_b32 v188, 16, v222\n"
"v_and_b32 v189, 0xffff0000, v222\n"
"v_lshlrev_b32 v190, 16, v223\n"
"v_and_b32 v191, 0xffff0000, v223\n"
"v_lshlrev_b32 v192, 16, v224\n"
"v_and_b32 v193, 0xffff0000, v224\n"
"v_lshlrev_b32 v194, 16, v225\n"
"v_and_b32 v195, 0xffff0000, v225\n"
"v_lshlrev_b32 v196, 16, v226\n"
"v_and_b32 v197, 0xffff0000, v226\n"
"v_lshlrev_b32 v198, 16, v227\n"
"v_and_b32 v199, 0xffff0000, v227\n"
"v_lshlrev_b32 v200, 16, v228\n"
"v_and_b32 v201, 0xffff0000, v228\n"
"v_lshlrev_b32 v202, 16, v229\n"
"v_and_b32 v203, 0xffff0000, v229\n"
"s_mov_b32 s63, 3\n"
"buffer_load_dwordx4 v[100:103], v251, s[96:99], s72 offen offset:0 sc1\n"
"buffer_load_dwordx4 v[104:107], v251, s[96:99], s72 offen offset:1024 sc1\n"
"buffer_load_dwordx4 v[108:111], v251, s[96:99], s72 offen offset:2048 sc1\n"
"buffer_load_dwordx4 v[112:115], v251, s[96:99], s72 offen offset:3072 sc1\n"
"buffer_load_dwordx4 v[116:119], v251, s[96:99], s73 offen offset:0 sc1\n"
"s_mul_i32 s45, %2, 5120\n"
"s_add_u32 s37, s46, s45\n"
"s_add_u32 s37, s37, 0\n"
"s_add_u32 s45, s45, %3\n"
"s_add_u32 m0, s45, 0\n"
"s_nop 0\n"
"buffer_load_dwordx4 v251, s[96:99], s37 offen offset:0 sc1 lds\n"
"buffer_load_dwordx4 v251, s[96:99], s37 offen offset:1024 sc1 lds\n"
"buffer_load_dwordx4 v251, s[96:99], s37 offen offset:2048 sc1 lds\n"
"buffer_load_dwordx4 v251, s[96:99], s37 offen offset:3072 sc1 lds\n"
"s_add_u32 m0, s45, 4096\n"
"s_add_u32 s37, s37, 4096\n"
"buffer_load_dwordx4 v251, s[96:99], s37 offen offset:0 sc1 lds\n"
"s_mul_i32 s45, %2, 5120\n"
"s_add_u32 s37, s46, s45\n"
"s_add_u32 s37, s37, 20480\n"
"s_add_u32 s45, s45, %3\n"
"s_add_u32 m0, s45, 20480\n"
"s_nop 0\n"
"buffer_load_dwordx4 v251, s[96:99], s37 offen offset:0 sc1 lds\n"
"buffer_load_dwordx4 v251, s[96:99], s37 offen offset:1024 sc1 lds\n"
"buffer_load_dwordx4 v251, s[96:99], s37 offen offset:2048 sc1 lds\n"
"buffer_load_dwordx4 v251, s[96:99], s37 offen offset:3072 sc1 lds\n"
"s_add_u32 m0, s45, 24576\n"
"s_add_u32 s37, s37, 4096\n"
"buffer_load_dwordx4 v251, s[96:99], s37 offen offset:0 sc1 lds\n"
"s_mul_i32 s45, %2, 5120\n"
"s_add_u32 s37, s46, s45\n"
"s_add_u32 s37, s37, 40960\n"
"s_add_u32 s45, s45, %3\n"
"s_add_u32 m0, s45, 40960\n"
"s_nop 0\n"
"buffer_load_dwordx4 v251, s[96:99], s37 offen offset:0 sc1 lds\n"
"buffer_load_dwordx4 v251, s[96:99], s37 offen offset:1024 sc1 lds\n"
"buffer_load_dwordx4 v251, s[96:99], s37 offen offset:2048 sc1 lds\n"
"buffer_load_dwordx4 v251, s[96:99], s37 offen offset:3072 sc1 lds\n"
"s_add_u32 m0, s45, 45056\n"
"s_add_u32 s37, s37, 4096\n"
"buffer_load_dwordx4 v251, s[96:99], s37 offen offset:0 sc1 lds\n"
"s_mul_i32 s45, %2, 5120\n"
"s_add_u32 s37, s46, s45\n"
"s_add_u32 s37, s37, 61440\n"
"s_add_u32 s45, s45, %3\n"
"s_add_u32 m0, s45, 61440\n"
"s_nop 0\n"
"buffer_load_dwordx4 v251, s[96:99], s37 offen offset:0 sc1 lds\n"
"buffer_load_dwordx4 v251, s[96:99], s37 offen offset:1024 sc1 lds\n"
"buffer_load_dwordx4 v251, s[96:99], s37 offen offset:2048 sc1 lds\n"
"buffer_load_dwordx4 v251, s[96:99], s37 offen offset:3072 sc1 lds\n"
"s_add_u32 m0, s45, 65536\n"
"s_add_u32 s37, s37, 4096\n"
"buffer_load_dwordx4 v251, s[96:99], s37 offen offset:0 sc1 lds\n"
"s_mul_i32 s45, %2, 5120\n"
"s_add_u32 s37, s47, s45\n"
"s_add_u32 s37, s37, 0\n"
"s_add_u32 s45, s45, %3\n"
"s_add_u32 m0, s45, 81920\n"
"s_nop 0\n"
"buffer_load_dwordx4 v251, s[96:99], s37 offen offset:0 sc1 lds\n"
"buffer_load_dwordx4 v251, s[96:99], s37 offen offset:1024 sc1 lds\n"
"buffer_load_dwordx4 v251, s[96:99], s37 offen offset:2048 sc1 lds\n"
"buffer_load_dwordx4 v251, s[96:99], s37 offen offset:3072 sc1 lds\n"
"s_add_u32 m0, s45, 86016\n"
"s_add_u32 s37, s37, 4096\n"
"buffer_load_dwordx4 v251, s[96:99], s37 offen offset:0 sc1 lds\n"
"s_waitcnt vmcnt(29)\n"
"v_max_u32 v242, v100, v101\n"
"v_max3_u32 v242, v242, v102, v103\n"
"s_waitcnt vmcnt(28)\n"
"v_max3_u32 v242, v242, v104, v105\n"
"v_max3_u32 v242, v242, v106, v107\n"
"s_waitcnt vmcnt(27)\n"
"v_max3_u32 v242, v242, v108, v109\n"
"v_max3_u32 v242, v242, v110, v111\n"
"s_waitcnt vmcnt(26)\n"
"v_max3_u32 v242, v242, v112, v113\n"
"v_max3_u32 v242, v242, v114, v115\n"
"s_waitcnt vmcnt(25)\n"
"v_max3_u32 v242, v242, v116, v117\n"
"v_max3_u32 v242, v242, v118, v119\n"
"v_cmp_eq_u32 vcc, -1, v242\n"
"s_nop 0\n"
"s_cbranch_vccnz SLOW_OX_%=\n"
"OK_OX_%=:\n"
"v_pk_add_f32 v[0:1], v[0:1], v[100:101]\n"
"v_pk_add_f32 v[2:3], v[2:3], v[102:103]\n"
"v_pk_add_f32 v[4:5], v[4:5], v[104:105]\n"
"v_pk_add_f32 v[6:7], v[6:7], v[106:107]\n"
"v_pk_add_f32 v[8:9], v[8:9], v[108:109]\n"
"v_pk_add_f32 v[10:11], v[10:11], v[110:111]\n"
"v_pk_add_f32 v[12:13], v[12:13], v[112:113]\n"
"v_pk_add_f32 v[14:15], v[14:15], v[114:115]\n"
"v_pk_add_f32 v[16:17], v[16:17], v[116:117]\n"
"v_pk_add_f32 v[18:19], v[18:19], v[118:119]\n"
"v_pk_fma_f32 v[0:1], v[0:1], v[124:125], v[184:185]\n"
"v_pk_fma_f32 v[2:3], v[2:3], v[126:127], v[186:187]\n"
"v_pk_fma_f32 v[4:5], v[4:5], v[128:129], v[188:189]\n"
"v_pk_fma_f32 v[6:7], v[6:7], v[130:131], v[190:191]\n"
"v_pk_fma_f32 v[8:9], v[8:9], v[132:133], v[192:193]\n"
"v_pk_fma_f32 v[10:11], v[10:11], v[134:135], v[194:195]\n"
"v_pk_fma_f32 v[12:13], v[12:13], v[136:137], v[196:197]\n"
"v_pk_fma_f32 v[14:15], v[14:15], v[138:139], v[198:199]\n"
"v_pk_fma_f32 v[16:17], v[16:17], v[140:141], v[200:201]\n"
"v_pk_fma_f32 v[18:19], v[18:19], v[142:143], v[202:203]\n"
"v_pk_mul_f32 v[20:21], v[0:1], v[144:145]\n"
"v_pk_mul_f32 v[22:23], v[2:3], v[146:147]\n"
"v_pk_mul_f32 v[24:25], v[4:5], v[148:149]\n"
"v_pk_mul_f32 v[26:27], v[6:7], v[150:151]\n"
"v_pk_mul_f32 v[28:29], v[8:9], v[152:153]\n"
"v_pk_mul_f32 v[30:31], v[10:11], v[154:155]\n"
"v_pk_mul_f32 v[32:33], v[12:13], v[156:157]\n"
"v_pk_mul_f32 v[34:35], v[14:15], v[158:159]\n"
"v_pk_mul_f32 v[36:37], v[16:17], v[160:161]\n"
"v_pk_mul_f32 v[38:39], v[18:19], v[162:163]\n"
"v_cvt_pk_bf16_f32 v40, v20, v21\n"
"v_cvt_pk_bf16_f32 v41, v22, v23\n"
"v_cvt_pk_bf16_f32 v42, v24, v25\n"
"v_cvt_pk_bf16_f32 v43, v26, v27\n"
"v_cvt_pk_bf16_f32 v44, v28, v29\n"
"v_cvt_pk_bf16_f32 v45, v30, v31\n"
"v_cvt_pk_bf16_f32 v46, v32, v33\n"
"v_cvt_pk_bf16_f32 v47, v34, v35\n"
"v_cvt_pk_bf16_f32 v48, v36, v37\n"
"v_cvt_pk_bf16_f32 v49, v38, v39\n"
"s_mul_i32 s38, s42, 20480\n"
"s_add_u32 s38, s38, 106954752\n"
"s_add_u32 s39, s38, s41\n"
"s_add_u32 s38, s38, s40\n"
"buffer_store_dwordx4 v[40:43], v251, s[96:99], s38 offen offset:0 sc1\n"
"buffer_store_dwordx4 v[44:47], v251, s[96:99], s38 offen offset:1024 sc1\n"
"buffer_store_dwordx2 v[48:49], v250, s[96:99], s39 offen sc1\n"
"v_pk_add_f32 v[52:53], v[0:1], v[2:3]\n"
"v_pk_mul_f32 v[54:55], v[0:1], v[0:1]\n"
"v_pk_add_f32 v[56:57], v[20:21], v[22:23]\n"
"v_pk_mul_f32 v[58:59], v[20:21], v[20:21]\n"
"v_pk_mul_f32 v[60:61], v[20:21], v[144:145]\n"
"v_pk_mul_f32 v[62:63], v[20:21], v[164:165]\n"
"v_pk_fma_f32 v[54:55], v[2:3], v[2:3], v[54:55]\n"
"v_pk_fma_f32 v[58:59], v[22:23], v[22:23], v[58:59]\n"
"v_pk_fma_f32 v[60:61], v[22:23], v[146:147], v[60:61]\n"
"v_pk_fma_f32 v[62:63], v[22:23], v[166:167], v[62:63]\n"
"v_pk_add_f32 v[52:53], v[52:53], v[4:5]\n"
"v_pk_add_f32 v[56:57], v[56:57], v[24:25]\n"
"v_pk_fma_f32 v[54:55], v[4:5], v[4:5], v[54:55]\n"
"v_pk_fma_f32 v[58:59], v[24:25], v[24:25], v[58:59]\n"
"v_pk_fma_f32 v[60:61], v[24:25], v[148:149], v[60:61]\n"
"v_pk_fma_f32 v[62:63], v[24:25], v[168:169], v[62:63]\n"
"v_pk_add_f32 v[52:53], v[52:53], v[6:7]\n"
"v_pk_add_f32 v[56:57], v[56:57], v[26:27]\n"
"v_pk_fma_f32 v[54:55], v[6:7], v[6:7], v[54:55]\n"
"v_pk_fma_f32 v[58:59], v[26:27], v[26:27], v[58:59]\n"
"v_pk_fma_f32 v[60:61], v[26:27], v[150:151], v[60:61]\n"
"v_pk_fma_f32 v[62:63], v[26:27], v[170:171], v[62:63]\n"
"v_pk_add_f32 v[52:53], v[52:53], v[8:9]\n"
"v_pk_add_f32 v[56:57], v[56:57], v[28:29]\n"
"v_pk_fma_f32 v[54:55], v[8:9], v[8:9], v[54:55]\n"
"v_pk_fma_f32 v[58:59], v[28:29], v[28:29], v[58:59]\n"
"v_pk_fma_f32 v[60:61], v[28:29], v[152:153], v[60:61]\n"
"v_pk_fma_f32 v[62:63], v[28:29], v[172:173], v[62:63]\n"
"v_pk_add_f32 v[52:53], v[52:53], v[10:11]\n"
"v_pk_add_f32 v[56:57], v[56:57], v[30:31]\n"
"v_pk_fma_f32 v[54:55], v[10:11], v[10:11], v[54:55]\n"
"v_pk_fma_f32 v[58:59], v[30:31], v[30:31], v[58:59]\n"
"v_pk_fma_f32 v[60:61], v[30:31], v[154:155], v[60:61]\n"
"v_pk_fma_f32 v[62:63], v[30:31], v[174:175], v[62:63]\n"
"v_pk_add_f32 v[52:53], v[52:53], v[12:13]\n"
"v_pk_add_f32 v[56:57], v[56:57], v[32:33]\n"
"v_pk_fma_f32 v[54:55], v[12:13], v[12:13], v[54:55]\n"
"v_pk_fma_f32 v[58:59], v[32:33], v[32:33], v[58:59]\n"
"v_pk_fma_f32 v[60:61], v[32:33], v[156:157], v[60:61]\n"
"v_pk_fma_f32 v[62:63], v[32:33], v[176:177], v[62:63]\n"
"v_pk_add_f32 v[52:53], v[52:53], v[14:15]\n"
"v_pk_add_f32 v[56:57], v[56:57], v[34:35]\n"
"v_pk_fma_f32 v[54:55], v[14:15], v[14:15], v[54:55]\n"
"v_pk_fma_f32 v[58:59], v[34:35], v[34:35], v[58:59]\n"
"v_pk_fma_f32 v[60:61], v[34:35], v[158:159], v[60:61]\n"
"v_pk_fma_f32 v[62:63], v[34:35], v[178:179], v[62:63]\n"
"v_pk_add_f32 v[52:53], v[52:53], v[16:17]\n"
"v_pk_add_f32 v[56:57], v[56:57], v[36:37]\n"
"v_pk_fma_f32 v[54:55], v[16:17], v[16:17], v[54:55]\n"
"v_pk_fma_f32 v[58:59], v[36:37], v[36:37], v[58:59]\n"
"v_pk_fma_f32 v[60:61], v[36:37], v[160:161], v[60:61]\n"
"v_pk_fma_f32 v[62:63], v[36:37], v[180:181], v[62:63]\n"
"v_pk_add_f32 v[52:53], v[52:53], v[18:19]\n"
"v_pk_add_f32 v[56:57], v[56:57], v[38:39]\n"
"v_pk_fma_f32 v[54:55], v[18:19], v[18:19], v[54:55]\n"
"v_pk_fma_f32 v[58:59], v[38:39], v[38:39], v[58:59]\n"
"v_pk_fma_f32 v[60:61], v[38:39], v[162:163], v[60:61]\n"
"v_pk_fma_f32 v[62:63], v[38:39], v[182:183], v[62:63]\n"
"v_add_f32 v64, v52, v53\n"
"v_add_f32 v65, v54, v55\n"
"v_add_f32 v66, v56, v57\n"
"v_add_f32 v67, v58, v59\n"
"v_add_f32 v68, v60, v61\n"
"v_add_f32 v69, v62, v63\n"
"v_mov_b32 v232, v64\n"
"v_mov_b32 v233, v65\n"
"v_mov_b32 v234, v66\n"
"v_mov_b32 v235, v67\n"
"v_mov_b32 v236, v68\n"
"v_mov_b32 v237, v69\n"
"v_permlane16_swap_b32 v64, v232\n"
"v_permlane16_swap_b32 v65, v233\n"
"v_permlane16_swap_b32 v66, v234\n"
"v_permlane16_swap_b32 v67, v235\n"
"v_permlane16_swap_b32 v68, v236\n"
"v_permlane16_swap_b32 v69, v237\n"
"v_add_f32 v64, v64, v232\n"
"v_add_f32 v65, v65, v233\n"
"v_add_f32 v66, v66, v234\n"
"v_add_f32 v67, v67, v235\n"
"v_add_f32 v68, v68, v236\n"
"v_add_f32 v69, v69, v237\n"
"v_mov_b32 v232, v64\n"
"v_mov_b32 v233, v65\n"
"v_mov_b32 v234, v66\n"
"v_mov_b32 v235, v67\n"
"v_mov_b32 v236, v68\n"
"v_mov_b32 v237, v69\n"
"v_permlane32_swap_b32 v64, v232\n"
"v_permlane32_swap_b32 v65, v233\n"
"v_permlane32_swap_b32 v66, v234\n"
"v_permlane32_swap_b32 v67, v235\n"
"v_permlane32_swap_b32 v68, v236\n"
"v_permlane32_swap_b32 v69, v237\n"
"v_add_f32 v64, v64, v232\n"
"v_add_f32 v65, v65, v233\n"
"v_add_f32 v66, v66, v234\n"
"v_add_f32 v67, v67, v235\n"
"v_add_f32 v68, v68, v236\n"
"v_add_f32 v69, v69, v237\n"
"v_cmp_eq_u32 vcc, 1, v248\n"
"s_lshl_b32 s38, s65, 5\n"
"s_add_u32 s38, s38, 122159104\n"
"v_cndmask_b32 v64, v64, v68, vcc\n"
"v_cndmask_b32 v65, v65, v69, vcc\n"
"buffer_store_dwordx4 v[64:67], v205, s[96:99], s38 offen sc1\n"
"s_mul_i32 s47, s65, 102400\n"
"s_add_u32 s47, s47, 74448896\n"
"s_mul_i32 s38, s43, 20480\n"
"s_add_u32 s46, s47, s38\n"
"s_cmp_eq_u32 s43, 0\n"
"s_cselect_b32 s38, 81920, 0\n"
"s_add_u32 s47, s47, s38\n"
"s_mul_i32 s45, %2, 15360\n"
"s_add_u32 s37, s46, s45\n"
"s_add_u32 s37, s37, 0\n"
"s_add_u32 s45, s45, %3\n"
"s_add_u32 m0, s45, 102400\n"
"s_nop 0\n"
"buffer_load_dwordx4 v251, s[96:99], s37 offen offset:0 sc1 lds\n"
"buffer_load_dwordx4 v251, s[96:99], s37 offen offset:1024 sc1 lds\n"
"buffer_load_dwordx4 v251, s[96:99], s37 offen offset:2048 sc1 lds\n"
"buffer_load_dwordx4 v251, s[96:99], s37 offen offset:3072 sc1 lds\n"
"v_cvt_pk_bf16_f32 v220, v0, v1\n"
"v_cvt_pk_bf16_f32 v221, v2, v3\n"
"v_cvt_pk_bf16_f32 v222, v4, v5\n"
"v_cvt_pk_bf16_f32 v223, v6, v7\n"
"v_cvt_pk_bf16_f32 v224, v8, v9\n"
"v_cvt_pk_bf16_f32 v225, v10, v11\n"
"v_cvt_pk_bf16_f32 v226, v12, v13\n"
"v_cvt_pk_bf16_f32 v227, v14, v15\n"
"v_cvt_pk_bf16_f32 v228, v16, v17\n"
"v_cvt_pk_bf16_f32 v229, v18, v19\n"
"s_waitcnt vmcnt(8)\n"
"s_barrier\n"
"ds_read_b128 a[160:163], v93 offset:0\n"
"ds_read_b128 a[164:167], v93 offset:1024\n"
"ds_read_b128 a[168:171], v93 offset:2048\n"
"ds_read_b128 a[172:175], v93 offset:3072\n"
"s_waitcnt lgkmcnt(3)\n"
"v_mfma_f32_16x16x32_bf16 v[72:75], a[160:163], a[176:179], 0\n"
"ds_read_b128 a[160:163], v93 offset:4096\n"
"s_waitcnt lgkmcnt(3)\n"
"v_mfma_f32_16x16x32_bf16 v[72:75], a[164:167], a[180:183], v[72:75]\n"
"ds_read_b128 a[164:167], v93 offset:5120\n"
"s_waitcnt lgkmcnt(3)\n"
"v_mfma_f32_16x16x32_bf16 v[72:75], a[168:171], a[184:187], v[72:75]\n"
"ds_read_b128 a[168:171], v93 offset:6144\n"
"s_waitcnt lgkmcnt(3)\n"
"v_mfma_f32_16x16x32_bf16 v[72:75], a[172:175], a[188:191], v[72:75]\n"
"ds_read_b128 a[172:175], v93 offset:7168\n"
"s_waitcnt lgkmcnt(3)\n"
"v_mfma_f32_16x16x32_bf16 v[72:75], a[160:163], a[192:195], v[72:75]\n"
"ds_read_b128 a[160:163], v93 offset:8192\n"
"s_waitcnt lgkmcnt(3)\n"
"v_mfma_f32_16x16x32_bf16 v[72:75], a[164:167], a[196:199], v[72:75]\n"
"ds_read_b128 a[164:167], v93 offset:9216\n"
"s_waitcnt lgkmcnt(3)\n"
"v_mfma_f32_16x16x32_bf16 v[72:75], a[168:171], a[200:203], v[72:75]\n"
"ds_read_b128 a[168:171], v93 offset:10240\n"
"s_waitcnt lgkmcnt(3)\n"
"v_mfma_f32_16x16x32_bf16 v[72:75], a[172:175], a[204:207], v[72:75]\n"
"ds_read_b128 a[172:175], v93 offset:11264\n"
"s_waitcnt lgkmcnt(3)\n"
"v_mfma_f32_16x16x32_bf16 v[72:75], a[160:163], a[208:211], v[72:75]\n"
"ds_read_b128 a[160:163], v93 offset:12288\n"
"s_waitcnt lgkmcnt(3)\n"
"v_mfma_f32_16x16x32_bf16 v[72:75], a[164:167], a[212:215], v[72:75]\n"
"ds_read_b128 a[164:167], v93 offset:13312\n"
"s_waitcnt lgkmcnt(3)\n"
"v_mfma_f32_16x16x32_bf16 v[72:75], a[168:171], a[216:219], v[72:75]\n"
"ds_read_b128 a[168:171], v93 offset:14336\n"
"s_waitcnt lgkmcnt(3)\n"
"v_mfma_f32_16x16x32_bf16 v[72:75], a[172:175], a[220:223], v[72:75]\n"
"ds_read_b128 a[172:175], v93 offset:15360\n"
"s_waitcnt lgkmcnt(3)\n"
"v_mfma_f32_16x16x32_bf16 v[72:75], a[160:163], a[224:227], v[72:75]\n"
"ds_read_b128 a[160:163], v93 offset:16384\n"
"s_waitcnt lgkmcnt(3)\n"
"v_mfma_f32_16x16x32_bf16 v[72:75], a[164:167], a[228:231], v[72:75]\n"
"ds_read_b128 a[164:167], v93 offset:17408\n"
"s_waitcnt lgkmcnt(3)\n"
"v_mfma_f32_16x16x32_bf16 v[72:75], a[168:171], a[232:235], v[72:75]\n"
"ds_read_b128 a[168:171], v93 offset:18432\n"
"s_waitcnt lgkmcnt(3)\n"
"v_mfma_f32_16x16x32_bf16 v[72:75], a[172:175], a[236:239], v[72:75]\n"
"ds_read_b128 a[172:175], v93 offset:19456\n"
"s_waitcnt lgkmcnt(3)\n"
"v_mfma_f32_16x16x32_bf16 v[72:75], a[160:163], a[240:243], v[72:75]\n"
"s_waitcnt lgkmcnt(2)\n"
"v_mfma_f32_16x16x32_bf16 v[72:75], a[164:167], a[244:247], v[72:75]\n"
"s_waitcnt lgkmcnt(1)\n"
"v_mfma_f32_16x16x32_bf16 v[72:75], a[168:171], a[248:251], v[72:75]\n"
"s_waitcnt lgkmcnt(0)\n"
"v_mfma_f32_16x16x32_bf16 v[72:75], a[172:175], a[252:255], v[72:75]\n"
"ds_read_b128 a[160:163], v93 offset:20480\n"
"ds_read_b128 a[164:167], v93 offset:21504\n"
"ds_read_b128 a[168:171], v93 offset:22528\n"
"ds_read_b128 a[172:175], v93 offset:23552\n"
"s_waitcnt lgkmcnt(3)\n"
"v_mfma_f32_16x16x32_bf16 v[76:79], a[160:163], a[176:179], 0\n"
"PG20_%=:\n"
"s_cbranch_execz RPG20_%=\n"
"ds_read_b128 a[160:163], v93 offset:24576\n"
"s_waitcnt lgkmcnt(3)\n"
"v_mfma_f32_16x16x32_bf16 v[76:79], a[164:167], a[180:183], v[76:79]\n"
"ds_read_b128 a[164:167], v93 offset:25600\n"
"s_waitcnt lgkmcnt(3)\n"
"v_mfma_f32_16x16x32_bf16 v[76:79], a[168:171], a[184:187], v[76:79]\n"
"ds_read_b128 a[168:171], v93 offset:26624\n"
"s_waitcnt lgkmcnt(3)\n"
"v_mfma_f32_16x16x32_bf16 v[76:79], a[172:175], a[188:191], v[76:79]\n"
"ds_read_b128 a[172:175], v93 offset:27648\n"
"s_waitcnt lgkmcnt(3)\n"
"v_mfma_f32_16x16x32_bf16 v[76:79], a[160:163], a[192:195], v[76:79]\n"
"ds_read_b128 a[160:163], v93 offset:28672\n"
"s_waitcnt lgkmcnt(3)\n"
"v_mfma_f32_16x16x32_bf16 v[76:79], a[164:167], a[196:199], v[76:79]\n"
"ds_read_b128 a[164:167], v93 offset:29696\n"
"s_waitcnt lgkmcnt(3)\n"
"v_mfma_f32_16x16x32_bf16 v[76:79], a[168:171], a[200:203], v[76:79]\n"
"ds_read_b128 a[168:171], v93 offset:30720\n"
"s_waitcnt lgkmcnt(3)\n"
"v_mfma_f32_16x16x32_bf16 v[76:79], a[172:175], a[204:207], v[76:79]\n"
"ds_read_b128 a[172:175], v93 offset:31744\n"
"s_waitcnt lgkmcnt(3)\n"
"v_mfma_f32_16x16x32_bf16 v[76:79], a[160:163], a[208:211], v[76:79]\n"
"ds_read_b128 a[160:163], v93 offset:32768\n"
"s_waitcnt lgkmcnt(3)\n"
"v_mfma_f32_16x16x32_bf16 v[76:79], a[164:167], a[212:215], v[76:79]\n"
"ds_read_b128 a[164:167], v93 offset:33792\n"
"s_waitcnt lgkmcnt(3)\n"
"v_mfma_f32_16x16x32_bf16 v[76:79], a[168:171], a[216:219], v[76:79]\n"
"ds_read_b128 a[168:171], v93 offset:34816\n"
"s_waitcnt lgkmcnt(3)\n"
"v_mfma_f32_16x16x32_bf16 v[76:79], a[172:175], a[220:223], v[76:79]\n"
"ds_read_b128 a[172:175], v93 offset:35840\n"
"s_waitcnt lgkmcnt(3)\n"
"v_mfma_f32_16x16x32_bf16 v[76:79], a[160:163], a[224:227], v[76:79]\n"
"ds_read_b128 a[160:163], v93 offset:36864\n"
"s_waitcnt lgkmcnt(3)\n"
"v_mfma_f32_16x16x32_bf16 v[76:79], a[164:167], a[228:231], v[76:79]\n"
"ds_read_b128 a[164:167], v93 offset:37888\n"
"s_waitcnt lgkmcnt(3)\n"
"v_mfma_f32_16x16x32_bf16 v[76:79], a[168:171], a[232:235], v[76:79]\n"
"ds_read_b128 a[168:171], v93 offset:38912\n"
"s_waitcnt lgkmcnt(3)\n"
"v_mfma_f32_16x16x32_bf16 v[76:79], a[172:175], a[236:239], v[76:79]\n"
"ds_read_b128 a[172:175], v93 offset:39936\n"
"s_waitcnt lgkmcnt(3)\n"
"v_mfma_f32_16x16x32_bf16 v[76:79], a[160:163], a[240:243], v[76:79]\n"
"s_waitcnt lgkmcnt(2)\n"
"v_mfma_f32_16x16x32_bf16 v[76:79], a[164:167], a[244:247], v[76:79]\n"
"s_waitcnt lgkmcnt(1)\n"
"v_mfma_f32_16x16x32_bf16 v[76:79], a[168:171], a[248:251], v[76:79]\n"
"s_waitcnt lgkmcnt(0)\n"
"v_mfma_f32_16x16x32_bf16 v[76:79], a[172:175], a[252:255], v[76:79]\n"
"ds_read_b128 a[160:163], v93 offset:40960\n"
"ds_read_b128 a[164:167], v93 offset:41984\n"
"ds_read_b128 a[168:171], v93 offset:43008\n"
"ds_read_b128 a[172:175], v93 offset:44032\n"
"s_waitcnt lgkmcnt(3)\n"
"v_mfma_f32_16x16x32_bf16 v[80:83], a[160:163], a[176:179], 0\n"
"ds_read_b128 a[160:163], v93 offset:45056\n"
"s_waitcnt lgkmcnt(3)\n"
"v_mfma_f32_16x16x32_bf16 v[80:83], a[164:167], a[180:183], v[80:83]\n"
"ds_read_b128 a[164:167], v93 offset:46080\n"
"s_waitcnt lgkmcnt(3)\n"
"v_mfma_f32_16x16x32_bf16 v[80:83], a[168:171], a[184:187], v[80:83]\n"
"ds_read_b128 a[168:171], v93 offset:47104\n"
"s_waitcnt lgkmcnt(3)\n"
"v_mfma_f32_16x16x32_bf16 v[80:83], a[172:175], a[188:191], v[80:83]\n"
"ds_read_b128 a[172:175], v93 offset:48128\n"
"s_waitcnt lgkmcnt(3)\n"
"v_mfma_f32_16x16x32_bf16 v[80:83], a[160:163], a[192:195], v[80:83]\n"
"ds_read_b128 a[160:163], v93 offset:49152\n"
"s_waitcnt lgkmcnt(3)\n"
"v_mfma_f32_16x16x32_bf16 v[80:83], a[164:167], a[196:199], v[80:83]\n"
"ds_read_b128 a[164:167], v93 offset:50176\n"
"s_waitcnt lgkmcnt(3)\n"
"v_mfma_f32_16x16x32_bf16 v[80:83], a[168:171], a[200:203], v[80:83]\n"
"ds_read_b128 a[168:171], v93 offset:51200\n"
"s_waitcnt lgkmcnt(3)\n"
"v_mfma_f32_16x16x32_bf16 v[80:83], a[172:175], a[204:207], v[80:83]\n"
"ds_read_b128 a[172:175], v93 offset:52224\n"
"s_waitcnt lgkmcnt(3)\n"
"v_mfma_f32_16x16x32_bf16 v[80:83], a[160:163], a[208:211], v[80:83]\n"
"ds_read_b128 a[160:163], v93 offset:53248\n"
"s_waitcnt lgkmcnt(3)\n"
"v_mfma_f32_16x16x32_bf16 v[80:83], a[164:167], a[212:215], v[80:83]\n"
"ds_read_b128 a[164:167], v93 offset:54272\n"
"s_waitcnt lgkmcnt(3)\n"
"v_mfma_f32_16x16x32_bf16 v[80:83], a[168:171], a[216:219], v[80:83]\n"
"ds_read_b128 a[168:171], v93 offset:55296\n"
"s_waitcnt lgkmcnt(3)\n"
"v_mfma_f32_16x16x32_bf16 v[80:83], a[172:175], a[220:223], v[80:83]\n"
"ds_read_b128 a[172:175], v93 offset:56320\n"
"s_waitcnt lgkmcnt(3)\n"
"v_mfma_f32_16x16x32_bf16 v[80:83], a[160:163], a[224:227], v[80:83]\n"
"ds_read_b128 a[160:163], v93 offset:57344\n"
"s_waitcnt lgkmcnt(3)\n"
"v_mfma_f32_16x16x32_bf16 v[80:83], a[164:167], a[228:231], v[80:83]\n"
"ds_read_b128 a[164:167], v93 offset:58368\n"
"s_waitcnt lgkmcnt(3)\n"
"v_mfma_f32_16x16x32_bf16 v[80:83], a[168:171], a[232:235], v[80:83]\n"
"ds_read_b128 a[168:171], v93 offset:59392\n"
"s_waitcnt lgkmcnt(3)\n"
"v_mfma_f32_16x16x32_bf16 v[80:83], a[172:175], a[236:239], v[80:83]\n"
"ds_read_b128 a[172:175], v93 offset:60416\n"
"s_waitcnt lgkmcnt(3)\n"
"v_mfma_f32_16x16x32_bf16 v[80:83], a[160:163], a[240:243], v[80:83]\n"
"s_waitcnt lgkmcnt(2)\n"
"v_mfma_f32_16x16x32_bf16 v[80:83], a[164:167], a[244:247], v[80:83]\n"
"s_waitcnt lgkmcnt(1)\n"
"v_mfma_f32_16x16x32_bf16 v[80:83], a[168:171], a[248:251], v[80:83]\n"
"s_waitcnt lgkmcnt(0)\n"
"v_mfma_f32_16x16x32_bf16 v[80:83], a[172:175], a[252:255], v[80:83]\n"
"ds_read_b128 a[160:163], v94 offset:0\n"
"ds_read_b128 a[164:167], v94 offset:1024\n"
"ds_read_b128 a[168:171], v94 offset:2048\n"
"ds_read_b128 a[172:175], v94 offset:3072\n"
"s_waitcnt lgkmcnt(3)\n"
"v_mfma_f32_16x16x32_bf16 v[84:87], a[160:163], a[176:179], 0\n"
"ds_read_b128 a[160:163], v94 offset:4096\n"
"s_waitcnt lgkmcnt(3)\n"
"v_mfma_f32_16x16x32_bf16 v[84:87], a[164:167], a[180:183], v[84:87]\n"
"ds_read_b128 a[164:167], v94 offset:5120\n"
"s_waitcnt lgkmcnt(3)\n"
"v_mfma_f32_16x16x32_bf16 v[84:87], a[168:171], a[184:187], v[84:87]\n"
"ds_read_b128 a[168:171], v94 offset:6144\n"
"s_waitcnt lgkmcnt(3)\n"
"v_mfma_f32_16x16x32_bf16 v[84:87], a[172:175], a[188:191], v[84:87]\n"
"ds_read_b128 a[172:175], v94 offset:7168\n"
"s_waitcnt lgkmcnt(3)\n"
"v_mfma_f32_16x16x32_bf16 v[84:87], a[160:163], a[192:195], v[84:87]\n"
"ds_read_b128 a[160:163], v94 offset:8192\n"
"s_waitcnt lgkmcnt(3)\n"
"v_mfma_f32_16x16x32_bf16 v[84:87], a[164:167], a[196:199], v[84:87]\n"
"ds_read_b128 a[164:167], v94 offset:9216\n"
"s_waitcnt lgkmcnt(3)\n"
"v_mfma_f32_16x16x32_bf16 v[84:87], a[168:171], a[200:203], v[84:87]\n"
"ds_read_b128 a[168:171], v94 offset:10240\n"
"s_waitcnt lgkmcnt(3)\n"
"v_mfma_f32_16x16x32_bf16 v[84:87], a[172:175], a[204:207], v[84:87]\n"
"ds_read_b128 a[172:175], v94 offset:11264\n"
"s_waitcnt lgkmcnt(3)\n"
"v_mfma_f32_16x16x32_bf16 v[84:87], a[160:163], a[208:211], v[84:87]\n"
"ds_read_b128 a[160:163], v94 offset:12288\n"
"s_waitcnt lgkmcnt(3)\n"
"v_mfma_f32_16x16x32_bf16 v[84:87], a[164:167], a[212:215], v[84:87]\n"
"ds_read_b128 a[164:167], v94 offset:13312\n"
"s_waitcnt lgkmcnt(3)\n"
"v_mfma_f32_16x16x32_bf16 v[84:87], a[168:171], a[216:219], v[84:87]\n"
"ds_read_b128 a[168:171], v94 offset:14336\n"
"s_waitcnt lgkmcnt(3)\n"
"v_mfma_f32_16x16x32_bf16 v[84:87], a[172:175], a[220:223], v[84:87]\n"
"ds_read_b128 a[172:175], v94 offset:15360\n"
"s_waitcnt lgkmcnt(3)\n"
"v_mfma_f32_16x16x32_bf16 v[84:87], a[160:163], a[224:227], v[84:87]\n"
"ds_read_b128 a[160:163], v94 offset:16384\n"
"s_waitcnt lgkmcnt(3)\n"
"v_mfma_f32_16x16x32_bf16 v[84:87], a[164:167], a[228:231], v[84:87]\n"
"ds_read_b128 a[164:167], v94 offset:17408\n"
"s_waitcnt lgkmcnt(3)\n"
"v_mfma_f32_16x16x32_bf16 v[84:87], a[168:171], a[232:235], v[84:87]\n"
"ds_read_b128 a[168:171], v94 offset:18432\n"
"s_waitcnt lgkmcnt(3)\n"
"v_mfma_f32_16x16x32_bf16 v[84:87], a[172:175], a[236:239], v[84:87]\n"
"ds_read_b128 a[172:175], v94 offset:19456\n"
"s_waitcnt lgkmcnt(3)\n"
"v_mfma_f32_16x16x32_bf16 v[84:87], a[160:163], a[240:243], v[84:87]\n"
"s_waitcnt lgkmcnt(2)\n"
"v_mfma_f32_16x16x32_bf16 v[84:87], a[164:167], a[244:247], v[84:87]\n"
"s_waitcnt lgkmcnt(1)\n"
"v_mfma_f32_16x16x32_bf16 v[84:87], a[168:171], a[248:251], v[84:87]\n"
"s_waitcnt lgkmcnt(0)\n"
"v_mfma_f32_16x16x32_bf16 v[84:87], a[172:175], a[252:255], v[84:87]\n"
"ds_read_b128 a[160:163], v94 offset:20480\n"
"ds_read_b128 a[164:167], v94 offset:21504\n"
"ds_read_b128 a[168:171], v94 offset:22528\n"
"ds_read_b128 a[172:175], v94 offset:23552\n"
"s_waitcnt lgkmcnt(3)\n"
"v_mfma_f32_16x16x32_bf16 v[88:91], a[160:163], a[176:179], 0\n"
"ds_read_b128 a[160:163], v94 offset:24576\n"
"s_waitcnt lgkmcnt(3)\n"
"v_mfma_f32_16x16x32_bf16 v[88:91], a[164:167], a[180:183], v[88:91]\n"
"ds_read_b128 a[164:167], v94 offset:25600\n"
"s_waitcnt lgkmcnt(3)\n"
"v_mfma_f32_16x16x32_bf16 v[88:91], a[168:171], a[184:187], v[88:91]\n"
"ds_read_b128 a[168:171], v94 offset:26624\n"
"s_waitcnt lgkmcnt(3)\n"
"v_mfma_f32_16x16x32_bf16 v[88:91], a[172:175], a[188:191], v[88:91]\n"
"ds_read_b128 a[172:175], v94 offset:27648\n"
"s_waitcnt lgkmcnt(3)\n"
"v_mfma_f32_16x16x32_bf16 v[88:91], a[160:163], a[192:195], v[88:91]\n"
"ds_read_b128 a[160:163], v94 offset:28672\n"
"s_waitcnt lgkmcnt(3)\n"
"v_mfma_f32_16x16x32_bf16 v[88:91], a[164:167], a[196:199], v[88:91]\n"
"ds_read_b128 a[164:167], v94 offset:29696\n"
"s_waitcnt lgkmcnt(3)\n"
"v_mfma_f32_16x16x32_bf16 v[88:91], a[168:171], a[200:203], v[88:91]\n"
"ds_read_b128 a[168:171], v94 offset:30720\n"
"s_waitcnt lgkmcnt(3)\n"
"v_mfma_f32_16x16x32_bf16 v[88:91], a[172:175], a[204:207], v[88:91]\n"
"ds_read_b128 a[172:175], v94 offset:31744\n"
"s_waitcnt lgkmcnt(3)\n"
"v_mfma_f32_16x16x32_bf16 v[88:91], a[160:163], a[208:211], v[88:91]\n"
"ds_read_b128 a[160:163], v94 offset:32768\n"
"s_waitcnt lgkmcnt(3)\n"
"v_mfma_f32_16x16x32_bf16 v[88:91], a[164:167], a[212:215], v[88:91]\n"
"ds_read_b128 a[164:167], v94 offset:33792\n"
"s_waitcnt lgkmcnt(3)\n"
"v_mfma_f32_16x16x32_bf16 v[88:91], a[168:171], a[216:219], v[88:91]\n"
"ds_read_b128 a[168:171], v94 offset:34816\n"
"s_waitcnt lgkmcnt(3)\n"
"v_mfma_f32_16x16x32_bf16 v[88:91], a[172:175], a[220:223], v[88:91]\n"
"ds_read_b128 a[172:175], v94 offset:35840\n"
"s_waitcnt lgkmcnt(3)\n"
"v_mfma_f32_16x16x32_bf16 v[88:91], a[160:163], a[224:227], v[88:91]\n"
"ds_read_b128 a[160:163], v94 offset:36864\n"
"s_waitcnt lgkmcnt(3)\n"
"v_mfma_f32_16x16x32_bf16 v[88:91], a[164:167], a[228:231], v[88:91]\n"
"ds_read_b128 a[164:167], v94 offset:37888\n"
"s_waitcnt lgkmcnt(3)\n"
"v_mfma_f32_16x16x32_bf16 v[88:91], a[168:171], a[232:235], v[88:91]\n"
"ds_read_b128 a[168:171], v94 offset:38912\n"
"s_waitcnt lgkmcnt(3)\n"
"v_mfma_f32_16x16x32_bf16 v[88:91], a[172:175], a[236:239], v[88:91]\n"
"ds_read_b128 a[172:175], v94 offset:39936\n"
"s_waitcnt lgkmcnt(3)\n"
"v_mfma_f32_16x16x32_bf16 v[88:91], a[160:163], a[240:243], v[88:91]\n"
"s_waitcnt lgkmcnt(2)\n"
"v_mfma_f32_16x16x32_bf16 v[88:91], a[164:167], a[244:247], v[88:91]\n"
"s_waitcnt lgkmcnt(1)\n"
"v_mfma_f32_16x16x32_bf16 v[88:91], a[168:171], a[248:251], v[88:91]\n"
"s_waitcnt lgkmcnt(0)\n"
"v_mfma_f32_16x16x32_bf16 v[88:91], a[172:175], a[252:255], v[88:91]\n"
"v_mov_b32_dpp v124, v72 row_newbcast:0 row_mask:0xf bank_mask:0xf\n"
"v_mov_b32_dpp v125, v73 row_newbcast:0 row_mask:0xf bank_mask:0xf\n"
"v_mov_b32_dpp v126, v74 row_newbcast:0 row_mask:0xf bank_mask:0xf\n"
"v_mov_b32_dpp v127, v75 row_newbcast:0 row_mask:0xf bank_mask:0xf\n"
"v_mov_b32_dpp v128, v76 row_newbcast:0 row_mask:0xf bank_mask:0xf\n"
"v_mov_b32_dpp v129, v77 row_newbcast:0 row_mask:0xf bank_mask:0xf\n"
"v_mov_b32_dpp v130, v78 row_newbcast:0 row_mask:0xf bank_mask:0xf\n"
"v_mov_b32_dpp v131, v79 row_newbcast:0 row_mask:0xf bank_mask:0xf\n"
"v_mov_b32_dpp v132, v80 row_newbcast:0 row_mask:0xf bank_mask:0xf\n"
"v_mov_b32_dpp v133, v81 row_newbcast:0 row_mask:0xf bank_mask:0xf\n"
"v_mov_b32_dpp v134, v82 row_newbcast:0 row_mask:0xf bank_mask:0xf\n"
"v_mov_b32_dpp v135, v83 row_newbcast:0 row_mask:0xf bank_mask:0xf\n"
"v_mov_b32_dpp v136, v84 row_newbcast:0 row_mask:0xf bank_mask:0xf\n"
"v_mov_b32_dpp v137, v85 row_newbcast:0 row_mask:0xf bank_mask:0xf\n"
"v_mov_b32_dpp v138, v86 row_newbcast:0 row_mask:0xf bank_mask:0xf\n"
"v_mov_b32_dpp v139, v87 row_newbcast:0 row_mask:0xf bank_mask:0xf\n"
"v_mov_b32_dpp v140, v88 row_newbcast:0 row_mask:0xf bank_mask:0xf\n"
"v_mov_b32_dpp v141, v89 row_newbcast:0 row_mask:0xf bank_mask:0xf\n"
"v_mov_b32_dpp v142, v90 row_newbcast:0 row_mask:0xf bank_mask:0xf\n"
"v_mov_b32_dpp v143, v91 row_newbcast:0 row_mask:0xf bank_mask:0xf\n"
"v_mov_b32_dpp v144, v72 row_newbcast:1 row_mask:0xf bank_mask:0xf\n"
"v_mov_b32_dpp v145, v73 row_newbcast:1 row_mask:0xf bank_mask:0xf\n"
"v_mov_b32_dpp v146, v74 row_newbcast:1 row_mask:0xf bank_mask:0xf\n"
"v_mov_b32_dpp v147, v75 row_newbcast:1 row_mask:0xf bank_mask:0xf\n"
"v_mov_b32_dpp v148, v76 row_newbcast:1 row_mask:0xf bank_mask:0xf\n"
"v_mov_b32_dpp v149, v77 row_newbcast:1 row_mask:0xf bank_mask:0xf\n"
"v_mov_b32_dpp v150, v78 row_newbcast:1 row_mask:0xf bank_mask:0xf\n"
"v_mov_b32_dpp v151, v79 row_newbcast:1 row_mask:0xf bank_mask:0xf\n"
"v_mov_b32_dpp v152, v80 row_newbcast:1 row_mask:0xf bank_mask:0xf\n"
"v_mov_b32_dpp v153, v81 row_newbcast:1 row_mask:0xf bank_mask:0xf\n"
"v_mov_b32_dpp v154, v82 row_newbcast:1 row_mask:0xf bank_mask:0xf\n"
"v_mov_b32_dpp v155, v83 row_newbcast:1 row_mask:0xf bank_mask:0xf\n"
"v_mov_b32_dpp v156, v84 row_newbcast:1 row_mask:0xf bank_mask:0xf\n"
"v_mov_b32_dpp v157, v85 row_newbcast:1 row_mask:0xf bank_mask:0xf\n"
"v_mov_b32_dpp v158, v86 row_newbcast:1 row_mask:0xf bank_mask:0xf\n"
"v_mov_b32_dpp v159, v87 row_newbcast:1 row_mask:0xf bank_mask:0xf\n"
"v_mov_b32_dpp v160, v88 row_newbcast:1 row_mask:0xf bank_mask:0xf\n"
"v_mov_b32_dpp v161, v89 row_newbcast:1 row_mask:0xf bank_mask:0xf\n"
"v_mov_b32_dpp v162, v90 row_newbcast:1 row_mask:0xf bank_mask:0xf\n"
"v_mov_b32_dpp v163, v91 row_newbcast:1 row_mask:0xf bank_mask:0xf\n"
"v_mov_b32_dpp v164, v72 row_newbcast:2 row_mask:0xf bank_mask:0xf\n"
"v_mov_b32_dpp v165, v73 row_newbcast:2 row_mask:0xf bank_mask:0xf\n"
"v_mov_b32_dpp v166, v74 row_newbcast:2 row_mask:0xf bank_mask:0xf\n"
"v_mov_b32_dpp v167, v75 row_newbcast:2 row_mask:0xf bank_mask:0xf\n"
"v_mov_b32_dpp v168, v76 row_newbcast:2 row_mask:0xf bank_mask:0xf\n"
"v_mov_b32_dpp v169, v77 row_newbcast:2 row_mask:0xf bank_mask:0xf\n"
"v_mov_b32_dpp v170, v78 row_newbcast:2 row_mask:0xf bank_mask:0xf\n"
"v_mov_b32_dpp v171, v79 row_newbcast:2 row_mask:0xf bank_mask:0xf\n"
"v_mov_b32_dpp v172, v80 row_newbcast:2 row_mask:0xf bank_mask:0xf\n"
"v_mov_b32_dpp v173, v81 row_newbcast:2 row_mask:0xf bank_mask:0xf\n"
"v_mov_b32_dpp v174, v82 row_newbcast:2 row_mask:0xf bank_mask:0xf\n"
"v_mov_b32_dpp v175, v83 row_newbcast:2 row_mask:0xf bank_mask:0xf\n"
"v_mov_b32_dpp v176, v84 row_newbcast:2 row_mask:0xf bank_mask:0xf\n"
"v_mov_b32_dpp v177, v85 row_newbcast:2 row_mask:0xf bank_mask:0xf\n"
"v_mov_b32_dpp v178, v86 row_newbcast:2 row_mask:0xf bank_mask:0xf\n"
"v_mov_b32_dpp v179, v87 row_newbcast:2 row_mask:0xf bank_mask:0xf\n"
"v_mov_b32_dpp v180, v88 row_newbcast:2 row_mask:0xf bank_mask:0xf\n"
"v_mov_b32_dpp v181, v89 row_newbcast:2 row_mask:0xf bank_mask:0xf\n"
"v_mov_b32_dpp v182, v90 row_newbcast:2 row_mask:0xf bank_mask:0xf\n"
"v_mov_b32_dpp v183, v91 row_newbcast:2 row_mask:0xf bank_mask:0xf\n"
"s_lshr_b32 s38, s65, 2\n"
"s_add_u32 s38, s38, 1\n"
"s_sub_u32 s39, 0, s38\n"
"s_bitcmp1_b32 s65, 1\n"
"s_cselect_b32 s38, s39, s38\n"
"s_lshl_b32 s39, s42, 4\n"
"s_add_u32 s38, s38, s39\n"
"v_add_u32 v232, s38, v249\n"
"s_movk_i32 s39, 2048\n"
"v_cmp_gt_u32 s[78:79], s39, v232\n"
"v_and_b32 v233, 15, v232\n"
"v_bfe_u32 v234, v232, 4, 7\n"
"v_mul_u32_u24 v234, 20480, v234\n"
"v_lshl_add_u32 v204, v248, 4, v233\n"
"v_lshl_add_u32 v204, v204, 4, v234\n"
"v_lshl_add_u32 v205, s43, 3, v204\n"
"v_lshl_add_u32 v203, s42, 4, v249\n"
"v_lshlrev_b32 v203, 8, v203\n"
"v_lshl_add_u32 v203, v248, 6, v203\n"
"s_mov_b32 s60, 122159104\n"
"s_mov_b64 exec, 0\n"
"s_cmp_lg_u32 %2, 0\n"
"s_cbranch_scc1 TWD0_%=\n"
"s_branch PG21_%=\n"
"RPG21_%=:\n"
"s_nop 0\n"
"s_nop 0\n"
"s_nop 0\n"
"s_nop 0\n"
"s_nop 0\n"
"TWD0_%=:\n"
"s_cmp_lg_u32 %2, 1\n"
"s_cbranch_scc1 TWD1_%=\n"
"s_branch PG22_%=\n"
"RPG22_%=:\n"
"s_nop 0\n"
"s_nop 0\n"
"s_nop 0\n"
"s_nop 0\n"
"s_nop 0\n"
"TWD1_%=:\n"
"s_cmp_lg_u32 %2, 2\n"
"s_cbranch_scc1 TWD2_%=\n"
"s_nop 0\n"
"s_nop 0\n"
"s_nop 0\n"
"s_nop 0\n"
"s_nop 0\n"
"s_nop 0\n"
"TWD2_%=:\n"
"s_cmp_lg_u32 %2, 3\n"
"s_cbranch_scc1 TWD3_%=\n"
"s_nop 0\n"
"s_nop 0\n"
"s_nop 0\n"
"s_nop 0\n"
"s_nop 0\n"
"s_nop 0\n"
"TWD3_%=:\n"
"s_mov_b64 exec, -1\n"
"T_G1_%=:\n"
"s_mov_b32 s63, 3\n"
"buffer_load_dwordx4 v[184:187], v203, s[96:99], s60 offen offset:0 sc1\n"
"buffer_load_dwordx4 v[188:191], v203, s[96:99], s60 offen offset:16 sc1\n"
"buffer_load_dwordx4 v[192:195], v203, s[96:99], s60 offen offset:32 sc1\n"
"buffer_load_dwordx4 v[196:199], v203, s[96:99], s60 offen offset:48 sc1\n"
"s_add_u32 m0, s45, 106496\n"
"s_add_u32 s37, s37, 4096\n"
"buffer_load_dwordx4 v251, s[96:99], s37 offen offset:0 sc1 lds\n"
"buffer_load_dwordx4 v251, s[96:99], s37 offen offset:1024 sc1 lds\n"
"buffer_load_dwordx4 v251, s[96:99], s37 offen offset:2048 sc1 lds\n"
"buffer_load_dwordx4 v251, s[96:99], s37 offen offset:3072 sc1 lds\n"
"s_add_u32 m0, s45, 110592\n"
"s_add_u32 s37, s37, 4096\n"
"buffer_load_dwordx4 v251, s[96:99], s37 offen offset:0 sc1 lds\n"
"buffer_load_dwordx4 v251, s[96:99], s37 offen offset:1024 sc1 lds\n"
"buffer_load_dwordx4 v251, s[96:99], s37 offen offset:2048 sc1 lds\n"
"buffer_load_dwordx4 v251, s[96:99], s37 offen offset:3072 sc1 lds\n"
"s_add_u32 m0, s45, 114688\n"
"s_add_u32 s37, s37, 4096\n"
"buffer_load_dwordx4 v251, s[96:99], s37 offen offset:0 sc1 lds\n"
"buffer_load_dwordx4 v251, s[96:99], s37 offen offset:1024 sc1 lds\n"
"buffer_load_dwordx4 v251, s[96:99], s37 offen offset:2048 sc1 lds\n"
"s_waitcnt vmcnt(14)\n"
"v_max_u32 v242, v184, v185\n"
"v_max3_u32 v242, v242, v186, v187\n"
"s_waitcnt vmcnt(13)\n"
"v_max3_u32 v242, v242, v188, v189\n"
"v_max3_u32 v242, v242, v190, v191\n"
"s_waitcnt vmcnt(12)\n"
"v_max3_u32 v242, v242, v192, v193\n"
"v_max3_u32 v242, v242, v194, v195\n"
"s_waitcnt vmcnt(11)\n"
"v_max3_u32 v242, v242, v196, v197\n"
"v_max3_u32 v242, v242, v198, v199\n"
"v_cmp_eq_u32 vcc, -1, v242\n"
"s_nop 0\n"
"s_cbranch_vccnz SLOW_G1_%=\n"
"OK_G1_%=:\n"
"s_mul_i32 s36, s42, 20480\n"
"s_add_u32 s36, s36, 106954752\n"
"buffer_load_dwordx4 a[0:3], v251, s[96:99], s36 offen offset:0 sc1\n"
"buffer_load_dwordx4 a[4:7], v251, s[96:99], s36 offen offset:1024 sc1\n"
"buffer_load_dwordx4 a[8:11], v251, s[96:99], s36 offen offset:2048 sc1\n"
"buffer_load_dwordx4 a[12:15], v251, s[96:99], s36 offen offset:3072 sc1\n"
"s_add_u32 s36, s36, 4096\n"
"buffer_load_dwordx4 a[16:19], v251, s[96:99], s36 offen offset:0 sc1\n"
"buffer_load_dwordx4 a[20:23], v251, s[96:99], s36 offen offset:1024 sc1\n"
"buffer_load_dwordx4 a[24:27], v251, s[96:99], s36 offen offset:2048 sc1\n"
"buffer_load_dwordx4 a[28:31], v251, s[96:99], s36 offen offset:3072 sc1\n"
"s_add_u32 s36, s36, 4096\n"
"buffer_load_dwordx4 a[32:35], v251, s[96:99], s36 offen offset:0 sc1\n"
"buffer_load_dwordx4 a[36:39], v251, s[96:99], s36 offen offset:1024 sc1\n"
"buffer_load_dwordx4 a[40:43], v251, s[96:99], s36 offen offset:2048 sc1\n"
"buffer_load_dwordx4 a[44:47], v251, s[96:99], s36 offen offset:3072 sc1\n"
"s_add_u32 s36, s36, 4096\n"
"buffer_load_dwordx4 a[48:51], v251, s[96:99], s36 offen offset:0 sc1\n"
"buffer_load_dwordx4 a[52:55], v251, s[96:99], s36 offen offset:1024 sc1\n"
"buffer_load_dwordx4 a[56:59], v251, s[96:99], s36 offen offset:2048 sc1\n"
"buffer_load_dwordx4 a[60:63], v251, s[96:99], s36 offen offset:3072 sc1\n"
"s_add_u32 s36, s36, 4096\n"
"buffer_load_dwordx4 a[64:67], v251, s[96:99], s36 offen offset:0 sc1\n"
"buffer_load_dwordx4 a[68:71], v251, s[96:99], s36 offen offset:1024 sc1\n"
"buffer_load_dwordx4 a[72:75], v251, s[96:99], s36 offen offset:2048 sc1\n"
"buffer_load_dwordx4 a[76:79], v251, s[96:99], s36 offen offset:3072 sc1\n"
"v_add_f32 v96, v184, v192\n"
"v_add_f32 v97, v185, v193\n"
"v_add_f32 v98, v186, v194\n"
"v_add_f32 v99, v187, v195\n"
"v_add_f32 v100, v188, v196\n"
"v_add_f32 v101, v189, v197\n"
"v_mov_b32 v232, v96\n"
"v_mov_b32 v233, v97\n"
"v_mov_b32 v234, v98\n"
"v_mov_b32 v235, v99\n"
"v_mov_b32 v236, v100\n"
"v_mov_b32 v237, v101\n"
"v_permlane16_swap_b32 v96, v232\n"
"v_permlane16_swap_b32 v97, v233\n"
"v_permlane16_swap_b32 v98, v234\n"
"v_permlane16_swap_b32 v99, v235\n"
"v_permlane16_swap_b32 v100, v236\n"
"v_permlane16_swap_b32 v101, v237\n"
"v_add_f32 v96, v96, v232\n"
"v_add_f32 v97, v97, v233\n"
"v_add_f32 v98, v98, v234\n"
"v_add_f32 v99, v99, v235\n"
"v_add_f32 v100, v100, v236\n"
"v_add_f32 v101, v101, v237\n"
"v_mov_b32 v232, v96\n"
"v_mov_b32 v233, v97\n"
"v_mov_b32 v234, v98\n"
"v_mov_b32 v235, v99\n"
"v_mov_b32 v236, v100\n"
"v_mov_b32 v237, v101\n"
"v_permlane32_swap_b32 v96, v232\n"
"v_permlane32_swap_b32 v97, v233\n"
"v_permlane32_swap_b32 v98, v234\n"
"v_permlane32_swap_b32 v99, v235\n"
"v_permlane32_swap_b32 v100, v236\n"
"v_permlane32_swap_b32 v101, v237\n"
"v_add_f32 v96, v96, v232\n"
"v_add_f32 v97, v97, v233\n"
"v_add_f32 v98, v98, v234\n"
"v_add_f32 v99, v99, v235\n"
"v_add_f32 v100, v100, v236\n"
"v_add_f32 v101, v101, v237\n"
"v_mul_f32 v230, 0x3acccccd, v96\n"
"v_mul_f32 v231, 0x3acccccd, v97\n"
"v_fma_f32 v231, -v230, v230, v231\n"
"v_max_f32 v231, 0, v231\n"
"v_add_f32 v231, 0x3727c5ac, v231\n"
"v_rsq_f32 v231, v231\n"
"v_fma_f32 v232, -v230, s51, v98\n"
"v_mov_b32 v234, s52\n"
"v_fma_f32 v232, v232, v231, v234\n"
"v_mul_f32 v234, s53, v230\n"
"v_fma_f32 v234, -2.0, v100, v234\n"
"v_fma_f32 v234, v234, v230, v99\n"
"v_fma_f32 v235, -v230, s54, v101\n"
"v_add_f32 v235, v235, v235\n"
"v_fma_f32 v235, v234, v231, v235\n"
"v_mov_b32 v234, s55\n"
"v_fma_f32 v233, v235, v231, v234\n"
"v_mul_f32 v234, 0x3acccccd, v232\n"
"v_mul_f32 v20, 0x3acccccd, v233\n"
"v_fma_f32 v20, -v234, v234, v20\n"
"v_max_f32 v20, 0, v20\n"
"v_add_f32 v20, 0x3727c5ac, v20\n"
"v_rsq_f32 v20, v20\n"
"s_nop 0\n"
"v_mul_f32 v26, v231, v20\n"
"v_mul_f32 v24, v234, v20\n"
"v_mul_f32 v22, v230, v26\n"
"v_sub_f32 v24, 0, v24\n"
"v_sub_f32 v22, 0, v22\n"
"v_mov_b32 v21, v20\n"
"v_mov_b32 v27, v26\n"
"v_mov_b32 v25, v24\n"
"v_mov_b32 v23, v22\n"
"v_pk_mul_f32 v[104:105], v[144:145], v[20:21]\n"
"v_pk_mul_f32 v[106:107], v[146:147], v[20:21]\n"
"v_pk_mul_f32 v[108:109], v[148:149], v[20:21]\n"
"v_pk_mul_f32 v[110:111], v[150:151], v[20:21]\n"
"v_pk_mul_f32 v[112:113], v[152:153], v[20:21]\n"
"v_pk_mul_f32 v[114:115], v[154:155], v[20:21]\n"
"v_pk_mul_f32 v[116:117], v[156:157], v[20:21]\n"
"v_pk_mul_f32 v[118:119], v[158:159], v[20:21]\n"
"v_pk_mul_f32 v[120:121], v[160:161], v[20:21]\n"
"v_pk_mul_f32 v[122:123], v[162:163], v[20:21]\n"
"v_pk_fma_f32 v[104:105], v[124:125], v[22:23], v[104:105]\n"
"v_pk_fma_f32 v[106:107], v[126:127], v[22:23], v[106:107]\n"
"v_pk_fma_f32 v[108:109], v[128:129], v[22:23], v[108:109]\n"
"v_pk_fma_f32 v[110:111], v[130:131], v[22:23], v[110:111]\n"
"v_pk_fma_f32 v[112:113], v[132:133], v[22:23], v[112:113]\n"
"v_pk_fma_f32 v[114:115], v[134:135], v[22:23], v[114:115]\n"
"v_pk_fma_f32 v[116:117], v[136:137], v[22:23], v[116:117]\n"
"v_pk_fma_f32 v[118:119], v[138:139], v[22:23], v[118:119]\n"
"v_pk_fma_f32 v[120:121], v[140:141], v[22:23], v[120:121]\n"
"v_pk_fma_f32 v[122:123], v[142:143], v[22:23], v[122:123]\n"
"v_pk_fma_f32 v[104:105], v[164:165], v[24:25], v[104:105]\n"
"v_pk_fma_f32 v[106:107], v[166:167], v[24:25], v[106:107]\n"
"v_pk_fma_f32 v[108:109], v[168:169], v[24:25], v[108:109]\n"
"v_pk_fma_f32 v[110:111], v[170:171], v[24:25], v[110:111]\n"
"v_pk_fma_f32 v[112:113], v[172:173], v[24:25], v[112:113]\n"
"v_pk_fma_f32 v[114:115], v[174:175], v[24:25], v[114:115]\n"
"v_pk_fma_f32 v[116:117], v[176:177], v[24:25], v[116:117]\n"
"v_pk_fma_f32 v[118:119], v[178:179], v[24:25], v[118:119]\n"
"v_pk_fma_f32 v[120:121], v[180:181], v[24:25], v[120:121]\n"
"v_pk_fma_f32 v[122:123], v[182:183], v[24:25], v[122:123]\n"
"ds_read_b128 v[32:35], v93 offset:0\n"
"ds_read_b128 v[36:39], v93 offset:20480\n"
"ds_read_b128 v[40:43], v93 offset:40960\n"
"ds_read_b128 v[44:47], v94 offset:0\n"
"ds_read_b128 v[48:51], v94 offset:20480\n"
"ds_read_b128 v[52:55], v93 offset:1024\n"
"ds_read_b128 v[56:59], v93 offset:21504\n"
"ds_read_b128 v[60:63], v93 offset:41984\n"
"s_waitcnt vmcnt(19) lgkmcnt(7)\n"
"v_mfma_f32_16x16x32_bf16 v[0:3], v[32:35], a[0:3], 0\n"
"ds_read_b128 v[64:67], v94 offset:1024\n"
"s_waitcnt lgkmcnt(7)\n"
"v_mfma_f32_16x16x32_bf16 v[4:7], v[36:39], a[0:3], 0\n"
"ds_read_b128 v[68:71], v94 offset:21504\n"
"s_waitcnt lgkmcnt(7)\n"
"v_mfma_f32_16x16x32_bf16 v[8:11], v[40:43], a[0:3], 0\n"
"ds_read_b128 v[72:75], v93 offset:2048\n"
"s_waitcnt lgkmcnt(7)\n"
"v_mfma_f32_16x16x32_bf16 v[12:15], v[44:47], a[0:3], 0\n"
"ds_read_b128 v[76:79], v93 offset:22528\n"
"s_waitcnt lgkmcnt(7)\n"
"v_mfma_f32_16x16x32_bf16 v[16:19], v[48:51], a[0:3], 0\n"
"ds_read_b128 v[32:35], v93 offset:43008\n"
"s_waitcnt vmcnt(18) lgkmcnt(7)\n"
"v_mfma_f32_16x16x32_bf16 v[0:3], v[52:55], a[4:7], v[0:3]\n"
"ds_read_b128 v[36:39], v94 offset:2048\n"
"s_waitcnt lgkmcnt(7)\n"
"v_mfma_f32_16x16x32_bf16 v[4:7], v[56:59], a[4:7], v[4:7]\n"
"ds_read_b128 v[40:43], v94 offset:22528\n"
"s_waitcnt lgkmcnt(7)\n"
"v_mfma_f32_16x16x32_bf16 v[8:11], v[60:63], a[4:7], v[8:11]\n"
"ds_read_b128 v[44:47], v93 offset:3072\n"
"s_waitcnt lgkmcnt(7)\n"
"v_mfma_f32_16x16x32_bf16 v[12:15], v[64:67], a[4:7], v[12:15]\n"
"ds_read_b128 v[48:51], v93 offset:23552\n"
"s_waitcnt lgkmcnt(7)\n"
"v_mfma_f32_16x16x32_bf16 v[16:19], v[68:71], a[4:7], v[16:19]\n"
"ds_read_b128 v[52:55], v93 offset:44032\n"
"s_waitcnt vmcnt(17) lgkmcnt(7)\n"
"v_mfma_f32_16x16x32_bf16 v[0:3], v[72:75], a[8:11], v[0:3]\n"
"ds_read_b128 v[56:59], v94 offset:3072\n"
"s_waitcnt lgkmcnt(7)\n"
"v_mfma_f32_16x16x32_bf16 v[4:7], v[76:79], a[8:11], v[4:7]\n"
"ds_read_b128 v[60:63], v94 offset:23552\n"
"s_waitcnt lgkmcnt(7)\n"
"v_mfma_f32_16x16x32_bf16 v[8:11], v[32:35], a[8:11], v[8:11]\n"
"ds_read_b128 v[64:67], v93 offset:4096\n"
"s_waitcnt lgkmcnt(7)\n"
"v_mfma_f32_16x16x32_bf16 v[12:15], v[36:39], a[8:11], v[12:15]\n"
"ds_read_b128 v[68:71], v93 offset:24576\n"
"s_waitcnt lgkmcnt(7)\n"
"v_mfma_f32_16x16x32_bf16 v[16:19], v[40:43], a[8:11], v[16:19]\n"
"ds_read_b128 v[72:75], v93 offset:45056\n"
"s_waitcnt vmcnt(16) lgkmcnt(7)\n"
"v_mfma_f32_16x16x32_bf16 v[0:3], v[44:47], a[12:15], v[0:3]\n"
"ds_read_b128 v[76:79], v94 offset:4096\n"
"s_waitcnt lgkmcnt(7)\n"
"v_mfma_f32_16x16x32_bf16 v[4:7], v[48:51], a[12:15], v[4:7]\n"
"ds_read_b128 v[32:35], v94 offset:24576\n"
"s_waitcnt lgkmcnt(7)\n"
"v_mfma_f32_16x16x32_bf16 v[8:11], v[52:55], a[12:15], v[8:11]\n"
"ds_read_b128 v[36:39], v93 offset:5120\n"
"s_waitcnt lgkmcnt(7)\n"
"v_mfma_f32_16x16x32_bf16 v[12:15], v[56:59], a[12:15], v[12:15]\n"
"ds_read_b128 v[40:43], v93 offset:25600\n"
"s_waitcnt lgkmcnt(7)\n"
"v_mfma_f32_16x16x32_bf16 v[16:19], v[60:63], a[12:15], v[16:19]\n"
"ds_read_b128 v[44:47], v93 offset:46080\n"
"s_waitcnt vmcnt(15) lgkmcnt(7)\n"
"v_mfma_f32_16x16x32_bf16 v[0:3], v[64:67], a[16:19], v[0:3]\n"
"ds_read_b128 v[48:51], v94 offset:5120\n"
"s_waitcnt lgkmcnt(7)\n"
"v_mfma_f32_16x16x32_bf16 v[4:7], v[68:71], a[16:19], v[4:7]\n"
"ds_read_b128 v[52:55], v94 offset:25600\n"
"s_waitcnt lgkmcnt(7)\n"
"v_mfma_f32_16x16x32_bf16 v[8:11], v[72:75], a[16:19], v[8:11]\n"
"ds_read_b128 v[56:59], v93 offset:6144\n"
"s_waitcnt lgkmcnt(7)\n"
"v_mfma_f32_16x16x32_bf16 v[12:15], v[76:79], a[16:19], v[12:15]\n"
"ds_read_b128 v[60:63], v93 offset:26624\n"
"s_waitcnt lgkmcnt(7)\n"
"v_mfma_f32_16x16x32_bf16 v[16:19], v[32:35], a[16:19], v[16:19]\n"
"ds_read_b128 v[64:67], v93 offset:47104\n"
"s_waitcnt vmcnt(14) lgkmcnt(7)\n"
"v_mfma_f32_16x16x32_bf16 v[0:3], v[36:39], a[20:23], v[0:3]\n"
"ds_read_b128 v[68:71], v94 offset:6144\n"
"s_waitcnt lgkmcnt(7)\n"
"v_mfma_f32_16x16x32_bf16 v[4:7], v[40:43], a[20:23], v[4:7]\n"
"ds_read_b128 v[72:75], v94 offset:26624\n"
"s_waitcnt lgkmcnt(7)\n"
"v_mfma_f32_16x16x32_bf16 v[8:11], v[44:47], a[20:23], v[8:11]\n"
"ds_read_b128 v[76:79], v93 offset:7168\n"
"s_waitcnt lgkmcnt(7)\n"
"v_mfma_f32_16x16x32_bf16 v[12:15], v[48:51], a[20:23], v[12:15]\n"
"ds_read_b128 v[32:35], v93 offset:27648\n"
"s_waitcnt lgkmcnt(7)\n"
"v_mfma_f32_16x16x32_bf16 v[16:19], v[52:55], a[20:23], v[16:19]\n"
"ds_read_b128 v[36:39], v93 offset:48128\n"
"s_waitcnt vmcnt(13) lgkmcnt(7)\n"
"v_mfma_f32_16x16x32_bf16 v[0:3], v[56:59], a[24:27], v[0:3]\n"
"ds_read_b128 v[40:43], v94 offset:7168\n"
"s_waitcnt lgkmcnt(7)\n"
"v_mfma_f32_16x16x32_bf16 v[4:7], v[60:63], a[24:27], v[4:7]\n"
"ds_read_b128 v[44:47], v94 offset:27648\n"
"s_waitcnt lgkmcnt(7)\n"
"v_mfma_f32_16x16x32_bf16 v[8:11], v[64:67], a[24:27], v[8:11]\n"
"ds_read_b128 v[48:51], v93 offset:8192\n"
"s_waitcnt lgkmcnt(7)\n"
"v_mfma_f32_16x16x32_bf16 v[12:15], v[68:71], a[24:27], v[12:15]\n"
"PG21_%=:\n"
"s_cbranch_execz RPG21_%=\n"
"ds_read_b128 v[52:55], v93 offset:28672\n"
"s_waitcnt lgkmcnt(7)\n"
"v_mfma_f32_16x16x32_bf16 v[16:19], v[72:75], a[24:27], v[16:19]\n"
"ds_read_b128 v[56:59], v93 offset:49152\n"
"s_waitcnt vmcnt(12) lgkmcnt(7)\n"
"v_mfma_f32_16x16x32_bf16 v[0:3], v[76:79], a[28:31], v[0:3]\n"
"ds_read_b128 v[60:63], v94 offset:8192\n"
"s_waitcnt lgkmcnt(7)\n"
"v_mfma_f32_16x16x32_bf16 v[4:7], v[32:35], a[28:31], v[4:7]\n"
"ds_read_b128 v[64:67], v94 offset:28672\n"
"s_waitcnt lgkmcnt(7)\n"
"v_mfma_f32_16x16x32_bf16 v[8:11], v[36:39], a[28:31], v[8:11]\n"
"ds_read_b128 v[68:71], v93 offset:9216\n"
"s_waitcnt lgkmcnt(7)\n"
"v_mfma_f32_16x16x32_bf16 v[12:15], v[40:43], a[28:31], v[12:15]\n"
"ds_read_b128 v[72:75], v93 offset:29696\n"
"s_waitcnt lgkmcnt(7)\n"
"v_mfma_f32_16x16x32_bf16 v[16:19], v[44:47], a[28:31], v[16:19]\n"
"ds_read_b128 v[76:79], v93 offset:50176\n"
"s_waitcnt vmcnt(11) lgkmcnt(7)\n"
"v_mfma_f32_16x16x32_bf16 v[0:3], v[48:51], a[32:35], v[0:3]\n"
"ds_read_b128 v[32:35], v94 offset:9216\n"
"s_waitcnt lgkmcnt(7)\n"
"v_mfma_f32_16x16x32_bf16 v[4:7], v[52:55], a[32:35], v[4:7]\n"
"ds_read_b128 v[36:39], v94 offset:29696\n"
"s_waitcnt lgkmcnt(7)\n"
"v_mfma_f32_16x16x32_bf16 v[8:11], v[56:59], a[32:35], v[8:11]\n"
"ds_read_b128 v[40:43], v93 offset:10240\n"
"s_waitcnt lgkmcnt(7)\n"
"v_mfma_f32_16x16x32_bf16 v[12:15], v[60:63], a[32:35], v[12:15]\n"
"ds_read_b128 v[44:47], v93 offset:30720\n"
"s_waitcnt lgkmcnt(7)\n"
"v_mfma_f32_16x16x32_bf16 v[16:19], v[64:67], a[32:35], v[16:19]\n"
"ds_read_b128 v[48:51], v93 offset:51200\n"
"s_waitcnt vmcnt(10) lgkmcnt(7)\n"
"v_mfma_f32_16x16x32_bf16 v[0:3], v[68:71], a[36:39], v[0:3]\n"
"ds_read_b128 v[52:55], v94 offset:10240\n"
"s_waitcnt lgkmcnt(7)\n"
"v_mfma_f32_16x16x32_bf16 v[4:7], v[72:75], a[36:39], v[4:7]\n"
"ds_read_b128 v[56:59], v94 offset:30720\n"
"s_waitcnt lgkmcnt(7)\n"
"v_mfma_f32_16x16x32_bf16 v[8:11], v[76:79], a[36:39], v[8:11]\n"
"ds_read_b128 v[60:63], v93 offset:11264\n"
"s_waitcnt lgkmcnt(7)\n"
"v_mfma_f32_16x16x32_bf16 v[12:15], v[32:35], a[36:39], v[12:15]\n"
"ds_read_b128 v[64:67], v93 offset:31744\n"
"s_waitcnt lgkmcnt(7)\n"
"v_mfma_f32_16x16x32_bf16 v[16:19], v[36:39], a[36:39], v[16:19]\n"
"ds_read_b128 v[68:71], v93 offset:52224\n"
"s_waitcnt vmcnt(9) lgkmcnt(7)\n"
"v_mfma_f32_16x16x32_bf16 v[0:3], v[40:43], a[40:43], v[0:3]\n"
"ds_read_b128 v[72:75], v94 offset:11264\n"
"s_waitcnt lgkmcnt(7)\n"
"v_mfma_f32_16x16x32_bf16 v[4:7], v[44:47], a[40:43], v[4:7]\n"
"ds_read_b128 v[76:79], v94 offset:31744\n"
"s_waitcnt lgkmcnt(7)\n"
"v_mfma_f32_16x16x32_bf16 v[8:11], v[48:51], a[40:43], v[8:11]\n"
"ds_read_b128 v[32:35], v93 offset:12288\n"
"s_waitcnt lgkmcnt(7)\n"
"v_mfma_f32_16x16x32_bf16 v[12:15], v[52:55], a[40:43], v[12:15]\n"
"ds_read_b128 v[36:39], v93 offset:32768\n"
"s_waitcnt lgkmcnt(7)\n"
"v_mfma_f32_16x16x32_bf16 v[16:19], v[56:59], a[40:43], v[16:19]\n"
"ds_read_b128 v[40:43], v93 offset:53248\n"
"s_waitcnt vmcnt(8) lgkmcnt(7)\n"
"v_mfma_f32_16x16x32_bf16 v[0:3], v[60:63], a[44:47], v[0:3]\n"
"ds_read_b128 v[44:47], v94 offset:12288\n"
"s_waitcnt lgkmcnt(7)\n"
"v_mfma_f32_16x16x32_bf16 v[4:7], v[64:67], a[44:47], v[4:7]\n"
"ds_read_b128 v[48:51], v94 offset:32768\n"
"s_waitcnt lgkmcnt(7)\n"
"v_mfma_f32_16x16x32_bf16 v[8:11], v[68:71], a[44:47], v[8:11]\n"
"ds_read_b128 v[52:55], v93 offset:13312\n"
"s_waitcnt lgkmcnt(7)\n"
"v_mfma_f32_16x16x32_bf16 v[12:15], v[72:75], a[44:47], v[12:15]\n"
"ds_read_b128 v[56:59], v93 offset:33792\n"
"s_waitcnt lgkmcnt(7)\n"
"v_mfma_f32_16x16x32_bf16 v[16:19], v[76:79], a[44:47], v[16:19]\n"
"ds_read_b128 v[60:63], v93 offset:54272\n"
"s_waitcnt vmcnt(7) lgkmcnt(7)\n"
"v_mfma_f32_16x16x32_bf16 v[0:3], v[32:35], a[48:51], v[0:3]\n"
"ds_read_b128 v[64:67], v94 offset:13312\n"
"s_waitcnt lgkmcnt(7)\n"
"v_mfma_f32_16x16x32_bf16 v[4:7], v[36:39], a[48:51], v[4:7]\n"
"ds_read_b128 v[68:71], v94 offset:33792\n"
"s_waitcnt lgkmcnt(7)\n"
"v_mfma_f32_16x16x32_bf16 v[8:11], v[40:43], a[48:51], v[8:11]\n"
"ds_read_b128 v[72:75], v93 offset:14336\n"
"s_waitcnt lgkmcnt(7)\n"
"v_mfma_f32_16x16x32_bf16 v[12:15], v[44:47], a[48:51], v[12:15]\n"
"ds_read_b128 v[76:79], v93 offset:34816\n"
"s_waitcnt lgkmcnt(7)\n"
"v_mfma_f32_16x16x32_bf16 v[16:19], v[48:51], a[48:51], v[16:19]\n"
"ds_read_b128 v[32:35], v93 offset:55296\n"
"s_waitcnt vmcnt(6) lgkmcnt(7)\n"
"v_mfma_f32_16x16x32_bf16 v[0:3], v[52:55], a[52:55], v[0:3]\n"
"ds_read_b128 v[36:39], v94 offset:14336\n"
"s_waitcnt lgkmcnt(7)\n"
"v_mfma_f32_16x16x32_bf16 v[4:7], v[56:59], a[52:55], v[4:7]\n"
"ds_read_b128 v[40:43], v94 offset:34816\n"
"s_waitcnt lgkmcnt(7)\n"
"v_mfma_f32_16x16x32_bf16 v[8:11], v[60:63], a[52:55], v[8:11]\n"
"ds_read_b128 v[44:47], v93 offset:15360\n"
"s_waitcnt lgkmcnt(7)\n"
"v_mfma_f32_16x16x32_bf16 v[12:15], v[64:67], a[52:55], v[12:15]\n"
"ds_read_b128 v[48:51], v93 offset:35840\n"
"s_waitcnt lgkmcnt(7)\n"
"v_mfma_f32_16x16x32_bf16 v[16:19], v[68:71], a[52:55], v[16:19]\n"
"ds_read_b128 v[52:55], v93 offset:56320\n"
"s_waitcnt vmcnt(5) lgkmcnt(7)\n"
"v_mfma_f32_16x16x32_bf16 v[0:3], v[72:75], a[56:59], v[0:3]\n"
"ds_read_b128 v[56:59], v94 offset:15360\n"
"s_waitcnt lgkmcnt(7)\n"
"v_mfma_f32_16x16x32_bf16 v[4:7], v[76:79], a[56:59], v[4:7]\n"
"ds_read_b128 v[60:63], v94 offset:35840\n"
"s_waitcnt lgkmcnt(7)\n"
"v_mfma_f32_16x16x32_bf16 v[8:11], v[32:35], a[56:59], v[8:11]\n"
"ds_read_b128 v[64:67], v93 offset:16384\n"
"s_waitcnt lgkmcnt(7)\n"
"v_mfma_f32_16x16x32_bf16 v[12:15], v[36:39], a[56:59], v[12:15]\n"
"ds_read_b128 v[68:71], v93 offset:36864\n"
"s_waitcnt lgkmcnt(7)\n"
"v_mfma_f32_16x16x32_bf16 v[16:19], v[40:43], a[56:59], v[16:19]\n"
"ds_read_b128 v[72:75], v93 offset:57344\n"
"s_waitcnt vmcnt(4) lgkmcnt(7)\n"
"v_mfma_f32_16x16x32_bf16 v[0:3], v[44:47], a[60:63], v[0:3]\n"
"ds_read_b128 v[76:79], v94 offset:16384\n"
"s_waitcnt lgkmcnt(7)\n"
"v_mfma_f32_16x16x32_bf16 v[4:7], v[48:51], a[60:63], v[4:7]\n"
"ds_read_b128 v[32:35], v94 offset:36864\n"
"s_waitcnt lgkmcnt(7)\n"
"v_mfma_f32_16x16x32_bf16 v[8:11], v[52:55], a[60:63], v[8:11]\n"
"ds_read_b128 v[36:39], v93 offset:17408\n"
"s_waitcnt lgkmcnt(7)\n"
"v_mfma_f32_16x16x32_bf16 v[12:15], v[56:59], a[60:63], v[12:15]\n"
"ds_read_b128 v[40:43], v93 offset:37888\n"
"s_waitcnt lgkmcnt(7)\n"
"v_mfma_f32_16x16x32_bf16 v[16:19], v[60:63], a[60:63], v[16:19]\n"
"ds_read_b128 v[44:47], v93 offset:58368\n"
"s_waitcnt vmcnt(3) lgkmcnt(7)\n"
"v_mfma_f32_16x16x32_bf16 v[0:3], v[64:67], a[64:67], v[0:3]\n"
"ds_read_b128 v[48:51], v94 offset:17408\n"
"s_waitcnt lgkmcnt(7)\n"
"v_mfma_f32_16x16x32_bf16 v[4:7], v[68:71], a[64:67], v[4:7]\n"
"ds_read_b128 v[52:55], v94 offset:37888\n"
"s_waitcnt lgkmcnt(7)\n"
"v_mfma_f32_16x16x32_bf16 v[8:11], v[72:75], a[64:67], v[8:11]\n"
"ds_read_b128 v[56:59], v93 offset:18432\n"
"s_waitcnt lgkmcnt(7)\n"
"v_mfma_f32_16x16x32_bf16 v[12:15], v[76:79], a[64:67], v[12:15]\n"
"ds_read_b128 v[60:63], v93 offset:38912\n"
"s_waitcnt lgkmcnt(7)\n"
"v_mfma_f32_16x16x32_bf16 v[16:19], v[32:35], a[64:67], v[16:19]\n"
"ds_read_b128 v[64:67], v93 offset:59392\n"
"s_waitcnt vmcnt(2) lgkmcnt(7)\n"
"v_mfma_f32_16x16x32_bf16 v[0:3], v[36:39], a[68:71], v[0:3]\n"
"ds_read_b128 v[68:71], v94 offset:18432\n"
"s_waitcnt lgkmcnt(7)\n"
"v_mfma_f32_16x16x32_bf16 v[4:7], v[40:43], a[68:71], v[4:7]\n"
"ds_read_b128 v[72:75], v94 offset:38912\n"
"s_waitcnt lgkmcnt(7)\n"
"v_mfma_f32_16x16x32_bf16 v[8:11], v[44:47], a[68:71], v[8:11]\n"
"ds_read_b128 v[76:79], v93 offset:19456\n"
"s_waitcnt lgkmcnt(7)\n"
"v_mfma_f32_16x16x32_bf16 v[12:15], v[48:51], a[68:71], v[12:15]\n"
"ds_read_b128 v[32:35], v93 offset:39936\n"
"s_waitcnt lgkmcnt(7)\n"
"v_mfma_f32_16x16x32_bf16 v[16:19], v[52:55], a[68:71], v[16:19]\n"
"ds_read_b128 v[36:39], v93 offset:60416\n"
"s_waitcnt vmcnt(1) lgkmcnt(7)\n"
"v_mfma_f32_16x16x32_bf16 v[0:3], v[56:59], a[72:75], v[0:3]\n"
"ds_read_b128 v[40:43], v94 offset:19456\n"
"s_waitcnt lgkmcnt(7)\n"
"v_mfma_f32_16x16x32_bf16 v[4:7], v[60:63], a[72:75], v[4:7]\n"
"ds_read_b128 v[44:47], v94 offset:39936\n"
"s_waitcnt lgkmcnt(7)\n"
"v_mfma_f32_16x16x32_bf16 v[8:11], v[64:67], a[72:75], v[8:11]\n"
"s_waitcnt lgkmcnt(6)\n"
"v_mfma_f32_16x16x32_bf16 v[12:15], v[68:71], a[72:75], v[12:15]\n"
"s_waitcnt lgkmcnt(5)\n"
"v_mfma_f32_16x16x32_bf16 v[16:19], v[72:75], a[72:75], v[16:19]\n"
"s_waitcnt vmcnt(0) lgkmcnt(4)\n"
"v_mfma_f32_16x16x32_bf16 v[0:3], v[76:79], a[76:79], v[0:3]\n"
"s_waitcnt lgkmcnt(3)\n"
"v_mfma_f32_16x16x32_bf16 v[4:7], v[32:35], a[76:79], v[4:7]\n"
"s_waitcnt lgkmcnt(2)\n"
"v_mfma_f32_16x16x32_bf16 v[8:11], v[36:39], a[76:79], v[8:11]\n"
"s_waitcnt lgkmcnt(1)\n"
"v_mfma_f32_16x16x32_bf16 v[12:15], v[40:43], a[76:79], v[12:15]\n"
"s_waitcnt lgkmcnt(0)\n"
"v_mfma_f32_16x16x32_bf16 v[16:19], v[44:47], a[76:79], v[16:19]\n"
"s_nop 7\n"
"v_cmp_u_f32 vcc, v16, v16\n"
"s_nop 0\n"
"s_cbranch_vccnz REDO_G1_%=\n"
"DONE_G1_%=:\n"
"v_pk_fma_f32 v[0:1], v[0:1], v[26:27], v[104:105]\n"
"v_pk_fma_f32 v[2:3], v[2:3], v[26:27], v[106:107]\n"
"v_pk_fma_f32 v[4:5], v[4:5], v[26:27], v[108:109]\n"
"v_pk_fma_f32 v[6:7], v[6:7], v[26:27], v[110:111]\n"
"v_pk_fma_f32 v[8:9], v[8:9], v[26:27], v[112:113]\n"
"v_pk_fma_f32 v[10:11], v[10:11], v[26:27], v[114:115]\n"
"v_pk_fma_f32 v[12:13], v[12:13], v[26:27], v[116:117]\n"
"v_pk_fma_f32 v[14:15], v[14:15], v[26:27], v[118:119]\n"
"v_pk_fma_f32 v[16:17], v[16:17], v[26:27], v[120:121]\n"
"v_pk_fma_f32 v[18:19], v[18:19], v[26:27], v[122:123]\n"
"v_pk_mul_f32 v[164:165], v[0:1], v[0:1]\n"
"v_pk_mul_f32 v[166:167], v[2:3], v[2:3]\n"
"v_pk_mul_f32 v[168:169], v[4:5], v[4:5]\n"
"v_pk_mul_f32 v[170:171], v[6:7], v[6:7]\n"
"v_pk_mul_f32 v[172:173], v[8:9], v[8:9]\n"
"v_pk_mul_f32 v[174:175], v[10:11], v[10:11]\n"
"v_pk_mul_f32 v[176:177], v[12:13], v[12:13]\n"
"v_pk_mul_f32 v[178:179], v[14:15], v[14:15]\n"
"v_pk_mul_f32 v[180:181], v[16:17], v[16:17]\n"
"v_pk_mul_f32 v[182:183], v[18:19], v[18:19]\n"
"v_pk_fma_f32 v[164:165], v[164:165], s[56:57], v[206:207]\n"
"v_pk_fma_f32 v[166:167], v[166:167], s[56:57], v[206:207]\n"
"v_pk_fma_f32 v[168:169], v[168:169], s[56:57], v[206:207]\n"
"v_pk_fma_f32 v[170:171], v[170:171], s[56:57], v[206:207]\n"
"v_pk_fma_f32 v[172:173], v[172:173], s[56:57], v[206:207]\n"
"v_pk_fma_f32 v[174:175], v[174:175], s[56:57], v[206:207]\n"
"v_pk_fma_f32 v[176:177], v[176:177], s[56:57], v[206:207]\n"
"v_pk_fma_f32 v[178:179], v[178:179], s[56:57], v[206:207]\n"
"v_pk_fma_f32 v[180:181], v[180:181], s[56:57], v[206:207]\n"
"v_pk_fma_f32 v[182:183], v[182:183], s[56:57], v[206:207]\n"
"v_pk_mul_f32 v[164:165], v[164:165], v[0:1]\n"
"v_pk_mul_f32 v[166:167], v[166:167], v[2:3]\n"
"v_pk_mul_f32 v[168:169], v[168:169], v[4:5]\n"
"v_pk_mul_f32 v[170:171], v[170:171], v[6:7]\n"
"v_pk_mul_f32 v[172:173], v[172:173], v[8:9]\n"
"v_pk_mul_f32 v[174:175], v[174:175], v[10:11]\n"
"v_pk_mul_f32 v[176:177], v[176:177], v[12:13]\n"
"v_pk_mul_f32 v[178:179], v[178:179], v[14:15]\n"
"v_pk_mul_f32 v[180:181], v[180:181], v[16:17]\n"
"v_pk_mul_f32 v[182:183], v[182:183], v[18:19]\n"
"v_exp_f32 v164, v164\n"
"v_exp_f32 v165, v165\n"
"v_exp_f32 v166, v166\n"
"v_exp_f32 v167, v167\n"
"v_exp_f32 v168, v168\n"
"v_exp_f32 v169, v169\n"
"v_exp_f32 v170, v170\n"
"v_exp_f32 v171, v171\n"
"v_exp_f32 v172, v172\n"
"v_exp_f32 v173, v173\n"
"v_exp_f32 v174, v174\n"
"v_exp_f32 v175, v175\n"
"v_exp_f32 v176, v176\n"
"v_exp_f32 v177, v177\n"
"v_exp_f32 v178, v178\n"
"v_exp_f32 v179, v179\n"
"v_exp_f32 v180, v180\n"
"v_exp_f32 v181, v181\n"
"v_exp_f32 v182, v182\n"
"v_exp_f32 v183, v183\n"
"v_pk_add_f32 v[164:165], v[164:165], v[208:209]\n"
"v_pk_add_f32 v[166:167], v[166:167], v[208:209]\n"
"v_pk_add_f32 v[168:169], v[168:169], v[208:209]\n"
"v_pk_add_f32 v[170:171], v[170:171], v[208:209]\n"
"v_pk_add_f32 v[172:173], v[172:173], v[208:209]\n"
"v_pk_add_f32 v[174:175], v[174:175], v[208:209]\n"
"v_pk_add_f32 v[176:177], v[176:177], v[208:209]\n"
"v_pk_add_f32 v[178:179], v[178:179], v[208:209]\n"
"v_pk_add_f32 v[180:181], v[180:181], v[208:209]\n"
"v_pk_add_f32 v[182:183], v[182:183], v[208:209]\n"
"v_rcp_f32 v164, v164\n"
"v_rcp_f32 v165, v165\n"
"v_rcp_f32 v166, v166\n"
"v_rcp_f32 v167, v167\n"
"v_rcp_f32 v168, v168\n"
"v_rcp_f32 v169, v169\n"
"v_rcp_f32 v170, v170\n"
"v_rcp_f32 v171, v171\n"
"v_rcp_f32 v172, v172\n"
"v_rcp_f32 v173, v173\n"
"v_rcp_f32 v174, v174\n"
"v_rcp_f32 v175, v175\n"
"v_rcp_f32 v176, v176\n"
"v_rcp_f32 v177, v177\n"
"v_rcp_f32 v178, v178\n"
"v_rcp_f32 v179, v179\n"
"v_rcp_f32 v180, v180\n"
"v_rcp_f32 v181, v181\n"
"v_rcp_f32 v182, v182\n"
"v_rcp_f32 v183, v183\n"
"v_pk_mul_f32 v[0:1], v[0:1], v[164:165]\n"
"v_pk_mul_f32 v[2:3], v[2:3], v[166:167]\n"
"v_pk_mul_f32 v[4:5], v[4:5], v[168:169]\n"
"v_pk_mul_f32 v[6:7], v[6:7], v[170:171]\n"
"v_pk_mul_f32 v[8:9], v[8:9], v[172:173]\n"
"v_pk_mul_f32 v[10:11], v[10:11], v[174:175]\n"
"v_pk_mul_f32 v[12:13], v[12:13], v[176:177]\n"
"v_pk_mul_f32 v[14:15], v[14:15], v[178:179]\n"
"v_pk_mul_f32 v[16:17], v[16:17], v[180:181]\n"
"v_pk_mul_f32 v[18:19], v[18:19], v[182:183]\n"
"v_cvt_pk_bf16_f32 v32, v0, v1\n"
"v_cvt_pk_bf16_f32 v33, v2, v3\n"
"v_cvt_pk_bf16_f32 v34, v4, v5\n"
"v_cvt_pk_bf16_f32 v35, v6, v7\n"
"v_cvt_pk_bf16_f32 v36, v8, v9\n"
"v_cvt_pk_bf16_f32 v37, v10, v11\n"
"v_cvt_pk_bf16_f32 v38, v12, v13\n"
"v_cvt_pk_bf16_f32 v39, v14, v15\n"
"v_cvt_pk_bf16_f32 v40, v16, v17\n"
"v_cvt_pk_bf16_f32 v41, v18, v19\n"
"v_cndmask_b32_e64 v32, 0, v32, s[78:79]\n"
"v_cndmask_b32_e64 v33, 0, v33, s[78:79]\n"
"v_cndmask_b32_e64 v34, 0, v34, s[78:79]\n"
"v_cndmask_b32_e64 v35, 0, v35, s[78:79]\n"
"v_cndmask_b32_e64 v36, 0, v36, s[78:79]\n"
"v_cndmask_b32_e64 v37, 0, v37, s[78:79]\n"
"v_cndmask_b32_e64 v38, 0, v38, s[78:79]\n"
"v_cndmask_b32_e64 v39, 0, v39, s[78:79]\n"
"v_cndmask_b32_e64 v40, 0, v40, s[78:79]\n"
"v_cndmask_b32_e64 v41, 0, v41, s[78:79]\n"
"s_add_u32 s38, s40, 109576192\n"
"s_add_u32 s39, s41, 109576192\n"
"buffer_store_dwordx4 v[32:35], v204, s[96:99], s38 offen offset:0 sc1\n"
"buffer_store_dwordx4 v[36:39], v204, s[96:99], s38 offen offset:1024 sc1\n"
"buffer_store_dwordx2 v[40:41], v205, s[96:99], s39 offen sc1\n"
"s_barrier\n"
"s_mul_i32 s45, %2, 5120\n"
"s_add_u32 s37, s46, s45\n"
"s_add_u32 s37, s37, 61440\n"
"s_add_u32 s45, s45, %3\n"
"s_add_u32 m0, s45, 0\n"
"s_nop 0\n"
"buffer_load_dwordx4 v251, s[96:99], s37 offen offset:0 sc1 lds\n"
"buffer_load_dwordx4 v251, s[96:99], s37 offen offset:1024 sc1 lds\n"
"buffer_load_dwordx4 v251, s[96:99], s37 offen offset:2048 sc1 lds\n"
"buffer_load_dwordx4 v251, s[96:99], s37 offen offset:3072 sc1 lds\n"
"s_add_u32 m0, s45, 4096\n"
"s_add_u32 s37, s37, 4096\n"
"buffer_load_dwordx4 v251, s[96:99], s37 offen offset:0 sc1 lds\n"
"s_mul_i32 s45, %2, 5120\n"
"s_add_u32 s37, s47, s45\n"
"s_add_u32 s37, s37, 0\n"
"s_add_u32 s45, s45, %3\n"
"s_add_u32 m0, s45, 20480\n"
"s_nop 0\n"
"buffer_load_dwordx4 v251, s[96:99], s37 offen offset:0 sc1 lds\n"
"buffer_load_dwordx4 v251, s[96:99], s37 offen offset:1024 sc1 lds\n"
"buffer_load_dwordx4 v251, s[96:99], s37 offen offset:2048 sc1 lds\n"
"buffer_load_dwordx4 v251, s[96:99], s37 offen offset:3072 sc1 lds\n"
"s_add_u32 m0, s45, 24576\n"
"s_add_u32 s37, s37, 4096\n"
"buffer_load_dwordx4 v251, s[96:99], s37 offen offset:0 sc1 lds\n"
"s_mov_b32 s38, 104872960\n"
"buffer_load_dwordx4 v[96:99], v254, s[96:99], s38 offen offset:0 sc1\n"
"buffer_load_dwordx4 v[100:103], v254, s[96:99], s38 offen offset:16 sc1\n"
"buffer_load_dwordx4 v[104:107], v254, s[96:99], s38 offen offset:128 sc1\n"
"buffer_load_dwordx4 v[108:111], v254, s[96:99], s38 offen offset:144 sc1\n"
"buffer_load_dwordx4 v[112:115], v255, s[96:99], s38 offen sc1\n"
"s_mov_b32 s38, 104865280\n"
"buffer_load_dwordx4 v[116:119], v254, s[96:99], s38 offen offset:0 sc1\n"
"buffer_load_dwordx4 v[120:123], v254, s[96:99], s38 offen offset:16 sc1\n"
"buffer_load_dwordx4 v[124:127], v254, s[96:99], s38 offen offset:128 sc1\n"
"buffer_load_dwordx4 v[128:131], v254, s[96:99], s38 offen offset:144 sc1\n"
"buffer_load_dwordx4 v[132:135], v255, s[96:99], s38 offen sc1\n"
"v_lshl_add_u32 v205, s42, 4, v249\n"
"v_lshlrev_b32 v205, 6, v205\n"
"v_lshl_add_u32 v205, v248, 28, v205\n"
"v_sub_f32 v20, 0, v230\n"
"v_mov_b32 v22, v231\n"
"v_mov_b32 v23, v231\n"
"v_mov_b32 v21, v20\n"
"v_lshlrev_b32 v136, 16, v220\n"
"v_and_b32 v137, 0xffff0000, v220\n"
"v_lshlrev_b32 v138, 16, v221\n"
"v_and_b32 v139, 0xffff0000, v221\n"
"v_lshlrev_b32 v140, 16, v222\n"
"v_and_b32 v141, 0xffff0000, v222\n"
"v_lshlrev_b32 v142, 16, v223\n"
"v_and_b32 v143, 0xffff0000, v223\n"
"v_lshlrev_b32 v144, 16, v224\n"
"v_and_b32 v145, 0xffff0000, v224\n"
"v_lshlrev_b32 v146, 16, v225\n"
"v_and_b32 v147, 0xffff0000, v225\n"
"v_lshlrev_b32 v148, 16, v226\n"
"v_and_b32 v149, 0xffff0000, v226\n"
"v_lshlrev_b32 v150, 16, v227\n"
"v_and_b32 v151, 0xffff0000, v227\n"
"v_lshlrev_b32 v152, 16, v228\n"
"v_and_b32 v153, 0xffff0000, v228\n"
"v_lshlrev_b32 v154, 16, v229\n"
"v_and_b32 v155, 0xffff0000, v229\n"
"v_pk_add_f32 v[136:137], v[136:137], v[20:21]\n"
"v_pk_add_f32 v[138:139], v[138:139], v[20:21]\n"
"v_pk_add_f32 v[140:141], v[140:141], v[20:21]\n"
"v_pk_add_f32 v[142:143], v[142:143], v[20:21]\n"
"v_pk_add_f32 v[144:145], v[144:145], v[20:21]\n"
"v_pk_add_f32 v[146:147], v[146:147], v[20:21]\n"
"v_pk_add_f32 v[148:149], v[148:149], v[20:21]\n"
"v_pk_add_f32 v[150:151], v[150:151], v[20:21]\n"
"v_pk_add_f32 v[152:153], v[152:153], v[20:21]\n"
"v_pk_add_f32 v[154:155], v[154:155], v[20:21]\n"
"s_mul_i32 s47, s67, 143360\n"
"s_add_u32 s47, s47, 75497472\n"
"s_mul_i32 s38, s43, 20480\n"
"s_add_u32 s46, s47, s38\n"
"s_cmp_eq_u32 s43, 0\n"
"s_cselect_b32 s38, 122880, 0\n"
"s_add_u32 s47, s47, s38\n"
"s_waitcnt vmcnt(9)\n"
"v_pk_mul_f32 v[96:97], v[96:97], v[22:23]\n"
"v_pk_mul_f32 v[98:99], v[98:99], v[22:23]\n"
"s_waitcnt vmcnt(8)\n"
"v_pk_mul_f32 v[100:101], v[100:101], v[22:23]\n"
"v_pk_mul_f32 v[102:103], v[102:103], v[22:23]\n"
"s_waitcnt vmcnt(7)\n"
"v_pk_mul_f32 v[104:105], v[104:105], v[22:23]\n"
"v_pk_mul_f32 v[106:107], v[106:107], v[22:23]\n"
"s_waitcnt vmcnt(6)\n"
"v_pk_mul_f32 v[108:109], v[108:109], v[22:23]\n"
"v_pk_mul_f32 v[110:111], v[110:111], v[22:23]\n"
"s_waitcnt vmcnt(5)\n"
"v_pk_mul_f32 v[112:113], v[112:113], v[22:23]\n"
"v_pk_mul_f32 v[114:115], v[114:115], v[22:23]\n"
"s_waitcnt vmcnt(4)\n"
"v_pk_fma_f32 v[136:137], v[136:137], v[96:97], v[116:117]\n"
"v_pk_fma_f32 v[138:139], v[138:139], v[98:99], v[118:119]\n"
"s_waitcnt vmcnt(3)\n"
"v_pk_fma_f32 v[140:141], v[140:141], v[100:101], v[120:121]\n"
"v_pk_fma_f32 v[142:143], v[142:143], v[102:103], v[122:123]\n"
"s_waitcnt vmcnt(2)\n"
"v_pk_fma_f32 v[144:145], v[144:145], v[104:105], v[124:125]\n"
"v_pk_fma_f32 v[146:147], v[146:147], v[106:107], v[126:127]\n"
"s_waitcnt vmcnt(1)\n"
"v_pk_fma_f32 v[148:149], v[148:149], v[108:109], v[128:129]\n"
"v_pk_fma_f32 v[150:151], v[150:151], v[110:111], v[130:131]\n"
"s_waitcnt vmcnt(0)\n"
"v_pk_fma_f32 v[152:153], v[152:153], v[112:113], v[132:133]\n"
"v_pk_fma_f32 v[154:155], v[154:155], v[114:115], v[134:135]\n"
"s_mul_i32 s60, s42, 20480\n"
"s_add_u32 s60, s60, 109578240\n"
"v_add_u32 v177, 5120, v251\n"
"v_add_u32 v178, 10240, v251\n"
"v_add_u32 v179, 15360, v251\n"
"s_barrier\n"
"s_mov_b32 s63, 3\n"
"buffer_load_dwordx4 v[160:163], v251, s[96:99], s60 offen sc1\n"
"buffer_load_dwordx4 v[164:167], v177, s[96:99], s60 offen sc1\n"
"buffer_load_dwordx4 v[168:171], v178, s[96:99], s60 offen sc1\n"
"buffer_load_dwordx4 v[172:175], v179, s[96:99], s60 offen sc1\n"
"s_mul_i32 s45, %2, 15360\n"
"s_add_u32 s37, s46, s45\n"
"s_add_u32 s37, s37, 0\n"
"s_add_u32 s45, s45, %3\n"
"s_add_u32 m0, s45, 40960\n"
"s_nop 0\n"
"buffer_load_dwordx4 v251, s[96:99], s37 offen offset:0 sc1 lds\n"
"buffer_load_dwordx4 v251, s[96:99], s37 offen offset:1024 sc1 lds\n"
"buffer_load_dwordx4 v251, s[96:99], s37 offen offset:2048 sc1 lds\n"
"buffer_load_dwordx4 v251, s[96:99], s37 offen offset:3072 sc1 lds\n"
"s_add_u32 m0, s45, 45056\n"
"s_add_u32 s37, s37, 4096\n"
"s_waitcnt vmcnt(7)\n"
"v_max_u32 v242, v160, v161\n"
"v_max3_u32 v242, v242, v162, v163\n"
"s_waitcnt vmcnt(6)\n"
"v_max3_u32 v242, v242, v164, v165\n"
"v_max3_u32 v242, v242, v166, v167\n"
"s_waitcnt vmcnt(5)\n"
"v_max3_u32 v242, v242, v168, v169\n"
"v_max3_u32 v242, v242, v170, v171\n"
"s_waitcnt vmcnt(4)\n"
"v_max3_u32 v242, v242, v172, v173\n"
"v_max3_u32 v242, v242, v174, v175\n"
"v_cmp_eq_u32 vcc, -1, v242\n"
"s_nop 0\n"
"s_cbranch_vccnz SLOW_G2_%=\n"
"OK_G2_%=:\n"
"s_mul_i32 s36, s42, 20480\n"
"s_add_u32 s36, s36, 109576192\n"
"buffer_load_dwordx4 a[0:3], v251, s[96:99], s36 offen offset:0 sc1\n"
"buffer_load_dwordx4 a[4:7], v251, s[96:99], s36 offen offset:1024 sc1\n"
"buffer_load_dwordx4 a[12:15], v251, s[96:99], s36 offen offset:3072 sc1\n"
"s_add_u32 s36, s36, 4096\n"
"buffer_load_dwordx4 a[16:19], v251, s[96:99], s36 offen offset:0 sc1\n"
"buffer_load_dwordx4 a[20:23], v251, s[96:99], s36 offen offset:1024 sc1\n"
"buffer_load_dwordx4 a[24:27], v251, s[96:99], s36 offen offset:2048 sc1\n"
"s_add_u32 s36, s36, 4096\n"
"buffer_load_dwordx4 a[32:35], v251, s[96:99], s36 offen offset:0 sc1\n"
"buffer_load_dwordx4 a[36:39], v251, s[96:99], s36 offen offset:1024 sc1\n"
"buffer_load_dwordx4 a[40:43], v251, s[96:99], s36 offen offset:2048 sc1\n"
"buffer_load_dwordx4 a[44:47], v251, s[96:99], s36 offen offset:3072 sc1\n"
"s_add_u32 s36, s36, 4096\n"
"buffer_load_dwordx4 a[52:55], v251, s[96:99], s36 offen offset:1024 sc1\n"
"buffer_load_dwordx4 a[56:59], v251, s[96:99], s36 offen offset:2048 sc1\n"
"buffer_load_dwordx4 a[60:63], v251, s[96:99], s36 offen offset:3072 sc1\n"
"s_add_u32 s36, s36, 4096\n"
"buffer_load_dwordx4 a[64:67], v251, s[96:99], s36 offen offset:0 sc1\n"
"buffer_load_dwordx4 a[72:75], v251, s[96:99], s36 offen offset:2048 sc1\n"
"buffer_load_dwordx4 a[76:79], v251, s[96:99], s36 offen offset:3072 sc1\n"
"ds_read_b128 v[32:35], v94 offset:40960\n"
"ds_read_b128 v[36:39], v95 offset:0\n"
"ds_read_b128 v[40:43], v95 offset:20480\n"
"ds_read_b128 v[44:47], v93 offset:0\n"
"ds_read_b128 v[48:51], v93 offset:20480\n"
"ds_read_b128 v[52:55], v94 offset:41984\n"
"ds_read_b128 v[56:59], v95 offset:1024\n"
"ds_read_b128 v[60:63], v95 offset:21504\n"
"s_waitcnt vmcnt(15) lgkmcnt(7)\n"
"v_mfma_f32_16x16x32_bf16 v[0:3], v[32:35], a[0:3], 0\n"
"ds_read_b128 v[64:67], v93 offset:1024\n"
"s_waitcnt lgkmcnt(7)\n"
"v_mfma_f32_16x16x32_bf16 v[4:7], v[36:39], a[0:3], 0\n"
"ds_read_b128 v[68:71], v93 offset:21504\n"
"s_waitcnt lgkmcnt(7)\n"
"v_mfma_f32_16x16x32_bf16 v[8:11], v[40:43], a[0:3], 0\n"
"ds_read_b128 v[72:75], v94 offset:43008\n"
"s_waitcnt lgkmcnt(7)\n"
"v_mfma_f32_16x16x32_bf16 v[12:15], v[44:47], a[0:3], 0\n"
"ds_read_b128 v[76:79], v95 offset:2048\n"
"s_waitcnt lgkmcnt(7)\n"
"v_mfma_f32_16x16x32_bf16 v[16:19], v[48:51], a[0:3], 0\n"
"ds_read_b128 v[32:35], v95 offset:22528\n"
"s_waitcnt vmcnt(14) lgkmcnt(7)\n"
"v_mfma_f32_16x16x32_bf16 v[0:3], v[52:55], a[4:7], v[0:3]\n"
"ds_read_b128 v[36:39], v93 offset:2048\n"
"s_waitcnt lgkmcnt(7)\n"
"v_mfma_f32_16x16x32_bf16 v[4:7], v[56:59], a[4:7], v[4:7]\n"
"ds_read_b128 v[40:43], v93 offset:22528\n"
"s_waitcnt lgkmcnt(7)\n"
"v_mfma_f32_16x16x32_bf16 v[8:11], v[60:63], a[4:7], v[8:11]\n"
"ds_read_b128 v[44:47], v94 offset:44032\n"
"s_waitcnt lgkmcnt(7)\n"
"v_mfma_f32_16x16x32_bf16 v[12:15], v[64:67], a[4:7], v[12:15]\n"
"ds_read_b128 v[48:51], v95 offset:3072\n"
"s_waitcnt lgkmcnt(7)\n"
"v_mfma_f32_16x16x32_bf16 v[16:19], v[68:71], a[4:7], v[16:19]\n"
"ds_read_b128 v[52:55], v95 offset:23552\n"
"s_waitcnt lgkmcnt(7)\n"
"v_mfma_f32_16x16x32_bf16 v[0:3], v[72:75], v[160:163], v[0:3]\n"
"ds_read_b128 v[56:59], v93 offset:3072\n"
"s_waitcnt lgkmcnt(7)\n"
"v_mfma_f32_16x16x32_bf16 v[4:7], v[76:79], v[160:163], v[4:7]\n"
"ds_read_b128 v[60:63], v93 offset:23552\n"
"s_waitcnt lgkmcnt(7)\n"
"v_mfma_f32_16x16x32_bf16 v[8:11], v[32:35], v[160:163], v[8:11]\n"
"ds_read_b128 v[64:67], v94 offset:45056\n"
"s_waitcnt lgkmcnt(7)\n"
"v_mfma_f32_16x16x32_bf16 v[12:15], v[36:39], v[160:163], v[12:15]\n"
"ds_read_b128 v[68:71], v95 offset:4096\n"
"s_waitcnt lgkmcnt(7)\n"
"v_mfma_f32_16x16x32_bf16 v[16:19], v[40:43], v[160:163], v[16:19]\n"
"ds_read_b128 v[72:75], v95 offset:24576\n"
"s_waitcnt vmcnt(13) lgkmcnt(7)\n"
"v_mfma_f32_16x16x32_bf16 v[0:3], v[44:47], a[12:15], v[0:3]\n"
"ds_read_b128 v[76:79], v93 offset:4096\n"
"s_waitcnt lgkmcnt(7)\n"
"v_mfma_f32_16x16x32_bf16 v[4:7], v[48:51], a[12:15], v[4:7]\n"
"ds_read_b128 v[32:35], v93 offset:24576\n"
"s_waitcnt lgkmcnt(7)\n"
"v_mfma_f32_16x16x32_bf16 v[8:11], v[52:55], a[12:15], v[8:11]\n"
"ds_read_b128 v[36:39], v94 offset:46080\n"
"s_waitcnt lgkmcnt(7)\n"
"v_mfma_f32_16x16x32_bf16 v[12:15], v[56:59], a[12:15], v[12:15]\n"
"ds_read_b128 v[40:43], v95 offset:5120\n"
"s_waitcnt lgkmcnt(7)\n"
"v_mfma_f32_16x16x32_bf16 v[16:19], v[60:63], a[12:15], v[16:19]\n"
"ds_read_b128 v[44:47], v95 offset:25600\n"
"s_waitcnt vmcnt(12) lgkmcnt(7)\n"
"v_mfma_f32_16x16x32_bf16 v[0:3], v[64:67], a[16:19], v[0:3]\n"
"ds_read_b128 v[48:51], v93 offset:5120\n"
"s_waitcnt lgkmcnt(7)\n"
"v_mfma_f32_16x16x32_bf16 v[4:7], v[68:71], a[16:19], v[4:7]\n"
"ds_read_b128 v[52:55], v93 offset:25600\n"
"s_waitcnt lgkmcnt(7)\n"
"v_mfma_f32_16x16x32_bf16 v[8:11], v[72:75], a[16:19], v[8:11]\n"
"ds_read_b128 v[56:59], v94 offset:47104\n"
"s_waitcnt lgkmcnt(7)\n"
"v_mfma_f32_16x16x32_bf16 v[12:15], v[76:79], a[16:19], v[12:15]\n"
"ds_read_b128 v[60:63], v95 offset:6144\n"
"s_waitcnt lgkmcnt(7)\n"
"v_mfma_f32_16x16x32_bf16 v[16:19], v[32:35], a[16:19], v[16:19]\n"
"ds_read_b128 v[64:67], v95 offset:26624\n"
"s_waitcnt vmcnt(11) lgkmcnt(7)\n"
"v_mfma_f32_16x16x32_bf16 v[0:3], v[36:39], a[20:23], v[0:3]\n"
"ds_read_b128 v[68:71], v93 offset:6144\n"
"s_waitcnt lgkmcnt(7)\n"
"v_mfma_f32_16x16x32_bf16 v[4:7], v[40:43], a[20:23], v[4:7]\n"
"ds_read_b128 v[72:75], v93 offset:26624\n"
"s_waitcnt lgkmcnt(7)\n"
"v_mfma_f32_16x16x32_bf16 v[8:11], v[44:47], a[20:23], v[8:11]\n"
"ds_read_b128 v[76:79], v94 offset:48128\n"
"s_waitcnt lgkmcnt(7)\n"
"v_mfma_f32_16x16x32_bf16 v[12:15], v[48:51], a[20:23], v[12:15]\n"
"ds_read_b128 v[32:35], v95 offset:7168\n"
"s_waitcnt lgkmcnt(7)\n"
"v_mfma_f32_16x16x32_bf16 v[16:19], v[52:55], a[20:23], v[16:19]\n"
"ds_read_b128 v[36:39], v95 offset:27648\n"
"s_waitcnt vmcnt(10) lgkmcnt(7)\n"
"v_mfma_f32_16x16x32_bf16 v[0:3], v[56:59], a[24:27], v[0:3]\n"
"ds_read_b128 v[40:43], v93 offset:7168\n"
"s_waitcnt lgkmcnt(7)\n"
"v_mfma_f32_16x16x32_bf16 v[4:7], v[60:63], a[24:27], v[4:7]\n"
"ds_read_b128 v[44:47], v93 offset:27648\n"
"s_waitcnt lgkmcnt(7)\n"
"v_mfma_f32_16x16x32_bf16 v[8:11], v[64:67], a[24:27], v[8:11]\n"
"ds_read_b128 v[48:51], v94 offset:49152\n"
"s_waitcnt lgkmcnt(7)\n"
"v_mfma_f32_16x16x32_bf16 v[12:15], v[68:71], a[24:27], v[12:15]\n"
"ds_read_b128 v[52:55], v95 offset:8192\n"
"s_waitcnt lgkmcnt(7)\n"
"v_mfma_f32_16x16x32_bf16 v[16:19], v[72:75], a[24:27], v[16:19]\n"
"ds_read_b128 v[56:59], v95 offset:28672\n"
"s_waitcnt lgkmcnt(7)\n"
"v_mfma_f32_16x16x32_bf16 v[0:3], v[76:79], v[164:167], v[0:3]\n"
"ds_read_b128 v[60:63], v93 offset:8192\n"
"s_waitcnt lgkmcnt(7)\n"
"v_mfma_f32_16x16x32_bf16 v[4:7], v[32:35], v[164:167], v[4:7]\n"
"PG22_%=:\n"
"s_cbranch_execz RPG22_%=\n"
"ds_read_b128 v[64:67], v93 offset:28672\n"
"s_waitcnt lgkmcnt(7)\n"
"v_mfma_f32_16x16x32_bf16 v[8:11], v[36:39], v[164:167], v[8:11]\n"
"ds_read_b128 v[68:71], v94 offset:50176\n"
"s_waitcnt lgkmcnt(7)\n"
"v_mfma_f32_16x16x32_bf16 v[12:15], v[40:43], v[164:167], v[12:15]\n"
"ds_read_b128 v[72:75], v95 offset:9216\n"
"s_waitcnt lgkmcnt(7)\n"
"v_mfma_f32_16x16x32_bf16 v[16:19], v[44:47], v[164:167], v[16:19]\n"
"ds_read_b128 v[76:79], v95 offset:29696\n"
"s_waitcnt vmcnt(9) lgkmcnt(7)\n"
"v_mfma_f32_16x16x32_bf16 v[0:3], v[48:51], a[32:35], v[0:3]\n"
"ds_read_b128 v[32:35], v93 offset:9216\n"
"s_waitcnt lgkmcnt(7)\n"
"v_mfma_f32_16x16x32_bf16 v[4:7], v[52:55], a[32:35], v[4:7]\n"
"ds_read_b128 v[36:39], v93 offset:29696\n"
"s_waitcnt lgkmcnt(7)\n"
"v_mfma_f32_16x16x32_bf16 v[8:11], v[56:59], a[32:35], v[8:11]\n"
"ds_read_b128 v[40:43], v94 offset:51200\n"
"s_waitcnt lgkmcnt(7)\n"
"v_mfma_f32_16x16x32_bf16 v[12:15], v[60:63], a[32:35], v[12:15]\n"
"ds_read_b128 v[44:47], v95 offset:10240\n"
"s_waitcnt lgkmcnt(7)\n"
"v_mfma_f32_16x16x32_bf16 v[16:19], v[64:67], a[32:35], v[16:19]\n"
"ds_read_b128 v[48:51], v95 offset:30720\n"
"s_waitcnt vmcnt(8) lgkmcnt(7)\n"
"v_mfma_f32_16x16x32_bf16 v[0:3], v[68:71], a[36:39], v[0:3]\n"
"ds_read_b128 v[52:55], v93 offset:10240\n"
"s_waitcnt lgkmcnt(7)\n"
"v_mfma_f32_16x16x32_bf16 v[4:7], v[72:75], a[36:39], v[4:7]\n"
"ds_read_b128 v[56:59], v93 offset:30720\n"
"s_waitcnt lgkmcnt(7)\n"
"v_mfma_f32_16x16x32_bf16 v[8:11], v[76:79], a[36:39], v[8:11]\n"
"ds_read_b128 v[60:63], v94 offset:52224\n"
"s_waitcnt lgkmcnt(7)\n"
"v_mfma_f32_16x16x32_bf16 v[12:15], v[32:35], a[36:39], v[12:15]\n"
"ds_read_b128 v[64:67], v95 offset:11264\n"
"s_waitcnt lgkmcnt(7)\n"
"v_mfma_f32_16x16x32_bf16 v[16:19], v[36:39], a[36:39], v[16:19]\n"
"ds_read_b128 v[68:71], v95 offset:31744\n"
"s_waitcnt vmcnt(7) lgkmcnt(7)\n"
"v_mfma_f32_16x16x32_bf16 v[0:3], v[40:43], a[40:43], v[0:3]\n"
"ds_read_b128 v[72:75], v93 offset:11264\n"
"s_waitcnt lgkmcnt(7)\n"
"v_mfma_f32_16x16x32_bf16 v[4:7], v[44:47], a[40:43], v[4:7]\n"
"ds_read_b128 v[76:79], v93 offset:31744\n"
"s_waitcnt lgkmcnt(7)\n"
"v_mfma_f32_16x16x32_bf16 v[8:11], v[48:51], a[40:43], v[8:11]\n"
"ds_read_b128 v[32:35], v94 offset:53248\n"
"s_waitcnt lgkmcnt(7)\n"
"v_mfma_f32_16x16x32_bf16 v[12:15], v[52:55], a[40:43], v[12:15]\n"
"ds_read_b128 v[36:39], v95 offset:12288\n"
"s_waitcnt lgkmcnt(7)\n"
"v_mfma_f32_16x16x32_bf16 v[16:19], v[56:59], a[40:43], v[16:19]\n"
"ds_read_b128 v[40:43], v95 offset:32768\n"
"s_waitcnt vmcnt(6) lgkmcnt(7)\n"
"v_mfma_f32_16x16x32_bf16 v[0:3], v[60:63], a[44:47], v[0:3]\n"
"ds_read_b128 v[44:47], v93 offset:12288\n"
"s_waitcnt lgkmcnt(7)\n"
"v_mfma_f32_16x16x32_bf16 v[4:7], v[64:67], a[44:47], v[4:7]\n"
"ds_read_b128 v[48:51], v93 offset:32768\n"
"s_waitcnt lgkmcnt(7)\n"
"v_mfma_f32_16x16x32_bf16 v[8:11], v[68:71], a[44:47], v[8:11]\n"
"ds_read_b128 v[52:55], v94 offset:54272\n"
"s_waitcnt lgkmcnt(7)\n"
"v_mfma_f32_16x16x32_bf16 v[12:15], v[72:75], a[44:47], v[12:15]\n"
"ds_read_b128 v[56:59], v95 offset:13312\n"
"s_waitcnt lgkmcnt(7)\n"
"v_mfma_f32_16x16x32_bf16 v[16:19], v[76:79], a[44:47], v[16:19]\n"
"ds_read_b128 v[60:63], v95 offset:33792\n"
"s_waitcnt lgkmcnt(7)\n"
"v_mfma_f32_16x16x32_bf16 v[0:3], v[32:35], v[168:171], v[0:3]\n"
"ds_read_b128 v[64:67], v93 offset:13312\n"
"s_waitcnt lgkmcnt(7)\n"
"v_mfma_f32_16x16x32_bf16 v[4:7], v[36:39], v[168:171], v[4:7]\n"
"ds_read_b128 v[68:71], v93 offset:33792\n"
"s_waitcnt lgkmcnt(7)\n"
"v_mfma_f32_16x16x32_bf16 v[8:11], v[40:43], v[168:171], v[8:11]\n"
"ds_read_b128 v[72:75], v94 offset:55296\n"
"s_waitcnt lgkmcnt(7)\n"
"v_mfma_f32_16x16x32_bf16 v[12:15], v[44:47], v[168:171], v[12:15]\n"
"ds_read_b128 v[76:79], v95 offset:14336\n"
"s_waitcnt lgkmcnt(7)\n"
"v_mfma_f32_16x16x32_bf16 v[16:19], v[48:51], v[168:171], v[16:19]\n"
"ds_read_b128 v[32:35], v95 offset:34816\n"
"s_waitcnt vmcnt(5) lgkmcnt(7)\n"
"v_mfma_f32_16x16x32_bf16 v[0:3], v[52:55], a[52:55], v[0:3]\n"
"ds_read_b128 v[36:39], v93 offset:14336\n"
"s_waitcnt lgkmcnt(7)\n"
"v_mfma_f32_16x16x32_bf16 v[4:7], v[56:59], a[52:55], v[4:7]\n"
"ds_read_b128 v[40:43], v93 offset:34816\n"
"s_waitcnt lgkmcnt(7)\n"
"v_mfma_f32_16x16x32_bf16 v[8:11], v[60:63], a[52:55], v[8:11]\n"
"ds_read_b128 v[44:47], v94 offset:56320\n"
"s_waitcnt lgkmcnt(7)\n"
"v_mfma_f32_16x16x32_bf16 v[12:15], v[64:67], a[52:55], v[12:15]\n"
"ds_read_b128 v[48:51], v95 offset:15360\n"
"s_waitcnt lgkmcnt(7)\n"
"v_mfma_f32_16x16x32_bf16 v[16:19], v[68:71], a[52:55], v[16:19]\n"
"ds_read_b128 v[52:55], v95 offset:35840\n"
"s_waitcnt vmcnt(4) lgkmcnt(7)\n"
"v_mfma_f32_16x16x32_bf16 v[0:3], v[72:75], a[56:59], v[0:3]\n"
"ds_read_b128 v[56:59], v93 offset:15360\n"
"s_waitcnt lgkmcnt(7)\n"
"v_mfma_f32_16x16x32_bf16 v[4:7], v[76:79], a[56:59], v[4:7]\n"
"ds_read_b128 v[60:63], v93 offset:35840\n"
"s_waitcnt lgkmcnt(7)\n"
"v_mfma_f32_16x16x32_bf16 v[8:11], v[32:35], a[56:59], v[8:11]\n"
"ds_read_b128 v[64:67], v94 offset:57344\n"
"s_waitcnt lgkmcnt(7)\n"
"v_mfma_f32_16x16x32_bf16 v[12:15], v[36:39], a[56:59], v[12:15]\n"
"ds_read_b128 v[68:71], v95 offset:16384\n"
"s_waitcnt lgkmcnt(7)\n"
"v_mfma_f32_16x16x32_bf16 v[16:19], v[40:43], a[56:59], v[16:19]\n"
"ds_read_b128 v[72:75], v95 offset:36864\n"
"s_waitcnt vmcnt(3) lgkmcnt(7)\n"
"v_mfma_f32_16x16x32_bf16 v[0:3], v[44:47], a[60:63], v[0:3]\n"
"ds_read_b128 v[76:79], v93 offset:16384\n"
"s_waitcnt lgkmcnt(7)\n"
"v_mfma_f32_16x16x32_bf16 v[4:7], v[48:51], a[60:63], v[4:7]\n"
"ds_read_b128 v[32:35], v93 offset:36864\n"
"s_waitcnt lgkmcnt(7)\n"
"v_mfma_f32_16x16x32_bf16 v[8:11], v[52:55], a[60:63], v[8:11]\n"
"ds_read_b128 v[36:39], v94 offset:58368\n"
"s_waitcnt lgkmcnt(7)\n"
"v_mfma_f32_16x16x32_bf16 v[12:15], v[56:59], a[60:63], v[12:15]\n"
"ds_read_b128 v[40:43], v95 offset:17408\n"
"s_waitcnt lgkmcnt(7)\n"
"v_mfma_f32_16x16x32_bf16 v[16:19], v[60:63], a[60:63], v[16:19]\n"
"ds_read_b128 v[44:47], v95 offset:37888\n"
"s_waitcnt vmcnt(2) lgkmcnt(7)\n"
"v_mfma_f32_16x16x32_bf16 v[0:3], v[64:67], a[64:67], v[0:3]\n"
"ds_read_b128 v[48:51], v93 offset:17408\n"
"s_waitcnt lgkmcnt(7)\n"
"v_mfma_f32_16x16x32_bf16 v[4:7], v[68:71], a[64:67], v[4:7]\n"
"ds_read_b128 v[52:55], v93 offset:37888\n"
"s_waitcnt lgkmcnt(7)\n"
"v_mfma_f32_16x16x32_bf16 v[8:11], v[72:75], a[64:67], v[8:11]\n"
"ds_read_b128 v[56:59], v94 offset:59392\n"
"s_waitcnt lgkmcnt(7)\n"
"v_mfma_f32_16x16x32_bf16 v[12:15], v[76:79], a[64:67], v[12:15]\n"
"ds_read_b128 v[60:63], v95 offset:18432\n"
"s_waitcnt lgkmcnt(7)\n"
"v_mfma_f32_16x16x32_bf16 v[16:19], v[32:35], a[64:67], v[16:19]\n"
"ds_read_b128 v[64:67], v95 offset:38912\n"
"s_waitcnt lgkmcnt(7)\n"
"v_mfma_f32_16x16x32_bf16 v[0:3], v[36:39], v[172:175], v[0:3]\n"
"ds_read_b128 v[68:71], v93 offset:18432\n"
"s_waitcnt lgkmcnt(7)\n"
"v_mfma_f32_16x16x32_bf16 v[4:7], v[40:43], v[172:175], v[4:7]\n"
"ds_read_b128 v[72:75], v93 offset:38912\n"
"s_waitcnt lgkmcnt(7)\n"
"v_mfma_f32_16x16x32_bf16 v[8:11], v[44:47], v[172:175], v[8:11]\n"
"ds_read_b128 v[76:79], v94 offset:60416\n"
"s_waitcnt lgkmcnt(7)\n"
"v_mfma_f32_16x16x32_bf16 v[12:15], v[48:51], v[172:175], v[12:15]\n"
"ds_read_b128 v[32:35], v95 offset:19456\n"
"s_waitcnt lgkmcnt(7)\n"
"v_mfma_f32_16x16x32_bf16 v[16:19], v[52:55], v[172:175], v[16:19]\n"
"ds_read_b128 v[36:39], v95 offset:39936\n"
"s_waitcnt vmcnt(1) lgkmcnt(7)\n"
"v_mfma_f32_16x16x32_bf16 v[0:3], v[56:59], a[72:75], v[0:3]\n"
"ds_read_b128 v[40:43], v93 offset:19456\n"
"s_waitcnt lgkmcnt(7)\n"
"v_mfma_f32_16x16x32_bf16 v[4:7], v[60:63], a[72:75], v[4:7]\n"
"ds_read_b128 v[44:47], v93 offset:39936\n"
"s_waitcnt lgkmcnt(7)\n"
"v_mfma_f32_16x16x32_bf16 v[8:11], v[64:67], a[72:75], v[8:11]\n"
"s_waitcnt lgkmcnt(6)\n"
"v_mfma_f32_16x16x32_bf16 v[12:15], v[68:71], a[72:75], v[12:15]\n"
"s_waitcnt lgkmcnt(5)\n"
"v_mfma_f32_16x16x32_bf16 v[16:19], v[72:75], a[72:75], v[16:19]\n"
"s_waitcnt vmcnt(0) lgkmcnt(4)\n"
"v_mfma_f32_16x16x32_bf16 v[0:3], v[76:79], a[76:79], v[0:3]\n"
"s_waitcnt lgkmcnt(3)\n"
"v_mfma_f32_16x16x32_bf16 v[4:7], v[32:35], a[76:79], v[4:7]\n"
"s_waitcnt lgkmcnt(2)\n"
"v_mfma_f32_16x16x32_bf16 v[8:11], v[36:39], a[76:79], v[8:11]\n"
"s_waitcnt lgkmcnt(1)\n"
"v_mfma_f32_16x16x32_bf16 v[12:15], v[40:43], a[76:79], v[12:15]\n"
"s_waitcnt lgkmcnt(0)\n"
"v_mfma_f32_16x16x32_bf16 v[16:19], v[44:47], a[76:79], v[16:19]\n"
"s_nop 7\n"
"v_cmp_u_f32 vcc, v16, v16\n"
"s_nop 0\n"
"s_cbranch_vccnz REDO_G2_%=\n"
"DONE_G2_%=:\n"
"v_pk_add_f32 v[0:1], v[0:1], v[136:137]\n"
"v_pk_add_f32 v[2:3], v[2:3], v[138:139]\n"
"v_pk_add_f32 v[4:5], v[4:5], v[140:141]\n"
"v_pk_add_f32 v[6:7], v[6:7], v[142:143]\n"
"v_pk_add_f32 v[8:9], v[8:9], v[144:145]\n"
"v_pk_add_f32 v[10:11], v[10:11], v[146:147]\n"
"v_pk_add_f32 v[12:13], v[12:13], v[148:149]\n"
"v_pk_add_f32 v[14:15], v[14:15], v[150:151]\n"
"v_pk_add_f32 v[16:17], v[16:17], v[152:153]\n"
"v_pk_add_f32 v[18:19], v[18:19], v[154:155]\n"
"v_cvt_pk_bf16_f32 v210, v0, v1\n"
"v_cvt_pk_bf16_f32 v211, v2, v3\n"
"v_cvt_pk_bf16_f32 v212, v4, v5\n"
"v_cvt_pk_bf16_f32 v213, v6, v7\n"
"v_cvt_pk_bf16_f32 v214, v8, v9\n"
"v_cvt_pk_bf16_f32 v215, v10, v11\n"
"v_cvt_pk_bf16_f32 v216, v12, v13\n"
"v_cvt_pk_bf16_f32 v217, v14, v15\n"
"v_cvt_pk_bf16_f32 v218, v16, v17\n"
"v_cvt_pk_bf16_f32 v219, v18, v19\n"
"s_mul_i32 s38, s42, 20480\n"
"s_add_u32 s38, s38, 112197632\n"
"s_add_u32 s39, s38, s41\n"
"s_add_u32 s38, s38, s40\n"
"buffer_store_dwordx4 v[210:213], v251, s[96:99], s38 offen offset:0 sc1\n"
"buffer_store_dwordx4 v[214:217], v251, s[96:99], s38 offen offset:1024 sc1\n"
"buffer_store_dwordx2 v[218:219], v250, s[96:99], s39 offen sc1\n"
"v_pk_add_f32 v[24:25], v[0:1], v[2:3]\n"
"v_pk_mul_f32 v[26:27], v[0:1], v[0:1]\n"
"v_pk_fma_f32 v[26:27], v[2:3], v[2:3], v[26:27]\n"
"v_pk_add_f32 v[24:25], v[24:25], v[4:5]\n"
"v_pk_fma_f32 v[26:27], v[4:5], v[4:5], v[26:27]\n"
"v_pk_add_f32 v[24:25], v[24:25], v[6:7]\n"
"v_pk_fma_f32 v[26:27], v[6:7], v[6:7], v[26:27]\n"
"v_pk_add_f32 v[24:25], v[24:25], v[8:9]\n"
"v_pk_fma_f32 v[26:27], v[8:9], v[8:9], v[26:27]\n"
"v_pk_add_f32 v[24:25], v[24:25], v[10:11]\n"
"v_pk_fma_f32 v[26:27], v[10:11], v[10:11], v[26:27]\n"
"v_pk_add_f32 v[24:25], v[24:25], v[12:13]\n"
"v_pk_fma_f32 v[26:27], v[12:13], v[12:13], v[26:27]\n"
"v_pk_add_f32 v[24:25], v[24:25], v[14:15]\n"
"v_pk_fma_f32 v[26:27], v[14:15], v[14:15], v[26:27]\n"
"v_pk_add_f32 v[24:25], v[24:25], v[16:17]\n"
"v_pk_fma_f32 v[26:27], v[16:17], v[16:17], v[26:27]\n"
"v_pk_add_f32 v[24:25], v[24:25], v[18:19]\n"
"v_pk_fma_f32 v[26:27], v[18:19], v[18:19], v[26:27]\n"
"v_add_f32 v28, v24, v25\n"
"v_add_f32 v29, v26, v27\n"
"v_mov_b32 v232, v28\n"
"v_mov_b32 v233, v29\n"
"s_nop 0\n"
"v_permlane16_swap_b32 v28, v232\n"
"v_permlane16_swap_b32 v29, v233\n"
"v_add_f32 v28, v28, v232\n"
"v_add_f32 v29, v29, v233\n"
"v_mov_b32 v232, v28\n"
"v_mov_b32 v233, v29\n"
"s_nop 0\n"
"v_permlane32_swap_b32 v28, v232\n"
"v_permlane32_swap_b32 v29, v233\n"
"v_add_f32 v28, v28, v232\n"
"v_add_f32 v29, v29, v233\n"
"s_lshl_b32 s38, s65, 3\n"
"s_add_u32 s38, s38, 122683392\n"
"buffer_store_dwordx2 v[28:29], v205, s[96:99], s38 offen sc1\n"
"buffer_load_dwordx4 v251, s[96:99], s37 offen offset:0 sc1 lds\n"
"buffer_load_dwordx4 v251, s[96:99], s37 offen offset:1024 sc1 lds\n"
"buffer_load_dwordx4 v251, s[96:99], s37 offen offset:2048 sc1 lds\n"
"buffer_load_dwordx4 v251, s[96:99], s37 offen offset:3072 sc1 lds\n"
"s_add_u32 m0, s45, 49152\n"
"s_add_u32 s37, s37, 4096\n"
"buffer_load_dwordx4 v251, s[96:99], s37 offen offset:0 sc1 lds\n"
"buffer_load_dwordx4 v251, s[96:99], s37 offen offset:1024 sc1 lds\n"
"buffer_load_dwordx4 v251, s[96:99], s37 offen offset:2048 sc1 lds\n"
"buffer_load_dwordx4 v251, s[96:99], s37 offen offset:3072 sc1 lds\n"
"s_add_u32 m0, s45, 53248\n"
"s_add_u32 s37, s37, 4096\n"
"buffer_load_dwordx4 v251, s[96:99], s37 offen offset:0 sc1 lds\n"
"buffer_load_dwordx4 v251, s[96:99], s37 offen offset:1024 sc1 lds\n"
"buffer_load_dwordx4 v251, s[96:99], s37 offen offset:2048 sc1 lds\n"
"s_barrier\n"
"s_mul_i32 s45, %2, 15360\n"
"s_add_u32 s37, s46, s45\n"
"s_add_u32 s37, s37, 61440\n"
"s_add_u32 s45, s45, %3\n"
"s_add_u32 m0, s45, 102400\n"
"s_nop 0\n"
"buffer_load_dwordx4 v251, s[96:99], s37 offen offset:0 sc1 lds\n"
"buffer_load_dwordx4 v251, s[96:99], s37 offen offset:1024 sc1 lds\n"
"buffer_load_dwordx4 v251, s[96:99], s37 offen offset:2048 sc1 lds\n"
"buffer_load_dwordx4 v251, s[96:99], s37 offen offset:3072 sc1 lds\n"
"s_add_u32 m0, s45, 106496\n"
"s_add_u32 s37, s37, 4096\n"
"buffer_load_dwordx4 v251, s[96:99], s37 offen offset:0 sc1 lds\n"
"buffer_load_dwordx4 v251, s[96:99], s37 offen offset:1024 sc1 lds\n"
"buffer_load_dwordx4 v251, s[96:99], s37 offen offset:2048 sc1 lds\n"
"buffer_load_dwordx4 v251, s[96:99], s37 offen offset:3072 sc1 lds\n"
"s_add_u32 m0, s45, 110592\n"
"s_add_u32 s37, s37, 4096\n"
"buffer_load_dwordx4 v251, s[96:99], s37 offen offset:0 sc1 lds\n"
"buffer_load_dwordx4 v251, s[96:99], s37 offen offset:1024 sc1 lds\n"
"buffer_load_dwordx4 v251, s[96:99], s37 offen offset:2048 sc1 lds\n"
"buffer_load_dwordx4 v251, s[96:99], s37 offen offset:3072 sc1 lds\n"
"s_add_u32 m0, s45, 114688\n"
"s_add_u32 s37, s37, 4096\n"
"buffer_load_dwordx4 v251, s[96:99], s37 offen offset:0 sc1 lds\n"
"buffer_load_dwordx4 v251, s[96:99], s37 offen offset:1024 sc1 lds\n"
"buffer_load_dwordx4 v251, s[96:99], s37 offen offset:2048 sc1 lds\n"
"s_mul_i32 s45, %2, 5120\n"
"s_add_u32 s37, s47, s45\n"
"s_add_u32 s37, s37, 0\n"
"s_add_u32 s45, s45, %3\n"
"s_add_u32 m0, s45, 0\n"
"s_nop 0\n"
"buffer_load_dwordx4 v251, s[96:99], s37 offen offset:0 sc1 lds\n"
"buffer_load_dwordx4 v251, s[96:99], s37 offen offset:1024 sc1 lds\n"
"buffer_load_dwordx4 v251, s[96:99], s37 offen offset:2048 sc1 lds\n"
"buffer_load_dwordx4 v251, s[96:99], s37 offen offset:3072 sc1 lds\n"
"s_add_u32 m0, s45, 4096\n"
"s_add_u32 s37, s37, 4096\n"
"buffer_load_dwordx4 v251, s[96:99], s37 offen offset:0 sc1 lds\n"
"v_lshl_add_u32 v246, s42, 4, v249\n"
"v_mul_u32_u24 v246, 1280, v246\n"
"v_lshl_add_u32 v246, v248, 4, v246\n"
"s_lshl_b32 s38, s49, 1\n"
"v_add_u32 v204, s38, v246\n"
"s_lshl_b32 s38, s50, 1\n"
"v_add_u32 v205, s38, v246\n"
"s_mov_b32 s38, 104870400\n"
"buffer_load_dwordx4 v[184:187], v254, s[96:99], s38 offen offset:0 sc1\n"
"buffer_load_dwordx4 v[188:191], v254, s[96:99], s38 offen offset:16 sc1\n"
"buffer_load_dwordx4 v[192:195], v254, s[96:99], s38 offen offset:128 sc1\n"
"buffer_load_dwordx4 v[196:199], v254, s[96:99], s38 offen offset:144 sc1\n"
"buffer_load_dwordx4 v[200:203], v255, s[96:99], s38 offen sc1\n"
"s_mul_i32 s38, s67, 7\n"
"s_add_u32 s40, s38, s43\n"
"s_cmp_eq_u32 s43, 0\n"
"s_cselect_b32 s39, 6, 0\n"
"s_add_u32 s41, s38, s39\n"
"s_lshr_b32 s41, s41, 1\n"
"s_lshl_b32 s49, s40, 4\n"
"s_lshl_b32 s50, s41, 5\n"
"s_lshl_b32 s39, s43, 2\n"
"s_add_u32 s50, s50, s39\n"
"s_lshl_b32 s40, s40, 9\n"
"s_lshl_b32 s41, s41, 10\n"
"s_lshl_b32 s38, s49, 2\n"
"v_lshl_add_u32 v254, v248, 5, s38\n"
"s_lshl_b32 s38, s50, 2\n"
"v_lshl_add_u32 v255, v248, 5, s38\n"
"s_lshl_b32 s48, s66, 3\n"
"s_lshl_b32 s38, %2, 1\n"
"s_add_u32 s48, s48, s38\n"
"v_add_u32 v60, 20480, v251\n"
"v_lshl_add_u32 v61, s48, 4, v249\n"
"v_lshlrev_b32 v61, 6, v61\n"
"v_lshl_add_u32 v61, v248, 4, v61\n"
"s_mov_b32 s60, 122683392\n"
"s_mul_i32 s38, s43, 57344\n"
"v_add_u32 v84, s38, v251\n"
"v_add_u32 v85, 57344, v84\n"
"v_add_u32 v86, 114688, v84\n"
"v_add_u32 v87, 172032, v84\n"
"s_cmp_eq_u32 s43, 0\n"
"s_cselect_b32 s38, 229376, 0\n"
"v_add_u32 v88, s38, v251\n"
"s_mul_i32 s38, %2, 14336\n"
"s_mul_i32 s61, s65, 286720\n"
"s_add_u32 s61, s61, s38\n"
"s_add_u32 s61, s61, 78643200\n"
"s_mov_b32 s72, s61\n"
"v_mov_b32 v68, 0x3f803f80\n"
"v_mov_b32 v69, 0x3f803f80\n"
"v_mov_b32 v70, 0x3f803f80\n"
"v_mov_b32 v71, 0x3f803f80\n"
"s_mul_i32 s38, %2, 5120\n"
"v_add_u32 v72, s38, v93\n"
"v_add_u32 v73, s38, v94\n"
"v_add_u32 v74, s38, v95\n"
"v_lshlrev_b32 v75, 4, v248\n"
"s_add_u32 s38, %3, 20480\n"
"v_add_u32 v75, s38, v75\n"
"s_lshl_b32 s38, %2, 10\n"
"v_add_u32 v76, s38, v75\n"
"s_mov_b64 exec, 0\n"
"s_cmp_lg_u32 %2, 0\n"
"s_cbranch_scc1 TWE0_%=\n"
"s_branch PG23_%=\n"
"RPG23_%=:\n"
"s_nop 0\n"
"s_nop 0\n"
"s_nop 0\n"
"s_nop 0\n"
"s_nop 0\n"
"TWE0_%=:\n"
"s_cmp_lg_u32 %2, 1\n"
"s_cbranch_scc1 TWE1_%=\n"
"s_branch PG24_%=\n"
"RPG24_%=:\n"
"s_nop 0\n"
"s_nop 0\n"
"s_nop 0\n"
"s_nop 0\n"
"s_nop 0\n"
"TWE1_%=:\n"
"s_cmp_lg_u32 %2, 2\n"
"s_cbranch_scc1 TWE2_%=\n"
"s_branch PG25_%=\n"
"RPG25_%=:\n"
"s_nop 0\n"
"s_nop 0\n"
"s_nop 0\n"
"s_nop 0\n"
"s_nop 0\n"
"TWE2_%=:\n"
"s_cmp_lg_u32 %2, 3\n"
"s_cbranch_scc1 TWE3_%=\n"
"s_nop 0\n"
"s_nop 0\n"
"s_nop 0\n"
"s_nop 0\n"
"s_nop 0\n"
"s_nop 0\n"
"TWE3_%=:\n"
"s_mov_b64 exec, -1\n"
"s_waitcnt vmcnt(5)\n"
"s_barrier\n"
"T_G3_%=:\n"
"s_mov_b32 s63, 3\n"
"buffer_load_dwordx4 v[124:127], v61, s[96:99], s60 offen sc1\n"
"buffer_load_dwordx4 v[128:131], v61, s[96:99], s60 offen offset:1024 sc1\n"
"ds_read_b128 v[132:135], v72 offset:40960\n"
"ds_read_b128 v[136:139], v73 offset:0\n"
"ds_read_b128 v[140:143], v73 offset:20480\n"
"ds_read_b128 v[144:147], v73 offset:40960\n"
"ds_read_b128 v[148:151], v74 offset:0\n"
"ds_read_b128 v[152:155], v74 offset:20480\n"
"ds_read_b128 v[156:159], v72 offset:0\n"
"ds_read_b128 v[160:163], v72 offset:41984\n"
"s_waitcnt lgkmcnt(7)\n"
"v_mfma_f32_16x16x32_bf16 v[96:99], v[132:135], v[68:71], 0\n"
"ds_read_b128 v[164:167], v73 offset:1024\n"
"s_waitcnt lgkmcnt(7)\n"
"v_mfma_f32_16x16x32_bf16 v[100:103], v[136:139], v[68:71], 0\n"
"ds_read_b128 v[168:171], v73 offset:21504\n"
"s_waitcnt lgkmcnt(7)\n"
"v_mfma_f32_16x16x32_bf16 v[104:107], v[140:143], v[68:71], 0\n"
"ds_read_b128 v[172:175], v73 offset:41984\n"
"s_waitcnt lgkmcnt(7)\n"
"v_mfma_f32_16x16x32_bf16 v[108:111], v[144:147], v[68:71], 0\n"
"ds_read_b128 v[176:179], v74 offset:1024\n"
"s_waitcnt lgkmcnt(7)\n"
"v_mfma_f32_16x16x32_bf16 v[112:115], v[148:151], v[68:71], 0\n"
"ds_read_b128 v[132:135], v74 offset:21504\n"
"s_waitcnt lgkmcnt(7)\n"
"v_mfma_f32_16x16x32_bf16 v[116:119], v[152:155], v[68:71], 0\n"
"ds_read_b128 v[136:139], v72 offset:1024\n"
"s_waitcnt lgkmcnt(7)\n"
"v_mfma_f32_16x16x32_bf16 v[120:123], v[156:159], v[68:71], 0\n"
"ds_read_b128 v[140:143], v72 offset:43008\n"
"s_waitcnt lgkmcnt(7)\n"
"v_mfma_f32_16x16x32_bf16 v[96:99], v[160:163], v[68:71], v[96:99]\n"
"ds_read_b128 v[144:147], v73 offset:2048\n"
"s_waitcnt lgkmcnt(7)\n"
"v_mfma_f32_16x16x32_bf16 v[100:103], v[164:167], v[68:71], v[100:103]\n"
"ds_read_b128 v[148:151], v73 offset:22528\n"
"s_waitcnt lgkmcnt(7)\n"
"v_mfma_f32_16x16x32_bf16 v[104:107], v[168:171], v[68:71], v[104:107]\n"
"ds_read_b128 v[152:155], v73 offset:43008\n"
"s_waitcnt lgkmcnt(7)\n"
"v_mfma_f32_16x16x32_bf16 v[108:111], v[172:175], v[68:71], v[108:111]\n"
"ds_read_b128 v[156:159], v74 offset:2048\n"
"s_waitcnt lgkmcnt(7)\n"
"v_mfma_f32_16x16x32_bf16 v[112:115], v[176:179], v[68:71], v[112:115]\n"
"ds_read_b128 v[160:163], v74 offset:22528\n"
"s_waitcnt lgkmcnt(7)\n"
"v_mfma_f32_16x16x32_bf16 v[116:119], v[132:135], v[68:71], v[116:119]\n"
"ds_read_b128 v[164:167], v72 offset:2048\n"
"s_waitcnt lgkmcnt(7)\n"
"v_mfma_f32_16x16x32_bf16 v[120:123], v[136:139], v[68:71], v[120:123]\n"
"ds_read_b128 v[168:171], v72 offset:44032\n"
"s_waitcnt lgkmcnt(7)\n"
"v_mfma_f32_16x16x32_bf16 v[96:99], v[140:143], v[68:71], v[96:99]\n"
"ds_read_b128 v[172:175], v73 offset:3072\n"
"s_waitcnt lgkmcnt(7)\n"
"v_mfma_f32_16x16x32_bf16 v[100:103], v[144:147], v[68:71], v[100:103]\n"
"ds_read_b128 v[176:179], v73 offset:23552\n"
"s_waitcnt lgkmcnt(7)\n"
"v_mfma_f32_16x16x32_bf16 v[104:107], v[148:151], v[68:71], v[104:107]\n"
"ds_read_b128 v[132:135], v73 offset:44032\n"
"s_waitcnt lgkmcnt(7)\n"
"v_mfma_f32_16x16x32_bf16 v[108:111], v[152:155], v[68:71], v[108:111]\n"
"ds_read_b128 v[136:139], v74 offset:3072\n"
"s_waitcnt lgkmcnt(7)\n"
"v_mfma_f32_16x16x32_bf16 v[112:115], v[156:159], v[68:71], v[112:115]\n"
"ds_read_b128 v[140:143], v74 offset:23552\n"
"s_waitcnt lgkmcnt(7)\n"
"v_mfma_f32_16x16x32_bf16 v[116:119], v[160:163], v[68:71], v[116:119]\n"
"ds_read_b128 v[144:147], v72 offset:3072\n"
"s_waitcnt lgkmcnt(7)\n"
"v_mfma_f32_16x16x32_bf16 v[120:123], v[164:167], v[68:71], v[120:123]\n"
"ds_read_b128 v[148:151], v72 offset:45056\n"
"s_waitcnt lgkmcnt(7)\n"
"v_mfma_f32_16x16x32_bf16 v[96:99], v[168:171], v[68:71], v[96:99]\n"
"ds_read_b128 v[152:155], v73 offset:4096\n"
"s_waitcnt lgkmcnt(7)\n"
"v_mfma_f32_16x16x32_bf16 v[100:103], v[172:175], v[68:71], v[100:103]\n"
"ds_read_b128 v[156:159], v73 offset:24576\n"
"s_waitcnt lgkmcnt(7)\n"
"v_mfma_f32_16x16x32_bf16 v[104:107], v[176:179], v[68:71], v[104:107]\n"
"ds_read_b128 v[160:163], v73 offset:45056\n"
"s_waitcnt lgkmcnt(7)\n"
"v_mfma_f32_16x16x32_bf16 v[108:111], v[132:135], v[68:71], v[108:111]\n"
"ds_read_b128 v[164:167], v74 offset:4096\n"
"s_waitcnt lgkmcnt(7)\n"
"v_mfma_f32_16x16x32_bf16 v[112:115], v[136:139], v[68:71], v[112:115]\n"
"ds_read_b128 v[168:171], v74 offset:24576\n"
"s_waitcnt lgkmcnt(7)\n"
"v_mfma_f32_16x16x32_bf16 v[116:119], v[140:143], v[68:71], v[116:119]\n"
"ds_read_b128 v[172:175], v72 offset:4096\n"
"s_waitcnt lgkmcnt(7)\n"
"v_mfma_f32_16x16x32_bf16 v[120:123], v[144:147], v[68:71], v[120:123]\n"
"s_waitcnt lgkmcnt(6)\n"
"v_mfma_f32_16x16x32_bf16 v[96:99], v[148:151], v[68:71], v[96:99]\n"
"s_waitcnt lgkmcnt(5)\n"
"v_mfma_f32_16x16x32_bf16 v[100:103], v[152:155], v[68:71], v[100:103]\n"
"s_waitcnt lgkmcnt(4)\n"
"v_mfma_f32_16x16x32_bf16 v[104:107], v[156:159], v[68:71], v[104:107]\n"
"s_waitcnt lgkmcnt(3)\n"
"v_mfma_f32_16x16x32_bf16 v[108:111], v[160:163], v[68:71], v[108:111]\n"
"s_waitcnt lgkmcnt(2)\n"
"v_mfma_f32_16x16x32_bf16 v[112:115], v[164:167], v[68:71], v[112:115]\n"
"s_waitcnt lgkmcnt(1)\n"
"v_mfma_f32_16x16x32_bf16 v[116:119], v[168:171], v[68:71], v[116:119]\n"
"s_waitcnt lgkmcnt(0)\n"
"v_mfma_f32_16x16x32_bf16 v[120:123], v[172:175], v[68:71], v[120:123]\n"
"ds_write_b128 v76, v[96:99] offset:0\n"
"ds_write_b128 v76, v[100:103] offset:64\n"
"ds_write_b128 v76, v[104:107] offset:128\n"
"ds_write_b128 v76, v[108:111] offset:192\n"
"ds_write_b128 v76, v[112:115] offset:256\n"
"s_nop 0\n"
"ds_write_b128 v76, v[116:119] offset:320\n"
"s_nop 0\n"
"ds_write_b128 v76, v[120:123] offset:384\n"
"s_waitcnt lgkmcnt(0)\n"
"s_barrier\n"
"s_waitcnt vmcnt(1)\n"
"v_max_u32 v242, v124, v125\n"
"v_max3_u32 v242, v242, v126, v127\n"
"s_waitcnt vmcnt(0)\n"
"v_max3_u32 v242, v242, v128, v129\n"
"v_max3_u32 v242, v242, v130, v131\n"
"v_cmp_eq_u32 vcc, -1, v242\n"
"s_nop 0\n"
"s_cbranch_vccnz SLOW_G3_%=\n"
"OK_G3_%=:\n"
"s_mov_b32 s61, s72\n"
"s_mul_i32 s36, s48, 20480\n"
"s_add_u32 s36, s36, 112197632\n"
"buffer_load_dwordx4 a[0:3], v251, s[96:99], s36 offen offset:0 sc1\n"
"buffer_load_dwordx4 a[4:7], v60, s[96:99], s36 offen offset:0 sc1\n"
"buffer_load_dwordx4 a[8:11], v251, s[96:99], s36 offen offset:1024 sc1\n"
"buffer_load_dwordx4 a[12:15], v60, s[96:99], s36 offen offset:1024 sc1\n"
"buffer_load_dwordx4 a[16:19], v251, s[96:99], s36 offen offset:2048 sc1\n"
"buffer_load_dwordx4 a[20:23], v60, s[96:99], s36 offen offset:2048 sc1\n"
"buffer_load_dwordx4 a[24:27], v251, s[96:99], s36 offen offset:3072 sc1\n"
"buffer_load_dwordx4 a[28:31], v60, s[96:99], s36 offen offset:3072 sc1\n"
"s_add_u32 s36, s36, 4096\n"
"buffer_load_dwordx4 a[32:35], v251, s[96:99], s36 offen offset:0 sc1\n"
"buffer_load_dwordx4 a[36:39], v60, s[96:99], s36 offen offset:0 sc1\n"
"buffer_load_dwordx4 a[40:43], v251, s[96:99], s36 offen offset:1024 sc1\n"
"buffer_load_dwordx4 a[44:47], v60, s[96:99], s36 offen offset:1024 sc1\n"
"buffer_load_dwordx4 a[48:51], v251, s[96:99], s36 offen offset:2048 sc1\n"
"buffer_load_dwordx4 a[52:55], v60, s[96:99], s36 offen offset:2048 sc1\n"
"buffer_load_dwordx4 a[56:59], v251, s[96:99], s36 offen offset:3072 sc1\n"
"buffer_load_dwordx4 a[60:63], v60, s[96:99], s36 offen offset:3072 sc1\n"
"s_add_u32 s36, s36, 4096\n"
"buffer_load_dwordx4 a[64:67], v251, s[96:99], s36 offen offset:0 sc1\n"
"buffer_load_dwordx4 a[68:71], v60, s[96:99], s36 offen offset:0 sc1\n"
"buffer_load_dwordx4 a[72:75], v251, s[96:99], s36 offen offset:1024 sc1\n"
"buffer_load_dwordx4 a[76:79], v60, s[96:99], s36 offen offset:1024 sc1\n"
"buffer_load_dwordx4 a[80:83], v251, s[96:99], s36 offen offset:2048 sc1\n"
"buffer_load_dwordx4 a[84:87], v60, s[96:99], s36 offen offset:2048 sc1\n"
"buffer_load_dwordx4 a[88:91], v251, s[96:99], s36 offen offset:3072 sc1\n"
"buffer_load_dwordx4 a[92:95], v60, s[96:99], s36 offen offset:3072 sc1\n"
"s_add_u32 s36, s36, 4096\n"
"buffer_load_dwordx4 a[96:99], v251, s[96:99], s36 offen offset:0 sc1\n"
"buffer_load_dwordx4 a[100:103], v60, s[96:99], s36 offen offset:0 sc1\n"
"buffer_load_dwordx4 a[104:107], v251, s[96:99], s36 offen offset:1024 sc1\n"
"buffer_load_dwordx4 a[108:111], v60, s[96:99], s36 offen offset:1024 sc1\n"
"buffer_load_dwordx4 a[112:115], v251, s[96:99], s36 offen offset:2048 sc1\n"
"buffer_load_dwordx4 a[116:119], v60, s[96:99], s36 offen offset:2048 sc1\n"
"buffer_load_dwordx4 a[120:123], v251, s[96:99], s36 offen offset:3072 sc1\n"
"buffer_load_dwordx4 a[124:127], v60, s[96:99], s36 offen offset:3072 sc1\n"
"s_add_u32 s36, s36, 4096\n"
"buffer_load_dwordx4 a[128:131], v251, s[96:99], s36 offen offset:0 sc1\n"
"buffer_load_dwordx4 a[132:135], v60, s[96:99], s36 offen offset:0 sc1\n"
"buffer_load_dwordx4 a[136:139], v251, s[96:99], s36 offen offset:1024 sc1\n"
"buffer_load_dwordx4 a[140:143], v60, s[96:99], s36 offen offset:1024 sc1\n"
"buffer_load_dwordx4 a[144:147], v251, s[96:99], s36 offen offset:2048 sc1\n"
"buffer_load_dwordx4 a[148:151], v60, s[96:99], s36 offen offset:2048 sc1\n"
"buffer_load_dwordx4 a[152:155], v251, s[96:99], s36 offen offset:3072 sc1\n"
"buffer_load_dwordx4 a[156:159], v60, s[96:99], s36 offen offset:3072 sc1\n"
"v_add_f32 v62, v124, v126\n"
"v_add_f32 v63, v125, v127\n"
"v_mov_b32 v232, v62\n"
"v_mov_b32 v233, v63\n"
"s_nop 0\n"
"v_permlane16_swap_b32 v62, v232\n"
"v_permlane16_swap_b32 v63, v233\n"
"v_add_f32 v62, v62, v232\n"
"v_add_f32 v63, v63, v233\n"
"v_mov_b32 v232, v62\n"
"v_mov_b32 v233, v63\n"
"s_nop 0\n"
"v_permlane32_swap_b32 v62, v232\n"
"v_permlane32_swap_b32 v63, v233\n"
"v_add_f32 v62, v62, v232\n"
"v_add_f32 v63, v63, v233\n"
"v_mul_f32 v56, 0x3acccccd, v62\n"
"v_mul_f32 v58, 0x3acccccd, v63\n"
"v_fma_f32 v58, -v56, v56, v58\n"
"v_max_f32 v58, 0, v58\n"
"v_add_f32 v58, 0x3727c5ac, v58\n"
"v_rsq_f32 v58, v58\n"
"s_nop 0\n"
"v_mul_f32 v56, v56, v58\n"
"v_mov_b32 v59, v58\n"
"v_sub_f32 v56, 0, v56\n"
"v_mov_b32 v57, v56\n"
"v_add_f32 v62, v128, v130\n"
"v_add_f32 v63, v129, v131\n"
"v_mov_b32 v232, v62\n"
"v_mov_b32 v233, v63\n"
"s_nop 0\n"
"v_permlane16_swap_b32 v62, v232\n"
"v_permlane16_swap_b32 v63, v233\n"
"v_add_f32 v62, v62, v232\n"
"v_add_f32 v63, v63, v233\n"
"v_mov_b32 v232, v62\n"
"v_mov_b32 v233, v63\n"
"s_nop 0\n"
"v_permlane32_swap_b32 v62, v232\n"
"v_permlane32_swap_b32 v63, v233\n"
"v_add_f32 v62, v62, v232\n"
"v_add_f32 v63, v63, v233\n"
"v_mul_f32 v64, 0x3acccccd, v62\n"
"v_mul_f32 v66, 0x3acccccd, v63\n"
"v_fma_f32 v66, -v64, v64, v66\n"
"v_max_f32 v66, 0, v66\n"
"v_add_f32 v66, 0x3727c5ac, v66\n"
"v_rsq_f32 v66, v66\n"
"s_nop 0\n"
"v_mul_f32 v64, v64, v66\n"
"v_mov_b32 v67, v66\n"
"v_sub_f32 v64, 0, v64\n"
"v_mov_b32 v65, v64\n"
"ds_read_b128 v[132:135], v93 offset:40960\n"
"ds_read_b128 v[136:139], v94 offset:0\n"
"ds_read_b128 v[140:143], v94 offset:20480\n"
"ds_read_b128 v[144:147], v94 offset:40960\n"
"ds_read_b128 v[148:151], v95 offset:0\n"
"PG23_%=:\n"
"s_cbranch_execz RPG23_%=\n"
"ds_read_b128 v[152:155], v95 offset:20480\n"
"ds_read_b128 v[156:159], v93 offset:0\n"
"ds_read_b128 v[160:163], v93 offset:41984\n"
"s_waitcnt vmcnt(39) lgkmcnt(7)\n"
"v_mfma_f32_16x16x32_bf16 v[0:3], v[132:135], a[0:3], 0\n"
"ds_read_b128 v[164:167], v94 offset:1024\n"
"s_waitcnt vmcnt(38)\n"
"v_mfma_f32_16x16x32_bf16 v[28:31], v[132:135], a[4:7], 0\n"
"s_waitcnt lgkmcnt(7)\n"
"v_mfma_f32_16x16x32_bf16 v[4:7], v[136:139], a[0:3], 0\n"
"ds_read_b128 v[168:171], v94 offset:21504\n"
"v_mfma_f32_16x16x32_bf16 v[32:35], v[136:139], a[4:7], 0\n"
"s_waitcnt lgkmcnt(7)\n"
"v_mfma_f32_16x16x32_bf16 v[8:11], v[140:143], a[0:3], 0\n"
"ds_read_b128 v[172:175], v94 offset:41984\n"
"v_mfma_f32_16x16x32_bf16 v[36:39], v[140:143], a[4:7], 0\n"
"ds_read_b128 v[96:99], v75 offset:0\n"
"s_waitcnt lgkmcnt(8)\n"
"v_mfma_f32_16x16x32_bf16 v[12:15], v[144:147], a[0:3], 0\n"
"ds_read_b128 v[176:179], v95 offset:1024\n"
"v_mfma_f32_16x16x32_bf16 v[40:43], v[144:147], a[4:7], 0\n"
"ds_read_b128 v[100:103], v75 offset:64\n"
"s_waitcnt lgkmcnt(9)\n"
"v_mfma_f32_16x16x32_bf16 v[16:19], v[148:151], a[0:3], 0\n"
"ds_read_b128 v[132:135], v95 offset:21504\n"
"v_mfma_f32_16x16x32_bf16 v[44:47], v[148:151], a[4:7], 0\n"
"ds_read_b128 v[104:107], v75 offset:128\n"
"s_waitcnt lgkmcnt(9)\n"
"v_mfma_f32_16x16x32_bf16 v[20:23], v[152:155], a[0:3], 0\n"
"ds_read_b128 v[136:139], v93 offset:1024\n"
"v_mfma_f32_16x16x32_bf16 v[48:51], v[152:155], a[4:7], 0\n"
"ds_read_b128 v[108:111], v75 offset:192\n"
"v_mfma_f32_16x16x32_bf16 v[24:27], v[156:159], a[0:3], 0\n"
"ds_read_b128 v[140:143], v93 offset:43008\n"
"v_mfma_f32_16x16x32_bf16 v[52:55], v[156:159], a[4:7], 0\n"
"ds_read_b128 v[112:115], v75 offset:256\n"
"s_waitcnt vmcnt(37) lgkmcnt(10)\n"
"v_mfma_f32_16x16x32_bf16 v[0:3], v[160:163], a[8:11], v[0:3]\n"
"ds_read_b128 v[144:147], v94 offset:2048\n"
"s_waitcnt vmcnt(36)\n"
"v_mfma_f32_16x16x32_bf16 v[28:31], v[160:163], a[12:15], v[28:31]\n"
"v_mfma_f32_16x16x32_bf16 v[4:7], v[164:167], a[8:11], v[4:7]\n"
"ds_read_b128 v[148:151], v94 offset:22528\n"
"v_mfma_f32_16x16x32_bf16 v[32:35], v[164:167], a[12:15], v[32:35]\n"
"ds_read_b128 v[116:119], v75 offset:320\n"
"v_mfma_f32_16x16x32_bf16 v[8:11], v[168:171], a[8:11], v[8:11]\n"
"ds_read_b128 v[152:155], v94 offset:43008\n"
"v_mfma_f32_16x16x32_bf16 v[36:39], v[168:171], a[12:15], v[36:39]\n"
"ds_read_b128 v[120:123], v75 offset:384\n"
"s_waitcnt lgkmcnt(9)\n"
"v_mfma_f32_16x16x32_bf16 v[12:15], v[172:175], a[8:11], v[12:15]\n"
"ds_read_b128 v[156:159], v95 offset:2048\n"
"v_mfma_f32_16x16x32_bf16 v[40:43], v[172:175], a[12:15], v[40:43]\n"
"ds_read_b128 v[232:235], v75 offset:1024\n"
"v_mfma_f32_16x16x32_bf16 v[16:19], v[176:179], a[8:11], v[16:19]\n"
"ds_read_b128 v[160:163], v95 offset:22528\n"
"v_mfma_f32_16x16x32_bf16 v[44:47], v[176:179], a[12:15], v[44:47]\n"
"ds_read_b128 v[236:239], v75 offset:2048\n"
"v_mfma_f32_16x16x32_bf16 v[20:23], v[132:135], a[8:11], v[20:23]\n"
"ds_read_b128 v[164:167], v93 offset:2048\n"
"v_mfma_f32_16x16x32_bf16 v[48:51], v[132:135], a[12:15], v[48:51]\n"
"s_waitcnt lgkmcnt(9)\n"
"v_mfma_f32_16x16x32_bf16 v[24:27], v[136:139], a[8:11], v[24:27]\n"
"ds_read_b128 v[168:171], v93 offset:44032\n"
"v_mfma_f32_16x16x32_bf16 v[52:55], v[136:139], a[12:15], v[52:55]\n"
"ds_read_b128 v[240:243], v75 offset:3072\n"
"s_waitcnt vmcnt(35)\n"
"v_mfma_f32_16x16x32_bf16 v[0:3], v[140:143], a[16:19], v[0:3]\n"
"ds_read_b128 v[172:175], v94 offset:3072\n"
"s_waitcnt vmcnt(34)\n"
"v_mfma_f32_16x16x32_bf16 v[28:31], v[140:143], a[20:23], v[28:31]\n"
"s_waitcnt lgkmcnt(6)\n"
"v_add_f32 v96, v96, v232\n"
"v_mfma_f32_16x16x32_bf16 v[4:7], v[144:147], a[16:19], v[4:7]\n"
"ds_read_b128 v[176:179], v94 offset:23552\n"
"v_mfma_f32_16x16x32_bf16 v[32:35], v[144:147], a[20:23], v[32:35]\n"
"v_add_f32 v97, v97, v233\n"
"v_mfma_f32_16x16x32_bf16 v[8:11], v[148:151], a[16:19], v[8:11]\n"
"ds_read_b128 v[132:135], v94 offset:44032\n"
"v_mfma_f32_16x16x32_bf16 v[36:39], v[148:151], a[20:23], v[36:39]\n"
"v_add_f32 v98, v98, v234\n"
"v_mfma_f32_16x16x32_bf16 v[12:15], v[152:155], a[16:19], v[12:15]\n"
"ds_read_b128 v[136:139], v95 offset:3072\n"
"v_mfma_f32_16x16x32_bf16 v[40:43], v[152:155], a[20:23], v[40:43]\n"
"v_mfma_f32_16x16x32_bf16 v[16:19], v[156:159], a[16:19], v[16:19]\n"
"ds_read_b128 v[140:143], v95 offset:23552\n"
"v_mfma_f32_16x16x32_bf16 v[44:47], v[156:159], a[20:23], v[44:47]\n"
"v_add_f32 v99, v99, v235\n"
"s_waitcnt lgkmcnt(5)\n"
"v_mfma_f32_16x16x32_bf16 v[20:23], v[160:163], a[16:19], v[20:23]\n"
"ds_read_b128 v[144:147], v93 offset:3072\n"
"v_mfma_f32_16x16x32_bf16 v[48:51], v[160:163], a[20:23], v[48:51]\n"
"ds_read_b128 v[244:247], v75 offset:1088\n"
"v_mfma_f32_16x16x32_bf16 v[24:27], v[164:167], a[16:19], v[24:27]\n"
"ds_read_b128 v[148:151], v93 offset:45056\n"
"v_mfma_f32_16x16x32_bf16 v[52:55], v[164:167], a[20:23], v[52:55]\n"
"v_add_f32 v96, v96, v236\n"
"s_waitcnt vmcnt(33)\n"
"v_mfma_f32_16x16x32_bf16 v[0:3], v[168:171], a[24:27], v[0:3]\n"
"ds_read_b128 v[152:155], v94 offset:4096\n"
"s_waitcnt vmcnt(32)\n"
"v_mfma_f32_16x16x32_bf16 v[28:31], v[168:171], a[28:31], v[28:31]\n"
"v_add_f32 v97, v97, v237\n"
"s_waitcnt lgkmcnt(6)\n"
"v_mfma_f32_16x16x32_bf16 v[4:7], v[172:175], a[24:27], v[4:7]\n"
"ds_read_b128 v[156:159], v94 offset:24576\n"
"v_mfma_f32_16x16x32_bf16 v[32:35], v[172:175], a[28:31], v[32:35]\n"
"v_add_f32 v98, v98, v238\n"
"v_mfma_f32_16x16x32_bf16 v[8:11], v[176:179], a[24:27], v[8:11]\n"
"ds_read_b128 v[160:163], v94 offset:45056\n"
"v_mfma_f32_16x16x32_bf16 v[36:39], v[176:179], a[28:31], v[36:39]\n"
"v_mfma_f32_16x16x32_bf16 v[12:15], v[132:135], a[24:27], v[12:15]\n"
"ds_read_b128 v[164:167], v95 offset:4096\n"
"v_mfma_f32_16x16x32_bf16 v[40:43], v[132:135], a[28:31], v[40:43]\n"
"v_add_f32 v99, v99, v239\n"
"s_waitcnt lgkmcnt(5)\n"
"v_mfma_f32_16x16x32_bf16 v[16:19], v[136:139], a[24:27], v[16:19]\n"
"ds_read_b128 v[168:171], v95 offset:24576\n"
"v_mfma_f32_16x16x32_bf16 v[44:47], v[136:139], a[28:31], v[44:47]\n"
"ds_read_b128 v[232:235], v75 offset:2112\n"
"v_mfma_f32_16x16x32_bf16 v[20:23], v[140:143], a[24:27], v[20:23]\n"
"ds_read_b128 v[172:175], v93 offset:4096\n"
"v_mfma_f32_16x16x32_bf16 v[48:51], v[140:143], a[28:31], v[48:51]\n"
"v_add_f32 v96, v96, v240\n"
"v_mfma_f32_16x16x32_bf16 v[24:27], v[144:147], a[24:27], v[24:27]\n"
"ds_read_b128 v[176:179], v93 offset:46080\n"
"v_mfma_f32_16x16x32_bf16 v[52:55], v[144:147], a[28:31], v[52:55]\n"
"v_add_f32 v97, v97, v241\n"
"s_waitcnt vmcnt(31) lgkmcnt(6)\n"
"v_mfma_f32_16x16x32_bf16 v[0:3], v[148:151], a[32:35], v[0:3]\n"
"ds_read_b128 v[132:135], v94 offset:5120\n"
"s_waitcnt vmcnt(30)\n"
"v_mfma_f32_16x16x32_bf16 v[28:31], v[148:151], a[36:39], v[28:31]\n"
"v_mfma_f32_16x16x32_bf16 v[4:7], v[152:155], a[32:35], v[4:7]\n"
"ds_read_b128 v[136:139], v94 offset:25600\n"
"v_mfma_f32_16x16x32_bf16 v[32:35], v[152:155], a[36:39], v[32:35]\n"
"v_add_f32 v98, v98, v242\n"
"v_mfma_f32_16x16x32_bf16 v[8:11], v[156:159], a[32:35], v[8:11]\n"
"ds_read_b128 v[140:143], v94 offset:46080\n"
"v_mfma_f32_16x16x32_bf16 v[36:39], v[156:159], a[36:39], v[36:39]\n"
"v_add_f32 v99, v99, v243\n"
"s_waitcnt lgkmcnt(5)\n"
"v_mfma_f32_16x16x32_bf16 v[12:15], v[160:163], a[32:35], v[12:15]\n"
"ds_read_b128 v[144:147], v95 offset:5120\n"
"v_mfma_f32_16x16x32_bf16 v[40:43], v[160:163], a[36:39], v[40:43]\n"
"ds_read_b128 v[236:239], v75 offset:3136\n"
"v_mfma_f32_16x16x32_bf16 v[16:19], v[164:167], a[32:35], v[16:19]\n"
"ds_read_b128 v[148:151], v95 offset:25600\n"
"v_mfma_f32_16x16x32_bf16 v[44:47], v[164:167], a[36:39], v[44:47]\n"
"v_add_f32 v100, v100, v244\n"
"v_mfma_f32_16x16x32_bf16 v[20:23], v[168:171], a[32:35], v[20:23]\n"
"ds_read_b128 v[152:155], v93 offset:5120\n"
"v_mfma_f32_16x16x32_bf16 v[48:51], v[168:171], a[36:39], v[48:51]\n"
"s_waitcnt lgkmcnt(6)\n"
"v_mfma_f32_16x16x32_bf16 v[24:27], v[172:175], a[32:35], v[24:27]\n"
"ds_read_b128 v[156:159], v93 offset:47104\n"
"v_mfma_f32_16x16x32_bf16 v[52:55], v[172:175], a[36:39], v[52:55]\n"
"v_add_f32 v101, v101, v245\n"
"s_waitcnt vmcnt(29)\n"
"v_mfma_f32_16x16x32_bf16 v[0:3], v[176:179], a[40:43], v[0:3]\n"
"ds_read_b128 v[160:163], v94 offset:6144\n"
"s_waitcnt vmcnt(28)\n"
"v_mfma_f32_16x16x32_bf16 v[28:31], v[176:179], a[44:47], v[28:31]\n"
"v_add_f32 v102, v102, v246\n"
"v_mfma_f32_16x16x32_bf16 v[4:7], v[132:135], a[40:43], v[4:7]\n"
"ds_read_b128 v[164:167], v94 offset:26624\n"
"v_mfma_f32_16x16x32_bf16 v[32:35], v[132:135], a[44:47], v[32:35]\n"
"v_add_f32 v103, v103, v247\n"
"s_waitcnt lgkmcnt(5)\n"
"v_mfma_f32_16x16x32_bf16 v[8:11], v[136:139], a[40:43], v[8:11]\n"
"ds_read_b128 v[168:171], v94 offset:47104\n"
"v_mfma_f32_16x16x32_bf16 v[36:39], v[136:139], a[44:47], v[36:39]\n"
"ds_read_b128 v[240:243], v75 offset:1152\n"
"v_mfma_f32_16x16x32_bf16 v[12:15], v[140:143], a[40:43], v[12:15]\n"
"ds_read_b128 v[172:175], v95 offset:6144\n"
"v_mfma_f32_16x16x32_bf16 v[40:43], v[140:143], a[44:47], v[40:43]\n"
"v_add_f32 v100, v100, v232\n"
"v_mfma_f32_16x16x32_bf16 v[16:19], v[144:147], a[40:43], v[16:19]\n"
"ds_read_b128 v[176:179], v95 offset:26624\n"
"v_mfma_f32_16x16x32_bf16 v[44:47], v[144:147], a[44:47], v[44:47]\n"
"s_waitcnt lgkmcnt(6)\n"
"v_mfma_f32_16x16x32_bf16 v[20:23], v[148:151], a[40:43], v[20:23]\n"
"ds_read_b128 v[132:135], v93 offset:6144\n"
"v_mfma_f32_16x16x32_bf16 v[48:51], v[148:151], a[44:47], v[48:51]\n"
"v_add_f32 v101, v101, v233\n"
"v_mfma_f32_16x16x32_bf16 v[24:27], v[152:155], a[40:43], v[24:27]\n"
"ds_read_b128 v[136:139], v93 offset:48128\n"
"v_mfma_f32_16x16x32_bf16 v[52:55], v[152:155], a[44:47], v[52:55]\n"
"v_add_f32 v102, v102, v234\n"
"s_waitcnt vmcnt(27)\n"
"v_mfma_f32_16x16x32_bf16 v[0:3], v[156:159], a[48:51], v[0:3]\n"
"ds_read_b128 v[140:143], v94 offset:7168\n"
"s_waitcnt vmcnt(26)\n"
"v_mfma_f32_16x16x32_bf16 v[28:31], v[156:159], a[52:55], v[28:31]\n"
"v_add_f32 v103, v103, v235\n"
"s_waitcnt lgkmcnt(5)\n"
"v_mfma_f32_16x16x32_bf16 v[4:7], v[160:163], a[48:51], v[4:7]\n"
"ds_read_b128 v[144:147], v94 offset:27648\n"
"v_mfma_f32_16x16x32_bf16 v[32:35], v[160:163], a[52:55], v[32:35]\n"
"ds_read_b128 v[244:247], v75 offset:2176\n"
"v_mfma_f32_16x16x32_bf16 v[8:11], v[164:167], a[48:51], v[8:11]\n"
"ds_read_b128 v[148:151], v94 offset:48128\n"
"v_mfma_f32_16x16x32_bf16 v[36:39], v[164:167], a[52:55], v[36:39]\n"
"v_mfma_f32_16x16x32_bf16 v[12:15], v[168:171], a[48:51], v[12:15]\n"
"ds_read_b128 v[152:155], v95 offset:7168\n"
"v_mfma_f32_16x16x32_bf16 v[40:43], v[168:171], a[52:55], v[40:43]\n"
"v_add_f32 v100, v100, v236\n"
"s_waitcnt lgkmcnt(6)\n"
"v_mfma_f32_16x16x32_bf16 v[16:19], v[172:175], a[48:51], v[16:19]\n"
"ds_read_b128 v[156:159], v95 offset:27648\n"
"v_mfma_f32_16x16x32_bf16 v[44:47], v[172:175], a[52:55], v[44:47]\n"
"v_add_f32 v101, v101, v237\n"
"v_mfma_f32_16x16x32_bf16 v[20:23], v[176:179], a[48:51], v[20:23]\n"
"ds_read_b128 v[160:163], v93 offset:7168\n"
"v_mfma_f32_16x16x32_bf16 v[48:51], v[176:179], a[52:55], v[48:51]\n"
"v_add_f32 v102, v102, v238\n"
"v_mfma_f32_16x16x32_bf16 v[24:27], v[132:135], a[48:51], v[24:27]\n"
"ds_read_b128 v[164:167], v93 offset:49152\n"
"v_mfma_f32_16x16x32_bf16 v[52:55], v[132:135], a[52:55], v[52:55]\n"
"v_add_f32 v103, v103, v239\n"
"s_waitcnt vmcnt(25) lgkmcnt(5)\n"
"v_mfma_f32_16x16x32_bf16 v[0:3], v[136:139], a[56:59], v[0:3]\n"
"ds_read_b128 v[168:171], v94 offset:8192\n"
"s_waitcnt vmcnt(24)\n"
"v_mfma_f32_16x16x32_bf16 v[28:31], v[136:139], a[60:63], v[28:31]\n"
"v_mfma_f32_16x16x32_bf16 v[4:7], v[140:143], a[56:59], v[4:7]\n"
"ds_read_b128 v[172:175], v94 offset:28672\n"
"v_mfma_f32_16x16x32_bf16 v[32:35], v[140:143], a[60:63], v[32:35]\n"
"ds_read_b128 v[232:235], v75 offset:3200\n"
"v_mfma_f32_16x16x32_bf16 v[8:11], v[144:147], a[56:59], v[8:11]\n"
"ds_read_b128 v[176:179], v94 offset:49152\n"
"v_mfma_f32_16x16x32_bf16 v[36:39], v[144:147], a[60:63], v[36:39]\n"
"v_add_f32 v104, v104, v240\n"
"s_waitcnt lgkmcnt(6)\n"
"v_mfma_f32_16x16x32_bf16 v[12:15], v[148:151], a[56:59], v[12:15]\n"
"ds_read_b128 v[132:135], v95 offset:8192\n"
"v_mfma_f32_16x16x32_bf16 v[40:43], v[148:151], a[60:63], v[40:43]\n"
"v_add_f32 v105, v105, v241\n"
"v_mfma_f32_16x16x32_bf16 v[16:19], v[152:155], a[56:59], v[16:19]\n"
"ds_read_b128 v[136:139], v95 offset:28672\n"
"v_mfma_f32_16x16x32_bf16 v[44:47], v[152:155], a[60:63], v[44:47]\n"
"v_add_f32 v106, v106, v242\n"
"v_mfma_f32_16x16x32_bf16 v[20:23], v[156:159], a[56:59], v[20:23]\n"
"ds_read_b128 v[140:143], v93 offset:8192\n"
"v_mfma_f32_16x16x32_bf16 v[48:51], v[156:159], a[60:63], v[48:51]\n"
"v_add_f32 v107, v107, v243\n"
"s_waitcnt lgkmcnt(6)\n"
"v_mfma_f32_16x16x32_bf16 v[24:27], v[160:163], a[56:59], v[24:27]\n"
"ds_read_b128 v[144:147], v93 offset:50176\n"
"v_mfma_f32_16x16x32_bf16 v[52:55], v[160:163], a[60:63], v[52:55]\n"
"s_waitcnt vmcnt(23)\n"
"v_mfma_f32_16x16x32_bf16 v[0:3], v[164:167], a[64:67], v[0:3]\n"
"ds_read_b128 v[148:151], v94 offset:9216\n"
"s_waitcnt vmcnt(22)\n"
"v_mfma_f32_16x16x32_bf16 v[28:31], v[164:167], a[68:71], v[28:31]\n"
"ds_read_b128 v[236:239], v75 offset:1216\n"
"v_mfma_f32_16x16x32_bf16 v[4:7], v[168:171], a[64:67], v[4:7]\n"
"ds_read_b128 v[152:155], v94 offset:29696\n"
"v_mfma_f32_16x16x32_bf16 v[32:35], v[168:171], a[68:71], v[32:35]\n"
"v_add_f32 v104, v104, v244\n"
"s_waitcnt lgkmcnt(6)\n"
"v_mfma_f32_16x16x32_bf16 v[8:11], v[172:175], a[64:67], v[8:11]\n"
"ds_read_b128 v[156:159], v94 offset:50176\n"
"v_mfma_f32_16x16x32_bf16 v[36:39], v[172:175], a[68:71], v[36:39]\n"
"v_add_f32 v105, v105, v245\n"
"v_mfma_f32_16x16x32_bf16 v[12:15], v[176:179], a[64:67], v[12:15]\n"
"ds_read_b128 v[160:163], v95 offset:9216\n"
"v_mfma_f32_16x16x32_bf16 v[40:43], v[176:179], a[68:71], v[40:43]\n"
"v_add_f32 v106, v106, v246\n"
"v_mfma_f32_16x16x32_bf16 v[16:19], v[132:135], a[64:67], v[16:19]\n"
"ds_read_b128 v[164:167], v95 offset:29696\n"
"v_mfma_f32_16x16x32_bf16 v[44:47], v[132:135], a[68:71], v[44:47]\n"
"s_waitcnt lgkmcnt(6)\n"
"v_mfma_f32_16x16x32_bf16 v[20:23], v[136:139], a[64:67], v[20:23]\n"
"ds_read_b128 v[168:171], v93 offset:9216\n"
"v_mfma_f32_16x16x32_bf16 v[48:51], v[136:139], a[68:71], v[48:51]\n"
"v_add_f32 v107, v107, v247\n"
"v_mfma_f32_16x16x32_bf16 v[24:27], v[140:143], a[64:67], v[24:27]\n"
"ds_read_b128 v[172:175], v93 offset:51200\n"
"v_mfma_f32_16x16x32_bf16 v[52:55], v[140:143], a[68:71], v[52:55]\n"
"ds_read_b128 v[240:243], v75 offset:2240\n"
"s_waitcnt vmcnt(21)\n"
"v_mfma_f32_16x16x32_bf16 v[0:3], v[144:147], a[72:75], v[0:3]\n"
"ds_read_b128 v[176:179], v94 offset:10240\n"
"s_waitcnt vmcnt(20)\n"
"v_mfma_f32_16x16x32_bf16 v[28:31], v[144:147], a[76:79], v[28:31]\n"
"v_add_f32 v104, v104, v232\n"
"s_waitcnt lgkmcnt(6)\n"
"v_mfma_f32_16x16x32_bf16 v[4:7], v[148:151], a[72:75], v[4:7]\n"
"ds_read_b128 v[132:135], v94 offset:30720\n"
"v_mfma_f32_16x16x32_bf16 v[32:35], v[148:151], a[76:79], v[32:35]\n"
"v_add_f32 v105, v105, v233\n"
"v_mfma_f32_16x16x32_bf16 v[8:11], v[152:155], a[72:75], v[8:11]\n"
"ds_read_b128 v[136:139], v94 offset:51200\n"
"v_mfma_f32_16x16x32_bf16 v[36:39], v[152:155], a[76:79], v[36:39]\n"
"v_mfma_f32_16x16x32_bf16 v[12:15], v[156:159], a[72:75], v[12:15]\n"
"ds_read_b128 v[140:143], v95 offset:10240\n"
"v_mfma_f32_16x16x32_bf16 v[40:43], v[156:159], a[76:79], v[40:43]\n"
"v_add_f32 v106, v106, v234\n"
"s_waitcnt lgkmcnt(6)\n"
"v_mfma_f32_16x16x32_bf16 v[16:19], v[160:163], a[72:75], v[16:19]\n"
"ds_read_b128 v[144:147], v95 offset:30720\n"
"v_mfma_f32_16x16x32_bf16 v[44:47], v[160:163], a[76:79], v[44:47]\n"
"v_add_f32 v107, v107, v235\n"
"v_mfma_f32_16x16x32_bf16 v[20:23], v[164:167], a[72:75], v[20:23]\n"
"ds_read_b128 v[148:151], v93 offset:10240\n"
"v_mfma_f32_16x16x32_bf16 v[48:51], v[164:167], a[76:79], v[48:51]\n"
"ds_read_b128 v[244:247], v75 offset:3264\n"
"v_mfma_f32_16x16x32_bf16 v[24:27], v[168:171], a[72:75], v[24:27]\n"
"ds_read_b128 v[152:155], v93 offset:52224\n"
"v_mfma_f32_16x16x32_bf16 v[52:55], v[168:171], a[76:79], v[52:55]\n"
"v_add_f32 v108, v108, v236\n"
"s_waitcnt vmcnt(19) lgkmcnt(6)\n"
"v_mfma_f32_16x16x32_bf16 v[0:3], v[172:175], a[80:83], v[0:3]\n"
"ds_read_b128 v[156:159], v94 offset:11264\n"
"buffer_load_dwordx4 a[160:163], v84, s[96:99], s61 offen offset:0 sc1\n"
"s_waitcnt vmcnt(19)\n"
"v_mfma_f32_16x16x32_bf16 v[28:31], v[172:175], a[84:87], v[28:31]\n"
"v_mfma_f32_16x16x32_bf16 v[4:7], v[176:179], a[80:83], v[4:7]\n"
"ds_read_b128 v[160:163], v94 offset:31744\n"
"v_mfma_f32_16x16x32_bf16 v[32:35], v[176:179], a[84:87], v[32:35]\n"
"v_add_f32 v109, v109, v237\n"
"v_mfma_f32_16x16x32_bf16 v[8:11], v[132:135], a[80:83], v[8:11]\n"
"ds_read_b128 v[164:167], v94 offset:52224\n"
"v_mfma_f32_16x16x32_bf16 v[36:39], v[132:135], a[84:87], v[36:39]\n"
"buffer_load_dwordx4 a[164:167], v85, s[96:99], s61 offen offset:0 sc1\n"
"v_add_f32 v110, v110, v238\n"
"s_waitcnt lgkmcnt(6)\n"
"v_mfma_f32_16x16x32_bf16 v[12:15], v[136:139], a[80:83], v[12:15]\n"
"ds_read_b128 v[168:171], v95 offset:11264\n"
"v_mfma_f32_16x16x32_bf16 v[40:43], v[136:139], a[84:87], v[40:43]\n"
"v_add_f32 v111, v111, v239\n"
"v_mfma_f32_16x16x32_bf16 v[16:19], v[140:143], a[80:83], v[16:19]\n"
"ds_read_b128 v[172:175], v95 offset:31744\n"
"v_mfma_f32_16x16x32_bf16 v[44:47], v[140:143], a[84:87], v[44:47]\n"
"ds_read_b128 v[232:235], v75 offset:1280\n"
"v_mfma_f32_16x16x32_bf16 v[20:23], v[144:147], a[80:83], v[20:23]\n"
"ds_read_b128 v[176:179], v93 offset:11264\n"
"buffer_load_dwordx4 a[168:171], v86, s[96:99], s61 offen offset:0 sc1\n"
"v_mfma_f32_16x16x32_bf16 v[48:51], v[144:147], a[84:87], v[48:51]\n"
"v_add_f32 v108, v108, v240\n"
"s_waitcnt lgkmcnt(6)\n"
"v_mfma_f32_16x16x32_bf16 v[24:27], v[148:151], a[80:83], v[24:27]\n"
"ds_read_b128 v[132:135], v93 offset:53248\n"
"v_mfma_f32_16x16x32_bf16 v[52:55], v[148:151], a[84:87], v[52:55]\n"
"s_waitcnt vmcnt(20)\n"
"v_mfma_f32_16x16x32_bf16 v[0:3], v[152:155], a[88:91], v[0:3]\n"
"ds_read_b128 v[136:139], v94 offset:12288\n"
"s_waitcnt vmcnt(19)\n"
"v_mfma_f32_16x16x32_bf16 v[28:31], v[152:155], a[92:95], v[28:31]\n"
"v_add_f32 v109, v109, v241\n"
"v_mfma_f32_16x16x32_bf16 v[4:7], v[156:159], a[88:91], v[4:7]\n"
"ds_read_b128 v[140:143], v94 offset:32768\n"
"buffer_load_dwordx4 a[172:175], v87, s[96:99], s61 offen offset:0 sc1\n"
"v_mfma_f32_16x16x32_bf16 v[32:35], v[156:159], a[92:95], v[32:35]\n"
"v_add_f32 v110, v110, v242\n"
"s_waitcnt lgkmcnt(6)\n"
"v_mfma_f32_16x16x32_bf16 v[8:11], v[160:163], a[88:91], v[8:11]\n"
"ds_read_b128 v[144:147], v94 offset:53248\n"
"v_mfma_f32_16x16x32_bf16 v[36:39], v[160:163], a[92:95], v[36:39]\n"
"v_add_f32 v111, v111, v243\n"
"v_mfma_f32_16x16x32_bf16 v[12:15], v[164:167], a[88:91], v[12:15]\n"
"ds_read_b128 v[148:151], v95 offset:12288\n"
"v_mfma_f32_16x16x32_bf16 v[40:43], v[164:167], a[92:95], v[40:43]\n"
"ds_read_b128 v[236:239], v75 offset:2304\n"
"buffer_load_dwordx4 a[176:179], v88, s[96:99], s61 offen offset:0 sc1\n"
"v_mfma_f32_16x16x32_bf16 v[16:19], v[168:171], a[88:91], v[16:19]\n"
"ds_read_b128 v[152:155], v95 offset:32768\n"
"v_mfma_f32_16x16x32_bf16 v[44:47], v[168:171], a[92:95], v[44:47]\n"
"s_waitcnt lgkmcnt(6)\n"
"v_mfma_f32_16x16x32_bf16 v[20:23], v[172:175], a[88:91], v[20:23]\n"
"ds_read_b128 v[156:159], v93 offset:12288\n"
"v_mfma_f32_16x16x32_bf16 v[48:51], v[172:175], a[92:95], v[48:51]\n"
"v_add_f32 v108, v108, v244\n"
"v_mfma_f32_16x16x32_bf16 v[24:27], v[176:179], a[88:91], v[24:27]\n"
"ds_read_b128 v[160:163], v93 offset:54272\n"
"v_mfma_f32_16x16x32_bf16 v[52:55], v[176:179], a[92:95], v[52:55]\n"
"buffer_load_dwordx4 a[180:183], v84, s[96:99], s61 offen offset:1024 sc1\n"
"v_add_f32 v109, v109, v245\n"
"s_waitcnt vmcnt(21)\n"
"v_mfma_f32_16x16x32_bf16 v[0:3], v[132:135], a[96:99], v[0:3]\n"
"ds_read_b128 v[164:167], v94 offset:13312\n"
"s_waitcnt vmcnt(20)\n"
"v_mfma_f32_16x16x32_bf16 v[28:31], v[132:135], a[100:103], v[28:31]\n"
"v_add_f32 v110, v110, v246\n"
"s_waitcnt lgkmcnt(6)\n"
"v_mfma_f32_16x16x32_bf16 v[4:7], v[136:139], a[96:99], v[4:7]\n"
"ds_read_b128 v[168:171], v94 offset:33792\n"
"v_mfma_f32_16x16x32_bf16 v[32:35], v[136:139], a[100:103], v[32:35]\n"
"v_add_f32 v111, v111, v247\n"
"v_mfma_f32_16x16x32_bf16 v[8:11], v[140:143], a[96:99], v[8:11]\n"
"ds_read_b128 v[172:175], v94 offset:54272\n"
"buffer_load_dwordx4 a[184:187], v85, s[96:99], s61 offen offset:1024 sc1\n"
"v_mfma_f32_16x16x32_bf16 v[36:39], v[140:143], a[100:103], v[36:39]\n"
"v_mfma_f32_16x16x32_bf16 v[12:15], v[144:147], a[96:99], v[12:15]\n"
"ds_read_b128 v[176:179], v95 offset:13312\n"
"v_mfma_f32_16x16x32_bf16 v[40:43], v[144:147], a[100:103], v[40:43]\n"
"ds_read_b128 v[240:243], v75 offset:3328\n"
"s_waitcnt lgkmcnt(6)\n"
"v_mfma_f32_16x16x32_bf16 v[16:19], v[148:151], a[96:99], v[16:19]\n"
"ds_read_b128 v[132:135], v95 offset:33792\n"
"v_mfma_f32_16x16x32_bf16 v[44:47], v[148:151], a[100:103], v[44:47]\n"
"v_add_f32 v112, v112, v232\n"
"v_mfma_f32_16x16x32_bf16 v[20:23], v[152:155], a[96:99], v[20:23]\n"
"ds_read_b128 v[136:139], v93 offset:13312\n"
"buffer_load_dwordx4 a[188:191], v86, s[96:99], s61 offen offset:1024 sc1\n"
"v_mfma_f32_16x16x32_bf16 v[48:51], v[152:155], a[100:103], v[48:51]\n"
"v_add_f32 v113, v113, v233\n"
"v_mfma_f32_16x16x32_bf16 v[24:27], v[156:159], a[96:99], v[24:27]\n"
"ds_read_b128 v[140:143], v93 offset:55296\n"
"v_mfma_f32_16x16x32_bf16 v[52:55], v[156:159], a[100:103], v[52:55]\n"
"v_add_f32 v114, v114, v234\n"
"s_waitcnt vmcnt(21) lgkmcnt(6)\n"
"v_mfma_f32_16x16x32_bf16 v[0:3], v[160:163], a[104:107], v[0:3]\n"
"ds_read_b128 v[144:147], v94 offset:14336\n"
"s_waitcnt vmcnt(20)\n"
"v_mfma_f32_16x16x32_bf16 v[28:31], v[160:163], a[108:111], v[28:31]\n"
"buffer_load_dwordx4 a[192:195], v87, s[96:99], s61 offen offset:1024 sc1\n"
"v_add_f32 v115, v115, v235\n"
"v_mfma_f32_16x16x32_bf16 v[4:7], v[164:167], a[104:107], v[4:7]\n"
"ds_read_b128 v[148:151], v94 offset:34816\n"
"v_mfma_f32_16x16x32_bf16 v[32:35], v[164:167], a[108:111], v[32:35]\n"
"v_mfma_f32_16x16x32_bf16 v[8:11], v[168:171], a[104:107], v[8:11]\n"
"ds_read_b128 v[152:155], v94 offset:55296\n"
"v_mfma_f32_16x16x32_bf16 v[36:39], v[168:171], a[108:111], v[36:39]\n"
"ds_read_b128 v[244:247], v75 offset:1344\n"
"s_waitcnt lgkmcnt(6)\n"
"v_mfma_f32_16x16x32_bf16 v[12:15], v[172:175], a[104:107], v[12:15]\n"
"ds_read_b128 v[156:159], v95 offset:14336\n"
"buffer_load_dwordx4 a[196:199], v88, s[96:99], s61 offen offset:1024 sc1\n"
"v_mfma_f32_16x16x32_bf16 v[40:43], v[172:175], a[108:111], v[40:43]\n"
"v_add_f32 v112, v112, v236\n"
"v_mfma_f32_16x16x32_bf16 v[16:19], v[176:179], a[104:107], v[16:19]\n"
"ds_read_b128 v[160:163], v95 offset:34816\n"
"v_mfma_f32_16x16x32_bf16 v[44:47], v[176:179], a[108:111], v[44:47]\n"
"v_add_f32 v113, v113, v237\n"
"v_mfma_f32_16x16x32_bf16 v[20:23], v[132:135], a[104:107], v[20:23]\n"
"ds_read_b128 v[164:167], v93 offset:14336\n"
"v_mfma_f32_16x16x32_bf16 v[48:51], v[132:135], a[108:111], v[48:51]\n"
"v_add_f32 v114, v114, v238\n"
"s_waitcnt lgkmcnt(6)\n"
"v_mfma_f32_16x16x32_bf16 v[24:27], v[136:139], a[104:107], v[24:27]\n"
"ds_read_b128 v[168:171], v93 offset:56320\n"
"buffer_load_dwordx4 a[200:203], v84, s[96:99], s61 offen offset:2048 sc1\n"
"v_mfma_f32_16x16x32_bf16 v[52:55], v[136:139], a[108:111], v[52:55]\n"
"s_waitcnt vmcnt(22)\n"
"v_mfma_f32_16x16x32_bf16 v[0:3], v[140:143], a[112:115], v[0:3]\n"
"ds_read_b128 v[172:175], v94 offset:15360\n"
"s_waitcnt vmcnt(21)\n"
"v_mfma_f32_16x16x32_bf16 v[28:31], v[140:143], a[116:119], v[28:31]\n"
"v_add_f32 v115, v115, v239\n"
"v_mfma_f32_16x16x32_bf16 v[4:7], v[144:147], a[112:115], v[4:7]\n"
"ds_read_b128 v[176:179], v94 offset:35840\n"
"v_mfma_f32_16x16x32_bf16 v[32:35], v[144:147], a[116:119], v[32:35]\n"
"ds_read_b128 v[232:235], v75 offset:2368\n"
"buffer_load_dwordx4 a[204:207], v85, s[96:99], s61 offen offset:2048 sc1\n"
"s_waitcnt lgkmcnt(6)\n"
"v_mfma_f32_16x16x32_bf16 v[8:11], v[148:151], a[112:115], v[8:11]\n"
"ds_read_b128 v[132:135], v94 offset:56320\n"
"v_mfma_f32_16x16x32_bf16 v[36:39], v[148:151], a[116:119], v[36:39]\n"
"v_add_f32 v112, v112, v240\n"
"v_mfma_f32_16x16x32_bf16 v[12:15], v[152:155], a[112:115], v[12:15]\n"
"ds_read_b128 v[136:139], v95 offset:15360\n"
"v_mfma_f32_16x16x32_bf16 v[40:43], v[152:155], a[116:119], v[40:43]\n"
"v_add_f32 v113, v113, v241\n"
"v_mfma_f32_16x16x32_bf16 v[16:19], v[156:159], a[112:115], v[16:19]\n"
"ds_read_b128 v[140:143], v95 offset:35840\n"
"v_mfma_f32_16x16x32_bf16 v[44:47], v[156:159], a[116:119], v[44:47]\n"
"buffer_load_dwordx4 a[208:211], v86, s[96:99], s61 offen offset:2048 sc1\n"
"s_waitcnt lgkmcnt(6)\n"
"v_mfma_f32_16x16x32_bf16 v[20:23], v[160:163], a[112:115], v[20:23]\n"
"ds_read_b128 v[144:147], v93 offset:15360\n"
"v_mfma_f32_16x16x32_bf16 v[48:51], v[160:163], a[116:119], v[48:51]\n"
"v_add_f32 v114, v114, v242\n"
"v_mfma_f32_16x16x32_bf16 v[24:27], v[164:167], a[112:115], v[24:27]\n"
"ds_read_b128 v[148:151], v93 offset:57344\n"
"v_mfma_f32_16x16x32_bf16 v[52:55], v[164:167], a[116:119], v[52:55]\n"
"v_add_f32 v115, v115, v243\n"
"s_waitcnt vmcnt(22)\n"
"v_mfma_f32_16x16x32_bf16 v[0:3], v[168:171], a[120:123], v[0:3]\n"
"ds_read_b128 v[152:155], v94 offset:16384\n"
"buffer_load_dwordx4 a[212:215], v87, s[96:99], s61 offen offset:2048 sc1\n"
"s_waitcnt vmcnt(22)\n"
"v_mfma_f32_16x16x32_bf16 v[28:31], v[168:171], a[124:127], v[28:31]\n"
"ds_read_b128 v[236:239], v75 offset:3392\n"
"s_waitcnt lgkmcnt(6)\n"
"v_mfma_f32_16x16x32_bf16 v[4:7], v[172:175], a[120:123], v[4:7]\n"
"ds_read_b128 v[156:159], v94 offset:36864\n"
"v_mfma_f32_16x16x32_bf16 v[32:35], v[172:175], a[124:127], v[32:35]\n"
"v_add_f32 v116, v116, v244\n"
"v_mfma_f32_16x16x32_bf16 v[8:11], v[176:179], a[120:123], v[8:11]\n"
"ds_read_b128 v[160:163], v94 offset:57344\n"
"v_mfma_f32_16x16x32_bf16 v[36:39], v[176:179], a[124:127], v[36:39]\n"
"v_add_f32 v117, v117, v245\n"
"v_mfma_f32_16x16x32_bf16 v[12:15], v[132:135], a[120:123], v[12:15]\n"
"ds_read_b128 v[164:167], v95 offset:16384\n"
"buffer_load_dwordx4 a[216:219], v88, s[96:99], s61 offen offset:2048 sc1\n"
"v_mfma_f32_16x16x32_bf16 v[40:43], v[132:135], a[124:127], v[40:43]\n"
"s_waitcnt lgkmcnt(6)\n"
"v_mfma_f32_16x16x32_bf16 v[16:19], v[136:139], a[120:123], v[16:19]\n"
"ds_read_b128 v[168:171], v95 offset:36864\n"
"v_mfma_f32_16x16x32_bf16 v[44:47], v[136:139], a[124:127], v[44:47]\n"
"v_add_f32 v118, v118, v246\n"
"v_mfma_f32_16x16x32_bf16 v[20:23], v[140:143], a[120:123], v[20:23]\n"
"ds_read_b128 v[172:175], v93 offset:16384\n"
"v_mfma_f32_16x16x32_bf16 v[48:51], v[140:143], a[124:127], v[48:51]\n"
"buffer_load_dwordx4 a[220:223], v84, s[96:99], s61 offen offset:3072 sc1\n"
"v_add_f32 v119, v119, v247\n"
"v_mfma_f32_16x16x32_bf16 v[24:27], v[144:147], a[120:123], v[24:27]\n"
"ds_read_b128 v[176:179], v93 offset:58368\n"
"v_mfma_f32_16x16x32_bf16 v[52:55], v[144:147], a[124:127], v[52:55]\n"
"ds_read_b128 v[240:243], v75 offset:1408\n"
"s_waitcnt vmcnt(23) lgkmcnt(6)\n"
"v_mfma_f32_16x16x32_bf16 v[0:3], v[148:151], a[128:131], v[0:3]\n"
"ds_read_b128 v[132:135], v94 offset:17408\n"
"s_waitcnt vmcnt(22)\n"
"v_mfma_f32_16x16x32_bf16 v[28:31], v[148:151], a[132:135], v[28:31]\n"
"v_add_f32 v116, v116, v232\n"
"v_mfma_f32_16x16x32_bf16 v[4:7], v[152:155], a[128:131], v[4:7]\n"
"ds_read_b128 v[136:139], v94 offset:37888\n"
"v_mfma_f32_16x16x32_bf16 v[32:35], v[152:155], a[132:135], v[32:35]\n"
"buffer_load_dwordx4 a[224:227], v85, s[96:99], s61 offen offset:3072 sc1\n"
"v_mfma_f32_16x16x32_bf16 v[8:11], v[156:159], a[128:131], v[8:11]\n"
"ds_read_b128 v[140:143], v94 offset:58368\n"
"v_mfma_f32_16x16x32_bf16 v[36:39], v[156:159], a[132:135], v[36:39]\n"
"v_add_f32 v117, v117, v233\n"
"s_waitcnt lgkmcnt(6)\n"
"v_mfma_f32_16x16x32_bf16 v[12:15], v[160:163], a[128:131], v[12:15]\n"
"ds_read_b128 v[144:147], v95 offset:17408\n"
"v_mfma_f32_16x16x32_bf16 v[40:43], v[160:163], a[132:135], v[40:43]\n"
"v_add_f32 v118, v118, v234\n"
"v_mfma_f32_16x16x32_bf16 v[16:19], v[164:167], a[128:131], v[16:19]\n"
"ds_read_b128 v[148:151], v95 offset:37888\n"
"buffer_load_dwordx4 a[228:231], v86, s[96:99], s61 offen offset:3072 sc1\n"
"v_mfma_f32_16x16x32_bf16 v[44:47], v[164:167], a[132:135], v[44:47]\n"
"v_add_f32 v119, v119, v235\n"
"v_mfma_f32_16x16x32_bf16 v[20:23], v[168:171], a[128:131], v[20:23]\n"
"ds_read_b128 v[152:155], v93 offset:17408\n"
"v_mfma_f32_16x16x32_bf16 v[48:51], v[168:171], a[132:135], v[48:51]\n"
"ds_read_b128 v[244:247], v75 offset:2432\n"
"s_waitcnt lgkmcnt(6)\n"
"v_mfma_f32_16x16x32_bf16 v[24:27], v[172:175], a[128:131], v[24:27]\n"
"ds_read_b128 v[156:159], v93 offset:59392\n"
"v_mfma_f32_16x16x32_bf16 v[52:55], v[172:175], a[132:135], v[52:55]\n"
"buffer_load_dwordx4 a[232:235], v87, s[96:99], s61 offen offset:3072 sc1\n"
"s_waitcnt vmcnt(24)\n"
"v_mfma_f32_16x16x32_bf16 v[0:3], v[176:179], a[136:139], v[0:3]\n"
"ds_read_b128 v[160:163], v94 offset:18432\n"
"s_waitcnt vmcnt(23)\n"
"v_mfma_f32_16x16x32_bf16 v[28:31], v[176:179], a[140:143], v[28:31]\n"
"v_add_f32 v116, v116, v236\n"
"v_mfma_f32_16x16x32_bf16 v[4:7], v[132:135], a[136:139], v[4:7]\n"
"ds_read_b128 v[164:167], v94 offset:38912\n"
"v_mfma_f32_16x16x32_bf16 v[32:35], v[132:135], a[140:143], v[32:35]\n"
"v_add_f32 v117, v117, v237\n"
"s_waitcnt lgkmcnt(6)\n"
"v_mfma_f32_16x16x32_bf16 v[8:11], v[136:139], a[136:139], v[8:11]\n"
"ds_read_b128 v[168:171], v94 offset:59392\n"
"v_mfma_f32_16x16x32_bf16 v[36:39], v[136:139], a[140:143], v[36:39]\n"
"buffer_load_dwordx4 a[236:239], v88, s[96:99], s61 offen offset:3072 sc1\n"
"v_add_f32 v118, v118, v238\n"
"v_mfma_f32_16x16x32_bf16 v[12:15], v[140:143], a[136:139], v[12:15]\n"
"ds_read_b128 v[172:175], v95 offset:18432\n"
"v_mfma_f32_16x16x32_bf16 v[40:43], v[140:143], a[140:143], v[40:43]\n"
"v_add_f32 v119, v119, v239\n"
"v_mfma_f32_16x16x32_bf16 v[16:19], v[144:147], a[136:139], v[16:19]\n"
"ds_read_b128 v[176:179], v95 offset:38912\n"
"v_mfma_f32_16x16x32_bf16 v[44:47], v[144:147], a[140:143], v[44:47]\n"
"ds_read_b128 v[232:235], v75 offset:3456\n"
"s_waitcnt lgkmcnt(6)\n"
"v_mfma_f32_16x16x32_bf16 v[20:23], v[148:151], a[136:139], v[20:23]\n"
"ds_read_b128 v[132:135], v93 offset:18432\n"
"s_add_u32 s61, s61, 4096\n"
"v_mfma_f32_16x16x32_bf16 v[48:51], v[148:151], a[140:143], v[48:51]\n"
"v_mfma_f32_16x16x32_bf16 v[24:27], v[152:155], a[136:139], v[24:27]\n"
"ds_read_b128 v[136:139], v93 offset:60416\n"
"v_mfma_f32_16x16x32_bf16 v[52:55], v[152:155], a[140:143], v[52:55]\n"
"v_add_f32 v120, v120, v240\n"
"s_waitcnt vmcnt(23)\n"
"v_mfma_f32_16x16x32_bf16 v[0:3], v[156:159], a[144:147], v[0:3]\n"
"ds_read_b128 v[140:143], v94 offset:19456\n"
"s_waitcnt vmcnt(22)\n"
"v_mfma_f32_16x16x32_bf16 v[28:31], v[156:159], a[148:151], v[28:31]\n"
"v_add_f32 v121, v121, v241\n"
"s_waitcnt lgkmcnt(6)\n"
"v_mfma_f32_16x16x32_bf16 v[4:7], v[160:163], a[144:147], v[4:7]\n"
"ds_read_b128 v[144:147], v94 offset:39936\n"
"buffer_load_dwordx4 a[240:243], v84, s[96:99], s61 offen offset:0 sc1\n"
"PG24_%=:\n"
"s_cbranch_execz RPG24_%=\n"
"v_mfma_f32_16x16x32_bf16 v[32:35], v[160:163], a[148:151], v[32:35]\n"
"v_add_f32 v122, v122, v242\n"
"v_mfma_f32_16x16x32_bf16 v[8:11], v[164:167], a[144:147], v[8:11]\n"
"ds_read_b128 v[148:151], v94 offset:60416\n"
"v_mfma_f32_16x16x32_bf16 v[36:39], v[164:167], a[148:151], v[36:39]\n"
"v_add_f32 v123, v123, v243\n"
"v_mfma_f32_16x16x32_bf16 v[12:15], v[168:171], a[144:147], v[12:15]\n"
"ds_read_b128 v[152:155], v95 offset:19456\n"
"v_mfma_f32_16x16x32_bf16 v[40:43], v[168:171], a[148:151], v[40:43]\n"
"buffer_load_dwordx4 a[244:247], v85, s[96:99], s61 offen offset:0 sc1\n"
"s_waitcnt lgkmcnt(5)\n"
"v_mfma_f32_16x16x32_bf16 v[16:19], v[172:175], a[144:147], v[16:19]\n"
"ds_read_b128 v[156:159], v95 offset:39936\n"
"v_mfma_f32_16x16x32_bf16 v[44:47], v[172:175], a[148:151], v[44:47]\n"
"v_add_f32 v120, v120, v244\n"
"v_mfma_f32_16x16x32_bf16 v[20:23], v[176:179], a[144:147], v[20:23]\n"
"ds_read_b128 v[160:163], v93 offset:19456\n"
"v_mfma_f32_16x16x32_bf16 v[48:51], v[176:179], a[148:151], v[48:51]\n"
"v_add_f32 v121, v121, v245\n"
"v_mfma_f32_16x16x32_bf16 v[24:27], v[132:135], a[144:147], v[24:27]\n"
"v_add_f32 v122, v122, v246\n"
"v_mfma_f32_16x16x32_bf16 v[52:55], v[132:135], a[148:151], v[52:55]\n"
"buffer_load_dwordx4 a[248:251], v86, s[96:99], s61 offen offset:0 sc1\n"
"v_add_f32 v123, v123, v247\n"
"s_waitcnt vmcnt(24) lgkmcnt(4)\n"
"v_mfma_f32_16x16x32_bf16 v[0:3], v[136:139], a[152:155], v[0:3]\n"
"s_waitcnt vmcnt(23)\n"
"v_mfma_f32_16x16x32_bf16 v[28:31], v[136:139], a[156:159], v[28:31]\n"
"v_add_f32 v120, v120, v232\n"
"v_mfma_f32_16x16x32_bf16 v[4:7], v[140:143], a[152:155], v[4:7]\n"
"v_add_f32 v121, v121, v233\n"
"v_mfma_f32_16x16x32_bf16 v[32:35], v[140:143], a[156:159], v[32:35]\n"
"v_add_f32 v122, v122, v234\n"
"v_mfma_f32_16x16x32_bf16 v[8:11], v[144:147], a[152:155], v[8:11]\n"
"buffer_load_dwordx4 a[252:255], v87, s[96:99], s61 offen offset:0 sc1\n"
"v_add_f32 v123, v123, v235\n"
"v_mfma_f32_16x16x32_bf16 v[36:39], v[144:147], a[156:159], v[36:39]\n"
"s_waitcnt lgkmcnt(1)\n"
"v_mfma_f32_16x16x32_bf16 v[12:15], v[148:151], a[152:155], v[12:15]\n"
"v_mfma_f32_16x16x32_bf16 v[40:43], v[148:151], a[156:159], v[40:43]\n"
"v_mfma_f32_16x16x32_bf16 v[16:19], v[152:155], a[152:155], v[16:19]\n"
"v_mfma_f32_16x16x32_bf16 v[44:47], v[152:155], a[156:159], v[44:47]\n"
"v_mfma_f32_16x16x32_bf16 v[20:23], v[156:159], a[152:155], v[20:23]\n"
"v_mfma_f32_16x16x32_bf16 v[48:51], v[156:159], a[156:159], v[48:51]\n"
"s_waitcnt lgkmcnt(0)\n"
"v_mfma_f32_16x16x32_bf16 v[24:27], v[160:163], a[152:155], v[24:27]\n"
"v_mfma_f32_16x16x32_bf16 v[52:55], v[160:163], a[156:159], v[52:55]\n"
"s_nop 7\n"
"v_cmp_u_f32 vcc, v24, v52\n"
"s_nop 0\n"
"s_cbranch_vccnz REDO_G3_%=\n"
"DONE_G3_%=:\n"
"buffer_load_dwordx4 a[0:3], v88, s[96:99], s61 offen offset:0 sc1\n"
"buffer_load_dwordx4 a[4:7], v84, s[96:99], s61 offen offset:1024 sc1\n"
"buffer_load_dwordx4 a[8:11], v85, s[96:99], s61 offen offset:1024 sc1\n"
"buffer_load_dwordx4 a[12:15], v86, s[96:99], s61 offen offset:1024 sc1\n"
"buffer_load_dwordx4 a[16:19], v87, s[96:99], s61 offen offset:1024 sc1\n"
"buffer_load_dwordx4 a[20:23], v88, s[96:99], s61 offen offset:1024 sc1\n"
"buffer_load_dwordx4 a[24:27], v84, s[96:99], s61 offen offset:2048 sc1\n"
"buffer_load_dwordx4 a[28:31], v85, s[96:99], s61 offen offset:2048 sc1\n"
"buffer_load_dwordx4 a[32:35], v86, s[96:99], s61 offen offset:2048 sc1\n"
"buffer_load_dwordx4 a[36:39], v87, s[96:99], s61 offen offset:2048 sc1\n"
"buffer_load_dwordx4 a[40:43], v88, s[96:99], s61 offen offset:2048 sc1\n"
"buffer_load_dwordx4 a[44:47], v84, s[96:99], s61 offen offset:3072 sc1\n"
"v_pk_mul_f32 v[0:1], v[0:1], v[58:59]\n"
"v_pk_mul_f32 v[2:3], v[2:3], v[58:59]\n"
"v_pk_mul_f32 v[4:5], v[4:5], v[58:59]\n"
"v_pk_mul_f32 v[6:7], v[6:7], v[58:59]\n"
"v_pk_mul_f32 v[8:9], v[8:9], v[58:59]\n"
"v_pk_mul_f32 v[10:11], v[10:11], v[58:59]\n"
"v_pk_mul_f32 v[12:13], v[12:13], v[58:59]\n"
"v_pk_mul_f32 v[14:15], v[14:15], v[58:59]\n"
"v_pk_mul_f32 v[16:17], v[16:17], v[58:59]\n"
"v_pk_mul_f32 v[18:19], v[18:19], v[58:59]\n"
"v_pk_mul_f32 v[20:21], v[20:21], v[58:59]\n"
"v_pk_mul_f32 v[22:23], v[22:23], v[58:59]\n"
"v_pk_mul_f32 v[24:25], v[24:25], v[58:59]\n"
"v_pk_mul_f32 v[26:27], v[26:27], v[58:59]\n"
"v_pk_fma_f32 v[0:1], v[96:97], v[56:57], v[0:1]\n"
"v_pk_fma_f32 v[2:3], v[98:99], v[56:57], v[2:3]\n"
"v_pk_fma_f32 v[4:5], v[100:101], v[56:57], v[4:5]\n"
"v_pk_fma_f32 v[6:7], v[102:103], v[56:57], v[6:7]\n"
"v_pk_fma_f32 v[8:9], v[104:105], v[56:57], v[8:9]\n"
"v_pk_fma_f32 v[10:11], v[106:107], v[56:57], v[10:11]\n"
"v_pk_fma_f32 v[12:13], v[108:109], v[56:57], v[12:13]\n"
"v_pk_fma_f32 v[14:15], v[110:111], v[56:57], v[14:15]\n"
"v_pk_fma_f32 v[16:17], v[112:113], v[56:57], v[16:17]\n"
"v_pk_fma_f32 v[18:19], v[114:115], v[56:57], v[18:19]\n"
"v_pk_fma_f32 v[20:21], v[116:117], v[56:57], v[20:21]\n"
"v_pk_fma_f32 v[22:23], v[118:119], v[56:57], v[22:23]\n"
"v_pk_fma_f32 v[24:25], v[120:121], v[56:57], v[24:25]\n"
"v_pk_fma_f32 v[26:27], v[122:123], v[56:57], v[26:27]\n"
"v_pk_mul_f32 v[132:133], v[0:1], v[0:1]\n"
"v_pk_mul_f32 v[134:135], v[2:3], v[2:3]\n"
"v_pk_mul_f32 v[136:137], v[4:5], v[4:5]\n"
"v_pk_mul_f32 v[138:139], v[6:7], v[6:7]\n"
"v_pk_mul_f32 v[140:141], v[8:9], v[8:9]\n"
"v_pk_mul_f32 v[142:143], v[10:11], v[10:11]\n"
"v_pk_mul_f32 v[144:145], v[12:13], v[12:13]\n"
"v_pk_mul_f32 v[146:147], v[14:15], v[14:15]\n"
"v_pk_mul_f32 v[148:149], v[16:17], v[16:17]\n"
"v_pk_mul_f32 v[150:151], v[18:19], v[18:19]\n"
"v_pk_mul_f32 v[152:153], v[20:21], v[20:21]\n"
"v_pk_mul_f32 v[154:155], v[22:23], v[22:23]\n"
"v_pk_mul_f32 v[156:157], v[24:25], v[24:25]\n"
"v_pk_mul_f32 v[158:159], v[26:27], v[26:27]\n"
"v_pk_fma_f32 v[132:133], v[132:133], s[56:57], v[206:207]\n"
"v_pk_fma_f32 v[134:135], v[134:135], s[56:57], v[206:207]\n"
"v_pk_fma_f32 v[136:137], v[136:137], s[56:57], v[206:207]\n"
"v_pk_fma_f32 v[138:139], v[138:139], s[56:57], v[206:207]\n"
"v_pk_fma_f32 v[140:141], v[140:141], s[56:57], v[206:207]\n"
"v_pk_fma_f32 v[142:143], v[142:143], s[56:57], v[206:207]\n"
"v_pk_fma_f32 v[144:145], v[144:145], s[56:57], v[206:207]\n"
"v_pk_fma_f32 v[146:147], v[146:147], s[56:57], v[206:207]\n"
"v_pk_fma_f32 v[148:149], v[148:149], s[56:57], v[206:207]\n"
"v_pk_fma_f32 v[150:151], v[150:151], s[56:57], v[206:207]\n"
"v_pk_fma_f32 v[152:153], v[152:153], s[56:57], v[206:207]\n"
"v_pk_fma_f32 v[154:155], v[154:155], s[56:57], v[206:207]\n"
"v_pk_fma_f32 v[156:157], v[156:157], s[56:57], v[206:207]\n"
"v_pk_fma_f32 v[158:159], v[158:159], s[56:57], v[206:207]\n"
"v_pk_mul_f32 v[132:133], v[132:133], v[0:1]\n"
"v_pk_mul_f32 v[134:135], v[134:135], v[2:3]\n"
"v_pk_mul_f32 v[136:137], v[136:137], v[4:5]\n"
"v_pk_mul_f32 v[138:139], v[138:139], v[6:7]\n"
"v_pk_mul_f32 v[140:141], v[140:141], v[8:9]\n"
"v_pk_mul_f32 v[142:143], v[142:143], v[10:11]\n"
"v_pk_mul_f32 v[144:145], v[144:145], v[12:13]\n"
"v_pk_mul_f32 v[146:147], v[146:147], v[14:15]\n"
"v_pk_mul_f32 v[148:149], v[148:149], v[16:17]\n"
"v_pk_mul_f32 v[150:151], v[150:151], v[18:19]\n"
"v_pk_mul_f32 v[152:153], v[152:153], v[20:21]\n"
"v_pk_mul_f32 v[154:155], v[154:155], v[22:23]\n"
"v_pk_mul_f32 v[156:157], v[156:157], v[24:25]\n"
"v_pk_mul_f32 v[158:159], v[158:159], v[26:27]\n"
"v_exp_f32 v132, v132\n"
"v_exp_f32 v133, v133\n"
"v_exp_f32 v134, v134\n"
"v_exp_f32 v135, v135\n"
"v_exp_f32 v136, v136\n"
"v_exp_f32 v137, v137\n"
"v_exp_f32 v138, v138\n"
"v_exp_f32 v139, v139\n"
"v_exp_f32 v140, v140\n"
"v_exp_f32 v141, v141\n"
"v_exp_f32 v142, v142\n"
"v_exp_f32 v143, v143\n"
"v_exp_f32 v144, v144\n"
"v_exp_f32 v145, v145\n"
"v_exp_f32 v146, v146\n"
"v_exp_f32 v147, v147\n"
"v_exp_f32 v148, v148\n"
"v_exp_f32 v149, v149\n"
"v_exp_f32 v150, v150\n"
"v_exp_f32 v151, v151\n"
"v_exp_f32 v152, v152\n"
"v_exp_f32 v153, v153\n"
"v_exp_f32 v154, v154\n"
"v_exp_f32 v155, v155\n"
"v_exp_f32 v156, v156\n"
"v_exp_f32 v157, v157\n"
"v_exp_f32 v158, v158\n"
"v_exp_f32 v159, v159\n"
"v_pk_add_f32 v[132:133], v[132:133], v[208:209]\n"
"v_pk_add_f32 v[134:135], v[134:135], v[208:209]\n"
"v_pk_add_f32 v[136:137], v[136:137], v[208:209]\n"
"v_pk_add_f32 v[138:139], v[138:139], v[208:209]\n"
"v_pk_add_f32 v[140:141], v[140:141], v[208:209]\n"
"v_pk_add_f32 v[142:143], v[142:143], v[208:209]\n"
"v_pk_add_f32 v[144:145], v[144:145], v[208:209]\n"
"v_pk_add_f32 v[146:147], v[146:147], v[208:209]\n"
"v_pk_add_f32 v[148:149], v[148:149], v[208:209]\n"
"v_pk_add_f32 v[150:151], v[150:151], v[208:209]\n"
"v_pk_add_f32 v[152:153], v[152:153], v[208:209]\n"
"v_pk_add_f32 v[154:155], v[154:155], v[208:209]\n"
"v_pk_add_f32 v[156:157], v[156:157], v[208:209]\n"
"v_pk_add_f32 v[158:159], v[158:159], v[208:209]\n"
"v_rcp_f32 v132, v132\n"
"v_rcp_f32 v133, v133\n"
"v_rcp_f32 v134, v134\n"
"v_rcp_f32 v135, v135\n"
"v_rcp_f32 v136, v136\n"
"v_rcp_f32 v137, v137\n"
"v_rcp_f32 v138, v138\n"
"v_rcp_f32 v139, v139\n"
"v_rcp_f32 v140, v140\n"
"v_rcp_f32 v141, v141\n"
"v_rcp_f32 v142, v142\n"
"v_rcp_f32 v143, v143\n"
"v_rcp_f32 v144, v144\n"
"v_rcp_f32 v145, v145\n"
"v_rcp_f32 v146, v146\n"
"v_rcp_f32 v147, v147\n"
"v_rcp_f32 v148, v148\n"
"v_rcp_f32 v149, v149\n"
"v_rcp_f32 v150, v150\n"
"v_rcp_f32 v151, v151\n"
"v_rcp_f32 v152, v152\n"
"v_rcp_f32 v153, v153\n"
"v_rcp_f32 v154, v154\n"
"v_rcp_f32 v155, v155\n"
"v_rcp_f32 v156, v156\n"
"v_rcp_f32 v157, v157\n"
"v_rcp_f32 v158, v158\n"
"v_rcp_f32 v159, v159\n"
"v_pk_mul_f32 v[0:1], v[0:1], v[132:133]\n"
"v_pk_mul_f32 v[2:3], v[2:3], v[134:135]\n"
"v_pk_mul_f32 v[4:5], v[4:5], v[136:137]\n"
"v_pk_mul_f32 v[6:7], v[6:7], v[138:139]\n"
"v_pk_mul_f32 v[8:9], v[8:9], v[140:141]\n"
"v_pk_mul_f32 v[10:11], v[10:11], v[142:143]\n"
"v_pk_mul_f32 v[12:13], v[12:13], v[144:145]\n"
"v_pk_mul_f32 v[14:15], v[14:15], v[146:147]\n"
"v_pk_mul_f32 v[16:17], v[16:17], v[148:149]\n"
"v_pk_mul_f32 v[18:19], v[18:19], v[150:151]\n"
"v_pk_mul_f32 v[20:21], v[20:21], v[152:153]\n"
"v_pk_mul_f32 v[22:23], v[22:23], v[154:155]\n"
"v_pk_mul_f32 v[24:25], v[24:25], v[156:157]\n"
"v_pk_mul_f32 v[26:27], v[26:27], v[158:159]\n"
"v_cvt_pk_bf16_f32 v160, v0, v1\n"
"v_cvt_pk_bf16_f32 v161, v2, v3\n"
"v_cvt_pk_bf16_f32 v162, v4, v5\n"
"v_cvt_pk_bf16_f32 v163, v6, v7\n"
"v_cvt_pk_bf16_f32 v164, v8, v9\n"
"v_cvt_pk_bf16_f32 v165, v10, v11\n"
"v_cvt_pk_bf16_f32 v166, v12, v13\n"
"v_cvt_pk_bf16_f32 v167, v14, v15\n"
"v_cvt_pk_bf16_f32 v168, v16, v17\n"
"v_cvt_pk_bf16_f32 v169, v18, v19\n"
"v_cvt_pk_bf16_f32 v170, v20, v21\n"
"v_cvt_pk_bf16_f32 v171, v22, v23\n"
"v_cvt_pk_bf16_f32 v172, v24, v25\n"
"v_cvt_pk_bf16_f32 v173, v26, v27\n"
"s_mul_i32 s38, s48, 57344\n"
"s_add_u32 s38, s38, 114819072\n"
"s_add_u32 s39, s38, s41\n"
"s_add_u32 s38, s38, s40\n"
"buffer_store_dwordx4 v[160:163], v251, s[96:99], s38 offen offset:0 sc1\n"
"buffer_store_dwordx4 v[164:167], v251, s[96:99], s38 offen offset:1024 sc1\n"
"buffer_store_dwordx4 v[168:171], v251, s[96:99], s38 offen offset:2048 sc1\n"
"buffer_store_dwordx2 v[172:173], v250, s[96:99], s39 offen sc1\n"
"s_add_u32 s48, s48, 1\n"
"v_pk_mul_f32 v[28:29], v[28:29], v[66:67]\n"
"v_pk_mul_f32 v[30:31], v[30:31], v[66:67]\n"
"v_pk_mul_f32 v[32:33], v[32:33], v[66:67]\n"
"v_pk_mul_f32 v[34:35], v[34:35], v[66:67]\n"
"v_pk_mul_f32 v[36:37], v[36:37], v[66:67]\n"
"v_pk_mul_f32 v[38:39], v[38:39], v[66:67]\n"
"v_pk_mul_f32 v[40:41], v[40:41], v[66:67]\n"
"v_pk_mul_f32 v[42:43], v[42:43], v[66:67]\n"
"v_pk_mul_f32 v[44:45], v[44:45], v[66:67]\n"
"v_pk_mul_f32 v[46:47], v[46:47], v[66:67]\n"
"v_pk_mul_f32 v[48:49], v[48:49], v[66:67]\n"
"v_pk_mul_f32 v[50:51], v[50:51], v[66:67]\n"
"v_pk_mul_f32 v[52:53], v[52:53], v[66:67]\n"
"v_pk_mul_f32 v[54:55], v[54:55], v[66:67]\n"
"v_pk_fma_f32 v[28:29], v[96:97], v[64:65], v[28:29]\n"
"v_pk_fma_f32 v[30:31], v[98:99], v[64:65], v[30:31]\n"
"v_pk_fma_f32 v[32:33], v[100:101], v[64:65], v[32:33]\n"
"v_pk_fma_f32 v[34:35], v[102:103], v[64:65], v[34:35]\n"
"v_pk_fma_f32 v[36:37], v[104:105], v[64:65], v[36:37]\n"
"v_pk_fma_f32 v[38:39], v[106:107], v[64:65], v[38:39]\n"
"v_pk_fma_f32 v[40:41], v[108:109], v[64:65], v[40:41]\n"
"v_pk_fma_f32 v[42:43], v[110:111], v[64:65], v[42:43]\n"
"v_pk_fma_f32 v[44:45], v[112:113], v[64:65], v[44:45]\n"
"v_pk_fma_f32 v[46:47], v[114:115], v[64:65], v[46:47]\n"
"v_pk_fma_f32 v[48:49], v[116:117], v[64:65], v[48:49]\n"
"v_pk_fma_f32 v[50:51], v[118:119], v[64:65], v[50:51]\n"
"v_pk_fma_f32 v[52:53], v[120:121], v[64:65], v[52:53]\n"
"v_pk_fma_f32 v[54:55], v[122:123], v[64:65], v[54:55]\n"
"v_pk_mul_f32 v[132:133], v[28:29], v[28:29]\n"
"v_pk_mul_f32 v[134:135], v[30:31], v[30:31]\n"
"v_pk_mul_f32 v[136:137], v[32:33], v[32:33]\n"
"v_pk_mul_f32 v[138:139], v[34:35], v[34:35]\n"
"v_pk_mul_f32 v[140:141], v[36:37], v[36:37]\n"
"v_pk_mul_f32 v[142:143], v[38:39], v[38:39]\n"
"v_pk_mul_f32 v[144:145], v[40:41], v[40:41]\n"
"v_pk_mul_f32 v[146:147], v[42:43], v[42:43]\n"
"v_pk_mul_f32 v[148:149], v[44:45], v[44:45]\n"
"v_pk_mul_f32 v[150:151], v[46:47], v[46:47]\n"
"v_pk_mul_f32 v[152:153], v[48:49], v[48:49]\n"
"v_pk_mul_f32 v[154:155], v[50:51], v[50:51]\n"
"v_pk_mul_f32 v[156:157], v[52:53], v[52:53]\n"
"v_pk_mul_f32 v[158:159], v[54:55], v[54:55]\n"
"v_pk_fma_f32 v[132:133], v[132:133], s[56:57], v[206:207]\n"
"v_pk_fma_f32 v[134:135], v[134:135], s[56:57], v[206:207]\n"
"v_pk_fma_f32 v[136:137], v[136:137], s[56:57], v[206:207]\n"
"v_pk_fma_f32 v[138:139], v[138:139], s[56:57], v[206:207]\n"
"v_pk_fma_f32 v[140:141], v[140:141], s[56:57], v[206:207]\n"
"v_pk_fma_f32 v[142:143], v[142:143], s[56:57], v[206:207]\n"
"v_pk_fma_f32 v[144:145], v[144:145], s[56:57], v[206:207]\n"
"v_pk_fma_f32 v[146:147], v[146:147], s[56:57], v[206:207]\n"
"v_pk_fma_f32 v[148:149], v[148:149], s[56:57], v[206:207]\n"
"v_pk_fma_f32 v[150:151], v[150:151], s[56:57], v[206:207]\n"
"v_pk_fma_f32 v[152:153], v[152:153], s[56:57], v[206:207]\n"
"v_pk_fma_f32 v[154:155], v[154:155], s[56:57], v[206:207]\n"
"v_pk_fma_f32 v[156:157], v[156:157], s[56:57], v[206:207]\n"
"v_pk_fma_f32 v[158:159], v[158:159], s[56:57], v[206:207]\n"
"v_pk_mul_f32 v[132:133], v[132:133], v[28:29]\n"
"v_pk_mul_f32 v[134:135], v[134:135], v[30:31]\n"
"v_pk_mul_f32 v[136:137], v[136:137], v[32:33]\n"
"v_pk_mul_f32 v[138:139], v[138:139], v[34:35]\n"
"v_pk_mul_f32 v[140:141], v[140:141], v[36:37]\n"
"v_pk_mul_f32 v[142:143], v[142:143], v[38:39]\n"
"v_pk_mul_f32 v[144:145], v[144:145], v[40:41]\n"
"v_pk_mul_f32 v[146:147], v[146:147], v[42:43]\n"
"v_pk_mul_f32 v[148:149], v[148:149], v[44:45]\n"
"v_pk_mul_f32 v[150:151], v[150:151], v[46:47]\n"
"v_pk_mul_f32 v[152:153], v[152:153], v[48:49]\n"
"v_pk_mul_f32 v[154:155], v[154:155], v[50:51]\n"
"v_pk_mul_f32 v[156:157], v[156:157], v[52:53]\n"
"v_pk_mul_f32 v[158:159], v[158:159], v[54:55]\n"
"v_exp_f32 v132, v132\n"
"v_exp_f32 v133, v133\n"
"v_exp_f32 v134, v134\n"
"v_exp_f32 v135, v135\n"
"v_exp_f32 v136, v136\n"
"v_exp_f32 v137, v137\n"
"v_exp_f32 v138, v138\n"
"v_exp_f32 v139, v139\n"
"v_exp_f32 v140, v140\n"
"v_exp_f32 v141, v141\n"
"v_exp_f32 v142, v142\n"
"v_exp_f32 v143, v143\n"
"v_exp_f32 v144, v144\n"
"v_exp_f32 v145, v145\n"
"v_exp_f32 v146, v146\n"
"v_exp_f32 v147, v147\n"
"v_exp_f32 v148, v148\n"
"v_exp_f32 v149, v149\n"
"v_exp_f32 v150, v150\n"
"v_exp_f32 v151, v151\n"
"v_exp_f32 v152, v152\n"
"v_exp_f32 v153, v153\n"
"v_exp_f32 v154, v154\n"
"v_exp_f32 v155, v155\n"
"v_exp_f32 v156, v156\n"
"v_exp_f32 v157, v157\n"
"v_exp_f32 v158, v158\n"
"v_exp_f32 v159, v159\n"
"v_pk_add_f32 v[132:133], v[132:133], v[208:209]\n"
"v_pk_add_f32 v[134:135], v[134:135], v[208:209]\n"
"v_pk_add_f32 v[136:137], v[136:137], v[208:209]\n"
"v_pk_add_f32 v[138:139], v[138:139], v[208:209]\n"
"v_pk_add_f32 v[140:141], v[140:141], v[208:209]\n"
"v_pk_add_f32 v[142:143], v[142:143], v[208:209]\n"
"v_pk_add_f32 v[144:145], v[144:145], v[208:209]\n"
"v_pk_add_f32 v[146:147], v[146:147], v[208:209]\n"
"v_pk_add_f32 v[148:149], v[148:149], v[208:209]\n"
"v_pk_add_f32 v[150:151], v[150:151], v[208:209]\n"
"v_pk_add_f32 v[152:153], v[152:153], v[208:209]\n"
"v_pk_add_f32 v[154:155], v[154:155], v[208:209]\n"
"v_pk_add_f32 v[156:157], v[156:157], v[208:209]\n"
"v_pk_add_f32 v[158:159], v[158:159], v[208:209]\n"
"v_rcp_f32 v132, v132\n"
"v_rcp_f32 v133, v133\n"
"v_rcp_f32 v134, v134\n"
"v_rcp_f32 v135, v135\n"
"v_rcp_f32 v136, v136\n"
"v_rcp_f32 v137, v137\n"
"v_rcp_f32 v138, v138\n"
"v_rcp_f32 v139, v139\n"
"v_rcp_f32 v140, v140\n"
"v_rcp_f32 v141, v141\n"
"v_rcp_f32 v142, v142\n"
"v_rcp_f32 v143, v143\n"
"v_rcp_f32 v144, v144\n"
"v_rcp_f32 v145, v145\n"
"v_rcp_f32 v146, v146\n"
"v_rcp_f32 v147, v147\n"
"v_rcp_f32 v148, v148\n"
"v_rcp_f32 v149, v149\n"
"v_rcp_f32 v150, v150\n"
"v_rcp_f32 v151, v151\n"
"v_rcp_f32 v152, v152\n"
"v_rcp_f32 v153, v153\n"
"v_rcp_f32 v154, v154\n"
"v_rcp_f32 v155, v155\n"
"v_rcp_f32 v156, v156\n"
"v_rcp_f32 v157, v157\n"
"v_rcp_f32 v158, v158\n"
"v_rcp_f32 v159, v159\n"
"v_pk_mul_f32 v[28:29], v[28:29], v[132:133]\n"
"v_pk_mul_f32 v[30:31], v[30:31], v[134:135]\n"
"v_pk_mul_f32 v[32:33], v[32:33], v[136:137]\n"
"v_pk_mul_f32 v[34:35], v[34:35], v[138:139]\n"
"v_pk_mul_f32 v[36:37], v[36:37], v[140:141]\n"
"v_pk_mul_f32 v[38:39], v[38:39], v[142:143]\n"
"v_pk_mul_f32 v[40:41], v[40:41], v[144:145]\n"
"v_pk_mul_f32 v[42:43], v[42:43], v[146:147]\n"
"v_pk_mul_f32 v[44:45], v[44:45], v[148:149]\n"
"v_pk_mul_f32 v[46:47], v[46:47], v[150:151]\n"
"v_pk_mul_f32 v[48:49], v[48:49], v[152:153]\n"
"v_pk_mul_f32 v[50:51], v[50:51], v[154:155]\n"
"v_pk_mul_f32 v[52:53], v[52:53], v[156:157]\n"
"v_pk_mul_f32 v[54:55], v[54:55], v[158:159]\n"
"v_cvt_pk_bf16_f32 v132, v28, v29\n"
"v_cvt_pk_bf16_f32 v133, v30, v31\n"
"v_cvt_pk_bf16_f32 v134, v32, v33\n"
"v_cvt_pk_bf16_f32 v135, v34, v35\n"
"v_cvt_pk_bf16_f32 v136, v36, v37\n"
"v_cvt_pk_bf16_f32 v137, v38, v39\n"
"v_cvt_pk_bf16_f32 v138, v40, v41\n"
"v_cvt_pk_bf16_f32 v139, v42, v43\n"
"v_cvt_pk_bf16_f32 v140, v44, v45\n"
"v_cvt_pk_bf16_f32 v141, v46, v47\n"
"v_cvt_pk_bf16_f32 v142, v48, v49\n"
"v_cvt_pk_bf16_f32 v143, v50, v51\n"
"v_cvt_pk_bf16_f32 v144, v52, v53\n"
"v_cvt_pk_bf16_f32 v145, v54, v55\n"
"s_mul_i32 s38, s48, 57344\n"
"s_add_u32 s38, s38, 114819072\n"
"s_add_u32 s39, s38, s41\n"
"s_add_u32 s38, s38, s40\n"
"buffer_store_dwordx4 v[132:135], v251, s[96:99], s38 offen offset:0 sc1\n"
"buffer_store_dwordx4 v[136:139], v251, s[96:99], s38 offen offset:1024 sc1\n"
"buffer_store_dwordx4 v[140:143], v251, s[96:99], s38 offen offset:2048 sc1\n"
"buffer_store_dwordx2 v[144:145], v250, s[96:99], s39 offen sc1\n"
"s_add_u32 s48, s48, 1\n"
"buffer_load_dwordx4 a[48:51], v85, s[96:99], s61 offen offset:3072 sc1\n"
"buffer_load_dwordx4 a[52:55], v86, s[96:99], s61 offen offset:3072 sc1\n"
"buffer_load_dwordx4 a[56:59], v87, s[96:99], s61 offen offset:3072 sc1\n"
"buffer_load_dwordx4 a[60:63], v88, s[96:99], s61 offen offset:3072 sc1\n"
"s_add_u32 s61, s61, 4096\n"
"buffer_load_dwordx4 a[64:67], v84, s[96:99], s61 offen offset:0 sc1\n"
"buffer_load_dwordx4 a[68:71], v85, s[96:99], s61 offen offset:0 sc1\n"
"buffer_load_dwordx4 a[72:75], v86, s[96:99], s61 offen offset:0 sc1\n"
"buffer_load_dwordx4 a[76:79], v87, s[96:99], s61 offen offset:0 sc1\n"
"buffer_load_dwordx4 a[80:83], v88, s[96:99], s61 offen offset:0 sc1\n"
"buffer_load_dwordx4 a[84:87], v84, s[96:99], s61 offen offset:1024 sc1\n"
"buffer_load_dwordx4 a[88:91], v85, s[96:99], s61 offen offset:1024 sc1\n"
"buffer_load_dwordx4 a[92:95], v86, s[96:99], s61 offen offset:1024 sc1\n"
"buffer_load_dwordx4 a[96:99], v87, s[96:99], s61 offen offset:1024 sc1\n"
"buffer_load_dwordx4 a[100:103], v88, s[96:99], s61 offen offset:1024 sc1\n"
"buffer_load_dwordx4 a[104:107], v84, s[96:99], s61 offen offset:2048 sc1\n"
"buffer_load_dwordx4 a[108:111], v85, s[96:99], s61 offen offset:2048 sc1\n"
"buffer_load_dwordx4 a[112:115], v86, s[96:99], s61 offen offset:2048 sc1\n"
"s_add_u32 s38, %2, 0\n"
"s_and_b32 s38, s38, 3\n"
"s_mul_i32 s39, s38, 57344\n"
"v_add_u32 v80, s39, v251\n"
"s_add_u32 s38, %2, 1\n"
"s_and_b32 s38, s38, 3\n"
"s_mul_i32 s39, s38, 57344\n"
"v_add_u32 v81, s39, v251\n"
"s_mul_i32 s39, s38, 15360\n"
"s_add_u32 s39, s39, %3\n"
"s_add_u32 s39, s39, 0\n"
"v_add_u32 v89, s39, v251\n"
"s_add_u32 s38, %2, 2\n"
"s_and_b32 s38, s38, 3\n"
"s_mul_i32 s39, s38, 57344\n"
"v_add_u32 v82, s39, v251\n"
"s_mul_i32 s39, s38, 15360\n"
"s_add_u32 s39, s39, %3\n"
"s_add_u32 s39, s39, 5120\n"
"v_add_u32 v90, s39, v251\n"
"s_add_u32 s38, %2, 3\n"
"s_and_b32 s38, s38, 3\n"
"s_mul_i32 s39, s38, 57344\n"
"v_add_u32 v83, s39, v251\n"
"s_mul_i32 s39, s38, 15360\n"
"s_add_u32 s39, s39, %3\n"
"s_add_u32 s39, s39, 10240\n"
"v_add_u32 v91, s39, v251\n"
"s_mul_i32 s38, %2, 14336\n"
"s_mul_i32 s62, s64, 229376\n"
"s_add_u32 s62, s62, s38\n"
"s_add_u32 s62, s62, 114819072\n"
"s_mul_i32 s38, %2, 15360\n"
"s_add_u32 s38, s38, %3\n"
"v_add_u32 v92, s38, v251\n"
"s_add_u32 s60, s62, 3072\n"
"s_add_u32 s70, s62, 10240\n"
"s_mov_b32 s71, s62\n"
"s_barrier\n"
"s_mov_b32 s63, 3\n"
"buffer_load_dwordx4 v[20:23], v80, s[96:99], s60 offen sc1\n"
"buffer_load_dwordx4 v[24:27], v81, s[96:99], s60 offen sc1\n"
"buffer_load_dwordx4 v[28:31], v82, s[96:99], s60 offen sc1\n"
"buffer_load_dwordx4 v[32:35], v83, s[96:99], s60 offen sc1\n"
"buffer_load_dwordx4 v[36:39], v80, s[96:99], s70 offen sc1\n"
"buffer_load_dwordx4 v[40:43], v81, s[96:99], s70 offen sc1\n"
"buffer_load_dwordx4 v[44:47], v82, s[96:99], s70 offen sc1\n"
"buffer_load_dwordx4 v[48:51], v83, s[96:99], s70 offen sc1\n"
"buffer_load_dwordx4 a[116:119], v87, s[96:99], s61 offen offset:2048 sc1\n"
"buffer_load_dwordx4 a[120:123], v88, s[96:99], s61 offen offset:2048 sc1\n"
"buffer_load_dwordx4 a[124:127], v84, s[96:99], s61 offen offset:3072 sc1\n"
"buffer_load_dwordx4 a[128:131], v85, s[96:99], s61 offen offset:3072 sc1\n"
"buffer_load_dwordx4 a[132:135], v86, s[96:99], s61 offen offset:3072 sc1\n"
"buffer_load_dwordx4 a[136:139], v87, s[96:99], s61 offen offset:3072 sc1\n"
"buffer_load_dwordx4 a[140:143], v88, s[96:99], s61 offen offset:3072 sc1\n"
"s_add_u32 s61, s61, 4096\n"
"buffer_load_dwordx4 a[144:147], v84, s[96:99], s61 offen offset:0 sc1\n"
"buffer_load_dwordx4 a[148:151], v85, s[96:99], s61 offen offset:0 sc1\n"
"buffer_load_dwordx4 a[152:155], v86, s[96:99], s61 offen offset:0 sc1\n"
"buffer_load_dwordx4 a[156:159], v87, s[96:99], s61 offen offset:0 sc1\n"
"buffer_load_dwordx4 v[96:99], v88, s[96:99], s61 offen offset:0 sc1\n"
"buffer_load_dwordx4 v[100:103], v84, s[96:99], s61 offen offset:1024 sc1\n"
"buffer_load_dwordx4 v[104:107], v85, s[96:99], s61 offen offset:1024 sc1\n"
"buffer_load_dwordx4 v[108:111], v86, s[96:99], s61 offen offset:1024 sc1\n"
"buffer_load_dwordx4 v[112:115], v87, s[96:99], s61 offen offset:1024 sc1\n"
"buffer_load_dwordx4 v[116:119], v88, s[96:99], s61 offen offset:1024 sc1\n"
"s_waitcnt vmcnt(24)\n"
"v_max_u32 v242, v20, v21\n"
"v_max3_u32 v242, v242, v22, v23\n"
"s_waitcnt vmcnt(23)\n"
"v_max3_u32 v242, v242, v24, v25\n"
"v_max3_u32 v242, v242, v26, v27\n"
"s_waitcnt vmcnt(22)\n"
"v_max3_u32 v242, v242, v28, v29\n"
"v_max3_u32 v242, v242, v30, v31\n"
"s_waitcnt vmcnt(21)\n"
"v_max3_u32 v242, v242, v32, v33\n"
"v_max3_u32 v242, v242, v34, v35\n"
"s_waitcnt vmcnt(20)\n"
"v_max3_u32 v242, v242, v36, v37\n"
"v_max3_u32 v242, v242, v38, v39\n"
"s_waitcnt vmcnt(19)\n"
"v_max3_u32 v242, v242, v40, v41\n"
"v_max3_u32 v242, v242, v42, v43\n"
"s_waitcnt vmcnt(18)\n"
"v_max3_u32 v242, v242, v44, v45\n"
"v_max3_u32 v242, v242, v46, v47\n"
"s_waitcnt vmcnt(17)\n"
"v_max3_u32 v242, v242, v48, v49\n"
"v_max3_u32 v242, v242, v50, v51\n"
"v_cmp_eq_u32 vcc, -1, v242\n"
"s_nop 0\n"
"s_cbranch_vccnz SLOW_G4_%=\n"
"OK_G4_%=:\n"
"s_mov_b32 s62, s71\n"
"buffer_load_dwordx4 v[120:123], v80, s[96:99], s62 offen offset:0 sc1\n"
"buffer_load_dwordx4 v[124:127], v81, s[96:99], s62 offen offset:0 sc1\n"
"buffer_load_dwordx4 v[128:131], v82, s[96:99], s62 offen offset:0 sc1\n"
"buffer_load_dwordx4 v[132:135], v83, s[96:99], s62 offen offset:0 sc1\n"
"buffer_load_dwordx4 v[136:139], v80, s[96:99], s62 offen offset:1024 sc1\n"
"buffer_load_dwordx4 v[140:143], v81, s[96:99], s62 offen offset:1024 sc1\n"
"buffer_load_dwordx4 v[144:147], v82, s[96:99], s62 offen offset:1024 sc1\n"
"buffer_load_dwordx4 v[148:151], v83, s[96:99], s62 offen offset:1024 sc1\n"
"buffer_load_dwordx4 v[152:155], v80, s[96:99], s62 offen offset:2048 sc1\n"
"buffer_load_dwordx4 v[156:159], v81, s[96:99], s62 offen offset:2048 sc1\n"
"buffer_load_dwordx4 v[160:163], v82, s[96:99], s62 offen offset:2048 sc1\n"
"buffer_load_dwordx4 v[164:167], v83, s[96:99], s62 offen offset:2048 sc1\n"
"buffer_load_dwordx4 v[168:171], v80, s[96:99], s62 offen offset:3072 sc1\n"
"buffer_load_dwordx4 v[172:175], v81, s[96:99], s62 offen offset:3072 sc1\n"
"buffer_load_dwordx4 v[176:179], v82, s[96:99], s62 offen offset:3072 sc1\n"
"buffer_load_dwordx4 v[180:183], v83, s[96:99], s62 offen offset:3072 sc1\n"
"s_add_u32 s62, s62, 4096\n"
"s_waitcnt vmcnt(15)\n"
"v_mfma_f32_16x16x32_bf16 v[0:3], a[160:163], v[120:123], 0\n"
"s_waitcnt vmcnt(14)\n"
"v_mfma_f32_16x16x32_bf16 v[20:23], a[160:163], v[124:127], 0\n"
"s_waitcnt vmcnt(13)\n"
"v_mfma_f32_16x16x32_bf16 v[40:43], a[160:163], v[128:131], 0\n"
"s_waitcnt vmcnt(12)\n"
"v_mfma_f32_16x16x32_bf16 v[60:63], a[160:163], v[132:135], 0\n"
"v_mfma_f32_16x16x32_bf16 v[4:7], a[164:167], v[120:123], 0\n"
"v_mfma_f32_16x16x32_bf16 v[24:27], a[164:167], v[124:127], 0\n"
"v_mfma_f32_16x16x32_bf16 v[44:47], a[164:167], v[128:131], 0\n"
"v_mfma_f32_16x16x32_bf16 v[64:67], a[164:167], v[132:135], 0\n"
"v_mfma_f32_16x16x32_bf16 v[8:11], a[168:171], v[120:123], 0\n"
"v_mfma_f32_16x16x32_bf16 v[28:31], a[168:171], v[124:127], 0\n"
"v_mfma_f32_16x16x32_bf16 v[48:51], a[168:171], v[128:131], 0\n"
"v_mfma_f32_16x16x32_bf16 v[68:71], a[168:171], v[132:135], 0\n"
"v_mfma_f32_16x16x32_bf16 v[12:15], a[172:175], v[120:123], 0\n"
"v_mfma_f32_16x16x32_bf16 v[32:35], a[172:175], v[124:127], 0\n"
"v_mfma_f32_16x16x32_bf16 v[52:55], a[172:175], v[128:131], 0\n"
"v_mfma_f32_16x16x32_bf16 v[72:75], a[172:175], v[132:135], 0\n"
"v_mfma_f32_16x16x32_bf16 v[16:19], a[176:179], v[120:123], 0\n"
"v_mfma_f32_16x16x32_bf16 v[36:39], a[176:179], v[124:127], 0\n"
"v_mfma_f32_16x16x32_bf16 v[56:59], a[176:179], v[128:131], 0\n"
"v_mfma_f32_16x16x32_bf16 v[76:79], a[176:179], v[132:135], 0\n"
"buffer_load_dwordx4 v[120:123], v80, s[96:99], s62 offen offset:0 sc1\n"
"buffer_load_dwordx4 v[124:127], v81, s[96:99], s62 offen offset:0 sc1\n"
"buffer_load_dwordx4 v[128:131], v82, s[96:99], s62 offen offset:0 sc1\n"
"buffer_load_dwordx4 v[132:135], v83, s[96:99], s62 offen offset:0 sc1\n"
"s_waitcnt vmcnt(15)\n"
"v_mfma_f32_16x16x32_bf16 v[0:3], a[180:183], v[136:139], v[0:3]\n"
"s_waitcnt vmcnt(14)\n"
"v_mfma_f32_16x16x32_bf16 v[20:23], a[180:183], v[140:143], v[20:23]\n"
"s_waitcnt vmcnt(13)\n"
"v_mfma_f32_16x16x32_bf16 v[40:43], a[180:183], v[144:147], v[40:43]\n"
"s_waitcnt vmcnt(12)\n"
"v_mfma_f32_16x16x32_bf16 v[60:63], a[180:183], v[148:151], v[60:63]\n"
"v_mfma_f32_16x16x32_bf16 v[4:7], a[184:187], v[136:139], v[4:7]\n"
"v_mfma_f32_16x16x32_bf16 v[24:27], a[184:187], v[140:143], v[24:27]\n"
"v_mfma_f32_16x16x32_bf16 v[44:47], a[184:187], v[144:147], v[44:47]\n"
"v_mfma_f32_16x16x32_bf16 v[64:67], a[184:187], v[148:151], v[64:67]\n"
"v_mfma_f32_16x16x32_bf16 v[8:11], a[188:191], v[136:139], v[8:11]\n"
"v_mfma_f32_16x16x32_bf16 v[28:31], a[188:191], v[140:143], v[28:31]\n"
"v_mfma_f32_16x16x32_bf16 v[48:51], a[188:191], v[144:147], v[48:51]\n"
"v_mfma_f32_16x16x32_bf16 v[68:71], a[188:191], v[148:151], v[68:71]\n"
"v_mfma_f32_16x16x32_bf16 v[12:15], a[192:195], v[136:139], v[12:15]\n"
"v_mfma_f32_16x16x32_bf16 v[32:35], a[192:195], v[140:143], v[32:35]\n"
"v_mfma_f32_16x16x32_bf16 v[52:55], a[192:195], v[144:147], v[52:55]\n"
"v_mfma_f32_16x16x32_bf16 v[72:75], a[192:195], v[148:151], v[72:75]\n"
"v_mfma_f32_16x16x32_bf16 v[16:19], a[196:199], v[136:139], v[16:19]\n"
"v_mfma_f32_16x16x32_bf16 v[36:39], a[196:199], v[140:143], v[36:39]\n"
"v_mfma_f32_16x16x32_bf16 v[56:59], a[196:199], v[144:147], v[56:59]\n"
"v_mfma_f32_16x16x32_bf16 v[76:79], a[196:199], v[148:151], v[76:79]\n"
"buffer_load_dwordx4 v[136:139], v80, s[96:99], s62 offen offset:1024 sc1\n"
"buffer_load_dwordx4 v[140:143], v81, s[96:99], s62 offen offset:1024 sc1\n"
"buffer_load_dwordx4 v[144:147], v82, s[96:99], s62 offen offset:1024 sc1\n"
"buffer_load_dwordx4 v[148:151], v83, s[96:99], s62 offen offset:1024 sc1\n"
"s_waitcnt vmcnt(15)\n"
"PG25_%=:\n"
"s_cbranch_execz RPG25_%=\n"
"v_mfma_f32_16x16x32_bf16 v[0:3], a[200:203], v[152:155], v[0:3]\n"
"s_waitcnt vmcnt(14)\n"
"v_mfma_f32_16x16x32_bf16 v[20:23], a[200:203], v[156:159], v[20:23]\n"
"s_waitcnt vmcnt(13)\n"
"v_mfma_f32_16x16x32_bf16 v[40:43], a[200:203], v[160:163], v[40:43]\n"
"s_waitcnt vmcnt(12)\n"
"v_mfma_f32_16x16x32_bf16 v[60:63], a[200:203], v[164:167], v[60:63]\n"
"v_mfma_f32_16x16x32_bf16 v[4:7], a[204:207], v[152:155], v[4:7]\n"
"v_mfma_f32_16x16x32_bf16 v[24:27], a[204:207], v[156:159], v[24:27]\n"
"v_mfma_f32_16x16x32_bf16 v[44:47], a[204:207], v[160:163], v[44:47]\n"
"v_mfma_f32_16x16x32_bf16 v[64:67], a[204:207], v[164:167], v[64:67]\n"
"v_mfma_f32_16x16x32_bf16 v[8:11], a[208:211], v[152:155], v[8:11]\n"
"v_mfma_f32_16x16x32_bf16 v[28:31], a[208:211], v[156:159], v[28:31]\n"
"v_mfma_f32_16x16x32_bf16 v[48:51], a[208:211], v[160:163], v[48:51]\n"
"v_mfma_f32_16x16x32_bf16 v[68:71], a[208:211], v[164:167], v[68:71]\n"
"v_mfma_f32_16x16x32_bf16 v[12:15], a[212:215], v[152:155], v[12:15]\n"
"v_mfma_f32_16x16x32_bf16 v[32:35], a[212:215], v[156:159], v[32:35]\n"
"v_mfma_f32_16x16x32_bf16 v[52:55], a[212:215], v[160:163], v[52:55]\n"
"v_mfma_f32_16x16x32_bf16 v[72:75], a[212:215], v[164:167], v[72:75]\n"
"v_mfma_f32_16x16x32_bf16 v[16:19], a[216:219], v[152:155], v[16:19]\n"
"v_mfma_f32_16x16x32_bf16 v[36:39], a[216:219], v[156:159], v[36:39]\n"
"v_mfma_f32_16x16x32_bf16 v[56:59], a[216:219], v[160:163], v[56:59]\n"
"v_mfma_f32_16x16x32_bf16 v[76:79], a[216:219], v[164:167], v[76:79]\n"
"buffer_load_dwordx4 v[152:155], v80, s[96:99], s62 offen offset:2048 sc1\n"
"buffer_load_dwordx4 v[156:159], v81, s[96:99], s62 offen offset:2048 sc1\n"
"buffer_load_dwordx4 v[160:163], v82, s[96:99], s62 offen offset:2048 sc1\n"
"buffer_load_dwordx4 v[164:167], v83, s[96:99], s62 offen offset:2048 sc1\n"
"s_waitcnt vmcnt(15)\n"
"v_mfma_f32_16x16x32_bf16 v[0:3], a[220:223], v[168:171], v[0:3]\n"
"s_waitcnt vmcnt(14)\n"
"v_mfma_f32_16x16x32_bf16 v[20:23], a[220:223], v[172:175], v[20:23]\n"
"s_waitcnt vmcnt(13)\n"
"v_mfma_f32_16x16x32_bf16 v[40:43], a[220:223], v[176:179], v[40:43]\n"
"s_waitcnt vmcnt(12)\n"
"v_mfma_f32_16x16x32_bf16 v[60:63], a[220:223], v[180:183], v[60:63]\n"
"v_mfma_f32_16x16x32_bf16 v[4:7], a[224:227], v[168:171], v[4:7]\n"
"v_mfma_f32_16x16x32_bf16 v[24:27], a[224:227], v[172:175], v[24:27]\n"
"v_mfma_f32_16x16x32_bf16 v[44:47], a[224:227], v[176:179], v[44:47]\n"
"v_mfma_f32_16x16x32_bf16 v[64:67], a[224:227], v[180:183], v[64:67]\n"
"v_mfma_f32_16x16x32_bf16 v[8:11], a[228:231], v[168:171], v[8:11]\n"
"v_mfma_f32_16x16x32_bf16 v[28:31], a[228:231], v[172:175], v[28:31]\n"
"v_mfma_f32_16x16x32_bf16 v[48:51], a[228:231], v[176:179], v[48:51]\n"
"v_mfma_f32_16x16x32_bf16 v[68:71], a[228:231], v[180:183], v[68:71]\n"
"v_mfma_f32_16x16x32_bf16 v[12:15], a[232:235], v[168:171], v[12:15]\n"
"v_mfma_f32_16x16x32_bf16 v[32:35], a[232:235], v[172:175], v[32:35]\n"
"v_mfma_f32_16x16x32_bf16 v[52:55], a[232:235], v[176:179], v[52:55]\n"
"v_mfma_f32_16x16x32_bf16 v[72:75], a[232:235], v[180:183], v[72:75]\n"
"v_mfma_f32_16x16x32_bf16 v[16:19], a[236:239], v[168:171], v[16:19]\n"
"v_mfma_f32_16x16x32_bf16 v[36:39], a[236:239], v[172:175], v[36:39]\n"
"v_mfma_f32_16x16x32_bf16 v[56:59], a[236:239], v[176:179], v[56:59]\n"
"v_mfma_f32_16x16x32_bf16 v[76:79], a[236:239], v[180:183], v[76:79]\n"
"buffer_load_dwordx4 v[168:171], v80, s[96:99], s62 offen offset:3072 sc1\n"
"buffer_load_dwordx4 v[172:175], v81, s[96:99], s62 offen offset:3072 sc1\n"
"buffer_load_dwordx4 v[176:179], v82, s[96:99], s62 offen offset:3072 sc1\n"
"buffer_load_dwordx4 v[180:183], v83, s[96:99], s62 offen offset:3072 sc1\n"
"s_add_u32 s62, s62, 4096\n"
"s_waitcnt vmcnt(15)\n"
"v_mfma_f32_16x16x32_bf16 v[0:3], a[240:243], v[120:123], v[0:3]\n"
"s_waitcnt vmcnt(14)\n"
"v_mfma_f32_16x16x32_bf16 v[20:23], a[240:243], v[124:127], v[20:23]\n"
"s_waitcnt vmcnt(13)\n"
"v_mfma_f32_16x16x32_bf16 v[40:43], a[240:243], v[128:131], v[40:43]\n"
"s_waitcnt vmcnt(12)\n"
"v_mfma_f32_16x16x32_bf16 v[60:63], a[240:243], v[132:135], v[60:63]\n"
"v_mfma_f32_16x16x32_bf16 v[4:7], a[244:247], v[120:123], v[4:7]\n"
"v_mfma_f32_16x16x32_bf16 v[24:27], a[244:247], v[124:127], v[24:27]\n"
"v_mfma_f32_16x16x32_bf16 v[44:47], a[244:247], v[128:131], v[44:47]\n"
"v_mfma_f32_16x16x32_bf16 v[64:67], a[244:247], v[132:135], v[64:67]\n"
"v_mfma_f32_16x16x32_bf16 v[8:11], a[248:251], v[120:123], v[8:11]\n"
"v_mfma_f32_16x16x32_bf16 v[28:31], a[248:251], v[124:127], v[28:31]\n"
"v_mfma_f32_16x16x32_bf16 v[48:51], a[248:251], v[128:131], v[48:51]\n"
"v_mfma_f32_16x16x32_bf16 v[68:71], a[248:251], v[132:135], v[68:71]\n"
"v_mfma_f32_16x16x32_bf16 v[12:15], a[252:255], v[120:123], v[12:15]\n"
"v_mfma_f32_16x16x32_bf16 v[32:35], a[252:255], v[124:127], v[32:35]\n"
"v_mfma_f32_16x16x32_bf16 v[52:55], a[252:255], v[128:131], v[52:55]\n"
"v_mfma_f32_16x16x32_bf16 v[72:75], a[252:255], v[132:135], v[72:75]\n"
"v_mfma_f32_16x16x32_bf16 v[16:19], a[0:3], v[120:123], v[16:19]\n"
"v_mfma_f32_16x16x32_bf16 v[36:39], a[0:3], v[124:127], v[36:39]\n"
"v_mfma_f32_16x16x32_bf16 v[56:59], a[0:3], v[128:131], v[56:59]\n"
"v_mfma_f32_16x16x32_bf16 v[76:79], a[0:3], v[132:135], v[76:79]\n"
"buffer_load_dwordx4 v[120:123], v80, s[96:99], s62 offen offset:0 sc1\n"
"buffer_load_dwordx4 v[124:127], v81, s[96:99], s62 offen offset:0 sc1\n"
"buffer_load_dwordx4 v[128:131], v82, s[96:99], s62 offen offset:0 sc1\n"
"buffer_load_dwordx4 v[132:135], v83, s[96:99], s62 offen offset:0 sc1\n"
"s_waitcnt vmcnt(15)\n"
"v_mfma_f32_16x16x32_bf16 v[0:3], a[4:7], v[136:139], v[0:3]\n"
"s_waitcnt vmcnt(14)\n"
"v_mfma_f32_16x16x32_bf16 v[20:23], a[4:7], v[140:143], v[20:23]\n"
"s_waitcnt vmcnt(13)\n"
"v_mfma_f32_16x16x32_bf16 v[40:43], a[4:7], v[144:147], v[40:43]\n"
"s_waitcnt vmcnt(12)\n"
"v_mfma_f32_16x16x32_bf16 v[60:63], a[4:7], v[148:151], v[60:63]\n"
"v_mfma_f32_16x16x32_bf16 v[4:7], a[8:11], v[136:139], v[4:7]\n"
"v_mfma_f32_16x16x32_bf16 v[24:27], a[8:11], v[140:143], v[24:27]\n"
"v_mfma_f32_16x16x32_bf16 v[44:47], a[8:11], v[144:147], v[44:47]\n"
"v_mfma_f32_16x16x32_bf16 v[64:67], a[8:11], v[148:151], v[64:67]\n"
"v_mfma_f32_16x16x32_bf16 v[8:11], a[12:15], v[136:139], v[8:11]\n"
"v_mfma_f32_16x16x32_bf16 v[28:31], a[12:15], v[140:143], v[28:31]\n"
"v_mfma_f32_16x16x32_bf16 v[48:51], a[12:15], v[144:147], v[48:51]\n"
"v_mfma_f32_16x16x32_bf16 v[68:71], a[12:15], v[148:151], v[68:71]\n"
"v_mfma_f32_16x16x32_bf16 v[12:15], a[16:19], v[136:139], v[12:15]\n"
"v_mfma_f32_16x16x32_bf16 v[32:35], a[16:19], v[140:143], v[32:35]\n"
"v_mfma_f32_16x16x32_bf16 v[52:55], a[16:19], v[144:147], v[52:55]\n"
"v_mfma_f32_16x16x32_bf16 v[72:75], a[16:19], v[148:151], v[72:75]\n"
"v_mfma_f32_16x16x32_bf16 v[16:19], a[20:23], v[136:139], v[16:19]\n"
"v_mfma_f32_16x16x32_bf16 v[36:39], a[20:23], v[140:143], v[36:39]\n"
"v_mfma_f32_16x16x32_bf16 v[56:59], a[20:23], v[144:147], v[56:59]\n"
"v_mfma_f32_16x16x32_bf16 v[76:79], a[20:23], v[148:151], v[76:79]\n"
"buffer_load_dwordx4 v[136:139], v80, s[96:99], s62 offen offset:1024 sc1\n"
"buffer_load_dwordx4 v[140:143], v81, s[96:99], s62 offen offset:1024 sc1\n"
"buffer_load_dwordx4 v[144:147], v82, s[96:99], s62 offen offset:1024 sc1\n"
"buffer_load_dwordx4 v[148:151], v83, s[96:99], s62 offen offset:1024 sc1\n"
"s_waitcnt vmcnt(15)\n"
"v_mfma_f32_16x16x32_bf16 v[0:3], a[24:27], v[152:155], v[0:3]\n"
"s_waitcnt vmcnt(14)\n"
"v_mfma_f32_16x16x32_bf16 v[20:23], a[24:27], v[156:159], v[20:23]\n"
"s_waitcnt vmcnt(13)\n"
"v_mfma_f32_16x16x32_bf16 v[40:43], a[24:27], v[160:163], v[40:43]\n"
"s_waitcnt vmcnt(12)\n"
"v_mfma_f32_16x16x32_bf16 v[60:63], a[24:27], v[164:167], v[60:63]\n"
"v_mfma_f32_16x16x32_bf16 v[4:7], a[28:31], v[152:155], v[4:7]\n"
"v_mfma_f32_16x16x32_bf16 v[24:27], a[28:31], v[156:159], v[24:27]\n"
"v_mfma_f32_16x16x32_bf16 v[44:47], a[28:31], v[160:163], v[44:47]\n"
"v_mfma_f32_16x16x32_bf16 v[64:67], a[28:31], v[164:167], v[64:67]\n"
"v_mfma_f32_16x16x32_bf16 v[8:11], a[32:35], v[152:155], v[8:11]\n"
"v_mfma_f32_16x16x32_bf16 v[28:31], a[32:35], v[156:159], v[28:31]\n"
"v_mfma_f32_16x16x32_bf16 v[48:51], a[32:35], v[160:163], v[48:51]\n"
"v_mfma_f32_16x16x32_bf16 v[68:71], a[32:35], v[164:167], v[68:71]\n"
"v_mfma_f32_16x16x32_bf16 v[12:15], a[36:39], v[152:155], v[12:15]\n"
"v_mfma_f32_16x16x32_bf16 v[32:35], a[36:39], v[156:159], v[32:35]\n"
"v_mfma_f32_16x16x32_bf16 v[52:55], a[36:39], v[160:163], v[52:55]\n"
"v_mfma_f32_16x16x32_bf16 v[72:75], a[36:39], v[164:167], v[72:75]\n"
"v_mfma_f32_16x16x32_bf16 v[16:19], a[40:43], v[152:155], v[16:19]\n"
"v_mfma_f32_16x16x32_bf16 v[36:39], a[40:43], v[156:159], v[36:39]\n"
"v_mfma_f32_16x16x32_bf16 v[56:59], a[40:43], v[160:163], v[56:59]\n"
"v_mfma_f32_16x16x32_bf16 v[76:79], a[40:43], v[164:167], v[76:79]\n"
"buffer_load_dwordx4 v[152:155], v80, s[96:99], s62 offen offset:2048 sc1\n"
"buffer_load_dwordx4 v[156:159], v81, s[96:99], s62 offen offset:2048 sc1\n"
"buffer_load_dwordx4 v[160:163], v82, s[96:99], s62 offen offset:2048 sc1\n"
"buffer_load_dwordx4 v[164:167], v83, s[96:99], s62 offen offset:2048 sc1\n"
"s_waitcnt vmcnt(15)\n"
"v_mfma_f32_16x16x32_bf16 v[0:3], a[44:47], v[168:171], v[0:3]\n"
"s_waitcnt vmcnt(14)\n"
"v_mfma_f32_16x16x32_bf16 v[20:23], a[44:47], v[172:175], v[20:23]\n"
"s_waitcnt vmcnt(13)\n"
"v_mfma_f32_16x16x32_bf16 v[40:43], a[44:47], v[176:179], v[40:43]\n"
"s_waitcnt vmcnt(12)\n"
"v_mfma_f32_16x16x32_bf16 v[60:63], a[44:47], v[180:183], v[60:63]\n"
"v_mfma_f32_16x16x32_bf16 v[4:7], a[48:51], v[168:171], v[4:7]\n"
"v_mfma_f32_16x16x32_bf16 v[24:27], a[48:51], v[172:175], v[24:27]\n"
"v_mfma_f32_16x16x32_bf16 v[44:47], a[48:51], v[176:179], v[44:47]\n"
"v_mfma_f32_16x16x32_bf16 v[64:67], a[48:51], v[180:183], v[64:67]\n"
"v_mfma_f32_16x16x32_bf16 v[8:11], a[52:55], v[168:171], v[8:11]\n"
"v_mfma_f32_16x16x32_bf16 v[28:31], a[52:55], v[172:175], v[28:31]\n"
"v_mfma_f32_16x16x32_bf16 v[48:51], a[52:55], v[176:179], v[48:51]\n"
"v_mfma_f32_16x16x32_bf16 v[68:71], a[52:55], v[180:183], v[68:71]\n"
"v_mfma_f32_16x16x32_bf16 v[12:15], a[56:59], v[168:171], v[12:15]\n"
"v_mfma_f32_16x16x32_bf16 v[32:35], a[56:59], v[172:175], v[32:35]\n"
"v_mfma_f32_16x16x32_bf16 v[52:55], a[56:59], v[176:179], v[52:55]\n"
"v_mfma_f32_16x16x32_bf16 v[72:75], a[56:59], v[180:183], v[72:75]\n"
"v_mfma_f32_16x16x32_bf16 v[16:19], a[60:63], v[168:171], v[16:19]\n"
"v_mfma_f32_16x16x32_bf16 v[36:39], a[60:63], v[172:175], v[36:39]\n"
"v_mfma_f32_16x16x32_bf16 v[56:59], a[60:63], v[176:179], v[56:59]\n"
"v_mfma_f32_16x16x32_bf16 v[76:79], a[60:63], v[180:183], v[76:79]\n"
"buffer_load_dwordx4 v[168:171], v80, s[96:99], s62 offen offset:3072 sc1\n"
"buffer_load_dwordx4 v[172:175], v81, s[96:99], s62 offen offset:3072 sc1\n"
"buffer_load_dwordx4 v[176:179], v82, s[96:99], s62 offen offset:3072 sc1\n"
"buffer_load_dwordx4 v[180:183], v83, s[96:99], s62 offen offset:3072 sc1\n"
"s_add_u32 s62, s62, 4096\n"
"s_waitcnt vmcnt(15)\n"
"v_mfma_f32_16x16x32_bf16 v[0:3], a[64:67], v[120:123], v[0:3]\n"
"s_waitcnt vmcnt(14)\n"
"v_mfma_f32_16x16x32_bf16 v[20:23], a[64:67], v[124:127], v[20:23]\n"
"s_waitcnt vmcnt(13)\n"
"v_mfma_f32_16x16x32_bf16 v[40:43], a[64:67], v[128:131], v[40:43]\n"
"s_waitcnt vmcnt(12)\n"
"v_mfma_f32_16x16x32_bf16 v[60:63], a[64:67], v[132:135], v[60:63]\n"
"v_mfma_f32_16x16x32_bf16 v[4:7], a[68:71], v[120:123], v[4:7]\n"
"v_mfma_f32_16x16x32_bf16 v[24:27], a[68:71], v[124:127], v[24:27]\n"
"v_mfma_f32_16x16x32_bf16 v[44:47], a[68:71], v[128:131], v[44:47]\n"
"v_mfma_f32_16x16x32_bf16 v[64:67], a[68:71], v[132:135], v[64:67]\n"
"v_mfma_f32_16x16x32_bf16 v[8:11], a[72:75], v[120:123], v[8:11]\n"
"v_mfma_f32_16x16x32_bf16 v[28:31], a[72:75], v[124:127], v[28:31]\n"
"v_mfma_f32_16x16x32_bf16 v[48:51], a[72:75], v[128:131], v[48:51]\n"
"v_mfma_f32_16x16x32_bf16 v[68:71], a[72:75], v[132:135], v[68:71]\n"
"v_mfma_f32_16x16x32_bf16 v[12:15], a[76:79], v[120:123], v[12:15]\n"
"v_mfma_f32_16x16x32_bf16 v[32:35], a[76:79], v[124:127], v[32:35]\n"
"v_mfma_f32_16x16x32_bf16 v[52:55], a[76:79], v[128:131], v[52:55]\n"
"v_mfma_f32_16x16x32_bf16 v[72:75], a[76:79], v[132:135], v[72:75]\n"
"v_mfma_f32_16x16x32_bf16 v[16:19], a[80:83], v[120:123], v[16:19]\n"
"v_mfma_f32_16x16x32_bf16 v[36:39], a[80:83], v[124:127], v[36:39]\n"
"v_mfma_f32_16x16x32_bf16 v[56:59], a[80:83], v[128:131], v[56:59]\n"
"v_mfma_f32_16x16x32_bf16 v[76:79], a[80:83], v[132:135], v[76:79]\n"
"buffer_load_dwordx4 v[120:123], v80, s[96:99], s62 offen offset:0 sc1\n"
"buffer_load_dwordx4 v[124:127], v81, s[96:99], s62 offen offset:0 sc1\n"
"buffer_load_dwordx4 v[128:131], v82, s[96:99], s62 offen offset:0 sc1\n"
"buffer_load_dwordx4 v[132:135], v83, s[96:99], s62 offen offset:0 sc1\n"
"s_waitcnt vmcnt(15)\n"
"v_mfma_f32_16x16x32_bf16 v[0:3], a[84:87], v[136:139], v[0:3]\n"
"s_waitcnt vmcnt(14)\n"
"v_mfma_f32_16x16x32_bf16 v[20:23], a[84:87], v[140:143], v[20:23]\n"
"s_waitcnt vmcnt(13)\n"
"v_mfma_f32_16x16x32_bf16 v[40:43], a[84:87], v[144:147], v[40:43]\n"
"s_waitcnt vmcnt(12)\n"
"v_mfma_f32_16x16x32_bf16 v[60:63], a[84:87], v[148:151], v[60:63]\n"
"v_mfma_f32_16x16x32_bf16 v[4:7], a[88:91], v[136:139], v[4:7]\n"
"v_mfma_f32_16x16x32_bf16 v[24:27], a[88:91], v[140:143], v[24:27]\n"
"v_mfma_f32_16x16x32_bf16 v[44:47], a[88:91], v[144:147], v[44:47]\n"
"v_mfma_f32_16x16x32_bf16 v[64:67], a[88:91], v[148:151], v[64:67]\n"
"v_mfma_f32_16x16x32_bf16 v[8:11], a[92:95], v[136:139], v[8:11]\n"
"v_mfma_f32_16x16x32_bf16 v[28:31], a[92:95], v[140:143], v[28:31]\n"
"v_mfma_f32_16x16x32_bf16 v[48:51], a[92:95], v[144:147], v[48:51]\n"
"v_mfma_f32_16x16x32_bf16 v[68:71], a[92:95], v[148:151], v[68:71]\n"
"v_mfma_f32_16x16x32_bf16 v[12:15], a[96:99], v[136:139], v[12:15]\n"
"v_mfma_f32_16x16x32_bf16 v[32:35], a[96:99], v[140:143], v[32:35]\n"
"v_mfma_f32_16x16x32_bf16 v[52:55], a[96:99], v[144:147], v[52:55]\n"
"v_mfma_f32_16x16x32_bf16 v[72:75], a[96:99], v[148:151], v[72:75]\n"
"v_mfma_f32_16x16x32_bf16 v[16:19], a[100:103], v[136:139], v[16:19]\n"
"v_mfma_f32_16x16x32_bf16 v[36:39], a[100:103], v[140:143], v[36:39]\n"
"v_mfma_f32_16x16x32_bf16 v[56:59], a[100:103], v[144:147], v[56:59]\n"
"v_mfma_f32_16x16x32_bf16 v[76:79], a[100:103], v[148:151], v[76:79]\n"
"buffer_load_dwordx4 v[136:139], v80, s[96:99], s62 offen offset:1024 sc1\n"
"buffer_load_dwordx4 v[140:143], v81, s[96:99], s62 offen offset:1024 sc1\n"
"buffer_load_dwordx4 v[144:147], v82, s[96:99], s62 offen offset:1024 sc1\n"
"buffer_load_dwordx4 v[148:151], v83, s[96:99], s62 offen offset:1024 sc1\n"
"s_waitcnt vmcnt(15)\n"
"v_mfma_f32_16x16x32_bf16 v[0:3], a[104:107], v[152:155], v[0:3]\n"
"s_waitcnt vmcnt(14)\n"
"v_mfma_f32_16x16x32_bf16 v[20:23], a[104:107], v[156:159], v[20:23]\n"
"s_waitcnt vmcnt(13)\n"
"v_mfma_f32_16x16x32_bf16 v[40:43], a[104:107], v[160:163], v[40:43]\n"
"s_waitcnt vmcnt(12)\n"
"v_mfma_f32_16x16x32_bf16 v[60:63], a[104:107], v[164:167], v[60:63]\n"
"v_mfma_f32_16x16x32_bf16 v[4:7], a[108:111], v[152:155], v[4:7]\n"
"v_mfma_f32_16x16x32_bf16 v[24:27], a[108:111], v[156:159], v[24:27]\n"
"v_mfma_f32_16x16x32_bf16 v[44:47], a[108:111], v[160:163], v[44:47]\n"
"v_mfma_f32_16x16x32_bf16 v[64:67], a[108:111], v[164:167], v[64:67]\n"
"v_mfma_f32_16x16x32_bf16 v[8:11], a[112:115], v[152:155], v[8:11]\n"
"v_mfma_f32_16x16x32_bf16 v[28:31], a[112:115], v[156:159], v[28:31]\n"
"v_mfma_f32_16x16x32_bf16 v[48:51], a[112:115], v[160:163], v[48:51]\n"
"v_mfma_f32_16x16x32_bf16 v[68:71], a[112:115], v[164:167], v[68:71]\n"
"v_mfma_f32_16x16x32_bf16 v[12:15], a[116:119], v[152:155], v[12:15]\n"
"v_mfma_f32_16x16x32_bf16 v[32:35], a[116:119], v[156:159], v[32:35]\n"
"v_mfma_f32_16x16x32_bf16 v[52:55], a[116:119], v[160:163], v[52:55]\n"
"v_mfma_f32_16x16x32_bf16 v[72:75], a[116:119], v[164:167], v[72:75]\n"
"v_mfma_f32_16x16x32_bf16 v[16:19], a[120:123], v[152:155], v[16:19]\n"
"v_mfma_f32_16x16x32_bf16 v[36:39], a[120:123], v[156:159], v[36:39]\n"
"v_mfma_f32_16x16x32_bf16 v[56:59], a[120:123], v[160:163], v[56:59]\n"
"v_mfma_f32_16x16x32_bf16 v[76:79], a[120:123], v[164:167], v[76:79]\n"
"s_waitcnt vmcnt(11)\n"
"v_mfma_f32_16x16x32_bf16 v[0:3], a[124:127], v[168:171], v[0:3]\n"
"s_waitcnt vmcnt(10)\n"
"v_mfma_f32_16x16x32_bf16 v[20:23], a[124:127], v[172:175], v[20:23]\n"
"s_waitcnt vmcnt(9)\n"
"v_mfma_f32_16x16x32_bf16 v[40:43], a[124:127], v[176:179], v[40:43]\n"
"s_waitcnt vmcnt(8)\n"
"v_mfma_f32_16x16x32_bf16 v[60:63], a[124:127], v[180:183], v[60:63]\n"
"v_mfma_f32_16x16x32_bf16 v[4:7], a[128:131], v[168:171], v[4:7]\n"
"v_mfma_f32_16x16x32_bf16 v[24:27], a[128:131], v[172:175], v[24:27]\n"
"v_mfma_f32_16x16x32_bf16 v[44:47], a[128:131], v[176:179], v[44:47]\n"
"v_mfma_f32_16x16x32_bf16 v[64:67], a[128:131], v[180:183], v[64:67]\n"
"v_mfma_f32_16x16x32_bf16 v[8:11], a[132:135], v[168:171], v[8:11]\n"
"v_mfma_f32_16x16x32_bf16 v[28:31], a[132:135], v[172:175], v[28:31]\n"
"v_mfma_f32_16x16x32_bf16 v[48:51], a[132:135], v[176:179], v[48:51]\n"
"v_mfma_f32_16x16x32_bf16 v[68:71], a[132:135], v[180:183], v[68:71]\n"
"v_mfma_f32_16x16x32_bf16 v[12:15], a[136:139], v[168:171], v[12:15]\n"
"v_mfma_f32_16x16x32_bf16 v[32:35], a[136:139], v[172:175], v[32:35]\n"
"v_mfma_f32_16x16x32_bf16 v[52:55], a[136:139], v[176:179], v[52:55]\n"
"v_mfma_f32_16x16x32_bf16 v[72:75], a[136:139], v[180:183], v[72:75]\n"
"v_mfma_f32_16x16x32_bf16 v[16:19], a[140:143], v[168:171], v[16:19]\n"
"v_mfma_f32_16x16x32_bf16 v[36:39], a[140:143], v[172:175], v[36:39]\n"
"v_mfma_f32_16x16x32_bf16 v[56:59], a[140:143], v[176:179], v[56:59]\n"
"v_mfma_f32_16x16x32_bf16 v[76:79], a[140:143], v[180:183], v[76:79]\n"
"s_waitcnt vmcnt(7)\n"
"v_mfma_f32_16x16x32_bf16 v[0:3], a[144:147], v[120:123], v[0:3]\n"
"s_waitcnt vmcnt(6)\n"
"v_mfma_f32_16x16x32_bf16 v[20:23], a[144:147], v[124:127], v[20:23]\n"
"s_waitcnt vmcnt(5)\n"
"v_mfma_f32_16x16x32_bf16 v[40:43], a[144:147], v[128:131], v[40:43]\n"
"s_waitcnt vmcnt(4)\n"
"v_mfma_f32_16x16x32_bf16 v[60:63], a[144:147], v[132:135], v[60:63]\n"
"v_mfma_f32_16x16x32_bf16 v[4:7], a[148:151], v[120:123], v[4:7]\n"
"v_mfma_f32_16x16x32_bf16 v[24:27], a[148:151], v[124:127], v[24:27]\n"
"v_mfma_f32_16x16x32_bf16 v[44:47], a[148:151], v[128:131], v[44:47]\n"
"v_mfma_f32_16x16x32_bf16 v[64:67], a[148:151], v[132:135], v[64:67]\n"
"v_mfma_f32_16x16x32_bf16 v[8:11], a[152:155], v[120:123], v[8:11]\n"
"v_mfma_f32_16x16x32_bf16 v[28:31], a[152:155], v[124:127], v[28:31]\n"
"v_mfma_f32_16x16x32_bf16 v[48:51], a[152:155], v[128:131], v[48:51]\n"
"v_mfma_f32_16x16x32_bf16 v[68:71], a[152:155], v[132:135], v[68:71]\n"
"v_mfma_f32_16x16x32_bf16 v[12:15], a[156:159], v[120:123], v[12:15]\n"
"v_mfma_f32_16x16x32_bf16 v[32:35], a[156:159], v[124:127], v[32:35]\n"
"v_mfma_f32_16x16x32_bf16 v[52:55], a[156:159], v[128:131], v[52:55]\n"
"v_mfma_f32_16x16x32_bf16 v[72:75], a[156:159], v[132:135], v[72:75]\n"
"v_mfma_f32_16x16x32_bf16 v[16:19], v[96:99], v[120:123], v[16:19]\n"
"v_mfma_f32_16x16x32_bf16 v[36:39], v[96:99], v[124:127], v[36:39]\n"
"v_mfma_f32_16x16x32_bf16 v[56:59], v[96:99], v[128:131], v[56:59]\n"
"v_mfma_f32_16x16x32_bf16 v[76:79], v[96:99], v[132:135], v[76:79]\n"
"s_waitcnt vmcnt(3)\n"
"v_mfma_f32_16x16x32_bf16 v[0:3], v[100:103], v[136:139], v[0:3]\n"
"s_waitcnt vmcnt(2)\n"
"v_mfma_f32_16x16x32_bf16 v[20:23], v[100:103], v[140:143], v[20:23]\n"
"s_waitcnt vmcnt(1)\n"
"v_mfma_f32_16x16x32_bf16 v[40:43], v[100:103], v[144:147], v[40:43]\n"
"s_waitcnt vmcnt(0)\n"
"v_mfma_f32_16x16x32_bf16 v[60:63], v[100:103], v[148:151], v[60:63]\n"
"v_mfma_f32_16x16x32_bf16 v[4:7], v[104:107], v[136:139], v[4:7]\n"
"v_mfma_f32_16x16x32_bf16 v[24:27], v[104:107], v[140:143], v[24:27]\n"
"v_mfma_f32_16x16x32_bf16 v[44:47], v[104:107], v[144:147], v[44:47]\n"
"v_mfma_f32_16x16x32_bf16 v[64:67], v[104:107], v[148:151], v[64:67]\n"
"v_mfma_f32_16x16x32_bf16 v[8:11], v[108:111], v[136:139], v[8:11]\n"
"v_mfma_f32_16x16x32_bf16 v[28:31], v[108:111], v[140:143], v[28:31]\n"
"v_mfma_f32_16x16x32_bf16 v[48:51], v[108:111], v[144:147], v[48:51]\n"
"v_mfma_f32_16x16x32_bf16 v[68:71], v[108:111], v[148:151], v[68:71]\n"
"v_mfma_f32_16x16x32_bf16 v[12:15], v[112:115], v[136:139], v[12:15]\n"
"v_mfma_f32_16x16x32_bf16 v[32:35], v[112:115], v[140:143], v[32:35]\n"
"v_mfma_f32_16x16x32_bf16 v[52:55], v[112:115], v[144:147], v[52:55]\n"
"v_mfma_f32_16x16x32_bf16 v[72:75], v[112:115], v[148:151], v[72:75]\n"
"v_mfma_f32_16x16x32_bf16 v[16:19], v[116:119], v[136:139], v[16:19]\n"
"v_mfma_f32_16x16x32_bf16 v[36:39], v[116:119], v[140:143], v[36:39]\n"
"v_mfma_f32_16x16x32_bf16 v[56:59], v[116:119], v[144:147], v[56:59]\n"
"v_mfma_f32_16x16x32_bf16 v[76:79], v[116:119], v[148:151], v[76:79]\n"
"s_nop 5\n"
"v_cmp_u_f32 vcc, v16, v36\n"
"s_nop 0\n"
"v_cmp_u_f32 s[58:59], v56, v76\n"
"s_nop 0\n"
"s_or_b64 vcc, vcc, s[58:59]\n"
"s_nop 0\n"
"s_cbranch_vccnz REDO_G4_%=\n"
"DONE_G4_%=:\n"
"ds_write_b128 v89, v[20:23] offset:0\n"
"ds_write_b128 v89, v[24:27] offset:1024\n"
"ds_write_b128 v89, v[28:31] offset:2048\n"
"ds_write_b128 v89, v[32:35] offset:3072\n"
"ds_write_b128 v89, v[36:39] offset:4096\n"
"ds_write_b128 v90, v[40:43] offset:0\n"
"ds_write_b128 v90, v[44:47] offset:1024\n"
"ds_write_b128 v90, v[48:51] offset:2048\n"
"ds_write_b128 v90, v[52:55] offset:3072\n"
"ds_write_b128 v90, v[56:59] offset:4096\n"
"ds_write_b128 v91, v[60:63] offset:0\n"
"ds_write_b128 v91, v[64:67] offset:1024\n"
"ds_write_b128 v91, v[68:71] offset:2048\n"
"ds_write_b128 v91, v[72:75] offset:3072\n"
"ds_write_b128 v91, v[76:79] offset:4096\n"
"v_lshlrev_b32 v96, 16, v220\n"
"v_and_b32 v97, 0xffff0000, v220\n"
"v_lshlrev_b32 v98, 16, v221\n"
"v_and_b32 v99, 0xffff0000, v221\n"
"v_lshlrev_b32 v100, 16, v222\n"
"v_and_b32 v101, 0xffff0000, v222\n"
"v_lshlrev_b32 v102, 16, v223\n"
"v_and_b32 v103, 0xffff0000, v223\n"
"v_lshlrev_b32 v104, 16, v224\n"
"v_and_b32 v105, 0xffff0000, v224\n"
"v_lshlrev_b32 v106, 16, v225\n"
"v_and_b32 v107, 0xffff0000, v225\n"
"v_lshlrev_b32 v108, 16, v226\n"
"v_and_b32 v109, 0xffff0000, v226\n"
"v_lshlrev_b32 v110, 16, v227\n"
"v_and_b32 v111, 0xffff0000, v227\n"
"v_lshlrev_b32 v112, 16, v228\n"
"v_and_b32 v113, 0xffff0000, v228\n"
"v_lshlrev_b32 v114, 16, v229\n"
"v_and_b32 v115, 0xffff0000, v229\n"
"v_lshlrev_b32 v116, 16, v210\n"
"v_and_b32 v117, 0xffff0000, v210\n"
"v_lshlrev_b32 v118, 16, v211\n"
"v_and_b32 v119, 0xffff0000, v211\n"
"v_lshlrev_b32 v120, 16, v212\n"
"v_and_b32 v121, 0xffff0000, v212\n"
"v_lshlrev_b32 v122, 16, v213\n"
"v_and_b32 v123, 0xffff0000, v213\n"
"v_lshlrev_b32 v124, 16, v214\n"
"v_and_b32 v125, 0xffff0000, v214\n"
"v_lshlrev_b32 v126, 16, v215\n"
"v_and_b32 v127, 0xffff0000, v215\n"
"v_lshlrev_b32 v128, 16, v216\n"
"v_and_b32 v129, 0xffff0000, v216\n"
"v_lshlrev_b32 v130, 16, v217\n"
"v_and_b32 v131, 0xffff0000, v217\n"
"v_lshlrev_b32 v132, 16, v218\n"
"v_and_b32 v133, 0xffff0000, v218\n"
"v_lshlrev_b32 v134, 16, v219\n"
"v_and_b32 v135, 0xffff0000, v219\n"
"s_waitcnt lgkmcnt(0)\n"
"s_barrier\n"
"ds_read_b128 v[20:23], v92 offset:0\n"
"ds_read_b128 v[24:27], v92 offset:1024\n"
"ds_read_b128 v[28:31], v92 offset:2048\n"
"ds_read_b128 v[32:35], v92 offset:3072\n"
"ds_read_b128 v[36:39], v92 offset:4096\n"
"ds_read_b128 v[40:43], v92 offset:5120\n"
"ds_read_b128 v[44:47], v92 offset:6144\n"
"ds_read_b128 v[48:51], v92 offset:7168\n"
"ds_read_b128 v[52:55], v92 offset:8192\n"
"ds_read_b128 v[56:59], v92 offset:9216\n"
"ds_read_b128 v[60:63], v92 offset:10240\n"
"ds_read_b128 v[64:67], v92 offset:11264\n"
"ds_read_b128 v[68:71], v92 offset:12288\n"
"ds_read_b128 v[72:75], v92 offset:13312\n"
"ds_read_b128 v[76:79], v92 offset:14336\n"
"s_waitcnt lgkmcnt(14)\n"
"v_pk_add_f32 v[0:1], v[0:1], v[20:21]\n"
"v_pk_add_f32 v[2:3], v[2:3], v[22:23]\n"
"s_waitcnt lgkmcnt(13)\n"
"v_pk_add_f32 v[4:5], v[4:5], v[24:25]\n"
"v_pk_add_f32 v[6:7], v[6:7], v[26:27]\n"
"s_waitcnt lgkmcnt(12)\n"
"v_pk_add_f32 v[8:9], v[8:9], v[28:29]\n"
"v_pk_add_f32 v[10:11], v[10:11], v[30:31]\n"
"s_waitcnt lgkmcnt(11)\n"
"v_pk_add_f32 v[12:13], v[12:13], v[32:33]\n"
"v_pk_add_f32 v[14:15], v[14:15], v[34:35]\n"
"s_waitcnt lgkmcnt(10)\n"
"v_pk_add_f32 v[16:17], v[16:17], v[36:37]\n"
"v_pk_add_f32 v[18:19], v[18:19], v[38:39]\n"
"s_waitcnt lgkmcnt(9)\n"
"v_pk_add_f32 v[0:1], v[0:1], v[40:41]\n"
"v_pk_add_f32 v[2:3], v[2:3], v[42:43]\n"
"s_waitcnt lgkmcnt(8)\n"
"v_pk_add_f32 v[4:5], v[4:5], v[44:45]\n"
"v_pk_add_f32 v[6:7], v[6:7], v[46:47]\n"
"s_waitcnt lgkmcnt(7)\n"
"v_pk_add_f32 v[8:9], v[8:9], v[48:49]\n"
"v_pk_add_f32 v[10:11], v[10:11], v[50:51]\n"
"s_waitcnt lgkmcnt(6)\n"
"v_pk_add_f32 v[12:13], v[12:13], v[52:53]\n"
"v_pk_add_f32 v[14:15], v[14:15], v[54:55]\n"
"s_waitcnt lgkmcnt(5)\n"
"v_pk_add_f32 v[16:17], v[16:17], v[56:57]\n"
"v_pk_add_f32 v[18:19], v[18:19], v[58:59]\n"
"s_waitcnt lgkmcnt(4)\n"
"v_pk_add_f32 v[0:1], v[0:1], v[60:61]\n"
"v_pk_add_f32 v[2:3], v[2:3], v[62:63]\n"
"s_waitcnt lgkmcnt(3)\n"
"v_pk_add_f32 v[4:5], v[4:5], v[64:65]\n"
"v_pk_add_f32 v[6:7], v[6:7], v[66:67]\n"
"s_waitcnt lgkmcnt(2)\n"
"v_pk_add_f32 v[8:9], v[8:9], v[68:69]\n"
"v_pk_add_f32 v[10:11], v[10:11], v[70:71]\n"
"s_waitcnt lgkmcnt(1)\n"
"v_pk_add_f32 v[12:13], v[12:13], v[72:73]\n"
"v_pk_add_f32 v[14:15], v[14:15], v[74:75]\n"
"s_waitcnt lgkmcnt(0)\n"
"v_pk_add_f32 v[16:17], v[16:17], v[76:77]\n"
"v_pk_add_f32 v[18:19], v[18:19], v[78:79]\n"
"v_pk_add_f32 v[0:1], v[0:1], v[116:117]\n"
"v_pk_add_f32 v[2:3], v[2:3], v[118:119]\n"
"v_pk_add_f32 v[4:5], v[4:5], v[120:121]\n"
"v_pk_add_f32 v[6:7], v[6:7], v[122:123]\n"
"v_pk_add_f32 v[8:9], v[8:9], v[124:125]\n"
"v_pk_add_f32 v[10:11], v[10:11], v[126:127]\n"
"v_pk_add_f32 v[12:13], v[12:13], v[128:129]\n"
"v_pk_add_f32 v[14:15], v[14:15], v[130:131]\n"
"v_pk_add_f32 v[16:17], v[16:17], v[132:133]\n"
"v_pk_add_f32 v[18:19], v[18:19], v[134:135]\n"
"v_pk_fma_f32 v[0:1], v[0:1], v[184:185], v[96:97]\n"
"v_pk_fma_f32 v[2:3], v[2:3], v[186:187], v[98:99]\n"
"v_pk_fma_f32 v[4:5], v[4:5], v[188:189], v[100:101]\n"
"v_pk_fma_f32 v[6:7], v[6:7], v[190:191], v[102:103]\n"
"v_pk_fma_f32 v[8:9], v[8:9], v[192:193], v[104:105]\n"
"v_pk_fma_f32 v[10:11], v[10:11], v[194:195], v[106:107]\n"
"v_pk_fma_f32 v[12:13], v[12:13], v[196:197], v[108:109]\n"
"v_pk_fma_f32 v[14:15], v[14:15], v[198:199], v[110:111]\n"
"v_pk_fma_f32 v[16:17], v[16:17], v[200:201], v[112:113]\n"
"v_pk_fma_f32 v[18:19], v[18:19], v[202:203], v[114:115]\n"
"v_cvt_pk_bf16_f32 v32, v0, v1\n"
"v_cvt_pk_bf16_f32 v33, v2, v3\n"
"v_cvt_pk_bf16_f32 v34, v4, v5\n"
"v_cvt_pk_bf16_f32 v35, v6, v7\n"
"v_cvt_pk_bf16_f32 v36, v8, v9\n"
"v_cvt_pk_bf16_f32 v37, v10, v11\n"
"v_cvt_pk_bf16_f32 v38, v12, v13\n"
"v_cvt_pk_bf16_f32 v39, v14, v15\n"
"v_cvt_pk_bf16_f32 v40, v16, v17\n"
"v_cvt_pk_bf16_f32 v41, v18, v19\n"
"buffer_store_dwordx4 v[32:35], v204, s[32:35], 0 offen offset:0\n"
"buffer_store_dwordx4 v[36:39], v204, s[32:35], 0 offen offset:64\n"
"buffer_store_dwordx2 v[40:41], v205, s[32:35], 0 offen\n"
"s_or_b32 s38, %1, %2\n"
"s_cmp_lg_u32 s38, 0\n"
"s_cbranch_scc1 NOBUMP_%=\n"
"s_mov_b32 s38, 105938944\n"
"v_min_u32 v243, 1, v252\n"
"v_lshlrev_b32 v243, 30, v243\n"
"v_mov_b32 v244, s14\n"
"v_mov_b32 v245, s15\n"
"buffer_store_dwordx2 v[244:245], v243, s[96:99], s38 offen sc1\n"
"NOBUMP_%=:\n"
"s_endpgm\n"
"CODE_END_%=:\n"
"SLOW_H_%=:\n"
"s_mov_b32 s87, 0x40000\n"
"SL_H_%=:\n"
"buffer_load_dwordx2 v[248:249], v231, s[96:99], s86 offen sc1\n"
"s_sub_u32 s87, s87, 1\n"
"s_waitcnt vmcnt(0)\n"
"v_cmp_ne_u64 vcc, s[14:15], v[248:249]\n"
"s_cbranch_scc1 OK_H_%=\n"
"s_nop 0\n"
"s_cbranch_vccnz SL_H_%=\n"
"s_branch OK_H_%=\n"
"SLOW_M_%=:\n"
"s_mov_b32 s87, 0x40000\n"
"SL_M_%=:\n"
"buffer_load_dwordx4 v[204:207], v251, s[96:99], s86 offen sc1\n"
"buffer_load_dwordx4 v[208:211], v251, s[96:99], s86 offen offset:1024 sc1\n"
"s_sub_u32 s87, s87, 1\n"
"s_waitcnt vmcnt(0)\n"
"v_xor_b32 v204, s14, v204\n"
"v_xor_b32 v205, s15, v205\n"
"v_xor_b32 v206, s14, v206\n"
"v_xor_b32 v207, s15, v207\n"
"v_xor_b32 v208, s14, v208\n"
"v_xor_b32 v209, s15, v209\n"
"v_xor_b32 v210, s14, v210\n"
"v_xor_b32 v211, s15, v211\n"
"v_or3_b32 v204, v204, v205, v206\n"
"v_or3_b32 v207, v207, v208, v209\n"
"v_or3_b32 v204, v204, v210, v211\n"
"v_or_b32 v204, v204, v207\n"
"v_cmp_ne_u32 vcc, 0, v204\n"
"s_cbranch_scc1 OK_M_%=\n"
"s_nop 0\n"
"s_cbranch_vccnz SL_M_%=\n"
"s_branch OK_M_%=\n"
"SLOW_KV_%=:\n"
"s_mov_b32 s87, 0x40000\n"
"SL_KV_%=:\n"
"buffer_load_dwordx2 v[248:249], v231, s[96:99], s86 offen sc1\n"
"s_sub_u32 s87, s87, 1\n"
"s_waitcnt vmcnt(0)\n"
"v_cmp_ne_u64 vcc, s[14:15], v[248:249]\n"
"s_cbranch_scc1 OK_KV_%=\n"
"s_nop 0\n"
"s_cbranch_vccnz SL_KV_%=\n"
"s_branch OK_KV_%=\n"
"SLOW_RT_%=:\n"
"s_mov_b32 s44, 0x40000\n"
"SL_RT_%=:\n"
"buffer_load_dwordx4 v[240:243], v251, s[96:99], s60 offen sc1\n"
"buffer_load_dwordx4 v[244:247], v251, s[96:99], s60 offen offset:1024 sc1\n"
"s_sub_u32 s44, s44, 1\n"
"s_waitcnt vmcnt(0)\n"
"v_xor_b32 v240, s14, v240\n"
"v_xor_b32 v241, s15, v241\n"
"v_xor_b32 v242, s14, v242\n"
"v_xor_b32 v243, s15, v243\n"
"v_xor_b32 v244, s14, v244\n"
"v_xor_b32 v245, s15, v245\n"
"v_xor_b32 v246, s14, v246\n"
"v_xor_b32 v247, s15, v247\n"
"v_or3_b32 v240, v240, v241, v242\n"
"v_or3_b32 v243, v243, v244, v245\n"
"v_or3_b32 v240, v240, v246, v247\n"
"v_or_b32 v240, v240, v243\n"
"v_cmp_ne_u32 vcc, 0, v240\n"
"s_cbranch_scc1 OK_RT_%=\n"
"s_nop 0\n"
"s_cbranch_vccnz SL_RT_%=\n"
"s_branch OK_RT_%=\n"
"SLOW_CTX_%=:\n"
"s_mov_b32 s44, 0x40000\n"
"SL_CTX_%=:\n"
"buffer_load_dwordx2 v[244:245], v243, s[96:99], s60 offen sc1\n"
"s_sub_u32 s44, s44, 1\n"
"s_waitcnt vmcnt(0)\n"
"v_cmp_ne_u64 vcc, s[14:15], v[244:245]\n"
"s_cbranch_scc1 OK_CTX_%=\n"
"s_nop 0\n"
"s_cbranch_vccnz SL_CTX_%=\n"
"s_branch OK_CTX_%=\n"
"SLOW_FL_%=:\n"
"s_mov_b32 s44, 0x40000\n"
"SL_FL_%=:\n"
"buffer_load_dwordx4 v[100:103], v251, s[96:99], s60 offen sc1\n"
"buffer_load_dwordx4 v[104:107], v251, s[96:99], s60 offen offset:1024 sc1\n"
"s_sub_u32 s44, s44, 1\n"
"s_waitcnt vmcnt(0)\n"
"v_xor_b32 v100, s14, v100\n"
"v_xor_b32 v101, s15, v101\n"
"v_xor_b32 v102, s14, v102\n"
"v_xor_b32 v103, s15, v103\n"
"v_xor_b32 v104, s14, v104\n"
"v_xor_b32 v105, s15, v105\n"
"v_xor_b32 v106, s14, v106\n"
"v_xor_b32 v107, s15, v107\n"
"v_or3_b32 v100, v100, v101, v102\n"
"v_or3_b32 v103, v103, v104, v105\n"
"v_or3_b32 v100, v100, v106, v107\n"
"v_or_b32 v100, v100, v103\n"
"v_cmp_ne_u32 vcc, 0, v100\n"
"s_cbranch_scc1 OK_FL_%=\n"
"s_nop 0\n"
"s_cbranch_vccnz SL_FL_%=\n"
"s_branch OK_FL_%=\n"
"SLOW_OX_%=:\n"
"s_mov_b32 s44, 0x40000\n"
"SL_OX_%=:\n"
"buffer_load_dwordx4 v[100:103], v251, s[96:99], s72 offen offset:0 sc1\n"
"buffer_load_dwordx4 v[104:107], v251, s[96:99], s72 offen offset:1024 sc1\n"
"buffer_load_dwordx4 v[108:111], v251, s[96:99], s72 offen offset:2048 sc1\n"
"buffer_load_dwordx4 v[112:115], v251, s[96:99], s72 offen offset:3072 sc1\n"
"buffer_load_dwordx4 v[116:119], v251, s[96:99], s73 offen offset:0 sc1\n"
"s_sub_u32 s44, s44, 1\n"
"s_waitcnt vmcnt(0)\n"
"v_max_u32 v242, v100, v101\n"
"v_max3_u32 v242, v242, v102, v103\n"
"v_max3_u32 v242, v242, v104, v105\n"
"v_max3_u32 v242, v242, v106, v107\n"
"v_max3_u32 v242, v242, v108, v109\n"
"v_max3_u32 v242, v242, v110, v111\n"
"v_max3_u32 v242, v242, v112, v113\n"
"PG26_%=:\n"
"s_nop 0\n"
"v_max3_u32 v242, v242, v114, v115\n"
"v_max3_u32 v242, v242, v116, v117\n"
"v_max3_u32 v242, v242, v118, v119\n"
"v_cmp_eq_u32 vcc, -1, v242\n"
"s_cbranch_scc1 OK_OX_%=\n"
"s_nop 0\n"
"s_cbranch_vccnz SL_OX_%=\n"
"s_branch OK_OX_%=\n"
"SLOW_G1_%=:\n"
"s_mov_b32 s44, 0x40000\n"
"SL_G1_%=:\n"
"buffer_load_dwordx4 v[184:187], v203, s[96:99], s60 offen offset:0 sc1\n"
"buffer_load_dwordx4 v[188:191], v203, s[96:99], s60 offen offset:16 sc1\n"
"buffer_load_dwordx4 v[192:195], v203, s[96:99], s60 offen offset:32 sc1\n"
"buffer_load_dwordx4 v[196:199], v203, s[96:99], s60 offen offset:48 sc1\n"
"s_sub_u32 s44, s44, 1\n"
"s_waitcnt vmcnt(0)\n"
"v_max_u32 v242, v184, v185\n"
"v_max3_u32 v242, v242, v186, v187\n"
"v_max3_u32 v242, v242, v188, v189\n"
"v_max3_u32 v242, v242, v190, v191\n"
"v_max3_u32 v242, v242, v192, v193\n"
"v_max3_u32 v242, v242, v194, v195\n"
"v_max3_u32 v242, v242, v196, v197\n"
"v_max3_u32 v242, v242, v198, v199\n"
"v_cmp_eq_u32 vcc, -1, v242\n"
"s_cbranch_scc1 OK_G1_%=\n"
"s_nop 0\n"
"s_cbranch_vccnz SL_G1_%=\n"
"s_branch OK_G1_%=\n"
"REDO_G1_%=:\n"
"s_sub_u32 s63, s63, 1\n"
"s_cbranch_scc1 DONE_G1_%=\n"
"s_waitcnt vmcnt(0) lgkmcnt(0)\n"
"s_sleep 8\n"
"s_branch OK_G1_%=\n"
"SLOW_G2_%=:\n"
"s_mov_b32 s44, 0x40000\n"
"SL_G2_%=:\n"
"buffer_load_dwordx4 v[160:163], v251, s[96:99], s60 offen sc1\n"
"buffer_load_dwordx4 v[164:167], v177, s[96:99], s60 offen sc1\n"
"buffer_load_dwordx4 v[168:171], v178, s[96:99], s60 offen sc1\n"
"buffer_load_dwordx4 v[172:175], v179, s[96:99], s60 offen sc1\n"
"s_sub_u32 s44, s44, 1\n"
"s_waitcnt vmcnt(0)\n"
"v_max_u32 v242, v160, v161\n"
"v_max3_u32 v242, v242, v162, v163\n"
"v_max3_u32 v242, v242, v164, v165\n"
"v_max3_u32 v242, v242, v166, v167\n"
"v_max3_u32 v242, v242, v168, v169\n"
"v_max3_u32 v242, v242, v170, v171\n"
"v_max3_u32 v242, v242, v172, v173\n"
"v_max3_u32 v242, v242, v174, v175\n"
"v_cmp_eq_u32 vcc, -1, v242\n"
"s_cbranch_scc1 OK_G2_%=\n"
"s_nop 0\n"
"s_cbranch_vccnz SL_G2_%=\n"
"s_branch OK_G2_%=\n"
"REDO_G2_%=:\n"
"s_sub_u32 s63, s63, 1\n"
"s_cbranch_scc1 DONE_G2_%=\n"
"s_waitcnt vmcnt(0) lgkmcnt(0)\n"
"s_sleep 8\n"
"s_branch OK_G2_%=\n"
"SLOW_G3_%=:\n"
"s_mov_b32 s44, 0x40000\n"
"SL_G3_%=:\n"
"buffer_load_dwordx4 v[124:127], v61, s[96:99], s60 offen sc1\n"
"buffer_load_dwordx4 v[128:131], v61, s[96:99], s60 offen offset:1024 sc1\n"
"s_sub_u32 s44, s44, 1\n"
"s_waitcnt vmcnt(0)\n"
"v_max_u32 v242, v124, v125\n"
"v_max3_u32 v242, v242, v126, v127\n"
"v_max3_u32 v242, v242, v128, v129\n"
"v_max3_u32 v242, v242, v130, v131\n"
"v_cmp_eq_u32 vcc, -1, v242\n"
"s_cbranch_scc1 OK_G3_%=\n"
"s_nop 0\n"
"s_cbranch_vccnz SL_G3_%=\n"
"s_branch OK_G3_%=\n"
"REDO_G3_%=:\n"
"s_sub_u32 s63, s63, 1\n"
"s_cbranch_scc1 DONE_G3_%=\n"
"s_waitcnt vmcnt(0) lgkmcnt(0)\n"
"s_sleep 8\n"
"s_branch OK_G3_%=\n"
"SLOW_G4_%=:\n"
"s_mov_b32 s44, 0x40000\n"
"SL_G4_%=:\n"
"buffer_load_dwordx4 v[20:23], v80, s[96:99], s60 offen sc1\n"
"buffer_load_dwordx4 v[24:27], v81, s[96:99], s60 offen sc1\n"
"buffer_load_dwordx4 v[28:31], v82, s[96:99], s60 offen sc1\n"
"buffer_load_dwordx4 v[32:35], v83, s[96:99], s60 offen sc1\n"
"buffer_load_dwordx4 v[36:39], v80, s[96:99], s70 offen sc1\n"
"buffer_load_dwordx4 v[40:43], v81, s[96:99], s70 offen sc1\n"
"buffer_load_dwordx4 v[44:47], v82, s[96:99], s70 offen sc1\n"
"buffer_load_dwordx4 v[48:51], v83, s[96:99], s70 offen sc1\n"
"s_sub_u32 s44, s44, 1\n"
"s_waitcnt vmcnt(0)\n"
"v_max_u32 v242, v20, v21\n"
"v_max3_u32 v242, v242, v22, v23\n"
"v_max3_u32 v242, v242, v24, v25\n"
"v_max3_u32 v242, v242, v26, v27\n"
"v_max3_u32 v242, v242, v28, v29\n"
"v_max3_u32 v242, v242, v30, v31\n"
"v_max3_u32 v242, v242, v32, v33\n"
"v_max3_u32 v242, v242, v34, v35\n"
"v_max3_u32 v242, v242, v36, v37\n"
"v_max3_u32 v242, v242, v38, v39\n"
"v_max3_u32 v242, v242, v40, v41\n"
"v_max3_u32 v242, v242, v42, v43\n"
"v_max3_u32 v242, v242, v44, v45\n"
"v_max3_u32 v242, v242, v46, v47\n"
"v_max3_u32 v242, v242, v48, v49\n"
"v_max3_u32 v242, v242, v50, v51\n"
"v_cmp_eq_u32 vcc, -1, v242\n"
"s_cbranch_scc1 OK_G4_%=\n"
"s_nop 0\n"
"s_cbranch_vccnz SL_G4_%=\n"
"s_branch OK_G4_%=\n"
"REDO_G4_%=:\n"
"s_sub_u32 s63, s63, 1\n"
"s_cbranch_scc1 DONE_G4_%=\n"
"s_waitcnt vmcnt(0) lgkmcnt(0)\n"
"s_sleep 8\n"
"s_branch OK_G4_%=\n"
"STUB_FINAL_1_%=:\n"
"s_mov_b32 s41, 0\n"
"s_branch RARE_0_%=\n"
"STUB_MID_0_%=:\n"
"s_mov_b32 s41, 1\n"
"s_branch RARE_1_%=\n"
"STUB_MIDM_0_%=:\n"
"s_mov_b32 s41, 2\n"
"s_branch RARE_1_%=\n"
"STUB_MIDR_0_%=:\n"
"s_mov_b32 s41, 3\n"
"s_branch RARE_1_%=\n"
"STUB_DRAINA_0_%=:\n"
"s_mov_b32 s41, 4\n"
"s_branch RARE_1_%=\n"
"STUB_MID_1_%=:\n"
"s_mov_b32 s41, 5\n"
"s_branch RARE_0_%=\n"
"STUB_MIDM_1_%=:\n"
"s_mov_b32 s41, 6\n"
"s_branch RARE_0_%=\n"
"STUB_MIDR_1_%=:\n"
"s_mov_b32 s41, 7\n"
"s_branch RARE_0_%=\n"
"STUB_DRAINA_1_%=:\n"
"s_mov_b32 s41, 8\n"
"s_branch RARE_0_%=\n"
"RARE_0_%=:\n"
"s_nop 15\n"
"s_nop 15\n"
"v_max_f32 v194, v0, v1\n"
"v_max3_f32 v194, v194, v2, v3\n"
"v_max3_f32 v194, v194, v4, v5\n"
"v_max3_f32 v194, v194, v6, v7\n"
"v_max3_f32 v194, v194, v8, v9\n"
"v_max3_f32 v194, v194, v10, v11\n"
"v_max3_f32 v194, v194, v12, v13\n"
"v_max3_f32 v194, v194, v14, v15\n"
"v_max3_f32 v194, v194, v32, v33\n"
"v_max3_f32 v194, v194, v34, v35\n"
"v_max3_f32 v194, v194, v36, v37\n"
"v_max3_f32 v194, v194, v38, v39\n"
"v_max3_f32 v194, v194, v40, v41\n"
"v_max3_f32 v194, v194, v42, v43\n"
"v_max3_f32 v194, v194, v44, v45\n"
"v_max3_f32 v194, v194, v46, v47\n"
"v_mov_b32 v196, v194\n"
"s_nop 1\n"
"v_permlane32_swap_b32 v194, v196\n"
"v_max3_f32 v194, v194, v196, 0\n"
"v_sub_f32 v196, 0, v194\n"
"v_exp_f32 v198, v196\n"
"v_add_f32 v222, v222, v194\n"
"v_mul_f32 v220, v220, v198\n"
"v_sub_f32 v128, 0, v222\n"
"v_sub_f32 v129, 0, v222\n"
"v_sub_f32 v130, 0, v222\n"
"v_sub_f32 v131, 0, v222\n"
"v_sub_f32 v132, 0, v222\n"
"v_sub_f32 v133, 0, v222\n"
"v_sub_f32 v134, 0, v222\n"
"v_sub_f32 v135, 0, v222\n"
"v_sub_f32 v136, 0, v222\n"
"v_sub_f32 v137, 0, v222\n"
"v_sub_f32 v138, 0, v222\n"
"v_sub_f32 v139, 0, v222\n"
"v_sub_f32 v140, 0, v222\n"
"v_sub_f32 v141, 0, v222\n"
"v_sub_f32 v142, 0, v222\n"
"v_sub_f32 v143, 0, v222\n"
"v_sub_f32 v0, v0, v194\n"
"v_sub_f32 v1, v1, v194\n"
"v_sub_f32 v2, v2, v194\n"
"v_sub_f32 v3, v3, v194\n"
"v_sub_f32 v4, v4, v194\n"
"v_sub_f32 v5, v5, v194\n"
"v_sub_f32 v6, v6, v194\n"
"v_sub_f32 v7, v7, v194\n"
"v_sub_f32 v8, v8, v194\n"
"v_sub_f32 v9, v9, v194\n"
"v_sub_f32 v10, v10, v194\n"
"v_sub_f32 v11, v11, v194\n"
"v_sub_f32 v12, v12, v194\n"
"v_sub_f32 v13, v13, v194\n"
"v_sub_f32 v14, v14, v194\n"
"v_sub_f32 v15, v15, v194\n"
"v_sub_f32 v32, v32, v194\n"
"v_sub_f32 v33, v33, v194\n"
"v_sub_f32 v34, v34, v194\n"
"v_sub_f32 v35, v35, v194\n"
"v_sub_f32 v36, v36, v194\n"
"v_sub_f32 v37, v37, v194\n"
"v_sub_f32 v38, v38, v194\n"
"v_sub_f32 v39, v39, v194\n"
"v_sub_f32 v40, v40, v194\n"
"v_sub_f32 v41, v41, v194\n"
"v_sub_f32 v42, v42, v194\n"
"v_sub_f32 v43, v43, v194\n"
"v_sub_f32 v44, v44, v194\n"
"v_sub_f32 v45, v45, v194\n"
"v_sub_f32 v46, v46, v194\n"
"v_sub_f32 v47, v47, v194\n"
"v_sub_f32 v64, v64, v194\n"
"v_sub_f32 v65, v65, v194\n"
"v_sub_f32 v66, v66, v194\n"
"v_sub_f32 v67, v67, v194\n"
"v_sub_f32 v68, v68, v194\n"
"v_sub_f32 v69, v69, v194\n"
"v_sub_f32 v70, v70, v194\n"
"v_sub_f32 v71, v71, v194\n"
"v_sub_f32 v72, v72, v194\n"
"v_sub_f32 v73, v73, v194\n"
"v_sub_f32 v74, v74, v194\n"
"v_sub_f32 v75, v75, v194\n"
"v_sub_f32 v76, v76, v194\n"
"v_sub_f32 v77, v77, v194\n"
"v_sub_f32 v78, v78, v194\n"
"v_sub_f32 v79, v79, v194\n"
"v_sub_f32 v96, v96, v194\n"
"v_sub_f32 v97, v97, v194\n"
"v_sub_f32 v98, v98, v194\n"
"v_sub_f32 v99, v99, v194\n"
"v_sub_f32 v100, v100, v194\n"
"v_sub_f32 v101, v101, v194\n"
"v_sub_f32 v102, v102, v194\n"
"v_sub_f32 v103, v103, v194\n"
"v_sub_f32 v104, v104, v194\n"
"v_sub_f32 v105, v105, v194\n"
"v_sub_f32 v106, v106, v194\n"
"v_sub_f32 v107, v107, v194\n"
"v_sub_f32 v108, v108, v194\n"
"v_sub_f32 v109, v109, v194\n"
"v_sub_f32 v110, v110, v194\n"
"v_sub_f32 v111, v111, v194\n"
"v_accvgpr_read_b32 v200, a0\n"
"v_mul_f32 v200, v200, v198\n"
"v_accvgpr_write_b32 a0, v200\n"
"v_accvgpr_read_b32 v201, a1\n"
"v_mul_f32 v201, v201, v198\n"
"v_accvgpr_write_b32 a1, v201\n"
"v_accvgpr_read_b32 v202, a2\n"
"v_mul_f32 v202, v202, v198\n"
"v_accvgpr_write_b32 a2, v202\n"
"v_accvgpr_read_b32 v203, a3\n"
"v_mul_f32 v203, v203, v198\n"
"v_accvgpr_write_b32 a3, v203\n"
"v_accvgpr_read_b32 v200, a4\n"
"v_mul_f32 v200, v200, v198\n"
"v_accvgpr_write_b32 a4, v200\n"
"v_accvgpr_read_b32 v201, a5\n"
"v_mul_f32 v201, v201, v198\n"
"v_accvgpr_write_b32 a5, v201\n"
"v_accvgpr_read_b32 v202, a6\n"
"v_mul_f32 v202, v202, v198\n"
"v_accvgpr_write_b32 a6, v202\n"
"v_accvgpr_read_b32 v203, a7\n"
"v_mul_f32 v203, v203, v198\n"
"v_accvgpr_write_b32 a7, v203\n"
"v_accvgpr_read_b32 v200, a8\n"
"v_mul_f32 v200, v200, v198\n"
"v_accvgpr_write_b32 a8, v200\n"
"v_accvgpr_read_b32 v201, a9\n"
"v_mul_f32 v201, v201, v198\n"
"v_accvgpr_write_b32 a9, v201\n"
"v_accvgpr_read_b32 v202, a10\n"
"v_mul_f32 v202, v202, v198\n"
"v_accvgpr_write_b32 a10, v202\n"
"v_accvgpr_read_b32 v203, a11\n"
"v_mul_f32 v203, v203, v198\n"
"v_accvgpr_write_b32 a11, v203\n"
"v_accvgpr_read_b32 v200, a12\n"
"v_mul_f32 v200, v200, v198\n"
"v_accvgpr_write_b32 a12, v200\n"
"v_accvgpr_read_b32 v201, a13\n"
"v_mul_f32 v201, v201, v198\n"
"v_accvgpr_write_b32 a13, v201\n"
"v_accvgpr_read_b32 v202, a14\n"
"v_mul_f32 v202, v202, v198\n"
"v_accvgpr_write_b32 a14, v202\n"
"v_accvgpr_read_b32 v203, a15\n"
"v_mul_f32 v203, v203, v198\n"
"v_accvgpr_write_b32 a15, v203\n"
"v_accvgpr_read_b32 v200, a32\n"
"v_mul_f32 v200, v200, v198\n"
"v_accvgpr_write_b32 a32, v200\n"
"v_accvgpr_read_b32 v201, a33\n"
"v_mul_f32 v201, v201, v198\n"
"v_accvgpr_write_b32 a33, v201\n"
"v_accvgpr_read_b32 v202, a34\n"
"v_mul_f32 v202, v202, v198\n"
"v_accvgpr_write_b32 a34, v202\n"
"v_accvgpr_read_b32 v203, a35\n"
"v_mul_f32 v203, v203, v198\n"
"v_accvgpr_write_b32 a35, v203\n"
"v_accvgpr_read_b32 v200, a36\n"
"v_mul_f32 v200, v200, v198\n"
"v_accvgpr_write_b32 a36, v200\n"
"v_accvgpr_read_b32 v201, a37\n"
"v_mul_f32 v201, v201, v198\n"
"v_accvgpr_write_b32 a37, v201\n"
"v_accvgpr_read_b32 v202, a38\n"
"v_mul_f32 v202, v202, v198\n"
"v_accvgpr_write_b32 a38, v202\n"
"v_accvgpr_read_b32 v203, a39\n"
"v_mul_f32 v203, v203, v198\n"
"v_accvgpr_write_b32 a39, v203\n"
"v_accvgpr_read_b32 v200, a40\n"
"v_mul_f32 v200, v200, v198\n"
"v_accvgpr_write_b32 a40, v200\n"
"v_accvgpr_read_b32 v201, a41\n"
"v_mul_f32 v201, v201, v198\n"
"v_accvgpr_write_b32 a41, v201\n"
"v_accvgpr_read_b32 v202, a42\n"
"v_mul_f32 v202, v202, v198\n"
"v_accvgpr_write_b32 a42, v202\n"
"v_accvgpr_read_b32 v203, a43\n"
"v_mul_f32 v203, v203, v198\n"
"v_accvgpr_write_b32 a43, v203\n"
"v_accvgpr_read_b32 v200, a44\n"
"v_mul_f32 v200, v200, v198\n"
"v_accvgpr_write_b32 a44, v200\n"
"v_accvgpr_read_b32 v201, a45\n"
"v_mul_f32 v201, v201, v198\n"
"v_accvgpr_write_b32 a45, v201\n"
"v_accvgpr_read_b32 v202, a46\n"
"v_mul_f32 v202, v202, v198\n"
"v_accvgpr_write_b32 a46, v202\n"
"v_accvgpr_read_b32 v203, a47\n"
"v_mul_f32 v203, v203, v198\n"
"v_accvgpr_write_b32 a47, v203\n"
"v_accvgpr_read_b32 v200, a64\n"
"v_mul_f32 v200, v200, v198\n"
"v_accvgpr_write_b32 a64, v200\n"
"v_accvgpr_read_b32 v201, a65\n"
"v_mul_f32 v201, v201, v198\n"
"v_accvgpr_write_b32 a65, v201\n"
"v_accvgpr_read_b32 v202, a66\n"
"v_mul_f32 v202, v202, v198\n"
"v_accvgpr_write_b32 a66, v202\n"
"v_accvgpr_read_b32 v203, a67\n"
"v_mul_f32 v203, v203, v198\n"
"v_accvgpr_write_b32 a67, v203\n"
"v_accvgpr_read_b32 v200, a68\n"
"v_mul_f32 v200, v200, v198\n"
"v_accvgpr_write_b32 a68, v200\n"
"v_accvgpr_read_b32 v201, a69\n"
"v_mul_f32 v201, v201, v198\n"
"v_accvgpr_write_b32 a69, v201\n"
"v_accvgpr_read_b32 v202, a70\n"
"v_mul_f32 v202, v202, v198\n"
"v_accvgpr_write_b32 a70, v202\n"
"v_accvgpr_read_b32 v203, a71\n"
"v_mul_f32 v203, v203, v198\n"
"v_accvgpr_write_b32 a71, v203\n"
"v_accvgpr_read_b32 v200, a72\n"
"v_mul_f32 v200, v200, v198\n"
"v_accvgpr_write_b32 a72, v200\n"
"v_accvgpr_read_b32 v201, a73\n"
"v_mul_f32 v201, v201, v198\n"
"v_accvgpr_write_b32 a73, v201\n"
"v_accvgpr_read_b32 v202, a74\n"
"v_mul_f32 v202, v202, v198\n"
"v_accvgpr_write_b32 a74, v202\n"
"v_accvgpr_read_b32 v203, a75\n"
"v_mul_f32 v203, v203, v198\n"
"v_accvgpr_write_b32 a75, v203\n"
"v_accvgpr_read_b32 v200, a76\n"
"v_mul_f32 v200, v200, v198\n"
"v_accvgpr_write_b32 a76, v200\n"
"v_accvgpr_read_b32 v201, a77\n"
"v_mul_f32 v201, v201, v198\n"
"v_accvgpr_write_b32 a77, v201\n"
"v_accvgpr_read_b32 v202, a78\n"
"v_mul_f32 v202, v202, v198\n"
"v_accvgpr_write_b32 a78, v202\n"
"v_accvgpr_read_b32 v203, a79\n"
"v_mul_f32 v203, v203, v198\n"
"v_accvgpr_write_b32 a79, v203\n"
"v_accvgpr_read_b32 v200, a96\n"
"v_mul_f32 v200, v200, v198\n"
"v_accvgpr_write_b32 a96, v200\n"
"v_accvgpr_read_b32 v201, a97\n"
"v_mul_f32 v201, v201, v198\n"
"v_accvgpr_write_b32 a97, v201\n"
"v_accvgpr_read_b32 v202, a98\n"
"v_mul_f32 v202, v202, v198\n"
"v_accvgpr_write_b32 a98, v202\n"
"v_accvgpr_read_b32 v203, a99\n"
"v_mul_f32 v203, v203, v198\n"
"v_accvgpr_write_b32 a99, v203\n"
"v_accvgpr_read_b32 v200, a100\n"
"v_mul_f32 v200, v200, v198\n"
"v_accvgpr_write_b32 a100, v200\n"
"v_accvgpr_read_b32 v201, a101\n"
"v_mul_f32 v201, v201, v198\n"
"v_accvgpr_write_b32 a101, v201\n"
"v_accvgpr_read_b32 v202, a102\n"
"v_mul_f32 v202, v202, v198\n"
"v_accvgpr_write_b32 a102, v202\n"
"v_accvgpr_read_b32 v203, a103\n"
"v_mul_f32 v203, v203, v198\n"
"v_accvgpr_write_b32 a103, v203\n"
"v_accvgpr_read_b32 v200, a104\n"
"v_mul_f32 v200, v200, v198\n"
"v_accvgpr_write_b32 a104, v200\n"
"v_accvgpr_read_b32 v201, a105\n"
"v_mul_f32 v201, v201, v198\n"
"v_accvgpr_write_b32 a105, v201\n"
"v_accvgpr_read_b32 v202, a106\n"
"v_mul_f32 v202, v202, v198\n"
"v_accvgpr_write_b32 a106, v202\n"
"v_accvgpr_read_b32 v203, a107\n"
"v_mul_f32 v203, v203, v198\n"
"v_accvgpr_write_b32 a107, v203\n"
"v_accvgpr_read_b32 v200, a108\n"
"v_mul_f32 v200, v200, v198\n"
"v_accvgpr_write_b32 a108, v200\n"
"v_accvgpr_read_b32 v201, a109\n"
"v_mul_f32 v201, v201, v198\n"
"v_accvgpr_write_b32 a109, v201\n"
"v_accvgpr_read_b32 v202, a110\n"
"v_mul_f32 v202, v202, v198\n"
"v_accvgpr_write_b32 a110, v202\n"
"v_accvgpr_read_b32 v203, a111\n"
"v_mul_f32 v203, v203, v198\n"
"v_accvgpr_write_b32 a111, v203\n"
"v_max_f32 v195, v16, v17\n"
"v_max3_f32 v195, v195, v18, v19\n"
"v_max3_f32 v195, v195, v20, v21\n"
"v_max3_f32 v195, v195, v22, v23\n"
"v_max3_f32 v195, v195, v24, v25\n"
"v_max3_f32 v195, v195, v26, v27\n"
"v_max3_f32 v195, v195, v28, v29\n"
"v_max3_f32 v195, v195, v30, v31\n"
"v_max3_f32 v195, v195, v48, v49\n"
"v_max3_f32 v195, v195, v50, v51\n"
"v_max3_f32 v195, v195, v52, v53\n"
"v_max3_f32 v195, v195, v54, v55\n"
"v_max3_f32 v195, v195, v56, v57\n"
"v_max3_f32 v195, v195, v58, v59\n"
"v_max3_f32 v195, v195, v60, v61\n"
"v_max3_f32 v195, v195, v62, v63\n"
"v_mov_b32 v196, v195\n"
"s_nop 1\n"
"v_permlane32_swap_b32 v195, v196\n"
"v_max3_f32 v195, v195, v196, 0\n"
"v_sub_f32 v196, 0, v195\n"
"v_exp_f32 v199, v196\n"
"v_add_f32 v223, v223, v195\n"
"v_mul_f32 v221, v221, v199\n"
"v_sub_f32 v144, 0, v223\n"
"v_sub_f32 v145, 0, v223\n"
"v_sub_f32 v146, 0, v223\n"
"v_sub_f32 v147, 0, v223\n"
"v_sub_f32 v148, 0, v223\n"
"v_sub_f32 v149, 0, v223\n"
"v_sub_f32 v150, 0, v223\n"
"v_sub_f32 v151, 0, v223\n"
"v_sub_f32 v152, 0, v223\n"
"v_sub_f32 v153, 0, v223\n"
"v_sub_f32 v154, 0, v223\n"
"v_sub_f32 v155, 0, v223\n"
"v_sub_f32 v156, 0, v223\n"
"v_sub_f32 v157, 0, v223\n"
"v_sub_f32 v158, 0, v223\n"
"v_sub_f32 v159, 0, v223\n"
"v_sub_f32 v16, v16, v195\n"
"v_sub_f32 v17, v17, v195\n"
"v_sub_f32 v18, v18, v195\n"
"v_sub_f32 v19, v19, v195\n"
"v_sub_f32 v20, v20, v195\n"
"v_sub_f32 v21, v21, v195\n"
"v_sub_f32 v22, v22, v195\n"
"v_sub_f32 v23, v23, v195\n"
"v_sub_f32 v24, v24, v195\n"
"v_sub_f32 v25, v25, v195\n"
"v_sub_f32 v26, v26, v195\n"
"v_sub_f32 v27, v27, v195\n"
"v_sub_f32 v28, v28, v195\n"
"v_sub_f32 v29, v29, v195\n"
"v_sub_f32 v30, v30, v195\n"
"v_sub_f32 v31, v31, v195\n"
"v_sub_f32 v48, v48, v195\n"
"v_sub_f32 v49, v49, v195\n"
"v_sub_f32 v50, v50, v195\n"
"v_sub_f32 v51, v51, v195\n"
"v_sub_f32 v52, v52, v195\n"
"v_sub_f32 v53, v53, v195\n"
"v_sub_f32 v54, v54, v195\n"
"v_sub_f32 v55, v55, v195\n"
"v_sub_f32 v56, v56, v195\n"
"v_sub_f32 v57, v57, v195\n"
"v_sub_f32 v58, v58, v195\n"
"v_sub_f32 v59, v59, v195\n"
"v_sub_f32 v60, v60, v195\n"
"v_sub_f32 v61, v61, v195\n"
"v_sub_f32 v62, v62, v195\n"
"v_sub_f32 v63, v63, v195\n"
"v_sub_f32 v80, v80, v195\n"
"v_sub_f32 v81, v81, v195\n"
"v_sub_f32 v82, v82, v195\n"
"v_sub_f32 v83, v83, v195\n"
"v_sub_f32 v84, v84, v195\n"
"v_sub_f32 v85, v85, v195\n"
"v_sub_f32 v86, v86, v195\n"
"v_sub_f32 v87, v87, v195\n"
"v_sub_f32 v88, v88, v195\n"
"v_sub_f32 v89, v89, v195\n"
"v_sub_f32 v90, v90, v195\n"
"v_sub_f32 v91, v91, v195\n"
"v_sub_f32 v92, v92, v195\n"
"v_sub_f32 v93, v93, v195\n"
"v_sub_f32 v94, v94, v195\n"
"v_sub_f32 v95, v95, v195\n"
"v_sub_f32 v112, v112, v195\n"
"v_sub_f32 v113, v113, v195\n"
"v_sub_f32 v114, v114, v195\n"
"v_sub_f32 v115, v115, v195\n"
"v_sub_f32 v116, v116, v195\n"
"v_sub_f32 v117, v117, v195\n"
"v_sub_f32 v118, v118, v195\n"
"v_sub_f32 v119, v119, v195\n"
"v_sub_f32 v120, v120, v195\n"
"v_sub_f32 v121, v121, v195\n"
"v_sub_f32 v122, v122, v195\n"
"v_sub_f32 v123, v123, v195\n"
"v_sub_f32 v124, v124, v195\n"
"v_sub_f32 v125, v125, v195\n"
"v_sub_f32 v126, v126, v195\n"
"v_sub_f32 v127, v127, v195\n"
"v_accvgpr_read_b32 v200, a16\n"
"v_mul_f32 v200, v200, v199\n"
"v_accvgpr_write_b32 a16, v200\n"
"v_accvgpr_read_b32 v201, a17\n"
"v_mul_f32 v201, v201, v199\n"
"v_accvgpr_write_b32 a17, v201\n"
"v_accvgpr_read_b32 v202, a18\n"
"v_mul_f32 v202, v202, v199\n"
"v_accvgpr_write_b32 a18, v202\n"
"v_accvgpr_read_b32 v203, a19\n"
"v_mul_f32 v203, v203, v199\n"
"v_accvgpr_write_b32 a19, v203\n"
"v_accvgpr_read_b32 v200, a20\n"
"v_mul_f32 v200, v200, v199\n"
"v_accvgpr_write_b32 a20, v200\n"
"v_accvgpr_read_b32 v201, a21\n"
"v_mul_f32 v201, v201, v199\n"
"v_accvgpr_write_b32 a21, v201\n"
"v_accvgpr_read_b32 v202, a22\n"
"v_mul_f32 v202, v202, v199\n"
"v_accvgpr_write_b32 a22, v202\n"
"v_accvgpr_read_b32 v203, a23\n"
"v_mul_f32 v203, v203, v199\n"
"v_accvgpr_write_b32 a23, v203\n"
"v_accvgpr_read_b32 v200, a24\n"
"v_mul_f32 v200, v200, v199\n"
"v_accvgpr_write_b32 a24, v200\n"
"v_accvgpr_read_b32 v201, a25\n"
"v_mul_f32 v201, v201, v199\n"
"v_accvgpr_write_b32 a25, v201\n"
"v_accvgpr_read_b32 v202, a26\n"
"v_mul_f32 v202, v202, v199\n"
"v_accvgpr_write_b32 a26, v202\n"
"v_accvgpr_read_b32 v203, a27\n"
"v_mul_f32 v203, v203, v199\n"
"v_accvgpr_write_b32 a27, v203\n"
"v_accvgpr_read_b32 v200, a28\n"
"v_mul_f32 v200, v200, v199\n"
"v_accvgpr_write_b32 a28, v200\n"
"v_accvgpr_read_b32 v201, a29\n"
"v_mul_f32 v201, v201, v199\n"
"v_accvgpr_write_b32 a29, v201\n"
"v_accvgpr_read_b32 v202, a30\n"
"v_mul_f32 v202, v202, v199\n"
"v_accvgpr_write_b32 a30, v202\n"
"v_accvgpr_read_b32 v203, a31\n"
"v_mul_f32 v203, v203, v199\n"
"v_accvgpr_write_b32 a31, v203\n"
"v_accvgpr_read_b32 v200, a48\n"
"v_mul_f32 v200, v200, v199\n"
"v_accvgpr_write_b32 a48, v200\n"
"v_accvgpr_read_b32 v201, a49\n"
"v_mul_f32 v201, v201, v199\n"
"v_accvgpr_write_b32 a49, v201\n"
"v_accvgpr_read_b32 v202, a50\n"
"v_mul_f32 v202, v202, v199\n"
"v_accvgpr_write_b32 a50, v202\n"
"v_accvgpr_read_b32 v203, a51\n"
"v_mul_f32 v203, v203, v199\n"
"v_accvgpr_write_b32 a51, v203\n"
"v_accvgpr_read_b32 v200, a52\n"
"v_mul_f32 v200, v200, v199\n"
"v_accvgpr_write_b32 a52, v200\n"
"v_accvgpr_read_b32 v201, a53\n"
"v_mul_f32 v201, v201, v199\n"
"v_accvgpr_write_b32 a53, v201\n"
"v_accvgpr_read_b32 v202, a54\n"
"v_mul_f32 v202, v202, v199\n"
"v_accvgpr_write_b32 a54, v202\n"
"v_accvgpr_read_b32 v203, a55\n"
"v_mul_f32 v203, v203, v199\n"
"v_accvgpr_write_b32 a55, v203\n"
"v_accvgpr_read_b32 v200, a56\n"
"v_mul_f32 v200, v200, v199\n"
"v_accvgpr_write_b32 a56, v200\n"
"v_accvgpr_read_b32 v201, a57\n"
"v_mul_f32 v201, v201, v199\n"
"v_accvgpr_write_b32 a57, v201\n"
"v_accvgpr_read_b32 v202, a58\n"
"v_mul_f32 v202, v202, v199\n"
"v_accvgpr_write_b32 a58, v202\n"
"v_accvgpr_read_b32 v203, a59\n"
"v_mul_f32 v203, v203, v199\n"
"v_accvgpr_write_b32 a59, v203\n"
"v_accvgpr_read_b32 v200, a60\n"
"v_mul_f32 v200, v200, v199\n"
"v_accvgpr_write_b32 a60, v200\n"
"v_accvgpr_read_b32 v201, a61\n"
"v_mul_f32 v201, v201, v199\n"
"v_accvgpr_write_b32 a61, v201\n"
"v_accvgpr_read_b32 v202, a62\n"
"v_mul_f32 v202, v202, v199\n"
"v_accvgpr_write_b32 a62, v202\n"
"v_accvgpr_read_b32 v203, a63\n"
"v_mul_f32 v203, v203, v199\n"
"v_accvgpr_write_b32 a63, v203\n"
"v_accvgpr_read_b32 v200, a80\n"
"v_mul_f32 v200, v200, v199\n"
"v_accvgpr_write_b32 a80, v200\n"
"v_accvgpr_read_b32 v201, a81\n"
"v_mul_f32 v201, v201, v199\n"
"v_accvgpr_write_b32 a81, v201\n"
"v_accvgpr_read_b32 v202, a82\n"
"v_mul_f32 v202, v202, v199\n"
"v_accvgpr_write_b32 a82, v202\n"
"v_accvgpr_read_b32 v203, a83\n"
"v_mul_f32 v203, v203, v199\n"
"v_accvgpr_write_b32 a83, v203\n"
"v_accvgpr_read_b32 v200, a84\n"
"v_mul_f32 v200, v200, v199\n"
"v_accvgpr_write_b32 a84, v200\n"
"v_accvgpr_read_b32 v201, a85\n"
"v_mul_f32 v201, v201, v199\n"
"v_accvgpr_write_b32 a85, v201\n"
"v_accvgpr_read_b32 v202, a86\n"
"v_mul_f32 v202, v202, v199\n"
"v_accvgpr_write_b32 a86, v202\n"
"v_accvgpr_read_b32 v203, a87\n"
"v_mul_f32 v203, v203, v199\n"
"v_accvgpr_write_b32 a87, v203\n"
"v_accvgpr_read_b32 v200, a88\n"
"v_mul_f32 v200, v200, v199\n"
"v_accvgpr_write_b32 a88, v200\n"
"v_accvgpr_read_b32 v201, a89\n"
"v_mul_f32 v201, v201, v199\n"
"v_accvgpr_write_b32 a89, v201\n"
"v_accvgpr_read_b32 v202, a90\n"
"v_mul_f32 v202, v202, v199\n"
"v_accvgpr_write_b32 a90, v202\n"
"v_accvgpr_read_b32 v203, a91\n"
"v_mul_f32 v203, v203, v199\n"
"v_accvgpr_write_b32 a91, v203\n"
"v_accvgpr_read_b32 v200, a92\n"
"v_mul_f32 v200, v200, v199\n"
"v_accvgpr_write_b32 a92, v200\n"
"v_accvgpr_read_b32 v201, a93\n"
"v_mul_f32 v201, v201, v199\n"
"v_accvgpr_write_b32 a93, v201\n"
"v_accvgpr_read_b32 v202, a94\n"
"v_mul_f32 v202, v202, v199\n"
"v_accvgpr_write_b32 a94, v202\n"
"v_accvgpr_read_b32 v203, a95\n"
"v_mul_f32 v203, v203, v199\n"
"v_accvgpr_write_b32 a95, v203\n"
"v_accvgpr_read_b32 v200, a112\n"
"v_mul_f32 v200, v200, v199\n"
"v_accvgpr_write_b32 a112, v200\n"
"v_accvgpr_read_b32 v201, a113\n"
"v_mul_f32 v201, v201, v199\n"
"v_accvgpr_write_b32 a113, v201\n"
"v_accvgpr_read_b32 v202, a114\n"
"v_mul_f32 v202, v202, v199\n"
"v_accvgpr_write_b32 a114, v202\n"
"v_accvgpr_read_b32 v203, a115\n"
"v_mul_f32 v203, v203, v199\n"
"v_accvgpr_write_b32 a115, v203\n"
"v_accvgpr_read_b32 v200, a116\n"
"v_mul_f32 v200, v200, v199\n"
"v_accvgpr_write_b32 a116, v200\n"
"v_accvgpr_read_b32 v201, a117\n"
"v_mul_f32 v201, v201, v199\n"
"v_accvgpr_write_b32 a117, v201\n"
"v_accvgpr_read_b32 v202, a118\n"
"PG27_%=:\n"
"s_nop 0\n"
"v_mul_f32 v202, v202, v199\n"
"v_accvgpr_write_b32 a118, v202\n"
"v_accvgpr_read_b32 v203, a119\n"
"v_mul_f32 v203, v203, v199\n"
"v_accvgpr_write_b32 a119, v203\n"
"v_accvgpr_read_b32 v200, a120\n"
"v_mul_f32 v200, v200, v199\n"
"v_accvgpr_write_b32 a120, v200\n"
"v_accvgpr_read_b32 v201, a121\n"
"v_mul_f32 v201, v201, v199\n"
"v_accvgpr_write_b32 a121, v201\n"
"v_accvgpr_read_b32 v202, a122\n"
"v_mul_f32 v202, v202, v199\n"
"v_accvgpr_write_b32 a122, v202\n"
"v_accvgpr_read_b32 v203, a123\n"
"v_mul_f32 v203, v203, v199\n"
"v_accvgpr_write_b32 a123, v203\n"
"v_accvgpr_read_b32 v200, a124\n"
"v_mul_f32 v200, v200, v199\n"
"v_accvgpr_write_b32 a124, v200\n"
"v_accvgpr_read_b32 v201, a125\n"
"v_mul_f32 v201, v201, v199\n"
"v_accvgpr_write_b32 a125, v201\n"
"v_accvgpr_read_b32 v202, a126\n"
"v_mul_f32 v202, v202, v199\n"
"v_accvgpr_write_b32 a126, v202\n"
"v_accvgpr_read_b32 v203, a127\n"
"v_mul_f32 v203, v203, v199\n"
"v_accvgpr_write_b32 a127, v203\n"
"v_exp_f32 v208, v0\n"
"v_exp_f32 v209, v1\n"
"v_exp_f32 v210, v2\n"
"v_mov_b32 v214, v208\n"
"v_exp_f32 v211, v3\n"
"v_mov_b32 v215, v209\n"
"v_cvt_pk_bf16_f32 v160, v208, v209\n"
"v_exp_f32 v212, v4\n"
"v_add_f32 v214, v214, v210\n"
"v_exp_f32 v213, v5\n"
"v_add_f32 v215, v215, v211\n"
"v_cvt_pk_bf16_f32 v161, v210, v211\n"
"v_exp_f32 v208, v6\n"
"v_add_f32 v214, v214, v212\n"
"v_exp_f32 v209, v7\n"
"v_add_f32 v215, v215, v213\n"
"v_cvt_pk_bf16_f32 v162, v212, v213\n"
"v_exp_f32 v210, v16\n"
"v_add_f32 v214, v214, v208\n"
"v_exp_f32 v211, v17\n"
"v_add_f32 v215, v215, v209\n"
"v_cvt_pk_bf16_f32 v163, v208, v209\n"
"v_exp_f32 v212, v18\n"
"v_mov_b32 v216, v210\n"
"v_exp_f32 v213, v19\n"
"v_mov_b32 v217, v211\n"
"v_cvt_pk_bf16_f32 v164, v210, v211\n"
"v_exp_f32 v208, v20\n"
"v_add_f32 v216, v216, v212\n"
"v_exp_f32 v209, v21\n"
"v_add_f32 v217, v217, v213\n"
"v_cvt_pk_bf16_f32 v165, v212, v213\n"
"v_exp_f32 v210, v22\n"
"v_add_f32 v216, v216, v208\n"
"v_exp_f32 v211, v23\n"
"v_add_f32 v217, v217, v209\n"
"v_cvt_pk_bf16_f32 v166, v208, v209\n"
"v_exp_f32 v212, v8\n"
"v_add_f32 v216, v216, v210\n"
"v_exp_f32 v213, v9\n"
"v_add_f32 v217, v217, v211\n"
"v_cvt_pk_bf16_f32 v167, v210, v211\n"
"v_exp_f32 v208, v10\n"
"v_add_f32 v214, v214, v212\n"
"v_exp_f32 v209, v11\n"
"v_add_f32 v215, v215, v213\n"
"v_cvt_pk_bf16_f32 v168, v212, v213\n"
"v_exp_f32 v210, v12\n"
"v_add_f32 v214, v214, v208\n"
"v_exp_f32 v211, v13\n"
"v_add_f32 v215, v215, v209\n"
"v_cvt_pk_bf16_f32 v169, v208, v209\n"
"v_exp_f32 v212, v14\n"
"v_add_f32 v214, v214, v210\n"
"v_exp_f32 v213, v15\n"
"v_add_f32 v215, v215, v211\n"
"v_cvt_pk_bf16_f32 v170, v210, v211\n"
"v_exp_f32 v208, v24\n"
"v_add_f32 v214, v214, v212\n"
"v_exp_f32 v209, v25\n"
"v_add_f32 v215, v215, v213\n"
"v_cvt_pk_bf16_f32 v171, v212, v213\n"
"v_exp_f32 v210, v26\n"
"v_add_f32 v216, v216, v208\n"
"v_exp_f32 v211, v27\n"
"v_add_f32 v217, v217, v209\n"
"v_cvt_pk_bf16_f32 v172, v208, v209\n"
"v_exp_f32 v212, v28\n"
"v_add_f32 v216, v216, v210\n"
"v_exp_f32 v213, v29\n"
"v_add_f32 v217, v217, v211\n"
"v_cvt_pk_bf16_f32 v173, v210, v211\n"
"v_exp_f32 v208, v30\n"
"v_add_f32 v216, v216, v212\n"
"v_exp_f32 v209, v31\n"
"v_add_f32 v217, v217, v213\n"
"v_cvt_pk_bf16_f32 v174, v212, v213\n"
"v_add_f32 v216, v216, v208\n"
"v_add_f32 v217, v217, v209\n"
"v_cvt_pk_bf16_f32 v175, v208, v209\n"
"v_exp_f32 v208, v32\n"
"v_exp_f32 v209, v33\n"
"v_exp_f32 v210, v34\n"
"v_add_f32 v214, v214, v208\n"
"v_exp_f32 v211, v35\n"
"v_add_f32 v215, v215, v209\n"
"v_cvt_pk_bf16_f32 v192, v208, v209\n"
"v_exp_f32 v212, v36\n"
"v_add_f32 v214, v214, v210\n"
"v_exp_f32 v213, v37\n"
"v_add_f32 v215, v215, v211\n"
"v_cvt_pk_bf16_f32 v193, v210, v211\n"
"v_exp_f32 v208, v38\n"
"v_add_f32 v214, v214, v212\n"
"v_exp_f32 v209, v39\n"
"v_add_f32 v215, v215, v213\n"
"v_cvt_pk_bf16_f32 v194, v212, v213\n"
"v_exp_f32 v210, v48\n"
"v_add_f32 v214, v214, v208\n"
"v_exp_f32 v211, v49\n"
"v_add_f32 v215, v215, v209\n"
"v_cvt_pk_bf16_f32 v195, v208, v209\n"
"v_exp_f32 v212, v50\n"
"v_add_f32 v216, v216, v210\n"
"v_exp_f32 v213, v51\n"
"v_add_f32 v217, v217, v211\n"
"v_cvt_pk_bf16_f32 v196, v210, v211\n"
"v_exp_f32 v208, v52\n"
"v_add_f32 v216, v216, v212\n"
"v_exp_f32 v209, v53\n"
"v_add_f32 v217, v217, v213\n"
"v_cvt_pk_bf16_f32 v197, v212, v213\n"
"v_exp_f32 v210, v54\n"
"v_add_f32 v216, v216, v208\n"
"v_exp_f32 v211, v55\n"
"v_add_f32 v217, v217, v209\n"
"v_cvt_pk_bf16_f32 v198, v208, v209\n"
"v_exp_f32 v212, v40\n"
"v_add_f32 v216, v216, v210\n"
"v_exp_f32 v213, v41\n"
"v_add_f32 v217, v217, v211\n"
"v_cvt_pk_bf16_f32 v199, v210, v211\n"
"v_exp_f32 v208, v42\n"
"v_add_f32 v214, v214, v212\n"
"v_exp_f32 v209, v43\n"
"v_add_f32 v215, v215, v213\n"
"v_cvt_pk_bf16_f32 v200, v212, v213\n"
"v_exp_f32 v210, v44\n"
"v_add_f32 v214, v214, v208\n"
"v_exp_f32 v211, v45\n"
"v_add_f32 v215, v215, v209\n"
"v_cvt_pk_bf16_f32 v201, v208, v209\n"
"v_exp_f32 v212, v46\n"
"v_add_f32 v214, v214, v210\n"
"v_exp_f32 v213, v47\n"
"v_add_f32 v215, v215, v211\n"
"v_cvt_pk_bf16_f32 v202, v210, v211\n"
"v_exp_f32 v208, v56\n"
"v_add_f32 v214, v214, v212\n"
"v_exp_f32 v209, v57\n"
"v_add_f32 v215, v215, v213\n"
"v_cvt_pk_bf16_f32 v203, v212, v213\n"
"v_exp_f32 v210, v58\n"
"v_add_f32 v216, v216, v208\n"
"v_exp_f32 v211, v59\n"
"v_add_f32 v217, v217, v209\n"
"v_cvt_pk_bf16_f32 v204, v208, v209\n"
"v_exp_f32 v212, v60\n"
"v_add_f32 v216, v216, v210\n"
"v_exp_f32 v213, v61\n"
"v_add_f32 v217, v217, v211\n"
"v_cvt_pk_bf16_f32 v205, v210, v211\n"
"v_exp_f32 v208, v62\n"
"v_add_f32 v216, v216, v212\n"
"v_exp_f32 v209, v63\n"
"v_add_f32 v217, v217, v213\n"
"v_cvt_pk_bf16_f32 v206, v212, v213\n"
"v_add_f32 v216, v216, v208\n"
"v_add_f32 v217, v217, v209\n"
"v_cvt_pk_bf16_f32 v207, v208, v209\n"
"v_add_f32 v218, v214, v215\n"
"v_add_f32 v219, v216, v217\n"
"s_nop 3\n"
"s_cmp_eq_u32 s41, 0\n"
"s_cbranch_scc1 RET_FINAL_1_%=\n"
"s_cmp_eq_u32 s41, 5\n"
"s_cbranch_scc1 RET_MID_1_%=\n"
"s_cmp_eq_u32 s41, 6\n"
"s_cbranch_scc1 RET_MIDM_1_%=\n"
"s_cmp_eq_u32 s41, 7\n"
"s_cbranch_scc1 RET_MIDR_1_%=\n"
"s_branch RET_DRAINA_1_%=\n"
"RARE_1_%=:\n"
"s_nop 15\n"
"s_nop 15\n"
"v_max_f32 v194, v64, v65\n"
"v_max3_f32 v194, v194, v66, v67\n"
"v_max3_f32 v194, v194, v68, v69\n"
"v_max3_f32 v194, v194, v70, v71\n"
"v_max3_f32 v194, v194, v72, v73\n"
"v_max3_f32 v194, v194, v74, v75\n"
"v_max3_f32 v194, v194, v76, v77\n"
"v_max3_f32 v194, v194, v78, v79\n"
"v_max3_f32 v194, v194, v96, v97\n"
"v_max3_f32 v194, v194, v98, v99\n"
"v_max3_f32 v194, v194, v100, v101\n"
"v_max3_f32 v194, v194, v102, v103\n"
"v_max3_f32 v194, v194, v104, v105\n"
"v_max3_f32 v194, v194, v106, v107\n"
"v_max3_f32 v194, v194, v108, v109\n"
"v_max3_f32 v194, v194, v110, v111\n"
"v_mov_b32 v196, v194\n"
"s_nop 1\n"
"v_permlane32_swap_b32 v194, v196\n"
"v_max3_f32 v194, v194, v196, 0\n"
"v_sub_f32 v196, 0, v194\n"
"v_exp_f32 v198, v196\n"
"v_add_f32 v222, v222, v194\n"
"v_mul_f32 v220, v220, v198\n"
"v_sub_f32 v128, 0, v222\n"
"v_sub_f32 v129, 0, v222\n"
"v_sub_f32 v130, 0, v222\n"
"v_sub_f32 v131, 0, v222\n"
"v_sub_f32 v132, 0, v222\n"
"v_sub_f32 v133, 0, v222\n"
"v_sub_f32 v134, 0, v222\n"
"v_sub_f32 v135, 0, v222\n"
"v_sub_f32 v136, 0, v222\n"
"v_sub_f32 v137, 0, v222\n"
"v_sub_f32 v138, 0, v222\n"
"v_sub_f32 v139, 0, v222\n"
"v_sub_f32 v140, 0, v222\n"
"v_sub_f32 v141, 0, v222\n"
"v_sub_f32 v142, 0, v222\n"
"v_sub_f32 v143, 0, v222\n"
"v_sub_f32 v0, v0, v194\n"
"v_sub_f32 v1, v1, v194\n"
"v_sub_f32 v2, v2, v194\n"
"v_sub_f32 v3, v3, v194\n"
"v_sub_f32 v4, v4, v194\n"
"v_sub_f32 v5, v5, v194\n"
"v_sub_f32 v6, v6, v194\n"
"v_sub_f32 v7, v7, v194\n"
"v_sub_f32 v8, v8, v194\n"
"v_sub_f32 v9, v9, v194\n"
"v_sub_f32 v10, v10, v194\n"
"v_sub_f32 v11, v11, v194\n"
"v_sub_f32 v12, v12, v194\n"
"v_sub_f32 v13, v13, v194\n"
"v_sub_f32 v14, v14, v194\n"
"v_sub_f32 v15, v15, v194\n"
"v_sub_f32 v32, v32, v194\n"
"v_sub_f32 v33, v33, v194\n"
"v_sub_f32 v34, v34, v194\n"
"v_sub_f32 v35, v35, v194\n"
"v_sub_f32 v36, v36, v194\n"
"v_sub_f32 v37, v37, v194\n"
"v_sub_f32 v38, v38, v194\n"
"v_sub_f32 v39, v39, v194\n"
"v_sub_f32 v40, v40, v194\n"
"v_sub_f32 v41, v41, v194\n"
"v_sub_f32 v42, v42, v194\n"
"v_sub_f32 v43, v43, v194\n"
"v_sub_f32 v44, v44, v194\n"
"v_sub_f32 v45, v45, v194\n"
"v_sub_f32 v46, v46, v194\n"
"v_sub_f32 v47, v47, v194\n"
"v_sub_f32 v64, v64, v194\n"
"v_sub_f32 v65, v65, v194\n"
"v_sub_f32 v66, v66, v194\n"
"v_sub_f32 v67, v67, v194\n"
"v_sub_f32 v68, v68, v194\n"
"v_sub_f32 v69, v69, v194\n"
"v_sub_f32 v70, v70, v194\n"
"v_sub_f32 v71, v71, v194\n"
"v_sub_f32 v72, v72, v194\n"
"v_sub_f32 v73, v73, v194\n"
"v_sub_f32 v74, v74, v194\n"
"v_sub_f32 v75, v75, v194\n"
"v_sub_f32 v76, v76, v194\n"
"v_sub_f32 v77, v77, v194\n"
"v_sub_f32 v78, v78, v194\n"
"v_sub_f32 v79, v79, v194\n"
"v_sub_f32 v96, v96, v194\n"
"v_sub_f32 v97, v97, v194\n"
"v_sub_f32 v98, v98, v194\n"
"v_sub_f32 v99, v99, v194\n"
"v_sub_f32 v100, v100, v194\n"
"v_sub_f32 v101, v101, v194\n"
"v_sub_f32 v102, v102, v194\n"
"v_sub_f32 v103, v103, v194\n"
"v_sub_f32 v104, v104, v194\n"
"v_sub_f32 v105, v105, v194\n"
"v_sub_f32 v106, v106, v194\n"
"v_sub_f32 v107, v107, v194\n"
"v_sub_f32 v108, v108, v194\n"
"v_sub_f32 v109, v109, v194\n"
"v_sub_f32 v110, v110, v194\n"
"v_sub_f32 v111, v111, v194\n"
"v_accvgpr_read_b32 v200, a0\n"
"v_mul_f32 v200, v200, v198\n"
"v_accvgpr_write_b32 a0, v200\n"
"v_accvgpr_read_b32 v201, a1\n"
"v_mul_f32 v201, v201, v198\n"
"v_accvgpr_write_b32 a1, v201\n"
"v_accvgpr_read_b32 v202, a2\n"
"v_mul_f32 v202, v202, v198\n"
"v_accvgpr_write_b32 a2, v202\n"
"v_accvgpr_read_b32 v203, a3\n"
"v_mul_f32 v203, v203, v198\n"
"v_accvgpr_write_b32 a3, v203\n"
"v_accvgpr_read_b32 v200, a4\n"
"v_mul_f32 v200, v200, v198\n"
"v_accvgpr_write_b32 a4, v200\n"
"v_accvgpr_read_b32 v201, a5\n"
"v_mul_f32 v201, v201, v198\n"
"v_accvgpr_write_b32 a5, v201\n"
"v_accvgpr_read_b32 v202, a6\n"
"v_mul_f32 v202, v202, v198\n"
"v_accvgpr_write_b32 a6, v202\n"
"v_accvgpr_read_b32 v203, a7\n"
"v_mul_f32 v203, v203, v198\n"
"v_accvgpr_write_b32 a7, v203\n"
"v_accvgpr_read_b32 v200, a8\n"
"v_mul_f32 v200, v200, v198\n"
"v_accvgpr_write_b32 a8, v200\n"
"v_accvgpr_read_b32 v201, a9\n"
"v_mul_f32 v201, v201, v198\n"
"v_accvgpr_write_b32 a9, v201\n"
"v_accvgpr_read_b32 v202, a10\n"
"v_mul_f32 v202, v202, v198\n"
"v_accvgpr_write_b32 a10, v202\n"
"v_accvgpr_read_b32 v203, a11\n"
"v_mul_f32 v203, v203, v198\n"
"v_accvgpr_write_b32 a11, v203\n"
"v_accvgpr_read_b32 v200, a12\n"
"v_mul_f32 v200, v200, v198\n"
"v_accvgpr_write_b32 a12, v200\n"
"v_accvgpr_read_b32 v201, a13\n"
"v_mul_f32 v201, v201, v198\n"
"v_accvgpr_write_b32 a13, v201\n"
"v_accvgpr_read_b32 v202, a14\n"
"v_mul_f32 v202, v202, v198\n"
"v_accvgpr_write_b32 a14, v202\n"
"v_accvgpr_read_b32 v203, a15\n"
"v_mul_f32 v203, v203, v198\n"
"v_accvgpr_write_b32 a15, v203\n"
"v_accvgpr_read_b32 v200, a32\n"
"v_mul_f32 v200, v200, v198\n"
"v_accvgpr_write_b32 a32, v200\n"
"v_accvgpr_read_b32 v201, a33\n"
"v_mul_f32 v201, v201, v198\n"
"v_accvgpr_write_b32 a33, v201\n"
"v_accvgpr_read_b32 v202, a34\n"
"v_mul_f32 v202, v202, v198\n"
"v_accvgpr_write_b32 a34, v202\n"
"v_accvgpr_read_b32 v203, a35\n"
"v_mul_f32 v203, v203, v198\n"
"v_accvgpr_write_b32 a35, v203\n"
"v_accvgpr_read_b32 v200, a36\n"
"v_mul_f32 v200, v200, v198\n"
"v_accvgpr_write_b32 a36, v200\n"
"v_accvgpr_read_b32 v201, a37\n"
"v_mul_f32 v201, v201, v198\n"
"v_accvgpr_write_b32 a37, v201\n"
"v_accvgpr_read_b32 v202, a38\n"
"v_mul_f32 v202, v202, v198\n"
"v_accvgpr_write_b32 a38, v202\n"
"v_accvgpr_read_b32 v203, a39\n"
"v_mul_f32 v203, v203, v198\n"
"v_accvgpr_write_b32 a39, v203\n"
"v_accvgpr_read_b32 v200, a40\n"
"v_mul_f32 v200, v200, v198\n"
"v_accvgpr_write_b32 a40, v200\n"
"v_accvgpr_read_b32 v201, a41\n"
"v_mul_f32 v201, v201, v198\n"
"v_accvgpr_write_b32 a41, v201\n"
"v_accvgpr_read_b32 v202, a42\n"
"v_mul_f32 v202, v202, v198\n"
"v_accvgpr_write_b32 a42, v202\n"
"v_accvgpr_read_b32 v203, a43\n"
"v_mul_f32 v203, v203, v198\n"
"v_accvgpr_write_b32 a43, v203\n"
"v_accvgpr_read_b32 v200, a44\n"
"v_mul_f32 v200, v200, v198\n"
"v_accvgpr_write_b32 a44, v200\n"
"v_accvgpr_read_b32 v201, a45\n"
"v_mul_f32 v201, v201, v198\n"
"v_accvgpr_write_b32 a45, v201\n"
"v_accvgpr_read_b32 v202, a46\n"
"v_mul_f32 v202, v202, v198\n"
"v_accvgpr_write_b32 a46, v202\n"
"v_accvgpr_read_b32 v203, a47\n"
"v_mul_f32 v203, v203, v198\n"
"v_accvgpr_write_b32 a47, v203\n"
"v_accvgpr_read_b32 v200, a64\n"
"v_mul_f32 v200, v200, v198\n"
"v_accvgpr_write_b32 a64, v200\n"
"v_accvgpr_read_b32 v201, a65\n"
"v_mul_f32 v201, v201, v198\n"
"v_accvgpr_write_b32 a65, v201\n"
"v_accvgpr_read_b32 v202, a66\n"
"v_mul_f32 v202, v202, v198\n"
"v_accvgpr_write_b32 a66, v202\n"
"v_accvgpr_read_b32 v203, a67\n"
"v_mul_f32 v203, v203, v198\n"
"v_accvgpr_write_b32 a67, v203\n"
"v_accvgpr_read_b32 v200, a68\n"
"v_mul_f32 v200, v200, v198\n"
"v_accvgpr_write_b32 a68, v200\n"
"v_accvgpr_read_b32 v201, a69\n"
"v_mul_f32 v201, v201, v198\n"
"v_accvgpr_write_b32 a69, v201\n"
"v_accvgpr_read_b32 v202, a70\n"
"v_mul_f32 v202, v202, v198\n"
"v_accvgpr_write_b32 a70, v202\n"
"v_accvgpr_read_b32 v203, a71\n"
"v_mul_f32 v203, v203, v198\n"
"v_accvgpr_write_b32 a71, v203\n"
"v_accvgpr_read_b32 v200, a72\n"
"v_mul_f32 v200, v200, v198\n"
"v_accvgpr_write_b32 a72, v200\n"
"v_accvgpr_read_b32 v201, a73\n"
"v_mul_f32 v201, v201, v198\n"
"v_accvgpr_write_b32 a73, v201\n"
"v_accvgpr_read_b32 v202, a74\n"
"v_mul_f32 v202, v202, v198\n"
"v_accvgpr_write_b32 a74, v202\n"
"v_accvgpr_read_b32 v203, a75\n"
"v_mul_f32 v203, v203, v198\n"
"v_accvgpr_write_b32 a75, v203\n"
"v_accvgpr_read_b32 v200, a76\n"
"v_mul_f32 v200, v200, v198\n"
"v_accvgpr_write_b32 a76, v200\n"
"v_accvgpr_read_b32 v201, a77\n"
"v_mul_f32 v201, v201, v198\n"
"v_accvgpr_write_b32 a77, v201\n"
"v_accvgpr_read_b32 v202, a78\n"
"v_mul_f32 v202, v202, v198\n"
"v_accvgpr_write_b32 a78, v202\n"
"v_accvgpr_read_b32 v203, a79\n"
"v_mul_f32 v203, v203, v198\n"
"v_accvgpr_write_b32 a79, v203\n"
"v_accvgpr_read_b32 v200, a96\n"
"v_mul_f32 v200, v200, v198\n"
"v_accvgpr_write_b32 a96, v200\n"
"v_accvgpr_read_b32 v201, a97\n"
"v_mul_f32 v201, v201, v198\n"
"v_accvgpr_write_b32 a97, v201\n"
"v_accvgpr_read_b32 v202, a98\n"
"v_mul_f32 v202, v202, v198\n"
"v_accvgpr_write_b32 a98, v202\n"
"v_accvgpr_read_b32 v203, a99\n"
"v_mul_f32 v203, v203, v198\n"
"v_accvgpr_write_b32 a99, v203\n"
"v_accvgpr_read_b32 v200, a100\n"
"v_mul_f32 v200, v200, v198\n"
"v_accvgpr_write_b32 a100, v200\n"
"v_accvgpr_read_b32 v201, a101\n"
"v_mul_f32 v201, v201, v198\n"
"v_accvgpr_write_b32 a101, v201\n"
"v_accvgpr_read_b32 v202, a102\n"
"v_mul_f32 v202, v202, v198\n"
"v_accvgpr_write_b32 a102, v202\n"
"v_accvgpr_read_b32 v203, a103\n"
"v_mul_f32 v203, v203, v198\n"
"v_accvgpr_write_b32 a103, v203\n"
"v_accvgpr_read_b32 v200, a104\n"
"v_mul_f32 v200, v200, v198\n"
"v_accvgpr_write_b32 a104, v200\n"
"v_accvgpr_read_b32 v201, a105\n"
"v_mul_f32 v201, v201, v198\n"
"v_accvgpr_write_b32 a105, v201\n"
"v_accvgpr_read_b32 v202, a106\n"
"v_mul_f32 v202, v202, v198\n"
"v_accvgpr_write_b32 a106, v202\n"
"v_accvgpr_read_b32 v203, a107\n"
"v_mul_f32 v203, v203, v198\n"
"v_accvgpr_write_b32 a107, v203\n"
"v_accvgpr_read_b32 v200, a108\n"
"v_mul_f32 v200, v200, v198\n"
"v_accvgpr_write_b32 a108, v200\n"
"v_accvgpr_read_b32 v201, a109\n"
"v_mul_f32 v201, v201, v198\n"
"v_accvgpr_write_b32 a109, v201\n"
"v_accvgpr_read_b32 v202, a110\n"
"v_mul_f32 v202, v202, v198\n"
"v_accvgpr_write_b32 a110, v202\n"
"v_accvgpr_read_b32 v203, a111\n"
"v_mul_f32 v203, v203, v198\n"
"v_accvgpr_write_b32 a111, v203\n"
"v_max_f32 v195, v80, v81\n"
"v_max3_f32 v195, v195, v82, v83\n"
"v_max3_f32 v195, v195, v84, v85\n"
"v_max3_f32 v195, v195, v86, v87\n"
"v_max3_f32 v195, v195, v88, v89\n"
"v_max3_f32 v195, v195, v90, v91\n"
"v_max3_f32 v195, v195, v92, v93\n"
"v_max3_f32 v195, v195, v94, v95\n"
"v_max3_f32 v195, v195, v112, v113\n"
"v_max3_f32 v195, v195, v114, v115\n"
"v_max3_f32 v195, v195, v116, v117\n"
"v_max3_f32 v195, v195, v118, v119\n"
"v_max3_f32 v195, v195, v120, v121\n"
"v_max3_f32 v195, v195, v122, v123\n"
"v_max3_f32 v195, v195, v124, v125\n"
"v_max3_f32 v195, v195, v126, v127\n"
"v_mov_b32 v196, v195\n"
"s_nop 1\n"
"v_permlane32_swap_b32 v195, v196\n"
"v_max3_f32 v195, v195, v196, 0\n"
"v_sub_f32 v196, 0, v195\n"
"v_exp_f32 v199, v196\n"
"v_add_f32 v223, v223, v195\n"
"v_mul_f32 v221, v221, v199\n"
"v_sub_f32 v144, 0, v223\n"
"v_sub_f32 v145, 0, v223\n"
"v_sub_f32 v146, 0, v223\n"
"v_sub_f32 v147, 0, v223\n"
"v_sub_f32 v148, 0, v223\n"
"v_sub_f32 v149, 0, v223\n"
"v_sub_f32 v150, 0, v223\n"
"v_sub_f32 v151, 0, v223\n"
"v_sub_f32 v152, 0, v223\n"
"v_sub_f32 v153, 0, v223\n"
"v_sub_f32 v154, 0, v223\n"
"v_sub_f32 v155, 0, v223\n"
"v_sub_f32 v156, 0, v223\n"
"v_sub_f32 v157, 0, v223\n"
"v_sub_f32 v158, 0, v223\n"
"v_sub_f32 v159, 0, v223\n"
"v_sub_f32 v16, v16, v195\n"
"v_sub_f32 v17, v17, v195\n"
"v_sub_f32 v18, v18, v195\n"
"v_sub_f32 v19, v19, v195\n"
"v_sub_f32 v20, v20, v195\n"
"v_sub_f32 v21, v21, v195\n"
"v_sub_f32 v22, v22, v195\n"
"v_sub_f32 v23, v23, v195\n"
"v_sub_f32 v24, v24, v195\n"
"v_sub_f32 v25, v25, v195\n"
"v_sub_f32 v26, v26, v195\n"
"v_sub_f32 v27, v27, v195\n"
"v_sub_f32 v28, v28, v195\n"
"v_sub_f32 v29, v29, v195\n"
"v_sub_f32 v30, v30, v195\n"
"v_sub_f32 v31, v31, v195\n"
"v_sub_f32 v48, v48, v195\n"
"v_sub_f32 v49, v49, v195\n"
"v_sub_f32 v50, v50, v195\n"
"v_sub_f32 v51, v51, v195\n"
"v_sub_f32 v52, v52, v195\n"
"v_sub_f32 v53, v53, v195\n"
"v_sub_f32 v54, v54, v195\n"
"v_sub_f32 v55, v55, v195\n"
"v_sub_f32 v56, v56, v195\n"
"v_sub_f32 v57, v57, v195\n"
"v_sub_f32 v58, v58, v195\n"
"v_sub_f32 v59, v59, v195\n"
"v_sub_f32 v60, v60, v195\n"
"v_sub_f32 v61, v61, v195\n"
"v_sub_f32 v62, v62, v195\n"
"v_sub_f32 v63, v63, v195\n"
"v_sub_f32 v80, v80, v195\n"
"v_sub_f32 v81, v81, v195\n"
"v_sub_f32 v82, v82, v195\n"
"v_sub_f32 v83, v83, v195\n"
"v_sub_f32 v84, v84, v195\n"
"v_sub_f32 v85, v85, v195\n"
"v_sub_f32 v86, v86, v195\n"
"v_sub_f32 v87, v87, v195\n"
"v_sub_f32 v88, v88, v195\n"
"v_sub_f32 v89, v89, v195\n"
"v_sub_f32 v90, v90, v195\n"
"v_sub_f32 v91, v91, v195\n"
"v_sub_f32 v92, v92, v195\n"
"v_sub_f32 v93, v93, v195\n"
"v_sub_f32 v94, v94, v195\n"
"v_sub_f32 v95, v95, v195\n"
"v_sub_f32 v112, v112, v195\n"
"v_sub_f32 v113, v113, v195\n"
"v_sub_f32 v114, v114, v195\n"
"v_sub_f32 v115, v115, v195\n"
"v_sub_f32 v116, v116, v195\n"
"v_sub_f32 v117, v117, v195\n"
"v_sub_f32 v118, v118, v195\n"
"v_sub_f32 v119, v119, v195\n"
"v_sub_f32 v120, v120, v195\n"
"v_sub_f32 v121, v121, v195\n"
"v_sub_f32 v122, v122, v195\n"
"v_sub_f32 v123, v123, v195\n"
"v_sub_f32 v124, v124, v195\n"
"v_sub_f32 v125, v125, v195\n"
"v_sub_f32 v126, v126, v195\n"
"v_sub_f32 v127, v127, v195\n"
"v_accvgpr_read_b32 v200, a16\n"
"v_mul_f32 v200, v200, v199\n"
"v_accvgpr_write_b32 a16, v200\n"
"v_accvgpr_read_b32 v201, a17\n"
"v_mul_f32 v201, v201, v199\n"
"v_accvgpr_write_b32 a17, v201\n"
"v_accvgpr_read_b32 v202, a18\n"
"v_mul_f32 v202, v202, v199\n"
"v_accvgpr_write_b32 a18, v202\n"
"v_accvgpr_read_b32 v203, a19\n"
"v_mul_f32 v203, v203, v199\n"
"v_accvgpr_write_b32 a19, v203\n"
"v_accvgpr_read_b32 v200, a20\n"
"v_mul_f32 v200, v200, v199\n"
"v_accvgpr_write_b32 a20, v200\n"
"v_accvgpr_read_b32 v201, a21\n"
"v_mul_f32 v201, v201, v199\n"
"v_accvgpr_write_b32 a21, v201\n"
"v_accvgpr_read_b32 v202, a22\n"
"v_mul_f32 v202, v202, v199\n"
"v_accvgpr_write_b32 a22, v202\n"
"v_accvgpr_read_b32 v203, a23\n"
"v_mul_f32 v203, v203, v199\n"
"v_accvgpr_write_b32 a23, v203\n"
"v_accvgpr_read_b32 v200, a24\n"
"v_mul_f32 v200, v200, v199\n"
"v_accvgpr_write_b32 a24, v200\n"
"v_accvgpr_read_b32 v201, a25\n"
"v_mul_f32 v201, v201, v199\n"
"v_accvgpr_write_b32 a25, v201\n"
"v_accvgpr_read_b32 v202, a26\n"
"v_mul_f32 v202, v202, v199\n"
"v_accvgpr_write_b32 a26, v202\n"
"v_accvgpr_read_b32 v203, a27\n"
"v_mul_f32 v203, v203, v199\n"
"v_accvgpr_write_b32 a27, v203\n"
"v_accvgpr_read_b32 v200, a28\n"
"v_mul_f32 v200, v200, v199\n"
"v_accvgpr_write_b32 a28, v200\n"
"v_accvgpr_read_b32 v201, a29\n"
"v_mul_f32 v201, v201, v199\n"
"v_accvgpr_write_b32 a29, v201\n"
"v_accvgpr_read_b32 v202, a30\n"
"v_mul_f32 v202, v202, v199\n"
"v_accvgpr_write_b32 a30, v202\n"
"v_accvgpr_read_b32 v203, a31\n"
"v_mul_f32 v203, v203, v199\n"
"v_accvgpr_write_b32 a31, v203\n"
"v_accvgpr_read_b32 v200, a48\n"
"v_mul_f32 v200, v200, v199\n"
"v_accvgpr_write_b32 a48, v200\n"
"v_accvgpr_read_b32 v201, a49\n"
"v_mul_f32 v201, v201, v199\n"
"v_accvgpr_write_b32 a49, v201\n"
"v_accvgpr_read_b32 v202, a50\n"
"v_mul_f32 v202, v202, v199\n"
"v_accvgpr_write_b32 a50, v202\n"
"v_accvgpr_read_b32 v203, a51\n"
"v_mul_f32 v203, v203, v199\n"
"v_accvgpr_write_b32 a51, v203\n"
"v_accvgpr_read_b32 v200, a52\n"
"v_mul_f32 v200, v200, v199\n"
"v_accvgpr_write_b32 a52, v200\n"
"v_accvgpr_read_b32 v201, a53\n"
"v_mul_f32 v201, v201, v199\n"
"v_accvgpr_write_b32 a53, v201\n"
"v_accvgpr_read_b32 v202, a54\n"
"v_mul_f32 v202, v202, v199\n"
"v_accvgpr_write_b32 a54, v202\n"
"v_accvgpr_read_b32 v203, a55\n"
"v_mul_f32 v203, v203, v199\n"
"v_accvgpr_write_b32 a55, v203\n"
"v_accvgpr_read_b32 v200, a56\n"
"v_mul_f32 v200, v200, v199\n"
"v_accvgpr_write_b32 a56, v200\n"
"v_accvgpr_read_b32 v201, a57\n"
"v_mul_f32 v201, v201, v199\n"
"v_accvgpr_write_b32 a57, v201\n"
"v_accvgpr_read_b32 v202, a58\n"
"v_mul_f32 v202, v202, v199\n"
"v_accvgpr_write_b32 a58, v202\n"
"v_accvgpr_read_b32 v203, a59\n"
"v_mul_f32 v203, v203, v199\n"
"v_accvgpr_write_b32 a59, v203\n"
"v_accvgpr_read_b32 v200, a60\n"
"v_mul_f32 v200, v200, v199\n"
"v_accvgpr_write_b32 a60, v200\n"
"v_accvgpr_read_b32 v201, a61\n"
"v_mul_f32 v201, v201, v199\n"
"v_accvgpr_write_b32 a61, v201\n"
"v_accvgpr_read_b32 v202, a62\n"
"v_mul_f32 v202, v202, v199\n"
"v_accvgpr_write_b32 a62, v202\n"
"v_accvgpr_read_b32 v203, a63\n"
"v_mul_f32 v203, v203, v199\n"
"v_accvgpr_write_b32 a63, v203\n"
"v_accvgpr_read_b32 v200, a80\n"
"v_mul_f32 v200, v200, v199\n"
"v_accvgpr_write_b32 a80, v200\n"
"v_accvgpr_read_b32 v201, a81\n"
"v_mul_f32 v201, v201, v199\n"
"v_accvgpr_write_b32 a81, v201\n"
"v_accvgpr_read_b32 v202, a82\n"
"v_mul_f32 v202, v202, v199\n"
"v_accvgpr_write_b32 a82, v202\n"
"v_accvgpr_read_b32 v203, a83\n"
"v_mul_f32 v203, v203, v199\n"
"v_accvgpr_write_b32 a83, v203\n"
"v_accvgpr_read_b32 v200, a84\n"
"v_mul_f32 v200, v200, v199\n"
"v_accvgpr_write_b32 a84, v200\n"
"v_accvgpr_read_b32 v201, a85\n"
"v_mul_f32 v201, v201, v199\n"
"v_accvgpr_write_b32 a85, v201\n"
"v_accvgpr_read_b32 v202, a86\n"
"v_mul_f32 v202, v202, v199\n"
"v_accvgpr_write_b32 a86, v202\n"
"v_accvgpr_read_b32 v203, a87\n"
"v_mul_f32 v203, v203, v199\n"
"v_accvgpr_write_b32 a87, v203\n"
"v_accvgpr_read_b32 v200, a88\n"
"v_mul_f32 v200, v200, v199\n"
"v_accvgpr_write_b32 a88, v200\n"
"v_accvgpr_read_b32 v201, a89\n"
"v_mul_f32 v201, v201, v199\n"
"v_accvgpr_write_b32 a89, v201\n"
"PG28_%=:\n"
"s_nop 0\n"
"v_accvgpr_read_b32 v202, a90\n"
"v_mul_f32 v202, v202, v199\n"
"v_accvgpr_write_b32 a90, v202\n"
"v_accvgpr_read_b32 v203, a91\n"
"v_mul_f32 v203, v203, v199\n"
"v_accvgpr_write_b32 a91, v203\n"
"v_accvgpr_read_b32 v200, a92\n"
"v_mul_f32 v200, v200, v199\n"
"v_accvgpr_write_b32 a92, v200\n"
"v_accvgpr_read_b32 v201, a93\n"
"v_mul_f32 v201, v201, v199\n"
"v_accvgpr_write_b32 a93, v201\n"
"v_accvgpr_read_b32 v202, a94\n"
"v_mul_f32 v202, v202, v199\n"
"v_accvgpr_write_b32 a94, v202\n"
"v_accvgpr_read_b32 v203, a95\n"
"v_mul_f32 v203, v203, v199\n"
"v_accvgpr_write_b32 a95, v203\n"
"v_accvgpr_read_b32 v200, a112\n"
"v_mul_f32 v200, v200, v199\n"
"v_accvgpr_write_b32 a112, v200\n"
"v_accvgpr_read_b32 v201, a113\n"
"v_mul_f32 v201, v201, v199\n"
"v_accvgpr_write_b32 a113, v201\n"
"v_accvgpr_read_b32 v202, a114\n"
"v_mul_f32 v202, v202, v199\n"
"v_accvgpr_write_b32 a114, v202\n"
"v_accvgpr_read_b32 v203, a115\n"
"v_mul_f32 v203, v203, v199\n"
"v_accvgpr_write_b32 a115, v203\n"
"v_accvgpr_read_b32 v200, a116\n"
"v_mul_f32 v200, v200, v199\n"
"v_accvgpr_write_b32 a116, v200\n"
"v_accvgpr_read_b32 v201, a117\n"
"v_mul_f32 v201, v201, v199\n"
"v_accvgpr_write_b32 a117, v201\n"
"v_accvgpr_read_b32 v202, a118\n"
"v_mul_f32 v202, v202, v199\n"
"v_accvgpr_write_b32 a118, v202\n"
"v_accvgpr_read_b32 v203, a119\n"
"v_mul_f32 v203, v203, v199\n"
"v_accvgpr_write_b32 a119, v203\n"
"v_accvgpr_read_b32 v200, a120\n"
"v_mul_f32 v200, v200, v199\n"
"v_accvgpr_write_b32 a120, v200\n"
"v_accvgpr_read_b32 v201, a121\n"
"v_mul_f32 v201, v201, v199\n"
"v_accvgpr_write_b32 a121, v201\n"
"v_accvgpr_read_b32 v202, a122\n"
"v_mul_f32 v202, v202, v199\n"
"v_accvgpr_write_b32 a122, v202\n"
"v_accvgpr_read_b32 v203, a123\n"
"v_mul_f32 v203, v203, v199\n"
"v_accvgpr_write_b32 a123, v203\n"
"v_accvgpr_read_b32 v200, a124\n"
"v_mul_f32 v200, v200, v199\n"
"v_accvgpr_write_b32 a124, v200\n"
"v_accvgpr_read_b32 v201, a125\n"
"v_mul_f32 v201, v201, v199\n"
"v_accvgpr_write_b32 a125, v201\n"
"v_accvgpr_read_b32 v202, a126\n"
"v_mul_f32 v202, v202, v199\n"
"v_accvgpr_write_b32 a126, v202\n"
"v_accvgpr_read_b32 v203, a127\n"
"v_mul_f32 v203, v203, v199\n"
"v_accvgpr_write_b32 a127, v203\n"
"v_exp_f32 v208, v64\n"
"v_exp_f32 v209, v65\n"
"v_exp_f32 v210, v66\n"
"v_mov_b32 v214, v208\n"
"v_exp_f32 v211, v67\n"
"v_mov_b32 v215, v209\n"
"v_cvt_pk_bf16_f32 v176, v208, v209\n"
"v_exp_f32 v212, v68\n"
"v_add_f32 v214, v214, v210\n"
"v_exp_f32 v213, v69\n"
"v_add_f32 v215, v215, v211\n"
"v_cvt_pk_bf16_f32 v177, v210, v211\n"
"v_exp_f32 v208, v70\n"
"v_add_f32 v214, v214, v212\n"
"v_exp_f32 v209, v71\n"
"v_add_f32 v215, v215, v213\n"
"v_cvt_pk_bf16_f32 v178, v212, v213\n"
"v_exp_f32 v210, v80\n"
"v_add_f32 v214, v214, v208\n"
"v_exp_f32 v211, v81\n"
"v_add_f32 v215, v215, v209\n"
"v_cvt_pk_bf16_f32 v179, v208, v209\n"
"v_exp_f32 v212, v82\n"
"v_mov_b32 v216, v210\n"
"v_exp_f32 v213, v83\n"
"v_mov_b32 v217, v211\n"
"v_cvt_pk_bf16_f32 v180, v210, v211\n"
"v_exp_f32 v208, v84\n"
"v_add_f32 v216, v216, v212\n"
"v_exp_f32 v209, v85\n"
"v_add_f32 v217, v217, v213\n"
"v_cvt_pk_bf16_f32 v181, v212, v213\n"
"v_exp_f32 v210, v86\n"
"v_add_f32 v216, v216, v208\n"
"v_exp_f32 v211, v87\n"
"v_add_f32 v217, v217, v209\n"
"v_cvt_pk_bf16_f32 v182, v208, v209\n"
"v_exp_f32 v212, v72\n"
"v_add_f32 v216, v216, v210\n"
"v_exp_f32 v213, v73\n"
"v_add_f32 v217, v217, v211\n"
"v_cvt_pk_bf16_f32 v183, v210, v211\n"
"v_exp_f32 v208, v74\n"
"v_add_f32 v214, v214, v212\n"
"v_exp_f32 v209, v75\n"
"v_add_f32 v215, v215, v213\n"
"v_cvt_pk_bf16_f32 v184, v212, v213\n"
"v_exp_f32 v210, v76\n"
"v_add_f32 v214, v214, v208\n"
"v_exp_f32 v211, v77\n"
"v_add_f32 v215, v215, v209\n"
"v_cvt_pk_bf16_f32 v185, v208, v209\n"
"v_exp_f32 v212, v78\n"
"v_add_f32 v214, v214, v210\n"
"v_exp_f32 v213, v79\n"
"v_add_f32 v215, v215, v211\n"
"v_cvt_pk_bf16_f32 v186, v210, v211\n"
"v_exp_f32 v208, v88\n"
"v_add_f32 v214, v214, v212\n"
"v_exp_f32 v209, v89\n"
"v_add_f32 v215, v215, v213\n"
"v_cvt_pk_bf16_f32 v187, v212, v213\n"
"v_exp_f32 v210, v90\n"
"v_add_f32 v216, v216, v208\n"
"v_exp_f32 v211, v91\n"
"v_add_f32 v217, v217, v209\n"
"v_cvt_pk_bf16_f32 v188, v208, v209\n"
"v_exp_f32 v212, v92\n"
"v_add_f32 v216, v216, v210\n"
"v_exp_f32 v213, v93\n"
"v_add_f32 v217, v217, v211\n"
"v_cvt_pk_bf16_f32 v189, v210, v211\n"
"v_exp_f32 v208, v94\n"
"v_add_f32 v216, v216, v212\n"
"v_exp_f32 v209, v95\n"
"v_add_f32 v217, v217, v213\n"
"v_cvt_pk_bf16_f32 v190, v212, v213\n"
"v_add_f32 v216, v216, v208\n"
"v_add_f32 v217, v217, v209\n"
"v_cvt_pk_bf16_f32 v191, v208, v209\n"
"v_exp_f32 v208, v96\n"
"v_exp_f32 v209, v97\n"
"v_exp_f32 v210, v98\n"
"v_add_f32 v214, v214, v208\n"
"v_exp_f32 v211, v99\n"
"v_add_f32 v215, v215, v209\n"
"v_cvt_pk_bf16_f32 v192, v208, v209\n"
"v_exp_f32 v212, v100\n"
"v_add_f32 v214, v214, v210\n"
"v_exp_f32 v213, v101\n"
"v_add_f32 v215, v215, v211\n"
"v_cvt_pk_bf16_f32 v193, v210, v211\n"
"v_exp_f32 v208, v102\n"
"v_add_f32 v214, v214, v212\n"
"v_exp_f32 v209, v103\n"
"v_add_f32 v215, v215, v213\n"
"v_cvt_pk_bf16_f32 v194, v212, v213\n"
"v_exp_f32 v210, v112\n"
"v_add_f32 v214, v214, v208\n"
"v_exp_f32 v211, v113\n"
"v_add_f32 v215, v215, v209\n"
"v_cvt_pk_bf16_f32 v195, v208, v209\n"
"v_exp_f32 v212, v114\n"
"v_add_f32 v216, v216, v210\n"
"v_exp_f32 v213, v115\n"
"v_add_f32 v217, v217, v211\n"
"v_cvt_pk_bf16_f32 v196, v210, v211\n"
"v_exp_f32 v208, v116\n"
"v_add_f32 v216, v216, v212\n"
"v_exp_f32 v209, v117\n"
"v_add_f32 v217, v217, v213\n"
"v_cvt_pk_bf16_f32 v197, v212, v213\n"
"v_exp_f32 v210, v118\n"
"v_add_f32 v216, v216, v208\n"
"v_exp_f32 v211, v119\n"
"v_add_f32 v217, v217, v209\n"
"v_cvt_pk_bf16_f32 v198, v208, v209\n"
"v_exp_f32 v212, v104\n"
"v_add_f32 v216, v216, v210\n"
"v_exp_f32 v213, v105\n"
"v_add_f32 v217, v217, v211\n"
"v_cvt_pk_bf16_f32 v199, v210, v211\n"
"v_exp_f32 v208, v106\n"
"v_add_f32 v214, v214, v212\n"
"v_exp_f32 v209, v107\n"
"v_add_f32 v215, v215, v213\n"
"v_cvt_pk_bf16_f32 v200, v212, v213\n"
"v_exp_f32 v210, v108\n"
"v_add_f32 v214, v214, v208\n"
"v_exp_f32 v211, v109\n"
"v_add_f32 v215, v215, v209\n"
"v_cvt_pk_bf16_f32 v201, v208, v209\n"
"v_exp_f32 v212, v110\n"
"v_add_f32 v214, v214, v210\n"
"v_exp_f32 v213, v111\n"
"v_add_f32 v215, v215, v211\n"
"v_cvt_pk_bf16_f32 v202, v210, v211\n"
"v_exp_f32 v208, v120\n"
"v_add_f32 v214, v214, v212\n"
"v_exp_f32 v209, v121\n"
"v_add_f32 v215, v215, v213\n"
"v_cvt_pk_bf16_f32 v203, v212, v213\n"
"v_exp_f32 v210, v122\n"
"v_add_f32 v216, v216, v208\n"
"v_exp_f32 v211, v123\n"
"v_add_f32 v217, v217, v209\n"
"v_cvt_pk_bf16_f32 v204, v208, v209\n"
"v_exp_f32 v212, v124\n"
"v_add_f32 v216, v216, v210\n"
"v_exp_f32 v213, v125\n"
"v_add_f32 v217, v217, v211\n"
"v_cvt_pk_bf16_f32 v205, v210, v211\n"
"v_exp_f32 v208, v126\n"
"v_add_f32 v216, v216, v212\n"
"v_exp_f32 v209, v127\n"
"v_add_f32 v217, v217, v213\n"
"v_cvt_pk_bf16_f32 v206, v212, v213\n"
"v_add_f32 v216, v216, v208\n"
"v_add_f32 v217, v217, v209\n"
"v_cvt_pk_bf16_f32 v207, v208, v209\n"
"v_add_f32 v218, v214, v215\n"
"v_add_f32 v219, v216, v217\n"
"s_nop 3\n"
"s_cmp_eq_u32 s41, 1\n"
"s_cbranch_scc1 RET_MID_0_%=\n"
"s_cmp_eq_u32 s41, 2\n"
"s_cbranch_scc1 RET_MIDM_0_%=\n"
"s_cmp_eq_u32 s41, 3\n"
"s_cbranch_scc1 RET_MIDR_0_%=\n"
"s_branch RET_DRAINA_0_%=\n"
    :: "s"(ka), "s"(b), "s"(w), "s"(lb)
    : "v0","v1","v2","v3","v4","v5","v6","v7","v8","v9","v10","v11","v12","v13","v14","v15","v16","v17","v18","v19","v20","v21","v22","v23","v24","v25","v26","v27","v28","v29","v30","v31","v32","v33","v34","v35","v36","v37","v38","v39","v40","v41","v42","v43","v44","v45","v46","v47","v48","v49","v50","v51","v52","v53","v54","v55","v56","v57","v58","v59","v60","v61","v62","v63","v64","v65","v66","v67","v68","v69","v70","v71","v72","v73","v74","v75","v76","v77","v78","v79","v80","v81","v82","v83","v84","v85","v86","v87","v88","v89","v90","v91","v92","v93","v94","v95","v96","v97","v98","v99","v100","v101","v102","v103","v104","v105","v106","v107","v108","v109","v110","v111","v112","v113","v114","v115","v116","v117","v118","v119","v120","v121","v122","v123","v124","v125","v126","v127","v128","v129","v130","v131","v132","v133","v134","v135","v136","v137","v138","v139","v140","v141","v142","v143","v144","v145","v146","v147","v148","v149","v150","v151","v152","v153","v154","v155","v156","v157","v158","v159","v160","v161","v162","v163","v164","v165","v166","v167","v168","v169","v170","v171","v172","v173","v174","v175","v176","v177","v178","v179","v180","v181","v182","v183","v184","v185","v186","v187","v188","v189","v190","v191","v192","v193","v194","v195","v196","v197","v198","v199","v200","v201","v202","v203","v204","v205","v206","v207","v208","v209","v210","v211","v212","v213","v214","v215","v216","v217","v218","v219","v220","v221","v222","v223","v224","v225","v226","v227","v228","v229","v230","v231","v232","v233","v234","v235","v236","v237","v238","v239","v240","v241","v242","v243","v244","v245","v246","v247","v248","v249","v250","v251","v252","v253","v254","v255","a0","a1","a2","a3","a4","a5","a6","a7","a8","a9","a10","a11","a12","a13","a14","a15","a16","a17","a18","a19","a20","a21","a22","a23","a24","a25","a26","a27","a28","a29","a30","a31","a32","a33","a34","a35","a36","a37","a38","a39","a40","a41","a42","a43","a44","a45","a46","a47","a48","a49","a50","a51","a52","a53","a54","a55","a56","a57","a58","a59","a60","a61","a62","a63","a64","a65","a66","a67","a68","a69","a70","a71","a72","a73","a74","a75","a76","a77","a78","a79","a80","a81","a82","a83","a84","a85","a86","a87","a88","a89","a90","a91","a92","a93","a94","a95","a96","a97","a98","a99","a100","a101","a102","a103","a104","a105","a106","a107","a108","a109","a110","a111","a112","a113","a114","a115","a116","a117","a118","a119","a120","a121","a122","a123","a124","a125","a126","a127","a128","a129","a130","a131","a132","a133","a134","a135","a136","a137","a138","a139","a140","a141","a142","a143","a144","a145","a146","a147","a148","a149","a150","a151","a152","a153","a154","a155","a156","a157","a158","a159","a160","a161","a162","a163","a164","a165","a166","a167","a168","a169","a170","a171","a172","a173","a174","a175","a176","a177","a178","a179","a180","a181","a182","a183","a184","a185","a186","a187","a188","a189","a190","a191","a192","a193","a194","a195","a196","a197","a198","a199","a200","a201","a202","a203","a204","a205","a206","a207","a208","a209","a210","a211","a212","a213","a214","a215","a216","a217","a218","a219","a220","a221","a222","a223","a224","a225","a226","a227","a228","a229","a230","a231","a232","a233","a234","a235","a236","a237","a238","a239","a240","a241","a242","a243","a244","a245","a246","a247","a248","a249","a250","a251","a252","a253","a254","a255","s8","s9","s10","s11","s12","s13","s14","s15","s16","s17","s18","s19","s20","s21","s22","s23","s24","s25","s26","s27","s28","s29","s30","s31","s32","s33","s34","s35","s36","s37","s38","s39","s40","s41","s42","s43","s44","s45","s46","s47","s48","s49","s50","s51","s52","s53","s54","s55","s56","s57","s58","s59","s60","s61","s62","s63","s64","s65","s66","s67","s68","s69","s70","s71","s72","s73","s74","s75","s76","s77","s78","s79","s80","s81","s82","s83","s84","s85","s86","s87","s88","s89","s90","s91","s92","s93","s94","s95","s96","s97","s98","s99","s100","s101","vcc","scc","memory");
}

constexpr int which = 0x1ff;
extern "C" void kernel_launch(void* const* d_in, const int* in_sizes, int n_in, void* d_out, int out_size, void* d_ws, size_t ws_size, hipStream_t st) {
  char* ws = (char*)d_ws;
  (void)hipFuncSetAttribute((const void*)mega, hipFuncAttributeMaxDynamicSharedMemorySize, 163840);
  if (which & 2) mega<<<256, 256, 163840, st>>>(d_in[1], d_in[3], d_in[4], d_in[2], ws, d_in[0], d_out, (unsigned*)(ws + (126u << 20)), d_in[5], d_in[7], d_in[8], d_in[9], d_in[10], d_in[11], d_in[12]);
}
```
